```python
import torch

def _softmax(x, axis=-1):
    return torch.softmax(x, dim=axis)
def _gelu(x):
    return torch.nn.functional.gelu(x, approximate="tanh")
def _silu(x):
    return torch.nn.functional.silu(x)
def _sigmoid(x):
    return torch.sigmoid(x)
def _layernorm(x, axis=-1, eps=1e-5):
    m = x.mean(dim=axis, keepdim=True)
    v = x.var(dim=axis, keepdim=True, unbiased=False)
    return (x - m) / torch.sqrt(v + eps)
def _scatter_add(tgt, idx, src, axis=0):
    return tgt.clone().index_add_(axis, idx, src)
def _segment_sum(src, seg, n):
    out = torch.zeros((n,) + src.shape[1:], dtype=src.dtype)
    return out.index_add_(0, seg, src)
def _pad(x, pw):
    flat = tuple(v for pair in reversed(pw) for v in pair)
    return torch.nn.functional.pad(x, flat)

kernel_name = 'recurrent_scan__gqa_block_cached__glu_gated_geglu__prompt'

def setup_inputs():

    _w = (lambda a: (lambda t: t.to(torch.bfloat16) if t.is_floating_point() else t)(torch.as_tensor(a)))
    def _A_setup():
        B, Sq, D = 1, 2048, 576
        Hq, Hkv, Sk, Dh = 32, 16, 2048, 128
        assert Hq % Hkv == 0, (Hq, Hkv)
        return {
            "x": _w((lambda *s: torch.randn(*s, dtype=torch.bfloat16) * 0.05)(B, Sq, D)),
            "k_cache": _w((lambda *s: torch.randn(*s, dtype=torch.bfloat16) * 3.2)(B, Hkv, Sk, Dh)),
            "v_cache": _w((lambda *s: torch.randn(*s, dtype=torch.bfloat16) * 3.2)(B, Hkv, Sk, Dh)),
            "wq": _w((lambda *s: torch.randn(*s, dtype=torch.bfloat16) * 3.2)(D, Hq * Dh) * (D ** -0.5)),
            "wo": _w((lambda *s: torch.randn(*s, dtype=torch.bfloat16) * 3.2)(Hq * Dh, D) * ((Hq * Dh) ** -0.5)),
        }
    _A_in = _A_setup()

    def _B_setup():
        B, S, Dm, Df = 1, 2048, 576, 4608
        return {
            "x": _w((lambda *s: torch.randn(*s, dtype=torch.bfloat16) * 0.05)(B, S, Dm)),
            "w_gate": _w((lambda *s: torch.randn(*s, dtype=torch.bfloat16) * 0.4)(Dm, Df) * (Dm ** -0.5)),
            "w_up": _w((lambda *s: torch.randn(*s, dtype=torch.bfloat16) * 0.4)(Dm, Df) * (Dm ** -0.5)),
            "w_down": _w((lambda *s: torch.randn(*s, dtype=torch.bfloat16) * 0.4)(Df, Dm) * (Df ** -0.5)),
        }
    _B_in = _B_setup()

    return {
        "A_x": _A_in["x"],
        "A_k_cache": _A_in["k_cache"],
        "A_v_cache": _A_in["v_cache"],
        "A_wq": _A_in["wq"],
        "A_wo": _A_in["wo"],
        "B_x": _B_in["x"],
        "B_w_gate": _B_in["w_gate"],
        "B_w_up": _B_in["w_up"],
        "B_w_down": _B_in["w_down"],
    }

def reference(A_x, A_k_cache, A_v_cache, A_wq, A_wo, B_x, B_w_gate, B_w_up, B_w_down):
    def _A_ref(x, k_cache, v_cache, wq, wo):
        B, Sq, D = x.shape
        Hq, Dh = 32, 128
        Hkv = k_cache.shape[1]
        rep = Hq // Hkv
        q = (lambda x, *d: x.permute(*d))((x @ wq).reshape(B, Sq, Hq, Dh), 0, 2, 1, 3)
        k = (lambda x, n, axis: torch.repeat_interleave(x, n, dim=axis))(k_cache, rep, axis=1)
        v = (lambda x, n, axis: torch.repeat_interleave(x, n, dim=axis))(v_cache, rep, axis=1)
        scores = torch.einsum("bhqd,bhkd->bhqk", q, k) * (Dh ** -0.5)
        p = _softmax(scores, axis=-1)
        o = torch.einsum("bhqk,bhkd->bhqd", p, v)
        return (lambda x, *d: x.permute(*d))(o, 0, 2, 1, 3).reshape(B, Sq, Hq * Dh) @ wo

    def _B_ref(x, w_gate, w_up, w_down):
        g = torch.einsum("bsd,df->bsf", x, w_gate)
        u = torch.einsum("bsd,df->bsf", x, w_up)
        h = _gelu(g) * u
        return torch.einsum("bsf,fd->bsd", h, w_down)

    x = A_x
    xn = _layernorm(x, axis=-1)
    s = _A_ref(xn, A_k_cache, A_v_cache, A_wq, A_wo)
    g = _silu(_B_ref(xn, B_w_gate, B_w_up, B_w_down))
    return x + s * g
```

```cpp
#include <hip/hip_runtime.h>
#include <hip/hip_bf16.h>
#include <stdint.h>
typedef unsigned short u16;
__device__ __forceinline__ float bf2f(u16 v){ return __uint_as_float(((unsigned)v)<<16); }
__device__ __forceinline__ u16 f2bf(float f){ unsigned u=__float_as_uint(f); u += 0x7FFF + ((u>>16)&1); return (u16)(u>>16); }
__device__ __forceinline__ float rbf(float f){ return bf2f(f2bf(f)); }
constexpr int S=2048, DM=576, HQ=32, HKV=16, DH=128, DF=4608;

__global__ void ln_k(const u16* x, u16* xn){
  int row=blockIdx.x, t=threadIdx.x;
  float v[9]; float s=0;
  for(int i=0;i<9;i++){ v[i]=bf2f(x[row*DM+t+64*i]); s+=v[i]; }
  for(int o=32;o;o>>=1) s+=__shfl_xor(s,o);
  float mean=s/DM; float q=0;
  for(int i=0;i<9;i++){ float d=v[i]-mean; q+=d*d; }
  for(int o=32;o;o>>=1) q+=__shfl_xor(q,o);
  float m=rbf(mean), var=rbf(q/DM);
  float sd=rbf(sqrtf(rbf(var+1e-5f)));
  for(int i=0;i<9;i++) xn[row*DM+t+64*i]=f2bf(rbf(v[i]-m)/sd);
}
__global__ void gemm_k(const u16* A, const u16* B, float* C, int M,int N,int K){
  __shared__ float As[16][65], Bs[16][65];
  int bm=blockIdx.y*64, bn=blockIdx.x*64, t=threadIdx.x, tx=t&15, ty=t>>4;
  float acc[4][4]={};
  for(int k0=0;k0<K;k0+=16){
    for(int i=t;i<64*16;i+=256){ int r=i>>4,c=i&15; As[c][r]=bf2f(A[(size_t)(bm+r)*K+k0+c]); }
    for(int i=t;i<64*16;i+=256){ int r=i>>6,c=i&63; Bs[r][c]=bf2f(B[(size_t)(k0+r)*N+bn+c]); }
    __syncthreads();
    for(int k=0;k<16;k++){
      float a[4],b[4];
      for(int i=0;i<4;i++){a[i]=As[k][ty*4+i]; b[i]=Bs[k][tx*4+i];}
      for(int i=0;i<4;i++)for(int j=0;j<4;j++)acc[i][j]+=a[i]*b[j];
    }
    __syncthreads();
  }
  for(int i=0;i<4;i++)for(int j=0;j<4;j++) C[(size_t)(bm+ty*4+i)*N+bn+tx*4+j]=acc[i][j];
}
__global__ void cvt_k(const float* a, u16* o, int n){ int i=blockIdx.x*256+threadIdx.x; if(i<n) o[i]=f2bf(a[i]); }
__global__ void geglu_k(const float* g, const float* u, u16* h, int n){ int i=blockIdx.x*256+threadIdx.x; if(i<n){
  float x=g[i]; float t=tanhf(0.7978845608028654f*(x+0.044715f*x*x*x)); h[i]=f2bf(0.5f*x*(1.f+t)*u[i]); } }
__global__ __launch_bounds__(128) void attn_k(const u16* q, const u16* k, const u16* v, u16* o){
  __shared__ float qs[16][DH]; __shared__ float ps[16][S/2];
  int h=blockIdx.y, q0=blockIdx.x*16, t=threadIdx.x, kv=h/2;
  for(int i=t;i<16*DH;i+=128){ int r=i/DH,c=i%DH; qs[r][c]=bf2f(q[(size_t)(q0+r)*(HQ*DH)+h*DH+c])*0.08838834764831845f; }
  __syncthreads();
  float acc[16]={}; float m[16], l[16];
  for(int r=0;r<16;r++){m[r]=-1e30f;l[r]=0;}
  __shared__ float red[16];
  for(int half=0;half<2;half++){
    for(int j=t;j<S/2;j+=128){
      const u16* kr=k+((size_t)kv*S+half*(S/2)+j)*DH; float s[16]={};
      for(int d=0;d<DH;d++){ float kk=bf2f(kr[d]); for(int r=0;r<16;r++) s[r]+=qs[r][d]*kk; }
      for(int r=0;r<16;r++) ps[r][j]=s[r];
    }
    __syncthreads();
    if(t<16){ float mx=-1e30f; for(int j=0;j<S/2;j++) mx=fmaxf(mx,ps[t][j]); red[t]=mx; }
    __syncthreads();
    for(int r=0;r<16;r++){ float mn=fmaxf(m[r],red[r]); float a=expf(m[r]-mn); acc[r]*=a; l[r]*=a; m[r]=mn; }
    __syncthreads();
    for(int i=t;i<16*(S/2);i+=128){ int r=i/(S/2), j=i%(S/2); float mm=0; for(int rr=0;rr<16;rr++) if(rr==r) mm=m[rr]; ps[r][j]=expf(ps[r][j]-mm); }
    __syncthreads();
    for(int j=0;j<S/2;j++){ float vv=bf2f(v[((size_t)kv*S+half*(S/2)+j)*DH+t]); for(int r=0;r<16;r++){ float p=ps[r][j]; acc[r]+=p*vv; l[r]+=p; } }
    __syncthreads();
  }
  for(int r=0;r<16;r++) o[(size_t)(q0+r)*(HQ*DH)+h*DH+t]=f2bf(acc[r]/l[r]);
}
__global__ void fin_k(const u16* x, const float* s, const float* f, u16* out, int n){ int i=blockIdx.x*256+threadIdx.x; if(i<n){
  float ff=f[i]; float g=ff/(1.f+expf(-ff)); out[i]=f2bf(bf2f(x[i])+s[i]*g); } }

extern "C" void kernel_launch(void* const* d_in, const int* in_sizes, int n_in, void* d_out, int out_size, void* d_ws, size_t ws_size, hipStream_t stream){
  const u16* x=(const u16*)d_in[0]; const u16* kc=(const u16*)d_in[1]; const u16* vc=(const u16*)d_in[2];
  const u16* wq=(const u16*)d_in[3]; const u16* wo=(const u16*)d_in[4];
  const u16* wg=(const u16*)d_in[6]; const u16* wu=(const u16*)d_in[7]; const u16* wd=(const u16*)d_in[8];
  char* ws=(char*)d_ws;
  u16* xn=(u16*)ws; ws+=S*DM*2;
  float* t0=(float*)ws; ws+=(size_t)S*DF*4;
  float* t1=(float*)ws; ws+=(size_t)S*DF*4;
  u16* qb=(u16*)ws; ws+=(size_t)S*HQ*DH*2;
  u16* ob=(u16*)ws; ws+=(size_t)S*HQ*DH*2;
  u16* hb=(u16*)ws; ws+=(size_t)S*DF*2;
  float* sf=(float*)ws; ws+=(size_t)S*DM*4;
  float* ff=(float*)ws; ws+=(size_t)S*DM*4;
  ln_k<<<S,64,0,stream>>>(x,xn);
  gemm_k<<<dim3(HQ*DH/64,S/64),256,0,stream>>>(xn,wq,t0,S,HQ*DH,DM);
  cvt_k<<<S*HQ*DH/256,256,0,stream>>>(t0,qb,S*HQ*DH);
  attn_k<<<dim3(S/16,HQ),128,0,stream>>>(qb,kc,vc,ob);
  gemm_k<<<dim3(DM/64,S/64),256,0,stream>>>(ob,wo,sf,S,DM,HQ*DH);
  gemm_k<<<dim3(DF/64,S/64),256,0,stream>>>(xn,wg,t0,S,DF,DM);
  gemm_k<<<dim3(DF/64,S/64),256,0,stream>>>(xn,wu,t1,S,DF,DM);
  geglu_k<<<S*DF/256,256,0,stream>>>(t0,t1,hb,S*DF);
  gemm_k<<<dim3(DM/64,S/64),256,0,stream>>>(hb,wd,ff,S,DM,DF);
  fin_k<<<S*DM/256,256,0,stream>>>(x,sf,ff,(u16*)d_out,S*DM);
}
```

```cpp
#include <hip/hip_runtime.h>
#include <hip/hip_bf16.h>
#include <stdint.h>
typedef unsigned short u16;
#define CLOB "v0","v1","v2","v3","v4","v5","v6","v7","v8","v9","v10","v11","v12","v13","v14","v15","v16","v17","v18","v19","v20","v21","v22","v23","v24","v25","v26","v27","v28","v29","v30","v31","v32","v33","v34","v35","v36","v37","v38","v39","v40","v41","v42","v43","v44","v45","v46","v47","v48","v49","v50","v51","v52","v53","v54","v55","v56","v57","v58","v59","v60","v61","v62","v63","v64","v65","v66","v67","v68","v69","v70","v71","v72","v73","v74","v75","v76","v77","v78","v79","v80","v81","v82","v83","v84","v85","v86","v87","v88","v89","v90","v91","v92","v93","v94","v95","v96","v97","v98","v99","v100","v101","v102","v103","v104","v105","v106","v107","v108","v109","v110","v111","v112","v113","v114","v115","v116","v117","v118","v119","v120","v121","v122","v123","v124","v125","v126","v127","v128","v129","v130","v131","v132","v133","v134","v135","v136","v137","v138","v139","v140","v141","v142","v143","v144","v145","v146","v147","v148","v149","v150","v151","v152","v153","v154","v155","v156","v157","v158","v159","v160","v161","v162","v163","v164","v165","v166","v167","v168","v169","v170","v171","v172","v173","v174","v175","v176","v177","v178","v179","v180","v181","v182","v183","v184","v185","v186","v187","v188","v189","v190","v191","v192","v193","v194","v195","v196","v197","v198","v199","v200","v201","v202","v203","v204","v205","v206","v207","v208","v209","v210","v211","v212","v213","v214","v215","v216","v217","v218","v219","v220","v221","v222","v223","v224","v225","v226","v227","v228","v229","v230","v231","v232","v233","v234","v235","v236","v237","v238","v239","v240","v241","v242","v243","v244","v245","v246","v247","v248","v249","v250","v251","v252","v253","v254","v255","a0","a1","a2","a3","a4","a5","a6","a7","a8","a9","a10","a11","a12","a13","a14","a15","a16","a17","a18","a19","a20","a21","a22","a23","a24","a25","a26","a27","a28","a29","a30","a31","a32","a33","a34","a35","a36","a37","a38","a39","a40","a41","a42","a43","a44","a45","a46","a47","a48","a49","a50","a51","a52","a53","a54","a55","a56","a57","a58","a59","a60","a61","a62","a63","a64","a65","a66","a67","a68","a69","a70","a71","a72","a73","a74","a75","a76","a77","a78","a79","a80","a81","a82","a83","a84","a85","a86","a87","a88","a89","a90","a91","a92","a93","a94","a95","a96","a97","a98","a99","a100","a101","a102","a103","a104","a105","a106","a107","a108","a109","a110","a111","a112","a113","a114","a115","a116","a117","a118","a119","a120","a121","a122","a123","a124","a125","a126","a127","a128","a129","a130","a131","a132","a133","a134","a135","a136","a137","a138","a139","a140","a141","a142","a143","a144","a145","a146","a147","a148","a149","a150","a151","a152","a153","a154","a155","a156","a157","a158","a159","a160","a161","a162","a163","a164","a165","a166","a167","a168","a169","a170","a171","a172","a173","a174","a175","a176","a177","a178","a179","a180","a181","a182","a183","a184","a185","a186","a187","a188","a189","a190","a191","a192","a193","a194","a195","a196","a197","a198","a199","a200","a201","a202","a203","a204","a205","a206","a207","a208","a209","a210","a211","a212","a213","a214","a215","a216","a217","a218","a219","a220","a221","a222","a223","a224","a225","a226","a227","a228","a229","a230","a231","a232","a233","a234","a235","a236","a237","a238","a239","a240","a241","a242","a243","a244","a245","a246","a247","a248","a249","a250","a251","a252","a253","a254","a255","s4","s5","s6","s7","s8","s9","s10","s11","s12","s13","s14","s15","s16","s17","s18","s19","s20","s21","s22","s23","s24","s100","s101","s25","s26","s27","s28","s29","s30","s31","s32","s33","s34","s35","s36","s37","s38","s39","s40","s41","s42","s43","s44","s45","s46","s47","s48","s49","s50","s51","s52","s53","s54","s55","s56","s57","s58","s59","s60","s61","s62","s63","s64","s65","s66","s67","s68","s69","s70","s71","s72","s73","s74","s75","s76","s77","s78","s79","s80","s81","s82","s83","s84","s85","s86","s87","s88","s89","s90","s91","s92","s93","s94","s95","s96","s97","s98","s99","vcc","scc","memory"
constexpr size_t WS_XN=0, WS_H=0x400000, WS_O=0x1800000, WS_P=0x2800000, WS_DBG=0x6000000;
#define FUSED 2
__global__ __launch_bounds__(256,1) void k1(const void* ws, const void* wq, const void* wg, const void* wu, const void* k, const void* v, const void* wd, const void* wo, const void* x, const void* out){
  unsigned w=__builtin_amdgcn_readfirstlane(threadIdx.x>>6);
  asm volatile(
    "s_load_dwordx16 s[4:19], %2, 0x0\n"
    "s_load_dwordx4 s[20:23], %2, 0x40\n"
    "s_mov_b32 s80, %0\n"
    "s_and_b32 s81, %1, 7\n"
    "s_lshl_b32 s81, s81, 2\n"
    "s_lshr_b32 s83, %1, 3\n"
    "s_lshr_b32 s82, s83, 2\n"
    "s_and_b32 s83, s83, 3\n"
    "s_add_u32 s81, s81, s83\n"
    "s_lshl_b32 s83, s82, 5\n"
    "s_add_u32 s83, s83, s81\n"
    "s_mul_i32 s83, s83, 9216\n"
    "v_mbcnt_lo_u32_b32 v12, -1, 0\n"
    "v_mbcnt_hi_u32_b32 v12, -1, v12\n"
    "s_cmp_eq_u32 s80, 3\n"
    "s_cselect_b32 s84, 0, 9216\n"
    "s_waitcnt lgkmcnt(0)\n"
    "s_mov_b64 s[24:25], s[20:21]\n"
    "s_add_u32 s24, s24, s83\n"
    "s_addc_u32 s25, s25, 0\n"
    "s_mov_b32 s26, s84\n"
    "s_mov_b32 s27, 0x20000\n"
    "v_and_b32 v13, 7, v12\n"
    "v_and_b32 v14, 24, v12\n"
    "v_and_b32 v15, 32, v12\n"
    "v_mul_u32_u24 v16, 1152, v13\n"
    "v_add_u32 v16, v16, v14\n"
    "v_lshl_add_u32 v16, v15, 1, v16\n"
    "s_mul_i32 s83, s80, 384\n"
    "buffer_load_dwordx2 v[20:21], v16, s[24:27], s83 offen offset:0\n"
    "buffer_load_dwordx2 v[22:23], v16, s[24:27], s83 offen offset:32\n"
    "buffer_load_dwordx2 v[24:25], v16, s[24:27], s83 offen offset:128\n"
    "buffer_load_dwordx2 v[26:27], v16, s[24:27], s83 offen offset:160\n"
    "buffer_load_dwordx2 v[28:29], v16, s[24:27], s83 offen offset:256\n"
    "buffer_load_dwordx2 v[30:31], v16, s[24:27], s83 offen offset:288\n"
    "s_mov_b64 s[76:77], s[4:5]\n"
    "s_add_u32 s76, s76, 3145728\n"
    "s_addc_u32 s77, s77, 0\n"
    "s_mov_b32 s78, 132096\n"
    "s_mov_b32 s79, 0x20000\n"
    "s_lshl_b32 s83, s82, 7\n"
    "s_add_u32 s83, s83, 131072\n"
    "buffer_load_dwordx2 v[46:47], off, s[76:79], s83 sc1\n"
    "v_lshlrev_b32 v17, 4, v13\n"
    "v_lshl_add_u32 v17, v14, 5, v17\n"
    "v_lshl_add_u32 v17, v15, 7, v17\n"
    "v_lshlrev_b32 v32, 3, v13\n"
    "v_add_u32 v32, 147456, v32\n"
    "s_lshl_b32 s83, s80, 6\n"
    "v_add_u32 v33, s83, v32\n"
    "v_mov_b32 v35, -1.0\n"
    "ds_write_b64 v33, v[34:35]\n"
    "v_and_b32 v19, 31, v12\n"
    "v_lshlrev_b32 v19, 7, v19\n"
    "s_lshl_b32 s84, s82, 2\n"
    "s_add_u32 s84, s84, s80\n"
    "s_lshl_b32 s83, s81, 8\n"
    "s_sub_u32 s85, 4718592, s83\n"
    "s_mov_b64 s[40:41], s[6:7]\n"
    "s_add_u32 s40, s40, s83\n"
    "s_addc_u32 s41, s41, 0\n"
    "s_mov_b32 s42, s85\n"
    "s_mov_b32 s43, 0x20000\n"
    "s_mul_i32 s83, s84, 73728\n"
    "s_mov_b64 s[36:37], s[4:5]\n"
    "s_add_u32 s36, s36, s83\n"
    "s_addc_u32 s37, s37, 0\n"
    "s_mov_b32 s38, 73728\n"
    "s_mov_b32 s39, 0x20000\n"
    "v_lshlrev_b32 v0, 4, v12\n"
    "v_lshrrev_b32 v13, 4, v12\n"
    "v_lshlrev_b32 v14, 2, v13\n"
    "v_and_b32 v15, 15, v12\n"
    "v_xor_b32 v14, v14, v15\n"
    "v_lshlrev_b32 v14, 4, v14\n"
    "s_lshl_b32 s83, s80, 3\n"
    "v_add_u32 v13, s83, v13\n"
    "v_lshlrev_b32 v13, 13, v13\n"
    "v_add_u32 v1, v13, v14\n"
    "v_add_u32 v2, 31744, v1\n"
    "s_lshl_b32 s86, s80, 11\n"
    "s_lshl_b32 s83, s80, 16\n"
    "v_subrev_u32 v1, s83, v1\n"
    "v_subrev_u32 v2, s83, v2\n"
    "s_mul_i32 s87, s83, 18\n"
    "s_mul_i32 m0, s86, 18\n"
    "s_waitcnt lgkmcnt(0)\n"
    "s_barrier\n"
    "s_lshl_b32 s83, s82, 5\n"
    "s_add_u32 s83, s83, s81\n"
    "s_lshl_b32 s83, s83, 2\n"
    "s_add_u32 s83, s83, s80\n"
    "s_lshl_b32 s83, s83, 7\n"
    "v_mov_b32 v11, 0x40000000\n"
    "v_writelane_b32 v11, s83, 0\n"
    "s_cmp_eq_u32 s80, 3\n"
    "s_cbranch_scc0 L_LN%=\n"
    "s_getpc_b64 s[32:33]\n"
    "L_PC%=:\n"
    "s_mov_b32 s34, L_END%=-L_PC%=\n"
    "s_mov_b32 s35, 0x20000\n"
    "s_lshr_b32 s83, %1, 3\n"
    "s_lshl_b32 s83, s83, 13\n"
    "v_lshlrev_b32 v34, 7, v12\n"
    "buffer_load_dword v60, v34, s[32:35], s83 offen\n"
    "s_sleep 8\n"
    "v_lshrrev_b32 v34, 1, v12\n"
    "v_lshlrev_b32 v34, 13, v34\n"
    "v_and_b32 v35, 1, v12\n"
    "v_lshl_add_u32 v34, v35, 7, v34\n"
    "s_mul_i32 s83, s82, 589824\n"
    "buffer_load_dword v60, v34, s[40:43], s83 offen\n"
    "s_add_u32 s83, s83, 262144\n"
    "buffer_load_dword v60, v34, s[40:43], s83 offen\n"
    "s_add_u32 s83, s83, 65536\n"
    "s_mov_b64 exec, 0xffff\n"
    "s_add_u32 s83, s83, 196608\n"
    "buffer_load_dword v60, v34, s[40:43], s83 offen\n"
    "s_mov_b64 exec, -1\n"
    "s_waitcnt vmcnt(4)\n"
    "v_readfirstlane_b32 s95, v46\n"
    "v_add_u32 v46, 1, v46\n"
    "buffer_store_dwordx2 v[46:47], v11, s[76:79], 0 offen sc1\n"
    "s_waitcnt vmcnt(0)\n"
    "L_D2%=:\n"
    "s_waitcnt vmcnt(60)\n"
    "buffer_load_dwordx4 v1, s[40:43], s87 offen lds\n"
    "buffer_load_dwordx4 v2, s[40:43], s87 offen offset:1024 lds\n"
    "s_add_u32 m0, m0, 2048\n"
    "s_add_u32 s87, s87, 65536\n"
    "s_cmp_lt_u32 s87, 4718592\n"
    "s_cbranch_scc1 L_D2%=\n"
    "s_lshl_b32 s84, s82, 2\n"
    "s_add_u32 s84, s84, s80\n"
    "s_mov_b64 s[44:45], s[8:9]\n"
    "s_mov_b32 s46, 5308416\n"
    "s_mov_b32 s47, 0x20000\n"
    "s_mov_b64 s[48:49], s[10:11]\n"
    "s_sub_u32 s48, s48, 1024\n"
    "s_subb_u32 s49, s49, 0\n"
    "s_mov_b32 s50, 5309440\n"
    "s_mov_b32 s51, 0x20000\n"
    "s_mul_i32 s83, s84, 589824\n"
    "s_mov_b64 s[52:53], s[4:5]\n"
    "s_add_u32 s52, s52, s83\n"
    "s_addc_u32 s53, s53, 0\n"
    "s_add_u32 s52, s52, 4194304\n"
    "s_addc_u32 s53, s53, 0\n"
    "s_mov_b32 s54, 589824\n"
    "s_mov_b32 s55, 0x20000\n"
    "s_lshl_b32 s83, s84, 19\n"
    "s_lshl_b32 s85, s81, 14\n"
    "s_add_u32 s83, s83, s85\n"
    "s_mov_b64 s[64:65], s[4:5]\n"
    "s_add_u32 s64, s64, s83\n"
    "s_addc_u32 s65, s65, 0\n"
    "s_add_u32 s64, s64, 25165824\n"
    "s_addc_u32 s65, s65, 0\n"
    "s_mov_b32 s66, 16384\n"
    "s_mov_b32 s67, 0x20000\n"
    "s_lshr_b32 s83, s81, 1\n"
    "s_lshl_b32 s83, s83, 19\n"
    "s_mov_b64 s[56:57], s[12:13]\n"
    "s_add_u32 s56, s56, s83\n"
    "s_addc_u32 s57, s57, 0\n"
    "s_mov_b32 s58, 524288\n"
    "s_mov_b32 s59, 0x20000\n"
    "s_mov_b64 s[60:61], s[14:15]\n"
    "s_add_u32 s60, s60, s83\n"
    "s_addc_u32 s61, s61, 0\n"
    "s_mov_b32 s62, 524288\n"
    "s_mov_b32 s63, 0x20000\n"
    "s_lshr_b32 s83, s81, 1\n"
    "s_mul_i32 s83, s83, 9\n"
    "s_and_b32 s85, s81, 1\n"
    "s_mul_i32 s85, s85, 5\n"
    "s_add_u32 s83, s83, s85\n"
    "s_lshl_b32 s89, s83, 12\n"
    "s_lshl_b32 s90, s83, 6\n"
    "v_lshlrev_b32 v10, 3, v12\n"
    "v_lshrrev_b32 v13, 2, v12\n"
    "v_and_b32 v14, 3, v12\n"
    "v_lshlrev_b32 v15, 8, v13\n"
    "v_lshl_add_u32 v15, v14, 4, v15\n"
    "v_and_b32 v8, 3, v13\n"
    "v_xor_b32 v3, 0, v8\n"
    "v_lshl_add_u32 v3, v3, 6, v15\n"
    "v_xor_b32 v4, 1, v8\n"
    "v_lshl_add_u32 v4, v4, 6, v15\n"
    "v_xor_b32 v5, 2, v8\n"
    "v_lshl_add_u32 v5, v5, 6, v15\n"
    "v_xor_b32 v6, 3, v8\n"
    "v_lshl_add_u32 v6, v6, 6, v15\n"
    "v_bfe_u32 v15, v12, 4, 1\n"
    "v_lshlrev_b32 v8, 6, v13\n"
    "v_lshl_add_u32 v8, v15, 5, v8\n"
    "v_lshl_add_u32 v8, v14, 3, v8\n"
    "v_add_u32 v9, 36864, v8\n"
    "v_lshlrev_b32 v15, 1, v15\n"
    "v_xor_b32 v15, v15, v14\n"
    "s_lshl_b32 s83, s80, 4\n"
    "v_add_u32 v13, s83, v13\n"
    "v_mul_u32_u24 v13, 9216, v13\n"
    "v_lshl_add_u32 v7, v15, 4, v13\n"
    "v_lshrrev_b32 v13, 4, v12\n"
    "v_and_b32 v14, 15, v12\n"
    "v_add_u32 v15, 0, v13\n"
    "v_xor_b32 v15, v15, v14\n"
    "v_lshlrev_b32 v15, 4, v15\n"
    "v_lshl_add_u32 v40, v13, 8, v15\n"
    "v_add_u32 v15, 4, v13\n"
    "v_xor_b32 v15, v15, v14\n"
    "v_lshlrev_b32 v15, 4, v15\n"
    "v_lshl_add_u32 v41, v13, 8, v15\n"
    "v_add_u32 v15, 8, v13\n"
    "v_xor_b32 v15, v15, v14\n"
    "v_lshlrev_b32 v15, 4, v15\n"
    "v_lshl_add_u32 v42, v13, 8, v15\n"
    "v_add_u32 v15, 12, v13\n"
    "v_xor_b32 v15, v15, v14\n"
    "v_lshlrev_b32 v15, 4, v15\n"
    "v_lshl_add_u32 v43, v13, 8, v15\n"
    "v_add_u32 v15, 0, v13\n"
    "v_lshlrev_b32 v15, 1, v15\n"
    "v_xor_b32 v15, v15, v14\n"
    "v_lshlrev_b32 v15, 4, v15\n"
    "v_lshl_add_u32 v44, v13, 8, v15\n"
    "v_add_u32 v15, 4, v13\n"
    "v_lshlrev_b32 v15, 1, v15\n"
    "v_xor_b32 v15, v15, v14\n"
    "v_lshlrev_b32 v15, 4, v15\n"
    "v_lshl_add_u32 v45, v13, 8, v15\n"
    "s_lshl_b32 s92, s80, 12\n"
    "s_mov_b32 s93, 0\n"
    "s_branch L_J%=\n"
    "L_LN%=:\n"
    "s_lshl_b32 s84, s82, 2\n"
    "s_add_u32 s84, s84, s80\n"
    "s_mov_b64 s[44:45], s[8:9]\n"
    "s_mov_b32 s46, 5308416\n"
    "s_mov_b32 s47, 0x20000\n"
    "s_mov_b64 s[48:49], s[10:11]\n"
    "s_sub_u32 s48, s48, 1024\n"
    "s_subb_u32 s49, s49, 0\n"
    "s_mov_b32 s50, 5309440\n"
    "s_mov_b32 s51, 0x20000\n"
    "s_mul_i32 s83, s84, 589824\n"
    "s_mov_b64 s[52:53], s[4:5]\n"
    "s_add_u32 s52, s52, s83\n"
    "s_addc_u32 s53, s53, 0\n"
    "s_add_u32 s52, s52, 4194304\n"
    "s_addc_u32 s53, s53, 0\n"
    "s_mov_b32 s54, 589824\n"
    "s_mov_b32 s55, 0x20000\n"
    "s_lshl_b32 s83, s84, 19\n"
    "s_lshl_b32 s85, s81, 14\n"
    "s_add_u32 s83, s83, s85\n"
    "s_mov_b64 s[64:65], s[4:5]\n"
    "s_add_u32 s64, s64, s83\n"
    "s_addc_u32 s65, s65, 0\n"
    "s_add_u32 s64, s64, 25165824\n"
    "s_addc_u32 s65, s65, 0\n"
    "s_mov_b32 s66, 16384\n"
    "s_mov_b32 s67, 0x20000\n"
    "s_lshr_b32 s83, s81, 1\n"
    "s_lshl_b32 s83, s83, 19\n"
    "s_mov_b64 s[56:57], s[12:13]\n"
    "s_add_u32 s56, s56, s83\n"
    "s_addc_u32 s57, s57, 0\n"
    "s_mov_b32 s58, 524288\n"
    "s_mov_b32 s59, 0x20000\n"
    "s_mov_b64 s[60:61], s[14:15]\n"
    "s_add_u32 s60, s60, s83\n"
    "s_addc_u32 s61, s61, 0\n"
    "s_mov_b32 s62, 524288\n"
    "s_mov_b32 s63, 0x20000\n"
    "s_lshr_b32 s83, s81, 1\n"
    "s_mul_i32 s83, s83, 9\n"
    "s_and_b32 s85, s81, 1\n"
    "s_mul_i32 s85, s85, 5\n"
    "s_add_u32 s83, s83, s85\n"
    "s_lshl_b32 s89, s83, 12\n"
    "s_lshl_b32 s90, s83, 6\n"
    "v_lshlrev_b32 v10, 3, v12\n"
    "v_lshrrev_b32 v13, 2, v12\n"
    "v_and_b32 v14, 3, v12\n"
    "v_lshlrev_b32 v15, 8, v13\n"
    "v_lshl_add_u32 v15, v14, 4, v15\n"
    "v_and_b32 v8, 3, v13\n"
    "v_xor_b32 v3, 0, v8\n"
    "v_lshl_add_u32 v3, v3, 6, v15\n"
    "v_xor_b32 v4, 1, v8\n"
    "v_lshl_add_u32 v4, v4, 6, v15\n"
    "v_xor_b32 v5, 2, v8\n"
    "v_lshl_add_u32 v5, v5, 6, v15\n"
    "v_xor_b32 v6, 3, v8\n"
    "v_lshl_add_u32 v6, v6, 6, v15\n"
    "v_bfe_u32 v15, v12, 4, 1\n"
    "v_lshlrev_b32 v8, 6, v13\n"
    "v_lshl_add_u32 v8, v15, 5, v8\n"
    "v_lshl_add_u32 v8, v14, 3, v8\n"
    "v_add_u32 v9, 36864, v8\n"
    "v_lshlrev_b32 v15, 1, v15\n"
    "v_xor_b32 v15, v15, v14\n"
    "s_lshl_b32 s83, s80, 4\n"
    "v_add_u32 v13, s83, v13\n"
    "v_mul_u32_u24 v13, 9216, v13\n"
    "v_lshl_add_u32 v7, v15, 4, v13\n"
    "v_lshrrev_b32 v13, 4, v12\n"
    "v_and_b32 v14, 15, v12\n"
    "v_add_u32 v15, 0, v13\n"
    "v_xor_b32 v15, v15, v14\n"
    "v_lshlrev_b32 v15, 4, v15\n"
    "v_lshl_add_u32 v40, v13, 8, v15\n"
    "v_add_u32 v15, 4, v13\n"
    "v_xor_b32 v15, v15, v14\n"
    "v_lshlrev_b32 v15, 4, v15\n"
    "v_lshl_add_u32 v41, v13, 8, v15\n"
    "v_add_u32 v15, 8, v13\n"
    "v_xor_b32 v15, v15, v14\n"
    "v_lshlrev_b32 v15, 4, v15\n"
    "v_lshl_add_u32 v42, v13, 8, v15\n"
    "v_add_u32 v15, 12, v13\n"
    "v_xor_b32 v15, v15, v14\n"
    "v_lshlrev_b32 v15, 4, v15\n"
    "v_lshl_add_u32 v43, v13, 8, v15\n"
    "v_add_u32 v15, 0, v13\n"
    "v_lshlrev_b32 v15, 1, v15\n"
    "v_xor_b32 v15, v15, v14\n"
    "v_lshlrev_b32 v15, 4, v15\n"
    "v_lshl_add_u32 v44, v13, 8, v15\n"
    "v_add_u32 v15, 4, v13\n"
    "v_lshlrev_b32 v15, 1, v15\n"
    "v_xor_b32 v15, v15, v14\n"
    "v_lshlrev_b32 v15, 4, v15\n"
    "v_lshl_add_u32 v45, v13, 8, v15\n"
    "s_lshl_b32 s92, s80, 12\n"
    "s_mov_b32 s93, 0\n"
    "s_lshl_b32 s83, s82, 2\n"
    "s_lshr_b32 s84, s81, 3\n"
    "s_add_u32 s83, s83, s84\n"
    "s_mul_i32 s83, s83, 3\n"
    "s_add_u32 s83, s83, s80\n"
    "s_mul_i32 s83, s83, 24576\n"
    "s_and_b32 s84, s81, 6\n"
    "s_lshl_b32 s84, s84, 9\n"
    "s_add_u32 s83, s83, s84\n"
    "s_and_b32 s84, s81, 1\n"
    "s_lshl_b32 s84, s84, 7\n"
    "s_add_u32 s83, s83, s84\n"
    "s_mov_b64 s[28:29], s[4:5]\n"
    "s_add_u32 s28, s28, s83\n"
    "s_addc_u32 s29, s29, 0\n"
    "s_mov_b32 s30, 24576\n"
    "s_mov_b32 s31, 0x20000\n"
    "s_waitcnt vmcnt(6)\n"
    "v_lshlrev_b32 v64, 16, v20\n"
    "v_and_b32 v65, 0xffff0000, v20\n"
    "v_add_f32 v34, v64, v65\n"
    "v_lshlrev_b32 v66, 16, v21\n"
    "v_and_b32 v67, 0xffff0000, v21\n"
    "v_add_f32 v34, v34, v66\n"
    "v_add_f32 v34, v34, v67\n"
    "s_waitcnt vmcnt(5)\n"
    "v_lshlrev_b32 v68, 16, v22\n"
    "v_and_b32 v69, 0xffff0000, v22\n"
    "v_add_f32 v34, v34, v68\n"
    "v_add_f32 v34, v34, v69\n"
    "v_lshlrev_b32 v70, 16, v23\n"
    "v_and_b32 v71, 0xffff0000, v23\n"
    "v_add_f32 v34, v34, v70\n"
    "v_add_f32 v34, v34, v71\n"
    "s_waitcnt vmcnt(4)\n"
    "v_lshlrev_b32 v72, 16, v24\n"
    "v_and_b32 v73, 0xffff0000, v24\n"
    "v_add_f32 v34, v34, v72\n"
    "v_add_f32 v34, v34, v73\n"
    "v_lshlrev_b32 v74, 16, v25\n"
    "v_and_b32 v75, 0xffff0000, v25\n"
    "v_add_f32 v34, v34, v74\n"
    "v_add_f32 v34, v34, v75\n"
    "s_waitcnt vmcnt(3)\n"
    "v_lshlrev_b32 v76, 16, v26\n"
    "v_and_b32 v77, 0xffff0000, v26\n"
    "v_add_f32 v34, v34, v76\n"
    "v_add_f32 v34, v34, v77\n"
    "v_lshlrev_b32 v78, 16, v27\n"
    "v_and_b32 v79, 0xffff0000, v27\n"
    "v_add_f32 v34, v34, v78\n"
    "v_add_f32 v34, v34, v79\n"
    "s_waitcnt vmcnt(2)\n"
    "v_lshlrev_b32 v80, 16, v28\n"
    "v_and_b32 v81, 0xffff0000, v28\n"
    "v_add_f32 v34, v34, v80\n"
    "v_add_f32 v34, v34, v81\n"
    "v_lshlrev_b32 v82, 16, v29\n"
    "v_and_b32 v83, 0xffff0000, v29\n"
    "v_add_f32 v34, v34, v82\n"
    "v_add_f32 v34, v34, v83\n"
    "s_waitcnt vmcnt(1)\n"
    "v_lshlrev_b32 v84, 16, v30\n"
    "v_and_b32 v85, 0xffff0000, v30\n"
    "v_add_f32 v34, v34, v84\n"
    "v_add_f32 v34, v34, v85\n"
    "v_lshlrev_b32 v86, 16, v31\n"
    "v_and_b32 v87, 0xffff0000, v31\n"
    "v_add_f32 v34, v34, v86\n"
    "v_add_f32 v34, v34, v87\n"
    "s_nop 1\n"
    "v_add_f32_dpp v34, v34, v34 row_ror:8 row_mask:0xf bank_mask:0xf\n"
    "v_mov_b32 v36, v34\n"
    "s_nop 1\n"
    "v_permlane16_swap_b32 v34, v36\n"
    "v_add_f32 v34, v34, v36\n"
    "v_mov_b32 v36, v34\n"
    "s_nop 1\n"
    "v_permlane32_swap_b32 v34, v36\n"
    "v_add_f32 v34, v34, v36\n"
    "v_mul_f32 v37, 0x3baaaaab, v34\n"
    "v_sub_f32 v38, v64, v37\n"
    "v_mul_f32 v35, v38, v38\n"
    "v_sub_f32 v39, v65, v37\n"
    "v_fmac_f32 v35, v39, v39\n"
    "v_sub_f32 v38, v66, v37\n"
    "v_fmac_f32 v35, v38, v38\n"
    "v_sub_f32 v39, v67, v37\n"
    "v_fmac_f32 v35, v39, v39\n"
    "v_sub_f32 v38, v68, v37\n"
    "v_fmac_f32 v35, v38, v38\n"
    "v_sub_f32 v39, v69, v37\n"
    "v_fmac_f32 v35, v39, v39\n"
    "v_sub_f32 v38, v70, v37\n"
    "v_fmac_f32 v35, v38, v38\n"
    "v_sub_f32 v39, v71, v37\n"
    "v_fmac_f32 v35, v39, v39\n"
    "v_sub_f32 v38, v72, v37\n"
    "v_fmac_f32 v35, v38, v38\n"
    "v_sub_f32 v39, v73, v37\n"
    "v_fmac_f32 v35, v39, v39\n"
    "v_sub_f32 v38, v74, v37\n"
    "v_fmac_f32 v35, v38, v38\n"
    "v_sub_f32 v39, v75, v37\n"
    "v_fmac_f32 v35, v39, v39\n"
    "v_sub_f32 v38, v76, v37\n"
    "v_fmac_f32 v35, v38, v38\n"
    "v_sub_f32 v39, v77, v37\n"
    "v_fmac_f32 v35, v39, v39\n"
    "v_sub_f32 v38, v78, v37\n"
    "v_fmac_f32 v35, v38, v38\n"
    "v_sub_f32 v39, v79, v37\n"
    "v_fmac_f32 v35, v39, v39\n"
    "v_sub_f32 v38, v80, v37\n"
    "v_fmac_f32 v35, v38, v38\n"
    "v_sub_f32 v39, v81, v37\n"
    "v_fmac_f32 v35, v39, v39\n"
    "v_sub_f32 v38, v82, v37\n"
    "v_fmac_f32 v35, v38, v38\n"
    "v_sub_f32 v39, v83, v37\n"
    "v_fmac_f32 v35, v39, v39\n"
    "v_sub_f32 v38, v84, v37\n"
    "v_fmac_f32 v35, v38, v38\n"
    "v_sub_f32 v39, v85, v37\n"
    "v_fmac_f32 v35, v39, v39\n"
    "v_sub_f32 v38, v86, v37\n"
    "v_fmac_f32 v35, v38, v38\n"
    "v_sub_f32 v39, v87, v37\n"
    "v_fmac_f32 v35, v39, v39\n"
    "s_nop 1\n"
    "v_add_f32_dpp v35, v35, v35 row_ror:8 row_mask:0xf bank_mask:0xf\n"
    "v_mov_b32 v36, v35\n"
    "s_nop 1\n"
    "v_permlane16_swap_b32 v35, v36\n"
    "v_add_f32 v35, v35, v36\n"
    "v_mov_b32 v36, v35\n"
    "s_nop 1\n"
    "v_permlane32_swap_b32 v35, v36\n"
    "v_add_f32 v35, v35, v36\n"
    "ds_write_b64 v33, v[34:35]\n"
    "s_waitcnt vmcnt(0)\n"
    "v_readfirstlane_b32 s95, v46\n"
    "v_add_u32 v46, 1, v46\n"
    "s_mov_b32 s96, 1048576\n"
    "L_LX%=:\n"
    "ds_read_b64 v[48:49], v32 offset:0\n"
    "ds_read_b64 v[50:51], v32 offset:64\n"
    "ds_read_b64 v[52:53], v32 offset:128\n"
    "s_sub_u32 s96, s96, 1\n"
    "s_cbranch_scc1 L_LY%=\n"
    "s_waitcnt lgkmcnt(0)\n"
    "v_cmp_eq_u32 vcc, -1.0, v49\n"
    "v_cmp_eq_u32 s[98:99], -1.0, v51\n"
    "s_nop 0\n"
    "s_or_b64 vcc, vcc, s[98:99]\n"
    "v_cmp_eq_u32 s[98:99], -1.0, v53\n"
    "s_nop 0\n"
    "s_or_b64 vcc, vcc, s[98:99]\n"
    "s_cbranch_scc1 L_LX%=\n"
    "L_LY%=:\n"
    "s_waitcnt lgkmcnt(0)\n"
    "v_add_f32 v34, v48, v50\n"
    "v_add_f32 v35, v49, v51\n"
    "v_add_f32 v34, v34, v52\n"
    "v_add_f32 v35, v35, v53\n"
    "v_mul_f32 v34, 0x3ae38e39, v34\n"
    "v_mov_b32 v36, 0x3baaaaab\n"
    "v_fma_f32 v48, v48, v36, -v34\n"
    "v_fma_f32 v50, v50, v36, -v34\n"
    "v_fma_f32 v52, v52, v36, -v34\n"
    "v_mul_f32 v37, v48, v48\n"
    "v_fmac_f32 v37, v50, v50\n"
    "v_fmac_f32 v37, v52, v52\n"
    "v_fmac_f32 v35, 0x43400000, v37\n"
    "v_mul_f32 v35, 0x3ae38e39, v35\n"
    "v_cvt_pk_bf16_f32 v35, v34, v35\n"
    "v_lshlrev_b32 v34, 16, v35\n"
    "v_and_b32 v35, 0xffff0000, v35\n"
    "v_add_f32 v35, 0x3727c5ac, v35\n"
    "v_cvt_pk_bf16_f32 v35, v35, v35\n"
    "v_and_b32 v35, 0xffff0000, v35\n"
    "v_sqrt_f32 v35, v35\n"
    "v_sub_f32 v64, v64, v34\n"
    "v_sub_f32 v65, v65, v34\n"
    "v_sub_f32 v66, v66, v34\n"
    "v_sub_f32 v67, v67, v34\n"
    "v_cvt_pk_bf16_f32 v35, v35, v35\n"
    "v_and_b32 v35, 0xffff0000, v35\n"
    "v_rcp_f32 v35, v35\n"
    "v_sub_f32 v68, v68, v34\n"
    "v_sub_f32 v69, v69, v34\n"
    "v_sub_f32 v70, v70, v34\n"
    "v_sub_f32 v71, v71, v34\n"
    "v_cvt_pk_bf16_f32 v20, v64, v65\n"
    "v_cvt_pk_bf16_f32 v21, v66, v67\n"
    "v_cvt_pk_bf16_f32 v22, v68, v69\n"
    "v_cvt_pk_bf16_f32 v23, v70, v71\n"
    "v_lshlrev_b32 v64, 16, v20\n"
    "v_and_b32 v65, 0xffff0000, v20\n"
    "v_lshlrev_b32 v66, 16, v21\n"
    "v_and_b32 v67, 0xffff0000, v21\n"
    "v_lshlrev_b32 v68, 16, v22\n"
    "v_and_b32 v69, 0xffff0000, v22\n"
    "v_lshlrev_b32 v70, 16, v23\n"
    "v_and_b32 v71, 0xffff0000, v23\n"
    "v_mul_f32 v64, v64, v35\n"
    "v_mul_f32 v65, v65, v35\n"
    "v_mul_f32 v66, v66, v35\n"
    "v_mul_f32 v67, v67, v35\n"
    "v_mul_f32 v68, v68, v35\n"
    "v_mul_f32 v69, v69, v35\n"
    "v_mul_f32 v70, v70, v35\n"
    "v_mul_f32 v71, v71, v35\n"
    "v_cvt_pk_bf16_f32 v20, v64, v65\n"
    "v_cvt_pk_bf16_f32 v21, v66, v67\n"
    "v_cvt_pk_bf16_f32 v22, v68, v69\n"
    "v_cvt_pk_bf16_f32 v23, v70, v71\n"
    "s_mov_b32 s83, 0\n"
    "buffer_store_dwordx4 v[20:23], v17, s[28:31], s83 offen sc1\n"
    "v_sub_f32 v72, v72, v34\n"
    "v_sub_f32 v73, v73, v34\n"
    "v_sub_f32 v74, v74, v34\n"
    "v_sub_f32 v75, v75, v34\n"
    "v_sub_f32 v76, v76, v34\n"
    "v_sub_f32 v77, v77, v34\n"
    "v_sub_f32 v78, v78, v34\n"
    "v_sub_f32 v79, v79, v34\n"
    "v_cvt_pk_bf16_f32 v24, v72, v73\n"
    "v_cvt_pk_bf16_f32 v25, v74, v75\n"
    "v_cvt_pk_bf16_f32 v26, v76, v77\n"
    "v_cvt_pk_bf16_f32 v27, v78, v79\n"
    "v_lshlrev_b32 v72, 16, v24\n"
    "v_and_b32 v73, 0xffff0000, v24\n"
    "v_lshlrev_b32 v74, 16, v25\n"
    "v_and_b32 v75, 0xffff0000, v25\n"
    "v_lshlrev_b32 v76, 16, v26\n"
    "v_and_b32 v77, 0xffff0000, v26\n"
    "v_lshlrev_b32 v78, 16, v27\n"
    "v_and_b32 v79, 0xffff0000, v27\n"
    "v_mul_f32 v72, v72, v35\n"
    "v_mul_f32 v73, v73, v35\n"
    "v_mul_f32 v74, v74, v35\n"
    "v_mul_f32 v75, v75, v35\n"
    "v_mul_f32 v76, v76, v35\n"
    "v_mul_f32 v77, v77, v35\n"
    "v_mul_f32 v78, v78, v35\n"
    "v_mul_f32 v79, v79, v35\n"
    "v_cvt_pk_bf16_f32 v24, v72, v73\n"
    "v_cvt_pk_bf16_f32 v25, v74, v75\n"
    "v_cvt_pk_bf16_f32 v26, v76, v77\n"
    "v_cvt_pk_bf16_f32 v27, v78, v79\n"
    "s_mov_b32 s83, 8192\n"
    "buffer_store_dwordx4 v[24:27], v17, s[28:31], s83 offen sc1\n"
    "v_sub_f32 v80, v80, v34\n"
    "v_sub_f32 v81, v81, v34\n"
    "v_sub_f32 v82, v82, v34\n"
    "v_sub_f32 v83, v83, v34\n"
    "v_sub_f32 v84, v84, v34\n"
    "v_sub_f32 v85, v85, v34\n"
    "v_sub_f32 v86, v86, v34\n"
    "v_sub_f32 v87, v87, v34\n"
    "v_cvt_pk_bf16_f32 v28, v80, v81\n"
    "v_cvt_pk_bf16_f32 v29, v82, v83\n"
    "v_cvt_pk_bf16_f32 v30, v84, v85\n"
    "v_cvt_pk_bf16_f32 v31, v86, v87\n"
    "v_lshlrev_b32 v80, 16, v28\n"
    "v_and_b32 v81, 0xffff0000, v28\n"
    "v_lshlrev_b32 v82, 16, v29\n"
    "v_and_b32 v83, 0xffff0000, v29\n"
    "v_lshlrev_b32 v84, 16, v30\n"
    "v_and_b32 v85, 0xffff0000, v30\n"
    "v_lshlrev_b32 v86, 16, v31\n"
    "v_and_b32 v87, 0xffff0000, v31\n"
    "v_mul_f32 v80, v80, v35\n"
    "v_mul_f32 v81, v81, v35\n"
    "v_mul_f32 v82, v82, v35\n"
    "v_mul_f32 v83, v83, v35\n"
    "v_mul_f32 v84, v84, v35\n"
    "v_mul_f32 v85, v85, v35\n"
    "v_mul_f32 v86, v86, v35\n"
    "v_mul_f32 v87, v87, v35\n"
    "v_cvt_pk_bf16_f32 v28, v80, v81\n"
    "v_cvt_pk_bf16_f32 v29, v82, v83\n"
    "v_cvt_pk_bf16_f32 v30, v84, v85\n"
    "v_cvt_pk_bf16_f32 v31, v86, v87\n"
    "s_mov_b32 s83, 16384\n"
    "buffer_store_dwordx4 v[28:31], v17, s[28:31], s83 offen sc1\n"
    "s_waitcnt vmcnt(0)\n"
    "buffer_store_dwordx2 v[46:47], v11, s[76:79], 0 offen sc1\n"
    "buffer_load_dwordx4 v1, s[40:43], s87 offen lds\n"
    "buffer_load_dwordx4 v2, s[40:43], s87 offen offset:1024 lds\n"
    "s_add_u32 m0, m0, 2048\n"
    "s_add_u32 s87, s87, 65536\n"
    "buffer_load_dwordx4 v1, s[40:43], s87 offen lds\n"
    "buffer_load_dwordx4 v2, s[40:43], s87 offen offset:1024 lds\n"
    "s_add_u32 m0, m0, 2048\n"
    "s_add_u32 s87, s87, 65536\n"
    "buffer_load_dwordx4 v1, s[40:43], s87 offen lds\n"
    "buffer_load_dwordx4 v2, s[40:43], s87 offen offset:1024 lds\n"
    "s_add_u32 m0, m0, 2048\n"
    "s_add_u32 s87, s87, 65536\n"
    "buffer_load_dwordx4 v1, s[40:43], s87 offen lds\n"
    "buffer_load_dwordx4 v2, s[40:43], s87 offen offset:1024 lds\n"
    "s_add_u32 m0, m0, 2048\n"
    "s_add_u32 s87, s87, 65536\n"
    "buffer_load_dwordx4 v1, s[40:43], s87 offen lds\n"
    "buffer_load_dwordx4 v2, s[40:43], s87 offen offset:1024 lds\n"
    "s_add_u32 m0, m0, 2048\n"
    "s_add_u32 s87, s87, 65536\n"
    "buffer_load_dwordx4 v1, s[40:43], s87 offen lds\n"
    "buffer_load_dwordx4 v2, s[40:43], s87 offen offset:1024 lds\n"
    "s_add_u32 m0, m0, 2048\n"
    "s_add_u32 s87, s87, 65536\n"
    "buffer_load_dwordx4 v1, s[40:43], s87 offen lds\n"
    "buffer_load_dwordx4 v2, s[40:43], s87 offen offset:1024 lds\n"
    "s_add_u32 m0, m0, 2048\n"
    "s_add_u32 s87, s87, 65536\n"
    "buffer_load_dwordx4 v1, s[40:43], s87 offen lds\n"
    "buffer_load_dwordx4 v2, s[40:43], s87 offen offset:1024 lds\n"
    "s_add_u32 m0, m0, 2048\n"
    "s_add_u32 s87, s87, 65536\n"
    "buffer_load_dwordx4 v1, s[40:43], s87 offen lds\n"
    "buffer_load_dwordx4 v2, s[40:43], s87 offen offset:1024 lds\n"
    "s_add_u32 m0, m0, 2048\n"
    "s_add_u32 s87, s87, 65536\n"
    "buffer_load_dwordx4 v1, s[40:43], s87 offen lds\n"
    "buffer_load_dwordx4 v2, s[40:43], s87 offen offset:1024 lds\n"
    "s_add_u32 m0, m0, 2048\n"
    "s_add_u32 s87, s87, 65536\n"
    "buffer_load_dwordx4 v1, s[40:43], s87 offen lds\n"
    "buffer_load_dwordx4 v2, s[40:43], s87 offen offset:1024 lds\n"
    "s_add_u32 m0, m0, 2048\n"
    "s_add_u32 s87, s87, 65536\n"
    "buffer_load_dwordx4 v1, s[40:43], s87 offen lds\n"
    "buffer_load_dwordx4 v2, s[40:43], s87 offen offset:1024 lds\n"
    "s_add_u32 m0, m0, 2048\n"
    "s_add_u32 s87, s87, 65536\n"
    "buffer_load_dwordx4 v1, s[40:43], s87 offen lds\n"
    "buffer_load_dwordx4 v2, s[40:43], s87 offen offset:1024 lds\n"
    "s_add_u32 m0, m0, 2048\n"
    "s_add_u32 s87, s87, 65536\n"
    "buffer_load_dwordx4 v1, s[40:43], s87 offen lds\n"
    "buffer_load_dwordx4 v2, s[40:43], s87 offen offset:1024 lds\n"
    "s_add_u32 m0, m0, 2048\n"
    "s_add_u32 s87, s87, 65536\n"
    "buffer_load_dwordx4 v1, s[40:43], s87 offen lds\n"
    "buffer_load_dwordx4 v2, s[40:43], s87 offen offset:1024 lds\n"
    "s_add_u32 m0, m0, 2048\n"
    "s_add_u32 s87, s87, 65536\n"
    "buffer_load_dwordx4 v1, s[40:43], s87 offen lds\n"
    "buffer_load_dwordx4 v2, s[40:43], s87 offen offset:1024 lds\n"
    "s_add_u32 m0, m0, 2048\n"
    "s_add_u32 s87, s87, 65536\n"
    "buffer_load_dwordx4 v1, s[40:43], s87 offen lds\n"
    "buffer_load_dwordx4 v2, s[40:43], s87 offen offset:1024 lds\n"
    "s_add_u32 m0, m0, 2048\n"
    "s_add_u32 s87, s87, 65536\n"
    "buffer_load_dwordx4 v1, s[40:43], s87 offen lds\n"
    "buffer_load_dwordx4 v2, s[40:43], s87 offen offset:1024 lds\n"
    "s_add_u32 m0, m0, 2048\n"
    "s_add_u32 s87, s87, 65536\n"
    "L_J%=:\n"
    "v_add_u32 v46, 1, v46\n"
    "v_mov_b32 v2, 0xbdd2d3e8\n"
    "s_lshl_b32 s84, s82, 2\n"
    "s_add_u32 s84, s84, s80\n"
    "s_lshl_b32 s84, s84, 12\n"
    "s_add_u32 s88, s95, 1\n"
    "s_mov_b32 s96, 1048576\n"
    "L_HP%=:\n"
    "buffer_load_dwordx2 v[14:15], v19, s[76:79], s84 offen sc1\n"
    "s_sub_u32 s96, s96, 1\n"
    "s_waitcnt vmcnt(0)\n"
    "v_subrev_u32 v14, s88, v14\n"
    "v_xor_b32 v15, v47, v15\n"
    "v_cmp_lt_u64 vcc, 15, v[14:15]\n"
    "s_cbranch_scc1 L_HX%=\n"
    "s_cbranch_vccnz L_HP%=\n"
    "L_HX%=:\n"
    "s_mov_b32 s83, 0\n"
    "buffer_load_dwordx4 a[0:3], v0, s[36:39], s83 offen sc1\n"
    "buffer_load_dwordx4 a[4:7], v0, s[36:39], s83 offen offset:1024 sc1\n"
    "buffer_load_dwordx4 a[8:11], v0, s[36:39], s83 offen offset:2048 sc1\n"
    "buffer_load_dwordx4 a[12:15], v0, s[36:39], s83 offen offset:3072 sc1\n"
    "s_mov_b32 s83, 4096\n"
    "buffer_load_dwordx4 a[16:19], v0, s[36:39], s83 offen sc1\n"
    "buffer_load_dwordx4 a[20:23], v0, s[36:39], s83 offen offset:1024 sc1\n"
    "buffer_load_dwordx4 a[24:27], v0, s[36:39], s83 offen offset:2048 sc1\n"
    "buffer_load_dwordx4 a[28:31], v0, s[36:39], s83 offen offset:3072 sc1\n"
    "s_barrier\n"
    "ds_read_b64_tr_b16 v[16:17], v3 offset:0\n"
    "ds_read_b64_tr_b16 v[18:19], v3 offset:4096\n"
    "ds_read_b64_tr_b16 v[20:21], v3 offset:8\n"
    "ds_read_b64_tr_b16 v[22:23], v3 offset:4104\n"
    "ds_read_b64_tr_b16 v[24:25], v4 offset:0\n"
    "ds_read_b64_tr_b16 v[26:27], v4 offset:4096\n"
    "s_mov_b32 s83, 8192\n"
    "buffer_load_dwordx4 a[32:35], v0, s[36:39], s83 offen sc1\n"
    "buffer_load_dwordx4 a[36:39], v0, s[36:39], s83 offen offset:1024 sc1\n"
    "buffer_load_dwordx4 a[40:43], v0, s[36:39], s83 offen offset:2048 sc1\n"
    "buffer_load_dwordx4 a[44:47], v0, s[36:39], s83 offen offset:3072 sc1\n"
    "s_mov_b32 s83, 12288\n"
    "buffer_load_dwordx4 a[48:51], v0, s[36:39], s83 offen sc1\n"
    "buffer_load_dwordx4 a[52:55], v0, s[36:39], s83 offen offset:1024 sc1\n"
    "buffer_load_dwordx4 a[56:59], v0, s[36:39], s83 offen offset:2048 sc1\n"
    "buffer_load_dwordx4 a[60:63], v0, s[36:39], s83 offen offset:3072 sc1\n"
    "s_mov_b32 s83, 16384\n"
    "buffer_load_dwordx4 a[64:67], v0, s[36:39], s83 offen sc1\n"
    "buffer_load_dwordx4 a[68:71], v0, s[36:39], s83 offen offset:1024 sc1\n"
    "buffer_load_dwordx4 a[72:75], v0, s[36:39], s83 offen offset:2048 sc1\n"
    "buffer_load_dwordx4 a[76:79], v0, s[36:39], s83 offen offset:3072 sc1\n"
    "s_mov_b32 s83, 20480\n"
    "buffer_load_dwordx4 a[80:83], v0, s[36:39], s83 offen sc1\n"
    "buffer_load_dwordx4 a[84:87], v0, s[36:39], s83 offen offset:1024 sc1\n"
    "buffer_load_dwordx4 a[88:91], v0, s[36:39], s83 offen offset:2048 sc1\n"
    "buffer_load_dwordx4 a[92:95], v0, s[36:39], s83 offen offset:3072 sc1\n"
    "s_waitcnt vmcnt(23) lgkmcnt(4)\n"
    "v_mfma_f32_16x16x32_bf16 v[128:131], v[16:19], a[0:3], 0\n"
    "s_waitcnt vmcnt(22)\n"
    "ds_read_b64_tr_b16 v[28:29], v4 offset:8\n"
    "v_mfma_f32_16x16x32_bf16 v[160:163], v[16:19], a[4:7], 0\n"
    "s_waitcnt vmcnt(21)\n"
    "ds_read_b64_tr_b16 v[30:31], v4 offset:4104\n"
    "v_mfma_f32_16x16x32_bf16 v[192:195], v[16:19], a[8:11], 0\n"
    "s_waitcnt vmcnt(20)\n"
    "s_mov_b32 s83, 24576\n"
    "buffer_load_dwordx4 a[96:99], v0, s[36:39], s83 offen sc1\n"
    "v_mfma_f32_16x16x32_bf16 v[224:227], v[16:19], a[12:15], 0\n"
    "s_waitcnt lgkmcnt(4)\n"
    "v_mfma_f32_16x16x32_bf16 v[132:135], v[20:23], a[0:3], 0\n"
    "ds_read_b64_tr_b16 v[32:33], v5 offset:0\n"
    "v_mfma_f32_16x16x32_bf16 v[164:167], v[20:23], a[4:7], 0\n"
    "ds_read_b64_tr_b16 v[34:35], v5 offset:4096\n"
    "v_mfma_f32_16x16x32_bf16 v[196:199], v[20:23], a[8:11], 0\n"
    "buffer_load_dwordx4 a[100:103], v0, s[36:39], s83 offen offset:1024 sc1\n"
    "v_mfma_f32_16x16x32_bf16 v[228:231], v[20:23], a[12:15], 0\n"
    "s_waitcnt lgkmcnt(4)\n"
    "v_mfma_f32_16x16x32_bf16 v[136:139], v[24:27], a[0:3], 0\n"
    "ds_read_b64_tr_b16 v[36:37], v5 offset:8\n"
    "v_mfma_f32_16x16x32_bf16 v[168:171], v[24:27], a[4:7], 0\n"
    "ds_read_b64_tr_b16 v[38:39], v5 offset:4104\n"
    "v_mfma_f32_16x16x32_bf16 v[200:203], v[24:27], a[8:11], 0\n"
    "buffer_load_dwordx4 a[104:107], v0, s[36:39], s83 offen offset:2048 sc1\n"
    "v_mfma_f32_16x16x32_bf16 v[232:235], v[24:27], a[12:15], 0\n"
    "s_waitcnt lgkmcnt(4)\n"
    "v_mfma_f32_16x16x32_bf16 v[140:143], v[28:31], a[0:3], 0\n"
    "ds_read_b64_tr_b16 v[48:49], v6 offset:0\n"
    "v_mfma_f32_16x16x32_bf16 v[172:175], v[28:31], a[4:7], 0\n"
    "ds_read_b64_tr_b16 v[50:51], v6 offset:4096\n"
    "v_mfma_f32_16x16x32_bf16 v[204:207], v[28:31], a[8:11], 0\n"
    "buffer_load_dwordx4 a[108:111], v0, s[36:39], s83 offen offset:3072 sc1\n"
    "v_mfma_f32_16x16x32_bf16 v[236:239], v[28:31], a[12:15], 0\n"
    "s_waitcnt lgkmcnt(4)\n"
    "v_mfma_f32_16x16x32_bf16 v[144:147], v[32:35], a[0:3], 0\n"
    "ds_read_b64_tr_b16 v[52:53], v6 offset:8\n"
    "v_mfma_f32_16x16x32_bf16 v[176:179], v[32:35], a[4:7], 0\n"
    "ds_read_b64_tr_b16 v[54:55], v6 offset:4104\n"
    "v_mfma_f32_16x16x32_bf16 v[208:211], v[32:35], a[8:11], 0\n"
    "s_mov_b32 s83, 28672\n"
    "buffer_load_dwordx4 a[112:115], v0, s[36:39], s83 offen sc1\n"
    "v_mfma_f32_16x16x32_bf16 v[240:243], v[32:35], a[12:15], 0\n"
    "s_waitcnt lgkmcnt(4)\n"
    "v_mfma_f32_16x16x32_bf16 v[148:151], v[36:39], a[0:3], 0\n"
    "ds_read_b64_tr_b16 v[56:57], v3 offset:8192\n"
    "v_mfma_f32_16x16x32_bf16 v[180:183], v[36:39], a[4:7], 0\n"
    "ds_read_b64_tr_b16 v[58:59], v3 offset:12288\n"
    "v_mfma_f32_16x16x32_bf16 v[212:215], v[36:39], a[8:11], 0\n"
    "buffer_load_dwordx4 a[116:119], v0, s[36:39], s83 offen offset:1024 sc1\n"
    "v_mfma_f32_16x16x32_bf16 v[244:247], v[36:39], a[12:15], 0\n"
    "s_waitcnt lgkmcnt(4)\n"
    "v_mfma_f32_16x16x32_bf16 v[152:155], v[48:51], a[0:3], 0\n"
    "ds_read_b64_tr_b16 v[16:17], v3 offset:8200\n"
    "v_mfma_f32_16x16x32_bf16 v[184:187], v[48:51], a[4:7], 0\n"
    "ds_read_b64_tr_b16 v[18:19], v3 offset:12296\n"
    "v_mfma_f32_16x16x32_bf16 v[216:219], v[48:51], a[8:11], 0\n"
    "buffer_load_dwordx4 a[120:123], v0, s[36:39], s83 offen offset:2048 sc1\n"
    "v_mfma_f32_16x16x32_bf16 v[248:251], v[48:51], a[12:15], 0\n"
    "s_waitcnt lgkmcnt(4)\n"
    "v_mfma_f32_16x16x32_bf16 v[156:159], v[52:55], a[0:3], 0\n"
    "ds_read_b64_tr_b16 v[20:21], v4 offset:8192\n"
    "v_mfma_f32_16x16x32_bf16 v[188:191], v[52:55], a[4:7], 0\n"
    "ds_read_b64_tr_b16 v[22:23], v4 offset:12288\n"
    "v_mfma_f32_16x16x32_bf16 v[220:223], v[52:55], a[8:11], 0\n"
    "buffer_load_dwordx4 a[124:127], v0, s[36:39], s83 offen offset:3072 sc1\n"
    "v_mfma_f32_16x16x32_bf16 v[252:255], v[52:55], a[12:15], 0\n"
    "s_waitcnt vmcnt(27) lgkmcnt(4)\n"
    "v_mfma_f32_16x16x32_bf16 v[128:131], v[56:59], a[16:19], v[128:131]\n"
    "s_waitcnt vmcnt(26)\n"
    "ds_read_b64_tr_b16 v[24:25], v4 offset:8200\n"
    "v_mfma_f32_16x16x32_bf16 v[160:163], v[56:59], a[20:23], v[160:163]\n"
    "s_waitcnt vmcnt(25)\n"
    "ds_read_b64_tr_b16 v[26:27], v4 offset:12296\n"
    "v_mfma_f32_16x16x32_bf16 v[192:195], v[56:59], a[24:27], v[192:195]\n"
    "s_waitcnt vmcnt(24)\n"
    "s_mov_b32 s83, 32768\n"
    "buffer_load_dwordx4 a[128:131], v0, s[36:39], s83 offen sc1\n"
    "v_mfma_f32_16x16x32_bf16 v[224:227], v[56:59], a[28:31], v[224:227]\n"
    "s_waitcnt lgkmcnt(4)\n"
    "v_mfma_f32_16x16x32_bf16 v[132:135], v[16:19], a[16:19], v[132:135]\n"
    "ds_read_b64_tr_b16 v[28:29], v5 offset:8192\n"
    "v_mfma_f32_16x16x32_bf16 v[164:167], v[16:19], a[20:23], v[164:167]\n"
    "ds_read_b64_tr_b16 v[30:31], v5 offset:12288\n"
    "v_mfma_f32_16x16x32_bf16 v[196:199], v[16:19], a[24:27], v[196:199]\n"
    "buffer_load_dwordx4 a[132:135], v0, s[36:39], s83 offen offset:1024 sc1\n"
    "v_mfma_f32_16x16x32_bf16 v[228:231], v[16:19], a[28:31], v[228:231]\n"
    "s_waitcnt lgkmcnt(4)\n"
    "s_bitcmp0_b32 s82, 2\n"
    "s_cselect_b64 s[32:33], s[8:9], s[10:11]\n"
    "v_mfma_f32_16x16x32_bf16 v[136:139], v[20:23], a[16:19], v[136:139]\n"
    "ds_read_b64_tr_b16 v[32:33], v5 offset:8200\n"
    "v_mfma_f32_16x16x32_bf16 v[168:171], v[20:23], a[20:23], v[168:171]\n"
    "s_and_b32 s34, s82, 3\n"
    "ds_read_b64_tr_b16 v[34:35], v5 offset:12296\n"
    "v_mfma_f32_16x16x32_bf16 v[200:203], v[20:23], a[24:27], v[200:203]\n"
    "buffer_load_dwordx4 a[136:139], v0, s[36:39], s83 offen offset:2048 sc1\n"
    "v_mfma_f32_16x16x32_bf16 v[232:235], v[20:23], a[28:31], v[232:235]\n"
    "s_waitcnt lgkmcnt(4)\n"
    "s_lshl_b32 s34, s34, 2\n"
    "v_mfma_f32_16x16x32_bf16 v[140:143], v[24:27], a[16:19], v[140:143]\n"
    "ds_read_b64_tr_b16 v[36:37], v6 offset:8192\n"
    "v_mfma_f32_16x16x32_bf16 v[172:175], v[24:27], a[20:23], v[172:175]\n"
    "s_and_b32 s35, s81, 3\n"
    "ds_read_b64_tr_b16 v[38:39], v6 offset:12288\n"
    "v_mfma_f32_16x16x32_bf16 v[204:207], v[24:27], a[24:27], v[204:207]\n"
    "buffer_load_dwordx4 a[140:143], v0, s[36:39], s83 offen offset:3072 sc1\n"
    "v_mfma_f32_16x16x32_bf16 v[236:239], v[24:27], a[28:31], v[236:239]\n"
    "s_waitcnt lgkmcnt(4)\n"
    "s_add_u32 s34, s34, s35\n"
    "v_mfma_f32_16x16x32_bf16 v[144:147], v[28:31], a[16:19], v[144:147]\n"
    "ds_read_b64_tr_b16 v[48:49], v6 offset:8200\n"
    "v_mfma_f32_16x16x32_bf16 v[176:179], v[28:31], a[20:23], v[176:179]\n"
    "s_mul_i32 s88, s34, 331776\n"
    "ds_read_b64_tr_b16 v[50:51], v6 offset:12296\n"
    "v_mfma_f32_16x16x32_bf16 v[208:211], v[28:31], a[24:27], v[208:211]\n"
    "s_mov_b32 s83, 36864\n"
    "buffer_load_dwordx4 a[144:147], v0, s[36:39], s83 offen sc1\n"
    "v_mfma_f32_16x16x32_bf16 v[240:243], v[28:31], a[28:31], v[240:243]\n"
    "s_waitcnt lgkmcnt(4)\n"
    "s_lshr_b32 s35, s81, 2\n"
    "v_mfma_f32_16x16x32_bf16 v[148:151], v[32:35], a[16:19], v[148:151]\n"
    "ds_read_b64_tr_b16 v[52:53], v3 offset:16384\n"
    "v_mfma_f32_16x16x32_bf16 v[180:183], v[32:35], a[20:23], v[180:183]\n"
    "s_mul_i32 s35, s35, 1152\n"
    "ds_read_b64_tr_b16 v[54:55], v3 offset:20480\n"
    "v_mfma_f32_16x16x32_bf16 v[212:215], v[32:35], a[24:27], v[212:215]\n"
    "buffer_load_dwordx4 a[148:151], v0, s[36:39], s83 offen offset:1024 sc1\n"
    "v_mfma_f32_16x16x32_bf16 v[244:247], v[32:35], a[28:31], v[244:247]\n"
    "s_waitcnt lgkmcnt(4)\n"
    "s_add_u32 s88, s88, s35\n"
    "v_mfma_f32_16x16x32_bf16 v[152:155], v[36:39], a[16:19], v[152:155]\n"
    "ds_read_b64_tr_b16 v[56:57], v3 offset:16392\n"
    "v_mfma_f32_16x16x32_bf16 v[184:187], v[36:39], a[20:23], v[184:187]\n"
    "s_mov_b32 s34, 5308416\n"
    "ds_read_b64_tr_b16 v[58:59], v3 offset:20488\n"
    "v_mfma_f32_16x16x32_bf16 v[216:219], v[36:39], a[24:27], v[216:219]\n"
    "buffer_load_dwordx4 a[152:155], v0, s[36:39], s83 offen offset:2048 sc1\n"
    "v_mfma_f32_16x16x32_bf16 v[248:251], v[36:39], a[28:31], v[248:251]\n"
    "s_waitcnt lgkmcnt(4)\n"
    "s_mov_b32 s35, 0x20000\n"
    "v_mfma_f32_16x16x32_bf16 v[156:159], v[48:51], a[16:19], v[156:159]\n"
    "ds_read_b64_tr_b16 v[16:17], v4 offset:16384\n"
    "v_mfma_f32_16x16x32_bf16 v[188:191], v[48:51], a[20:23], v[188:191]\n"
    "ds_read_b64_tr_b16 v[18:19], v4 offset:20480\n"
    "v_lshrrev_b32 v64, 4, v0\n"
    "v_mfma_f32_16x16x32_bf16 v[220:223], v[48:51], a[24:27], v[220:223]\n"
    "buffer_load_dwordx4 a[156:159], v0, s[36:39], s83 offen offset:3072 sc1\n"
    "v_mfma_f32_16x16x32_bf16 v[252:255], v[48:51], a[28:31], v[252:255]\n"
    "s_waitcnt vmcnt(31) lgkmcnt(4)\n"
    "s_mul_i32 s25, s80, 36\n"
    "v_mfma_f32_16x16x32_bf16 v[128:131], v[52:55], a[32:35], v[128:131]\n"
    "s_waitcnt vmcnt(30)\n"
    "ds_read_b64_tr_b16 v[20:21], v4 offset:16392\n"
    "v_mfma_f32_16x16x32_bf16 v[160:163], v[52:55], a[36:39], v[160:163]\n"
    "s_waitcnt vmcnt(29)\n"
    "ds_read_b64_tr_b16 v[22:23], v4 offset:20488\n"
    "v_add_u32 v65, s25, v64\n"
    "v_mfma_f32_16x16x32_bf16 v[192:195], v[52:55], a[40:43], v[192:195]\n"
    "s_waitcnt vmcnt(28)\n"
    "s_mov_b32 s83, 40960\n"
    "buffer_load_dwordx4 a[160:163], v0, s[36:39], s83 offen sc1\n"
    "v_mfma_f32_16x16x32_bf16 v[224:227], v[52:55], a[44:47], v[224:227]\n"
    "s_waitcnt lgkmcnt(4)\n"
    "v_mul_u32_u24 v66, 16385, v65\n"
    "v_mfma_f32_16x16x32_bf16 v[132:135], v[56:59], a[32:35], v[132:135]\n"
    "ds_read_b64_tr_b16 v[24:25], v5 offset:16384\n"
    "v_mfma_f32_16x16x32_bf16 v[164:167], v[56:59], a[36:39], v[164:167]\n"
    "ds_read_b64_tr_b16 v[26:27], v5 offset:20480\n"
    "v_lshrrev_b32 v66, 16, v66\n"
    "v_mfma_f32_16x16x32_bf16 v[196:199], v[56:59], a[40:43], v[196:199]\n"
    "buffer_load_dwordx4 a[164:167], v0, s[36:39], s83 offen offset:1024 sc1\n"
    "v_mfma_f32_16x16x32_bf16 v[228:231], v[56:59], a[44:47], v[228:231]\n"
    "s_waitcnt lgkmcnt(4)\n"
    "v_mul_u32_u24 v67, 4, v66\n"
    "v_mfma_f32_16x16x32_bf16 v[136:139], v[16:19], a[32:35], v[136:139]\n"
    "ds_read_b64_tr_b16 v[28:29], v5 offset:16392\n"
    "v_mfma_f32_16x16x32_bf16 v[168:171], v[16:19], a[36:39], v[168:171]\n"
    "ds_read_b64_tr_b16 v[30:31], v5 offset:20488\n"
    "v_sub_u32 v65, v65, v67\n"
    "v_mfma_f32_16x16x32_bf16 v[200:203], v[16:19], a[40:43], v[200:203]\n"
    "buffer_load_dwordx4 a[168:171], v0, s[36:39], s83 offen offset:2048 sc1\n"
    "v_mfma_f32_16x16x32_bf16 v[232:235], v[16:19], a[44:47], v[232:235]\n"
    "s_waitcnt lgkmcnt(4)\n"
    "v_lshlrev_b32 v65, 2, v65\n"
    "v_mfma_f32_16x16x32_bf16 v[140:143], v[20:23], a[32:35], v[140:143]\n"
    "ds_read_b64_tr_b16 v[32:33], v6 offset:16384\n"
    "v_mfma_f32_16x16x32_bf16 v[172:175], v[20:23], a[36:39], v[172:175]\n"
    "s_mov_b32 s25, 0x7420\n"
    "ds_read_b64_tr_b16 v[34:35], v6 offset:20480\n"
    "v_mfma_f32_16x16x32_bf16 v[204:207], v[20:23], a[40:43], v[204:207]\n"
    "buffer_load_dwordx4 a[172:175], v0, s[36:39], s83 offen offset:3072 sc1\n"
    "v_mfma_f32_16x16x32_bf16 v[236:239], v[20:23], a[44:47], v[236:239]\n"
    "s_waitcnt lgkmcnt(4)\n"
    "v_bfe_u32 v65, s25, v65, 4\n"
    "v_mfma_f32_16x16x32_bf16 v[144:147], v[24:27], a[32:35], v[144:147]\n"
    "ds_read_b64_tr_b16 v[36:37], v6 offset:16392\n"
    "v_mfma_f32_16x16x32_bf16 v[176:179], v[24:27], a[36:39], v[176:179]\n"
    "ds_read_b64_tr_b16 v[38:39], v6 offset:20488\n"
    "v_mul_u32_u24 v66, 9216, v66\n"
    "v_mfma_f32_16x16x32_bf16 v[208:211], v[24:27], a[40:43], v[208:211]\n"
    "s_mov_b32 s83, 45056\n"
    "buffer_load_dwordx4 a[176:179], v0, s[36:39], s83 offen sc1\n"
    "v_mfma_f32_16x16x32_bf16 v[240:243], v[24:27], a[44:47], v[240:243]\n"
    "s_waitcnt lgkmcnt(4)\n"
    "v_lshl_add_u32 v65, v65, 7, v66\n"
    "v_mfma_f32_16x16x32_bf16 v[148:151], v[28:31], a[32:35], v[148:151]\n"
    "ds_read_b64_tr_b16 v[48:49], v3 offset:24576\n"
    "v_mfma_f32_16x16x32_bf16 v[180:183], v[28:31], a[36:39], v[180:183]\n"
    "ds_read_b64_tr_b16 v[50:51], v3 offset:28672\n"
    "v_add_u32 v66, 28, v64\n"
    "v_mfma_f32_16x16x32_bf16 v[212:215], v[28:31], a[40:43], v[212:215]\n"
    "buffer_load_dwordx4 a[180:183], v0, s[36:39], s83 offen offset:1024 sc1\n"
    "v_mfma_f32_16x16x32_bf16 v[244:247], v[28:31], a[44:47], v[244:247]\n"
    "s_waitcnt lgkmcnt(4)\n"
    "v_lshrrev_b32 v66, 6, v66\n"
    "v_mfma_f32_16x16x32_bf16 v[152:155], v[32:35], a[32:35], v[152:155]\n"
    "ds_read_b64_tr_b16 v[52:53], v3 offset:24584\n"
    "v_mfma_f32_16x16x32_bf16 v[184:187], v[32:35], a[36:39], v[184:187]\n"
    "ds_read_b64_tr_b16 v[54:55], v3 offset:28680\n"
    "v_lshl_or_b32 v61, v66, 30, v65\n"
    "v_mfma_f32_16x16x32_bf16 v[216:219], v[32:35], a[40:43], v[216:219]\n"
    "buffer_load_dwordx4 a[184:187], v0, s[36:39], s83 offen offset:2048 sc1\n"
    "v_mfma_f32_16x16x32_bf16 v[248:251], v[32:35], a[44:47], v[248:251]\n"
    "s_waitcnt lgkmcnt(4)\n"
    "s_mul_i32 s25, s80, 18\n"
    "v_mfma_f32_16x16x32_bf16 v[156:159], v[36:39], a[32:35], v[156:159]\n"
    "ds_read_b64_tr_b16 v[56:57], v4 offset:24576\n"
    "v_mfma_f32_16x16x32_bf16 v[188:191], v[36:39], a[36:39], v[188:191]\n"
    "ds_read_b64_tr_b16 v[58:59], v4 offset:28672\n"
    "v_add_u32 v65, s25, v64\n"
    "v_mfma_f32_16x16x32_bf16 v[220:223], v[36:39], a[40:43], v[220:223]\n"
    "buffer_load_dwordx4 a[188:191], v0, s[36:39], s83 offen offset:3072 sc1\n"
    "v_mfma_f32_16x16x32_bf16 v[252:255], v[36:39], a[44:47], v[252:255]\n"
    "s_waitcnt vmcnt(35) lgkmcnt(4)\n"
    "v_mul_u32_u24 v66, 32769, v65\n"
    "v_mfma_f32_16x16x32_bf16 v[128:131], v[48:51], a[48:51], v[128:131]\n"
    "s_waitcnt vmcnt(34)\n"
    "ds_read_b64_tr_b16 v[16:17], v4 offset:24584\n"
    "v_mfma_f32_16x16x32_bf16 v[160:163], v[48:51], a[52:55], v[160:163]\n"
    "s_waitcnt vmcnt(33)\n"
    "ds_read_b64_tr_b16 v[18:19], v4 offset:28680\n"
    "v_lshrrev_b32 v66, 16, v66\n"
    "v_mfma_f32_16x16x32_bf16 v[192:195], v[48:51], a[56:59], v[192:195]\n"
    "s_waitcnt vmcnt(32)\n"
    "s_mov_b32 s83, 49152\n"
    "buffer_load_dwordx4 a[192:195], v0, s[36:39], s83 offen sc1\n"
    "v_mfma_f32_16x16x32_bf16 v[224:227], v[48:51], a[60:63], v[224:227]\n"
    "s_waitcnt lgkmcnt(4)\n"
    "v_mul_u32_u24 v67, 2, v66\n"
    "v_mfma_f32_16x16x32_bf16 v[132:135], v[52:55], a[48:51], v[132:135]\n"
    "ds_read_b64_tr_b16 v[20:21], v5 offset:24576\n"
    "v_mfma_f32_16x16x32_bf16 v[164:167], v[52:55], a[52:55], v[164:167]\n"
    "ds_read_b64_tr_b16 v[22:23], v5 offset:28672\n"
    "v_sub_u32 v65, v65, v67\n"
    "v_mfma_f32_16x16x32_bf16 v[196:199], v[52:55], a[56:59], v[196:199]\n"
    "buffer_load_dwordx4 a[196:199], v0, s[36:39], s83 offen offset:1024 sc1\n"
    "v_mfma_f32_16x16x32_bf16 v[228:231], v[52:55], a[60:63], v[228:231]\n"
    "s_waitcnt lgkmcnt(4)\n"
    "v_lshlrev_b32 v65, 2, v65\n"
    "v_mfma_f32_16x16x32_bf16 v[136:139], v[56:59], a[48:51], v[136:139]\n"
    "ds_read_b64_tr_b16 v[24:25], v5 offset:24584\n"
    "v_mfma_f32_16x16x32_bf16 v[168:171], v[56:59], a[52:55], v[168:171]\n"
    "s_mov_b32 s25, 0x53\n"
    "ds_read_b64_tr_b16 v[26:27], v5 offset:28680\n"
    "v_mfma_f32_16x16x32_bf16 v[200:203], v[56:59], a[56:59], v[200:203]\n"
    "buffer_load_dwordx4 a[200:203], v0, s[36:39], s83 offen offset:2048 sc1\n"
    "v_mfma_f32_16x16x32_bf16 v[232:235], v[56:59], a[60:63], v[232:235]\n"
    "s_waitcnt lgkmcnt(4)\n"
    "v_bfe_u32 v65, s25, v65, 4\n"
    "v_mfma_f32_16x16x32_bf16 v[140:143], v[16:19], a[48:51], v[140:143]\n"
    "ds_read_b64_tr_b16 v[28:29], v6 offset:24576\n"
    "v_mfma_f32_16x16x32_bf16 v[172:175], v[16:19], a[52:55], v[172:175]\n"
    "ds_read_b64_tr_b16 v[30:31], v6 offset:28672\n"
    "v_mul_u32_u24 v66, 9216, v66\n"
    "v_mfma_f32_16x16x32_bf16 v[204:207], v[16:19], a[56:59], v[204:207]\n"
    "buffer_load_dwordx4 a[204:207], v0, s[36:39], s83 offen offset:3072 sc1\n"
    "v_mfma_f32_16x16x32_bf16 v[236:239], v[16:19], a[60:63], v[236:239]\n"
    "s_waitcnt lgkmcnt(4)\n"
    "v_lshl_add_u32 v65, v65, 7, v66\n"
    "v_mfma_f32_16x16x32_bf16 v[144:147], v[20:23], a[48:51], v[144:147]\n"
    "ds_read_b64_tr_b16 v[32:33], v6 offset:24584\n"
    "v_mfma_f32_16x16x32_bf16 v[176:179], v[20:23], a[52:55], v[176:179]\n"
    "ds_read_b64_tr_b16 v[34:35], v6 offset:28680\n"
    "v_add_u32 v66, 46, v64\n"
    "v_mfma_f32_16x16x32_bf16 v[208:211], v[20:23], a[56:59], v[208:211]\n"
    "s_mov_b32 s83, 53248\n"
    "buffer_load_dwordx4 a[208:211], v0, s[36:39], s83 offen sc1\n"
    "v_mfma_f32_16x16x32_bf16 v[240:243], v[20:23], a[60:63], v[240:243]\n"
    "s_waitcnt lgkmcnt(4)\n"
    "v_lshrrev_b32 v66, 6, v66\n"
    "v_mfma_f32_16x16x32_bf16 v[148:151], v[24:27], a[48:51], v[148:151]\n"
    "ds_read_b64_tr_b16 v[36:37], v3 offset:32768\n"
    "v_mfma_f32_16x16x32_bf16 v[180:183], v[24:27], a[52:55], v[180:183]\n"
    "ds_read_b64_tr_b16 v[38:39], v3 offset:36864\n"
    "v_lshl_or_b32 v62, v66, 30, v65\n"
    "v_mfma_f32_16x16x32_bf16 v[212:215], v[24:27], a[56:59], v[212:215]\n"
    "buffer_load_dwordx4 a[212:215], v0, s[36:39], s83 offen offset:1024 sc1\n"
    "v_mfma_f32_16x16x32_bf16 v[244:247], v[24:27], a[60:63], v[244:247]\n"
    "s_waitcnt lgkmcnt(4)\n"
    "s_mul_i32 s25, s80, 27\n"
    "v_mfma_f32_16x16x32_bf16 v[152:155], v[28:31], a[48:51], v[152:155]\n"
    "ds_read_b64_tr_b16 v[48:49], v3 offset:32776\n"
    "v_mfma_f32_16x16x32_bf16 v[184:187], v[28:31], a[52:55], v[184:187]\n"
    "ds_read_b64_tr_b16 v[50:51], v3 offset:36872\n"
    "v_add_u32 v65, s25, v64\n"
    "v_mfma_f32_16x16x32_bf16 v[216:219], v[28:31], a[56:59], v[216:219]\n"
    "buffer_load_dwordx4 a[216:219], v0, s[36:39], s83 offen offset:2048 sc1\n"
    "v_mfma_f32_16x16x32_bf16 v[248:251], v[28:31], a[60:63], v[248:251]\n"
    "s_waitcnt lgkmcnt(4)\n"
    "v_mul_u32_u24 v66, 21846, v65\n"
    "v_mfma_f32_16x16x32_bf16 v[156:159], v[32:35], a[48:51], v[156:159]\n"
    "ds_read_b64_tr_b16 v[52:53], v4 offset:32768\n"
    "v_mfma_f32_16x16x32_bf16 v[188:191], v[32:35], a[52:55], v[188:191]\n"
    "ds_read_b64_tr_b16 v[54:55], v4 offset:36864\n"
    "v_lshrrev_b32 v66, 16, v66\n"
    "v_mfma_f32_16x16x32_bf16 v[220:223], v[32:35], a[56:59], v[220:223]\n"
    "buffer_load_dwordx4 a[220:223], v0, s[36:39], s83 offen offset:3072 sc1\n"
    "v_mfma_f32_16x16x32_bf16 v[252:255], v[32:35], a[60:63], v[252:255]\n"
    "s_waitcnt vmcnt(39) lgkmcnt(4)\n"
    "v_mul_u32_u24 v67, 3, v66\n"
    "v_mfma_f32_16x16x32_bf16 v[128:131], v[36:39], a[64:67], v[128:131]\n"
    "s_waitcnt vmcnt(38)\n"
    "ds_read_b64_tr_b16 v[56:57], v4 offset:32776\n"
    "v_mfma_f32_16x16x32_bf16 v[160:163], v[36:39], a[68:71], v[160:163]\n"
    "s_waitcnt vmcnt(37)\n"
    "ds_read_b64_tr_b16 v[58:59], v4 offset:36872\n"
    "v_sub_u32 v65, v65, v67\n"
    "v_mfma_f32_16x16x32_bf16 v[192:195], v[36:39], a[72:75], v[192:195]\n"
    "s_waitcnt vmcnt(36)\n"
    "s_mov_b32 s83, 57344\n"
    "buffer_load_dwordx4 a[224:227], v0, s[36:39], s83 offen sc1\n"
    "v_mfma_f32_16x16x32_bf16 v[224:227], v[36:39], a[76:79], v[224:227]\n"
    "s_waitcnt lgkmcnt(4)\n"
    "v_lshlrev_b32 v65, 2, v65\n"
    "v_mfma_f32_16x16x32_bf16 v[132:135], v[48:51], a[64:67], v[132:135]\n"
    "ds_read_b64_tr_b16 v[16:17], v5 offset:32768\n"
    "v_mfma_f32_16x16x32_bf16 v[164:167], v[48:51], a[68:71], v[164:167]\n"
    "s_mov_b32 s25, 0x681\n"
    "ds_read_b64_tr_b16 v[18:19], v5 offset:36864\n"
    "v_mfma_f32_16x16x32_bf16 v[196:199], v[48:51], a[72:75], v[196:199]\n"
    "buffer_load_dwordx4 a[228:231], v0, s[36:39], s83 offen offset:1024 sc1\n"
    "v_mfma_f32_16x16x32_bf16 v[228:231], v[48:51], a[76:79], v[228:231]\n"
    "s_waitcnt lgkmcnt(4)\n"
    "v_bfe_u32 v65, s25, v65, 4\n"
    "v_mfma_f32_16x16x32_bf16 v[136:139], v[52:55], a[64:67], v[136:139]\n"
    "ds_read_b64_tr_b16 v[20:21], v5 offset:32776\n"
    "v_mfma_f32_16x16x32_bf16 v[168:171], v[52:55], a[68:71], v[168:171]\n"
    "ds_read_b64_tr_b16 v[22:23], v5 offset:36872\n"
    "v_mul_u32_u24 v66, 9216, v66\n"
    "v_mfma_f32_16x16x32_bf16 v[200:203], v[52:55], a[72:75], v[200:203]\n"
    "buffer_load_dwordx4 a[232:235], v0, s[36:39], s83 offen offset:2048 sc1\n"
    "v_mfma_f32_16x16x32_bf16 v[232:235], v[52:55], a[76:79], v[232:235]\n"
    "s_waitcnt lgkmcnt(4)\n"
    "v_lshl_add_u32 v65, v65, 7, v66\n"
    "v_mfma_f32_16x16x32_bf16 v[140:143], v[56:59], a[64:67], v[140:143]\n"
    "ds_read_b64_tr_b16 v[24:25], v6 offset:32768\n"
    "v_mfma_f32_16x16x32_bf16 v[172:175], v[56:59], a[68:71], v[172:175]\n"
    "ds_read_b64_tr_b16 v[26:27], v6 offset:36864\n"
    "v_add_u32 v66, 37, v64\n"
    "v_mfma_f32_16x16x32_bf16 v[204:207], v[56:59], a[72:75], v[204:207]\n"
    "buffer_load_dwordx4 a[236:239], v0, s[36:39], s83 offen offset:3072 sc1\n"
    "v_mfma_f32_16x16x32_bf16 v[236:239], v[56:59], a[76:79], v[236:239]\n"
    "s_waitcnt lgkmcnt(4)\n"
    "v_lshrrev_b32 v66, 6, v66\n"
    "v_mfma_f32_16x16x32_bf16 v[144:147], v[16:19], a[64:67], v[144:147]\n"
    "ds_read_b64_tr_b16 v[28:29], v6 offset:32776\n"
    "v_mfma_f32_16x16x32_bf16 v[176:179], v[16:19], a[68:71], v[176:179]\n"
    "ds_read_b64_tr_b16 v[30:31], v6 offset:36872\n"
    "v_lshl_or_b32 v63, v66, 30, v65\n"
    "v_mfma_f32_16x16x32_bf16 v[208:211], v[16:19], a[72:75], v[208:211]\n"
    "s_mov_b32 s83, 61440\n"
    "buffer_load_dwordx4 a[240:243], v0, s[36:39], s83 offen sc1\n"
    "v_mfma_f32_16x16x32_bf16 v[240:243], v[16:19], a[76:79], v[240:243]\n"
    "s_waitcnt lgkmcnt(4)\n"
    "v_mfma_f32_16x16x32_bf16 v[148:151], v[20:23], a[64:67], v[148:151]\n"
    "ds_read_b64_tr_b16 v[32:33], v3 offset:40960\n"
    "v_mfma_f32_16x16x32_bf16 v[180:183], v[20:23], a[68:71], v[180:183]\n"
    "ds_read_b64_tr_b16 v[34:35], v3 offset:45056\n"
    "v_mfma_f32_16x16x32_bf16 v[212:215], v[20:23], a[72:75], v[212:215]\n"
    "buffer_load_dwordx4 a[244:247], v0, s[36:39], s83 offen offset:1024 sc1\n"
    "v_mfma_f32_16x16x32_bf16 v[244:247], v[20:23], a[76:79], v[244:247]\n"
    "s_waitcnt lgkmcnt(4)\n"
    "v_mfma_f32_16x16x32_bf16 v[152:155], v[24:27], a[64:67], v[152:155]\n"
    "ds_read_b64_tr_b16 v[36:37], v3 offset:40968\n"
    "v_mfma_f32_16x16x32_bf16 v[184:187], v[24:27], a[68:71], v[184:187]\n"
    "ds_read_b64_tr_b16 v[38:39], v3 offset:45064\n"
    "v_mfma_f32_16x16x32_bf16 v[216:219], v[24:27], a[72:75], v[216:219]\n"
    "buffer_load_dwordx4 a[248:251], v0, s[36:39], s83 offen offset:2048 sc1\n"
    "v_mfma_f32_16x16x32_bf16 v[248:251], v[24:27], a[76:79], v[248:251]\n"
    "s_waitcnt lgkmcnt(4)\n"
    "v_mfma_f32_16x16x32_bf16 v[156:159], v[28:31], a[64:67], v[156:159]\n"
    "ds_read_b64_tr_b16 v[48:49], v4 offset:40960\n"
    "v_mfma_f32_16x16x32_bf16 v[188:191], v[28:31], a[68:71], v[188:191]\n"
    "ds_read_b64_tr_b16 v[50:51], v4 offset:45056\n"
    "v_mfma_f32_16x16x32_bf16 v[220:223], v[28:31], a[72:75], v[220:223]\n"
    "buffer_load_dwordx4 a[252:255], v0, s[36:39], s83 offen offset:3072 sc1\n"
    "v_mfma_f32_16x16x32_bf16 v[252:255], v[28:31], a[76:79], v[252:255]\n"
    "s_waitcnt vmcnt(43) lgkmcnt(4)\n"
    "v_mfma_f32_16x16x32_bf16 v[128:131], v[32:35], a[80:83], v[128:131]\n"
    "s_waitcnt vmcnt(42)\n"
    "ds_read_b64_tr_b16 v[52:53], v4 offset:40968\n"
    "v_mfma_f32_16x16x32_bf16 v[160:163], v[32:35], a[84:87], v[160:163]\n"
    "s_waitcnt vmcnt(41)\n"
    "ds_read_b64_tr_b16 v[54:55], v4 offset:45064\n"
    "v_mfma_f32_16x16x32_bf16 v[192:195], v[32:35], a[88:91], v[192:195]\n"
    "s_waitcnt vmcnt(40)\n"
    "s_mov_b32 s83, 65536\n"
    "buffer_load_dwordx4 v[96:99], v0, s[36:39], s83 offen sc1\n"
    "v_mfma_f32_16x16x32_bf16 v[224:227], v[32:35], a[92:95], v[224:227]\n"
    "s_waitcnt lgkmcnt(4)\n"
    "v_mfma_f32_16x16x32_bf16 v[132:135], v[36:39], a[80:83], v[132:135]\n"
    "ds_read_b64_tr_b16 v[56:57], v5 offset:40960\n"
    "v_mfma_f32_16x16x32_bf16 v[164:167], v[36:39], a[84:87], v[164:167]\n"
    "ds_read_b64_tr_b16 v[58:59], v5 offset:45056\n"
    "v_mfma_f32_16x16x32_bf16 v[196:199], v[36:39], a[88:91], v[196:199]\n"
    "buffer_load_dwordx4 v[100:103], v0, s[36:39], s83 offen offset:1024 sc1\n"
    "v_mfma_f32_16x16x32_bf16 v[228:231], v[36:39], a[92:95], v[228:231]\n"
    "s_waitcnt lgkmcnt(4)\n"
    "v_mfma_f32_16x16x32_bf16 v[136:139], v[48:51], a[80:83], v[136:139]\n"
    "ds_read_b64_tr_b16 v[16:17], v5 offset:40968\n"
    "v_mfma_f32_16x16x32_bf16 v[168:171], v[48:51], a[84:87], v[168:171]\n"
    "ds_read_b64_tr_b16 v[18:19], v5 offset:45064\n"
    "v_mfma_f32_16x16x32_bf16 v[200:203], v[48:51], a[88:91], v[200:203]\n"
    "buffer_load_dwordx4 v[104:107], v0, s[36:39], s83 offen offset:2048 sc1\n"
    "v_mfma_f32_16x16x32_bf16 v[232:235], v[48:51], a[92:95], v[232:235]\n"
    "s_waitcnt lgkmcnt(4)\n"
    "v_mfma_f32_16x16x32_bf16 v[140:143], v[52:55], a[80:83], v[140:143]\n"
    "ds_read_b64_tr_b16 v[20:21], v6 offset:40960\n"
    "v_mfma_f32_16x16x32_bf16 v[172:175], v[52:55], a[84:87], v[172:175]\n"
    "ds_read_b64_tr_b16 v[22:23], v6 offset:45056\n"
    "v_mfma_f32_16x16x32_bf16 v[204:207], v[52:55], a[88:91], v[204:207]\n"
    "buffer_load_dwordx4 v[108:111], v0, s[36:39], s83 offen offset:3072 sc1\n"
    "v_mfma_f32_16x16x32_bf16 v[236:239], v[52:55], a[92:95], v[236:239]\n"
    "s_waitcnt lgkmcnt(4)\n"
    "v_mfma_f32_16x16x32_bf16 v[144:147], v[56:59], a[80:83], v[144:147]\n"
    "ds_read_b64_tr_b16 v[24:25], v6 offset:40968\n"
    "v_mfma_f32_16x16x32_bf16 v[176:179], v[56:59], a[84:87], v[176:179]\n"
    "ds_read_b64_tr_b16 v[26:27], v6 offset:45064\n"
    "v_mfma_f32_16x16x32_bf16 v[208:211], v[56:59], a[88:91], v[208:211]\n"
    "s_mov_b32 s83, 69632\n"
    "buffer_load_dwordx4 v[112:115], v0, s[36:39], s83 offen sc1\n"
    "v_mfma_f32_16x16x32_bf16 v[240:243], v[56:59], a[92:95], v[240:243]\n"
    "s_waitcnt lgkmcnt(4)\n"
    "v_mfma_f32_16x16x32_bf16 v[148:151], v[16:19], a[80:83], v[148:151]\n"
    "ds_read_b64_tr_b16 v[28:29], v3 offset:49152\n"
    "v_mfma_f32_16x16x32_bf16 v[180:183], v[16:19], a[84:87], v[180:183]\n"
    "ds_read_b64_tr_b16 v[30:31], v3 offset:53248\n"
    "v_mfma_f32_16x16x32_bf16 v[212:215], v[16:19], a[88:91], v[212:215]\n"
    "buffer_load_dwordx4 v[116:119], v0, s[36:39], s83 offen offset:1024 sc1\n"
    "v_mfma_f32_16x16x32_bf16 v[244:247], v[16:19], a[92:95], v[244:247]\n"
    "s_waitcnt lgkmcnt(4)\n"
    "v_mfma_f32_16x16x32_bf16 v[152:155], v[20:23], a[80:83], v[152:155]\n"
    "ds_read_b64_tr_b16 v[32:33], v3 offset:49160\n"
    "v_mfma_f32_16x16x32_bf16 v[184:187], v[20:23], a[84:87], v[184:187]\n"
    "ds_read_b64_tr_b16 v[34:35], v3 offset:53256\n"
    "v_mfma_f32_16x16x32_bf16 v[216:219], v[20:23], a[88:91], v[216:219]\n"
    "buffer_load_dwordx4 v[120:123], v0, s[36:39], s83 offen offset:2048 sc1\n"
    "v_mfma_f32_16x16x32_bf16 v[248:251], v[20:23], a[92:95], v[248:251]\n"
    "s_waitcnt lgkmcnt(4)\n"
    "v_mfma_f32_16x16x32_bf16 v[156:159], v[24:27], a[80:83], v[156:159]\n"
    "ds_read_b64_tr_b16 v[36:37], v4 offset:49152\n"
    "v_mfma_f32_16x16x32_bf16 v[188:191], v[24:27], a[84:87], v[188:191]\n"
    "ds_read_b64_tr_b16 v[38:39], v4 offset:53248\n"
    "v_mfma_f32_16x16x32_bf16 v[220:223], v[24:27], a[88:91], v[220:223]\n"
    "buffer_load_dwordx4 v[124:127], v0, s[36:39], s83 offen offset:3072 sc1\n"
    "v_mfma_f32_16x16x32_bf16 v[252:255], v[24:27], a[92:95], v[252:255]\n"
    "s_waitcnt vmcnt(47) lgkmcnt(4)\n"
    "v_mfma_f32_16x16x32_bf16 v[128:131], v[28:31], a[96:99], v[128:131]\n"
    "s_waitcnt vmcnt(46)\n"
    "ds_read_b64_tr_b16 v[48:49], v4 offset:49160\n"
    "v_mfma_f32_16x16x32_bf16 v[160:163], v[28:31], a[100:103], v[160:163]\n"
    "s_waitcnt vmcnt(45)\n"
    "ds_read_b64_tr_b16 v[50:51], v4 offset:53256\n"
    "v_mfma_f32_16x16x32_bf16 v[192:195], v[28:31], a[104:107], v[192:195]\n"
    "s_waitcnt vmcnt(44)\n"
    "v_mfma_f32_16x16x32_bf16 v[224:227], v[28:31], a[108:111], v[224:227]\n"
    "s_waitcnt lgkmcnt(4)\n"
    "v_mfma_f32_16x16x32_bf16 v[132:135], v[32:35], a[96:99], v[132:135]\n"
    "buffer_load_dword v60, v61, s[32:35], s88 offen\n"
    "v_mfma_f32_16x16x32_bf16 v[164:167], v[32:35], a[100:103], v[164:167]\n"
    "ds_read_b64_tr_b16 v[52:53], v5 offset:49152\n"
    "v_mfma_f32_16x16x32_bf16 v[196:199], v[32:35], a[104:107], v[196:199]\n"
    "ds_read_b64_tr_b16 v[54:55], v5 offset:53248\n"
    "v_mfma_f32_16x16x32_bf16 v[228:231], v[32:35], a[108:111], v[228:231]\n"
    "s_waitcnt lgkmcnt(4)\n"
    "v_mfma_f32_16x16x32_bf16 v[136:139], v[36:39], a[96:99], v[136:139]\n"
    "s_and_b32 s6, s81, 3\n"
    "ds_read_b64_tr_b16 v[56:57], v5 offset:49160\n"
    "v_mfma_f32_16x16x32_bf16 v[168:171], v[36:39], a[100:103], v[168:171]\n"
    "s_lshr_b32 s7, s81, 2\n"
    "ds_read_b64_tr_b16 v[58:59], v5 offset:53256\n"
    "v_mfma_f32_16x16x32_bf16 v[200:203], v[36:39], a[104:107], v[200:203]\n"
    "s_and_b32 s37, s7, 1\n"
    "v_mfma_f32_16x16x32_bf16 v[232:235], v[36:39], a[108:111], v[232:235]\n"
    "s_waitcnt lgkmcnt(4)\n"
    "s_cmp_eq_u32 s37, 0\n"
    "v_mfma_f32_16x16x32_bf16 v[140:143], v[48:51], a[96:99], v[140:143]\n"
    "s_cselect_b32 s85, 36, 32\n"
    "ds_read_b64_tr_b16 v[16:17], v6 offset:49152\n"
    "v_mfma_f32_16x16x32_bf16 v[172:175], v[48:51], a[100:103], v[172:175]\n"
    "s_cselect_b64 s[40:41], s[16:17], s[18:19]\n"
    "ds_read_b64_tr_b16 v[18:19], v6 offset:53248\n"
    "v_mfma_f32_16x16x32_bf16 v[204:207], v[48:51], a[104:107], v[204:207]\n"
    "s_lshl_b32 s18, s37, 30\n"
    "v_mfma_f32_16x16x32_bf16 v[236:239], v[48:51], a[108:111], v[236:239]\n"
    "s_waitcnt lgkmcnt(4)\n"
    "s_and_b32 s19, s81, 7\n"
    "v_mfma_f32_16x16x32_bf16 v[144:147], v[52:55], a[96:99], v[144:147]\n"
    "s_lshl_b32 s16, s19, 2\n"
    "ds_read_b64_tr_b16 v[20:21], v6 offset:49160\n"
    "v_mfma_f32_16x16x32_bf16 v[176:179], v[52:55], a[100:103], v[176:179]\n"
    "s_mul_i32 s16, s16, s37\n"
    "ds_read_b64_tr_b16 v[22:23], v6 offset:53256\n"
    "v_mfma_f32_16x16x32_bf16 v[208:211], v[52:55], a[104:107], v[208:211]\n"
    "s_lshl_b32 s19, s19, 14\n"
    "v_mfma_f32_16x16x32_bf16 v[240:243], v[52:55], a[108:111], v[240:243]\n"
    "s_waitcnt lgkmcnt(4)\n"
    "s_lshl_b32 s36, s80, 3\n"
    "v_mfma_f32_16x16x32_bf16 v[148:151], v[56:59], a[96:99], v[148:151]\n"
    "s_mul_i32 s17, s80, 9216\n"
    "ds_read_b64_tr_b16 v[24:25], v3 offset:57344\n"
    "v_mfma_f32_16x16x32_bf16 v[180:183], v[56:59], a[100:103], v[180:183]\n"
    "s_lshr_b32 s38, s81, 3\n"
    "ds_read_b64_tr_b16 v[26:27], v3 offset:61440\n"
    "v_mfma_f32_16x16x32_bf16 v[212:215], v[56:59], a[104:107], v[212:215]\n"
    "s_mul_i32 s39, s85, 288\n"
    "v_mfma_f32_16x16x32_bf16 v[244:247], v[56:59], a[108:111], v[244:247]\n"
    "s_waitcnt lgkmcnt(4)\n"
    "s_mul_i32 s85, s85, 36864\n"
    "v_mfma_f32_16x16x32_bf16 v[152:155], v[16:19], a[96:99], v[152:155]\n"
    "s_mul_i32 s83, s85, s38\n"
    "ds_read_b64_tr_b16 v[28:29], v3 offset:57352\n"
    "v_mfma_f32_16x16x32_bf16 v[184:187], v[16:19], a[100:103], v[184:187]\n"
    "s_mul_i32 s84, s6, 288\n"
    "ds_read_b64_tr_b16 v[30:31], v3 offset:61448\n"
    "v_add_u32 v3, 0x10000, v3\n"
    "v_mfma_f32_16x16x32_bf16 v[216:219], v[16:19], a[104:107], v[216:219]\n"
    "s_add_u32 s83, s83, s84\n"
    "v_mfma_f32_16x16x32_bf16 v[248:251], v[16:19], a[108:111], v[248:251]\n"
    "s_waitcnt lgkmcnt(4)\n"
    "s_add_u32 s40, s40, s83\n"
    "v_mfma_f32_16x16x32_bf16 v[156:159], v[20:23], a[96:99], v[156:159]\n"
    "s_addc_u32 s41, s41, 0\n"
    "ds_read_b64_tr_b16 v[32:33], v4 offset:57344\n"
    "v_mfma_f32_16x16x32_bf16 v[188:191], v[20:23], a[100:103], v[188:191]\n"
    "s_sub_u32 s42, s85, s84\n"
    "ds_read_b64_tr_b16 v[34:35], v4 offset:61440\n"
    "v_mfma_f32_16x16x32_bf16 v[220:223], v[20:23], a[104:107], v[220:223]\n"
    "s_mov_b32 s43, 0x20000\n"
    "v_mfma_f32_16x16x32_bf16 v[252:255], v[20:23], a[108:111], v[252:255]\n"
    "s_waitcnt vmcnt(44) lgkmcnt(4)\n"
    "s_and_b32 s83, s82, 7\n"
    "v_mfma_f32_16x16x32_bf16 v[128:131], v[24:27], a[112:115], v[128:131]\n"
    "s_waitcnt vmcnt(43)\n"
    "s_lshl_b32 s83, s83, 2\n"
    "ds_read_b64_tr_b16 v[36:37], v4 offset:57352\n"
    "v_mfma_f32_16x16x32_bf16 v[160:163], v[24:27], a[116:119], v[160:163]\n"
    "s_waitcnt vmcnt(42)\n"
    "s_add_u32 s83, s83, s6\n"
    "ds_read_b64_tr_b16 v[38:39], v4 offset:61448\n"
    "v_add_u32 v4, 0x10000, v4\n"
    "v_mfma_f32_16x16x32_bf16 v[192:195], v[24:27], a[120:123], v[192:195]\n"
    "s_waitcnt vmcnt(41)\n"
    "s_lshl_b32 s83, s83, 2\n"
    "v_mfma_f32_16x16x32_bf16 v[224:227], v[24:27], a[124:127], v[224:227]\n"
    "s_waitcnt lgkmcnt(4)\n"
    "s_add_u32 s83, s83, s80\n"
    "v_mfma_f32_16x16x32_bf16 v[132:135], v[28:31], a[112:115], v[132:135]\n"
    "s_mul_i32 s39, s39, s83\n"
    "ds_read_b64_tr_b16 v[48:49], v5 offset:57344\n"
    "v_mfma_f32_16x16x32_bf16 v[164:167], v[28:31], a[116:119], v[164:167]\n"
    "s_lshl_b32 s14, s37, 2\n"
    "ds_read_b64_tr_b16 v[50:51], v5 offset:61440\n"
    "v_mfma_f32_16x16x32_bf16 v[196:199], v[28:31], a[120:123], v[196:199]\n"
    "s_add_u32 s14, s14, s80\n"
    "v_mfma_f32_16x16x32_bf16 v[228:231], v[28:31], a[124:127], v[228:231]\n"
    "s_waitcnt lgkmcnt(4)\n"
    "s_cmp_eq_u32 s14, 3\n"
    "v_mfma_f32_16x16x32_bf16 v[136:139], v[32:35], a[112:115], v[136:139]\n"
    "s_addc_u32 s15, s7, 0\n"
    "ds_read_b64_tr_b16 v[52:53], v5 offset:57352\n"
    "v_mfma_f32_16x16x32_bf16 v[168:171], v[32:35], a[116:119], v[168:171]\n"
    "s_lshl_b32 s14, s14, 2\n"
    "ds_read_b64_tr_b16 v[54:55], v5 offset:61448\n"
    "v_add_u32 v5, 0x10000, v5\n"
    "v_mfma_f32_16x16x32_bf16 v[200:203], v[32:35], a[120:123], v[200:203]\n"
    "s_lshr_b32 s14, 0x33102210, s14\n"
    "v_mfma_f32_16x16x32_bf16 v[232:235], v[32:35], a[124:127], v[232:235]\n"
    "s_waitcnt lgkmcnt(4)\n"
    "s_and_b32 s14, s14, 3\n"
    "v_mfma_f32_16x16x32_bf16 v[140:143], v[36:39], a[112:115], v[140:143]\n"
    "s_lshl_b32 s83, s82, 3\n"
    "ds_read_b64_tr_b16 v[56:57], v6 offset:57344\n"
    "v_mfma_f32_16x16x32_bf16 v[172:175], v[36:39], a[116:119], v[172:175]\n"
    "s_add_u32 s83, s83, s15\n"
    "ds_read_b64_tr_b16 v[58:59], v6 offset:61440\n"
    "v_mfma_f32_16x16x32_bf16 v[204:207], v[36:39], a[120:123], v[204:207]\n"
    "s_mul_i32 s83, s83, 36864\n"
    "v_mfma_f32_16x16x32_bf16 v[236:239], v[36:39], a[124:127], v[236:239]\n"
    "s_waitcnt lgkmcnt(4)\n"
    "s_mul_i32 s84, s6, 288\n"
    "v_mfma_f32_16x16x32_bf16 v[144:147], v[48:51], a[112:115], v[144:147]\n"
    "s_mul_i32 s85, s14, 96\n"
    "ds_read_b64_tr_b16 v[16:17], v6 offset:57352\n"
    "v_mfma_f32_16x16x32_bf16 v[176:179], v[48:51], a[116:119], v[176:179]\n"
    "s_add_u32 s84, s84, s85\n"
    "ds_read_b64_tr_b16 v[18:19], v6 offset:61448\n"
    "v_add_u32 v6, 0x10000, v6\n"
    "v_mfma_f32_16x16x32_bf16 v[208:211], v[48:51], a[120:123], v[208:211]\n"
    "s_add_u32 s83, s83, s84\n"
    "v_mfma_f32_16x16x32_bf16 v[240:243], v[48:51], a[124:127], v[240:243]\n"
    "s_waitcnt lgkmcnt(4)\n"
    "s_sub_u32 s84, 36864, s84\n"
    "v_mfma_f32_16x16x32_bf16 v[148:151], v[52:55], a[112:115], v[148:151]\n"
    "s_cmp_eq_u32 s14, 3\n"
    "ds_read_b64_tr_b16 v[20:21], v3 offset:0\n"
    "v_mfma_f32_16x16x32_bf16 v[180:183], v[52:55], a[116:119], v[180:183]\n"
    "s_cselect_b32 s84, 0, s84\n"
    "ds_read_b64_tr_b16 v[22:23], v3 offset:4096\n"
    "v_mfma_f32_16x16x32_bf16 v[212:215], v[52:55], a[120:123], v[212:215]\n"
    "s_mov_b64 s[24:25], s[20:21]\n"
    "v_mfma_f32_16x16x32_bf16 v[244:247], v[52:55], a[124:127], v[244:247]\n"
    "s_waitcnt lgkmcnt(4)\n"
    "s_add_u32 s24, s24, s83\n"
    "v_mfma_f32_16x16x32_bf16 v[152:155], v[56:59], a[112:115], v[152:155]\n"
    "s_addc_u32 s25, s25, 0\n"
    "ds_read_b64_tr_b16 v[24:25], v3 offset:8\n"
    "v_mfma_f32_16x16x32_bf16 v[184:187], v[56:59], a[116:119], v[184:187]\n"
    "s_mov_b32 s26, s84\n"
    "ds_read_b64_tr_b16 v[26:27], v3 offset:4104\n"
    "v_mfma_f32_16x16x32_bf16 v[216:219], v[56:59], a[120:123], v[216:219]\n"
    "s_mov_b32 s27, 0x20000\n"
    "v_mfma_f32_16x16x32_bf16 v[248:251], v[56:59], a[124:127], v[248:251]\n"
    "s_waitcnt lgkmcnt(4)\n"
    "s_mov_b64 s[28:29], s[22:23]\n"
    "v_mfma_f32_16x16x32_bf16 v[156:159], v[16:19], a[112:115], v[156:159]\n"
    "s_add_u32 s28, s28, s83\n"
    "ds_read_b64_tr_b16 v[28:29], v4 offset:0\n"
    "v_mfma_f32_16x16x32_bf16 v[188:191], v[16:19], a[116:119], v[188:191]\n"
    "s_addc_u32 s29, s29, 0\n"
    "ds_read_b64_tr_b16 v[30:31], v4 offset:4096\n"
    "v_mfma_f32_16x16x32_bf16 v[220:223], v[16:19], a[120:123], v[220:223]\n"
    "s_mov_b32 s30, s84\n"
    "v_mfma_f32_16x16x32_bf16 v[252:255], v[16:19], a[124:127], v[252:255]\n"
    "s_waitcnt vmcnt(40) lgkmcnt(4)\n"
    "s_mov_b32 s31, 0x20000\n"
    "v_mfma_f32_16x16x32_bf16 v[128:131], v[20:23], a[128:131], v[128:131]\n"
    "s_waitcnt vmcnt(39)\n"
    "s_mul_i32 s83, s38, 147456\n"
    "ds_read_b64_tr_b16 v[32:33], v4 offset:8\n"
    "v_mfma_f32_16x16x32_bf16 v[160:163], v[20:23], a[132:135], v[160:163]\n"
    "s_waitcnt vmcnt(38)\n"
    "s_add_u32 s20, s52, s83\n"
    "ds_read_b64_tr_b16 v[34:35], v4 offset:4104\n"
    "v_mfma_f32_16x16x32_bf16 v[192:195], v[20:23], a[136:139], v[192:195]\n"
    "s_waitcnt vmcnt(37)\n"
    "s_addc_u32 s21, s53, 0\n"
    "v_mfma_f32_16x16x32_bf16 v[224:227], v[20:23], a[140:143], v[224:227]\n"
    "s_waitcnt lgkmcnt(4)\n"
    "s_mov_b32 s23, 0x20000\n"
    "v_mfma_f32_16x16x32_bf16 v[132:135], v[24:27], a[128:131], v[132:135]\n"
    "s_mov_b64 s[8:9], s[20:21]\n"
    "ds_read_b64_tr_b16 v[36:37], v5 offset:0\n"
    "v_mfma_f32_16x16x32_bf16 v[164:167], v[24:27], a[132:135], v[164:167]\n"
    "s_mov_b32 s10, 147456\n"
    "ds_read_b64_tr_b16 v[38:39], v5 offset:4096\n"
    "v_mfma_f32_16x16x32_bf16 v[196:199], v[24:27], a[136:139], v[196:199]\n"
    "s_mov_b32 s11, 0x20000\n"
    "v_mfma_f32_16x16x32_bf16 v[228:231], v[24:27], a[140:143], v[228:231]\n"
    "s_waitcnt lgkmcnt(4)\n"
    "s_sub_u32 s14, s64, s19\n"
    "v_mfma_f32_16x16x32_bf16 v[136:139], v[28:31], a[128:131], v[136:139]\n"
    "s_subb_u32 s15, s65, 0\n"
    "ds_read_b64_tr_b16 v[48:49], v5 offset:8\n"
    "v_mfma_f32_16x16x32_bf16 v[168:171], v[28:31], a[132:135], v[168:171]\n"
    "s_cmp_eq_u32 s37, 0\n"
    "ds_read_b64_tr_b16 v[50:51], v5 offset:4104\n"
    "v_mfma_f32_16x16x32_bf16 v[200:203], v[28:31], a[136:139], v[200:203]\n"
    "s_cselect_b64 s[20:21], s[20:21], s[14:15]\n"
    "v_mfma_f32_16x16x32_bf16 v[232:235], v[28:31], a[140:143], v[232:235]\n"
    "s_waitcnt lgkmcnt(4)\n"
    "s_cselect_b32 s22, 36, 32\n"
    "v_mfma_f32_16x16x32_bf16 v[140:143], v[32:35], a[128:131], v[140:143]\n"
    "s_lshl_b32 s22, s22, 12\n"
    "ds_read_b64_tr_b16 v[52:53], v6 offset:0\n"
    "v_mfma_f32_16x16x32_bf16 v[172:175], v[32:35], a[132:135], v[172:175]\n"
    "s_lshl_b32 s83, s82, 2\n"
    "ds_read_b64_tr_b16 v[54:55], v6 offset:4096\n"
    "v_mfma_f32_16x16x32_bf16 v[204:207], v[32:35], a[136:139], v[204:207]\n"
    "s_add_u32 s83, s83, s6\n"
    "v_mfma_f32_16x16x32_bf16 v[236:239], v[32:35], a[140:143], v[236:239]\n"
    "s_waitcnt lgkmcnt(4)\n"
    "s_mul_i32 s83, s83, 589824\n"
    "v_mfma_f32_16x16x32_bf16 v[144:147], v[36:39], a[128:131], v[144:147]\n"
    "s_mov_b64 s[72:73], s[4:5]\n"
    "ds_read_b64_tr_b16 v[56:57], v6 offset:8\n"
    "v_mfma_f32_16x16x32_bf16 v[176:179], v[36:39], a[132:135], v[176:179]\n"
    "s_add_u32 s72, s72, s83\n"
    "ds_read_b64_tr_b16 v[58:59], v6 offset:4104\n"
    "v_mfma_f32_16x16x32_bf16 v[208:211], v[36:39], a[136:139], v[208:211]\n"
    "s_addc_u32 s73, s73, 0\n"
    "v_mfma_f32_16x16x32_bf16 v[240:243], v[36:39], a[140:143], v[240:243]\n"
    "s_waitcnt lgkmcnt(4)\n"
    "s_add_u32 s72, s72, 41943040\n"
    "v_mfma_f32_16x16x32_bf16 v[148:151], v[48:51], a[128:131], v[148:151]\n"
    "s_addc_u32 s73, s73, 0\n"
    "ds_read_b64_tr_b16 v[16:17], v3 offset:8192\n"
    "v_mfma_f32_16x16x32_bf16 v[180:183], v[48:51], a[132:135], v[180:183]\n"
    "s_mov_b32 s74, 589824\n"
    "ds_read_b64_tr_b16 v[18:19], v3 offset:12288\n"
    "v_mfma_f32_16x16x32_bf16 v[212:215], v[48:51], a[136:139], v[212:215]\n"
    "s_mov_b32 s75, 0x20000\n"
    "v_mfma_f32_16x16x32_bf16 v[244:247], v[48:51], a[140:143], v[244:247]\n"
    "s_waitcnt lgkmcnt(4)\n"
    "s_lshl_b32 s14, s80, 4\n"
    "v_mfma_f32_16x16x32_bf16 v[152:155], v[52:55], a[128:131], v[152:155]\n"
    "s_add_u32 s14, s14, s7\n"
    "ds_read_b64_tr_b16 v[20:21], v3 offset:8200\n"
    "v_mfma_f32_16x16x32_bf16 v[184:187], v[52:55], a[132:135], v[184:187]\n"
    "s_mul_i32 s14, s14, 9216\n"
    "ds_read_b64_tr_b16 v[22:23], v3 offset:12296\n"
    "v_mfma_f32_16x16x32_bf16 v[216:219], v[52:55], a[136:139], v[216:219]\n"
    "s_add_u32 s15, s14, 73728\n"
    "v_mfma_f32_16x16x32_bf16 v[248:251], v[52:55], a[140:143], v[248:251]\n"
    "s_waitcnt lgkmcnt(4)\n"
    "s_lshl_b32 s83, s82, 5\n"
    "v_mfma_f32_16x16x32_bf16 v[156:159], v[56:59], a[128:131], v[156:159]\n"
    "s_and_b32 s84, s81, 24\n"
    "ds_read_b64_tr_b16 v[24:25], v4 offset:8192\n"
    "v_mfma_f32_16x16x32_bf16 v[188:191], v[56:59], a[132:135], v[188:191]\n"
    "s_add_u32 s84, s84, s83\n"
    "ds_read_b64_tr_b16 v[26:27], v4 offset:12288\n"
    "v_mfma_f32_16x16x32_bf16 v[220:223], v[56:59], a[136:139], v[220:223]\n"
    "s_lshl_b32 s84, s84, 2\n"
    "v_mfma_f32_16x16x32_bf16 v[252:255], v[56:59], a[140:143], v[252:255]\n"
    "s_waitcnt vmcnt(36) lgkmcnt(4)\n"
    "s_add_u32 s84, s84, s80\n"
    "v_mfma_f32_16x16x32_bf16 v[128:131], v[16:19], a[144:147], v[128:131]\n"
    "s_waitcnt vmcnt(35)\n"
    "s_lshl_b32 s12, s84, 7\n"
    "ds_read_b64_tr_b16 v[28:29], v4 offset:8200\n"
    "v_mfma_f32_16x16x32_bf16 v[160:163], v[16:19], a[148:151], v[160:163]\n"
    "s_waitcnt vmcnt(34)\n"
    "s_add_u32 s84, s83, s6\n"
    "ds_read_b64_tr_b16 v[30:31], v4 offset:12296\n"
    "v_mfma_f32_16x16x32_bf16 v[192:195], v[16:19], a[152:155], v[192:195]\n"
    "s_waitcnt vmcnt(33)\n"
    "s_lshl_b32 s84, s84, 2\n"
    "v_mfma_f32_16x16x32_bf16 v[224:227], v[16:19], a[156:159], v[224:227]\n"
    "s_waitcnt lgkmcnt(4)\n"
    "s_add_u32 s84, s84, s38\n"
    "v_mfma_f32_16x16x32_bf16 v[132:135], v[20:23], a[144:147], v[132:135]\n"
    "s_lshl_b32 s13, s84, 7\n"
    "ds_read_b64_tr_b16 v[32:33], v5 offset:8192\n"
    "v_mfma_f32_16x16x32_bf16 v[164:167], v[20:23], a[148:151], v[164:167]\n"
    "ds_read_b64_tr_b16 v[34:35], v5 offset:12288\n"
    "v_and_b32 v1, 0x70, v0\n"
    "v_mfma_f32_16x16x32_bf16 v[196:199], v[20:23], a[152:155], v[196:199]\n"
    "v_lshlrev_b32 v1, 5, v1\n"
    "v_mfma_f32_16x16x32_bf16 v[228:231], v[20:23], a[156:159], v[228:231]\n"
    "s_waitcnt lgkmcnt(4)\n"
    "s_lshl_b32 s83, s82, 7\n"
    "v_mfma_f32_16x16x32_bf16 v[136:139], v[24:27], a[144:147], v[136:139]\n"
    "s_add_u32 s83, s83, 131072\n"
    "ds_read_b64_tr_b16 v[36:37], v5 offset:8200\n"
    "v_mfma_f32_16x16x32_bf16 v[168:171], v[24:27], a[148:151], v[168:171]\n"
    "s_or_b32 s84, s81, s80\n"
    "ds_read_b64_tr_b16 v[38:39], v5 offset:12296\n"
    "v_mfma_f32_16x16x32_bf16 v[200:203], v[24:27], a[152:155], v[200:203]\n"
    "s_cselect_b32 s83, 0x40000000, s83\n"
    "v_mfma_f32_16x16x32_bf16 v[232:235], v[24:27], a[156:159], v[232:235]\n"
    "s_waitcnt lgkmcnt(4)\n"
    "v_mov_b32 v64, 0x40000000\n"
    "v_mfma_f32_16x16x32_bf16 v[140:143], v[28:31], a[144:147], v[140:143]\n"
    "ds_read_b64_tr_b16 v[48:49], v6 offset:8192\n"
    "v_writelane_b32 v64, s83, 0\n"
    "v_mfma_f32_16x16x32_bf16 v[172:175], v[28:31], a[148:151], v[172:175]\n"
    "ds_read_b64_tr_b16 v[50:51], v6 offset:12288\n"
    "v_add_u32 v65, 30, v46\n"
    "v_mfma_f32_16x16x32_bf16 v[204:207], v[28:31], a[152:155], v[204:207]\n"
    "buffer_store_dword v65, v64, s[76:79], 0 offen sc1\n"
    "v_mfma_f32_16x16x32_bf16 v[236:239], v[28:31], a[156:159], v[236:239]\n"
    "s_waitcnt lgkmcnt(4)\n"
    "s_and_b32 s83, s82, 3\n"
    "v_mfma_f32_16x16x32_bf16 v[144:147], v[32:35], a[144:147], v[144:147]\n"
    "s_lshl_b32 s83, s83, 1\n"
    "ds_read_b64_tr_b16 v[52:53], v6 offset:8200\n"
    "v_mfma_f32_16x16x32_bf16 v[176:179], v[32:35], a[148:151], v[176:179]\n"
    "s_and_b32 s84, s81, 1\n"
    "ds_read_b64_tr_b16 v[54:55], v6 offset:12296\n"
    "v_mfma_f32_16x16x32_bf16 v[208:211], v[32:35], a[152:155], v[208:211]\n"
    "s_add_u32 s83, s83, s84\n"
    "v_mfma_f32_16x16x32_bf16 v[240:243], v[32:35], a[156:159], v[240:243]\n"
    "s_waitcnt lgkmcnt(4)\n"
    "s_lshl_b32 s83, s83, 2\n"
    "v_mfma_f32_16x16x32_bf16 v[148:151], v[36:39], a[144:147], v[148:151]\n"
    "s_add_u32 s83, s83, s80\n"
    "ds_read_b64_tr_b16 v[56:57], v3 offset:16384\n"
    "v_mfma_f32_16x16x32_bf16 v[180:183], v[36:39], a[148:151], v[180:183]\n"
    "s_mul_i32 s83, s83, 2048\n"
    "ds_read_b64_tr_b16 v[58:59], v3 offset:20480\n"
    "v_mfma_f32_16x16x32_bf16 v[212:215], v[36:39], a[152:155], v[212:215]\n"
    "s_bitcmp0_b32 s82, 2\n"
    "v_mfma_f32_16x16x32_bf16 v[244:247], v[36:39], a[156:159], v[244:247]\n"
    "s_waitcnt lgkmcnt(4)\n"
    "s_cselect_b64 s[100:101], s[56:57], s[60:61]\n"
    "v_mfma_f32_16x16x32_bf16 v[152:155], v[48:51], a[144:147], v[152:155]\n"
    "s_add_u32 s100, s100, s83\n"
    "ds_read_b64_tr_b16 v[16:17], v3 offset:16392\n"
    "v_mfma_f32_16x16x32_bf16 v[184:187], v[48:51], a[148:151], v[184:187]\n"
    "s_addc_u32 s101, s101, 0\n"
    "ds_read_b64_tr_b16 v[18:19], v3 offset:20488\n"
    "v_mfma_f32_16x16x32_bf16 v[216:219], v[48:51], a[152:155], v[216:219]\n"
    "v_lshlrev_b32 v61, 3, v0\n"
    "v_mfma_f32_16x16x32_bf16 v[248:251], v[48:51], a[156:159], v[248:251]\n"
    "s_waitcnt lgkmcnt(4)\n"
    "v_mfma_f32_16x16x32_bf16 v[156:159], v[52:55], a[144:147], v[156:159]\n"
    "ds_read_b64_tr_b16 v[20:21], v4 offset:16384\n"
    "v_mfma_f32_16x16x32_bf16 v[188:191], v[52:55], a[148:151], v[188:191]\n"
    "ds_read_b64_tr_b16 v[22:23], v4 offset:20480\n"
    "v_mfma_f32_16x16x32_bf16 v[220:223], v[52:55], a[152:155], v[220:223]\n"
    "v_mfma_f32_16x16x32_bf16 v[252:255], v[52:55], a[156:159], v[252:255]\n"
    "s_waitcnt vmcnt(33) lgkmcnt(4)\n"
    "v_mfma_f32_16x16x32_bf16 v[128:131], v[56:59], a[160:163], v[128:131]\n"
    "s_waitcnt vmcnt(32)\n"
    "ds_read_b64_tr_b16 v[24:25], v4 offset:16392\n"
    "v_mfma_f32_16x16x32_bf16 v[160:163], v[56:59], a[164:167], v[160:163]\n"
    "s_waitcnt vmcnt(31)\n"
    "ds_read_b64_tr_b16 v[26:27], v4 offset:20488\n"
    "v_mfma_f32_16x16x32_bf16 v[192:195], v[56:59], a[168:171], v[192:195]\n"
    "s_waitcnt vmcnt(30)\n"
    "v_mfma_f32_16x16x32_bf16 v[224:227], v[56:59], a[172:175], v[224:227]\n"
    "s_waitcnt lgkmcnt(4)\n"
    "v_mfma_f32_16x16x32_bf16 v[132:135], v[16:19], a[160:163], v[132:135]\n"
    "ds_read_b64_tr_b16 v[28:29], v5 offset:16384\n"
    "v_mfma_f32_16x16x32_bf16 v[164:167], v[16:19], a[164:167], v[164:167]\n"
    "ds_read_b64_tr_b16 v[30:31], v5 offset:20480\n"
    "v_mfma_f32_16x16x32_bf16 v[196:199], v[16:19], a[168:171], v[196:199]\n"
    "v_mfma_f32_16x16x32_bf16 v[228:231], v[16:19], a[172:175], v[228:231]\n"
    "s_waitcnt lgkmcnt(4)\n"
    "v_mfma_f32_16x16x32_bf16 v[136:139], v[20:23], a[160:163], v[136:139]\n"
    "ds_read_b64_tr_b16 v[32:33], v5 offset:16392\n"
    "v_mfma_f32_16x16x32_bf16 v[168:171], v[20:23], a[164:167], v[168:171]\n"
    "ds_read_b64_tr_b16 v[34:35], v5 offset:20488\n"
    "v_mfma_f32_16x16x32_bf16 v[200:203], v[20:23], a[168:171], v[200:203]\n"
    "v_mfma_f32_16x16x32_bf16 v[232:235], v[20:23], a[172:175], v[232:235]\n"
    "s_waitcnt lgkmcnt(4)\n"
    "v_mfma_f32_16x16x32_bf16 v[140:143], v[24:27], a[160:163], v[140:143]\n"
    "ds_read_b64_tr_b16 v[36:37], v6 offset:16384\n"
    "v_mfma_f32_16x16x32_bf16 v[172:175], v[24:27], a[164:167], v[172:175]\n"
    "ds_read_b64_tr_b16 v[38:39], v6 offset:20480\n"
    "v_mfma_f32_16x16x32_bf16 v[204:207], v[24:27], a[168:171], v[204:207]\n"
    "v_mfma_f32_16x16x32_bf16 v[236:239], v[24:27], a[172:175], v[236:239]\n"
    "s_waitcnt lgkmcnt(4)\n"
    "v_mfma_f32_16x16x32_bf16 v[144:147], v[28:31], a[160:163], v[144:147]\n"
    "ds_read_b64_tr_b16 v[48:49], v6 offset:16392\n"
    "v_mfma_f32_16x16x32_bf16 v[176:179], v[28:31], a[164:167], v[176:179]\n"
    "ds_read_b64_tr_b16 v[50:51], v6 offset:20488\n"
    "v_mfma_f32_16x16x32_bf16 v[208:211], v[28:31], a[168:171], v[208:211]\n"
    "v_mfma_f32_16x16x32_bf16 v[240:243], v[28:31], a[172:175], v[240:243]\n"
    "s_waitcnt lgkmcnt(4)\n"
    "v_mfma_f32_16x16x32_bf16 v[148:151], v[32:35], a[160:163], v[148:151]\n"
    "ds_read_b64_tr_b16 v[52:53], v3 offset:24576\n"
    "v_mfma_f32_16x16x32_bf16 v[180:183], v[32:35], a[164:167], v[180:183]\n"
    "ds_read_b64_tr_b16 v[54:55], v3 offset:28672\n"
    "v_mfma_f32_16x16x32_bf16 v[212:215], v[32:35], a[168:171], v[212:215]\n"
    "v_mfma_f32_16x16x32_bf16 v[244:247], v[32:35], a[172:175], v[244:247]\n"
    "s_waitcnt lgkmcnt(4)\n"
    "v_mfma_f32_16x16x32_bf16 v[152:155], v[36:39], a[160:163], v[152:155]\n"
    "ds_read_b64_tr_b16 v[56:57], v3 offset:24584\n"
    "v_mfma_f32_16x16x32_bf16 v[184:187], v[36:39], a[164:167], v[184:187]\n"
    "ds_read_b64_tr_b16 v[58:59], v3 offset:28680\n"
    "v_mfma_f32_16x16x32_bf16 v[216:219], v[36:39], a[168:171], v[216:219]\n"
    "v_mfma_f32_16x16x32_bf16 v[248:251], v[36:39], a[172:175], v[248:251]\n"
    "s_waitcnt lgkmcnt(4)\n"
    "v_mfma_f32_16x16x32_bf16 v[156:159], v[48:51], a[160:163], v[156:159]\n"
    "ds_read_b64_tr_b16 v[16:17], v4 offset:24576\n"
    "v_mfma_f32_16x16x32_bf16 v[188:191], v[48:51], a[164:167], v[188:191]\n"
    "ds_read_b64_tr_b16 v[18:19], v4 offset:28672\n"
    "v_mfma_f32_16x16x32_bf16 v[220:223], v[48:51], a[168:171], v[220:223]\n"
    "v_mfma_f32_16x16x32_bf16 v[252:255], v[48:51], a[172:175], v[252:255]\n"
    "s_waitcnt vmcnt(29) lgkmcnt(4)\n"
    "v_mfma_f32_16x16x32_bf16 v[128:131], v[52:55], a[176:179], v[128:131]\n"
    "s_waitcnt vmcnt(28)\n"
    "ds_read_b64_tr_b16 v[20:21], v4 offset:24584\n"
    "v_mfma_f32_16x16x32_bf16 v[160:163], v[52:55], a[180:183], v[160:163]\n"
    "s_waitcnt vmcnt(27)\n"
    "ds_read_b64_tr_b16 v[22:23], v4 offset:28680\n"
    "v_mfma_f32_16x16x32_bf16 v[192:195], v[52:55], a[184:187], v[192:195]\n"
    "s_waitcnt vmcnt(26)\n"
    "v_mfma_f32_16x16x32_bf16 v[224:227], v[52:55], a[188:191], v[224:227]\n"
    "s_waitcnt lgkmcnt(4)\n"
    "v_mfma_f32_16x16x32_bf16 v[132:135], v[56:59], a[176:179], v[132:135]\n"
    "ds_read_b64_tr_b16 v[24:25], v5 offset:24576\n"
    "v_mfma_f32_16x16x32_bf16 v[164:167], v[56:59], a[180:183], v[164:167]\n"
    "ds_read_b64_tr_b16 v[26:27], v5 offset:28672\n"
    "v_mfma_f32_16x16x32_bf16 v[196:199], v[56:59], a[184:187], v[196:199]\n"
    "v_mfma_f32_16x16x32_bf16 v[228:231], v[56:59], a[188:191], v[228:231]\n"
    "s_waitcnt lgkmcnt(4)\n"
    "v_mfma_f32_16x16x32_bf16 v[136:139], v[16:19], a[176:179], v[136:139]\n"
    "ds_read_b64_tr_b16 v[28:29], v5 offset:24584\n"
    "v_mfma_f32_16x16x32_bf16 v[168:171], v[16:19], a[180:183], v[168:171]\n"
    "ds_read_b64_tr_b16 v[30:31], v5 offset:28680\n"
    "v_mfma_f32_16x16x32_bf16 v[200:203], v[16:19], a[184:187], v[200:203]\n"
    "v_mfma_f32_16x16x32_bf16 v[232:235], v[16:19], a[188:191], v[232:235]\n"
    "s_waitcnt lgkmcnt(4)\n"
    "v_mfma_f32_16x16x32_bf16 v[140:143], v[20:23], a[176:179], v[140:143]\n"
    "ds_read_b64_tr_b16 v[32:33], v6 offset:24576\n"
    "v_mfma_f32_16x16x32_bf16 v[172:175], v[20:23], a[180:183], v[172:175]\n"
    "ds_read_b64_tr_b16 v[34:35], v6 offset:28672\n"
    "v_mfma_f32_16x16x32_bf16 v[204:207], v[20:23], a[184:187], v[204:207]\n"
    "v_mfma_f32_16x16x32_bf16 v[236:239], v[20:23], a[188:191], v[236:239]\n"
    "s_waitcnt lgkmcnt(4)\n"
    "v_mfma_f32_16x16x32_bf16 v[144:147], v[24:27], a[176:179], v[144:147]\n"
    "ds_read_b64_tr_b16 v[36:37], v6 offset:24584\n"
    "v_mfma_f32_16x16x32_bf16 v[176:179], v[24:27], a[180:183], v[176:179]\n"
    "ds_read_b64_tr_b16 v[38:39], v6 offset:28680\n"
    "v_mfma_f32_16x16x32_bf16 v[208:211], v[24:27], a[184:187], v[208:211]\n"
    "v_mfma_f32_16x16x32_bf16 v[240:243], v[24:27], a[188:191], v[240:243]\n"
    "s_waitcnt lgkmcnt(4)\n"
    "s_barrier\n"
    "v_mfma_f32_16x16x32_bf16 v[148:151], v[28:31], a[176:179], v[148:151]\n"
    "s_mov_b32 s91, s86\n"
    "ds_read_b64_tr_b16 v[48:49], v3 offset:32768\n"
    "v_mfma_f32_16x16x32_bf16 v[180:183], v[28:31], a[180:183], v[180:183]\n"
    "s_add_u32 m0, s91, 0\n"
    "s_add_u32 s87, s90, 0\n"
    "buffer_load_dwordx4 v7, s[44:47], s87 offen lds\n"
    "v_mfma_f32_16x16x32_bf16 v[212:215], v[28:31], a[184:187], v[212:215]\n"
    "ds_read_b64_tr_b16 v[50:51], v3 offset:36864\n"
    "v_mfma_f32_16x16x32_bf16 v[244:247], v[28:31], a[188:191], v[244:247]\n"
    "s_waitcnt lgkmcnt(4)\n"
    "v_mfma_f32_16x16x32_bf16 v[152:155], v[32:35], a[176:179], v[152:155]\n"
    "ds_read_b64_tr_b16 v[52:53], v3 offset:32776\n"
    "v_mfma_f32_16x16x32_bf16 v[184:187], v[32:35], a[180:183], v[184:187]\n"
    "ds_read_b64_tr_b16 v[54:55], v3 offset:36872\n"
    "v_mfma_f32_16x16x32_bf16 v[216:219], v[32:35], a[184:187], v[216:219]\n"
    "v_mfma_f32_16x16x32_bf16 v[248:251], v[32:35], a[188:191], v[248:251]\n"
    "s_waitcnt lgkmcnt(4)\n"
    "v_mfma_f32_16x16x32_bf16 v[156:159], v[36:39], a[176:179], v[156:159]\n"
    "ds_read_b64_tr_b16 v[56:57], v4 offset:32768\n"
    "v_mfma_f32_16x16x32_bf16 v[188:191], v[36:39], a[180:183], v[188:191]\n"
    "ds_read_b64_tr_b16 v[58:59], v4 offset:36864\n"
    "v_mfma_f32_16x16x32_bf16 v[220:223], v[36:39], a[184:187], v[220:223]\n"
    "buffer_load_dwordx4 v7, s[48:51], s87 offen offset:1024 lds\n"
    "v_mfma_f32_16x16x32_bf16 v[252:255], v[36:39], a[188:191], v[252:255]\n"
    "s_waitcnt vmcnt(27) lgkmcnt(4)\n"
    "v_mfma_f32_16x16x32_bf16 v[128:131], v[48:51], a[192:195], v[128:131]\n"
    "s_waitcnt vmcnt(26)\n"
    "ds_read_b64_tr_b16 v[16:17], v4 offset:32776\n"
    "v_mfma_f32_16x16x32_bf16 v[160:163], v[48:51], a[196:199], v[160:163]\n"
    "s_waitcnt vmcnt(25)\n"
    "ds_read_b64_tr_b16 v[18:19], v4 offset:36872\n"
    "v_mfma_f32_16x16x32_bf16 v[192:195], v[48:51], a[200:203], v[192:195]\n"
    "s_waitcnt vmcnt(24)\n"
    "v_mfma_f32_16x16x32_bf16 v[224:227], v[48:51], a[204:207], v[224:227]\n"
    "s_waitcnt lgkmcnt(4)\n"
    "v_mfma_f32_16x16x32_bf16 v[132:135], v[52:55], a[192:195], v[132:135]\n"
    "ds_read_b64_tr_b16 v[20:21], v5 offset:32768\n"
    "v_mfma_f32_16x16x32_bf16 v[164:167], v[52:55], a[196:199], v[164:167]\n"
    "ds_read_b64_tr_b16 v[22:23], v5 offset:36864\n"
    "v_mfma_f32_16x16x32_bf16 v[196:199], v[52:55], a[200:203], v[196:199]\n"
    "v_mfma_f32_16x16x32_bf16 v[228:231], v[52:55], a[204:207], v[228:231]\n"
    "s_waitcnt lgkmcnt(4)\n"
    "s_add_u32 m0, s91, 8192\n"
    "s_add_u32 s87, s90, 589824\n"
    "buffer_load_dwordx4 v7, s[44:47], s87 offen lds\n"
    "v_mfma_f32_16x16x32_bf16 v[136:139], v[56:59], a[192:195], v[136:139]\n"
    "ds_read_b64_tr_b16 v[24:25], v5 offset:32776\n"
    "v_mfma_f32_16x16x32_bf16 v[168:171], v[56:59], a[196:199], v[168:171]\n"
    "ds_read_b64_tr_b16 v[26:27], v5 offset:36872\n"
    "v_mfma_f32_16x16x32_bf16 v[200:203], v[56:59], a[200:203], v[200:203]\n"
    "v_mfma_f32_16x16x32_bf16 v[232:235], v[56:59], a[204:207], v[232:235]\n"
    "s_waitcnt lgkmcnt(4)\n"
    "v_mfma_f32_16x16x32_bf16 v[140:143], v[16:19], a[192:195], v[140:143]\n"
    "ds_read_b64_tr_b16 v[28:29], v6 offset:32768\n"
    "v_mfma_f32_16x16x32_bf16 v[172:175], v[16:19], a[196:199], v[172:175]\n"
    "ds_read_b64_tr_b16 v[30:31], v6 offset:36864\n"
    "v_mfma_f32_16x16x32_bf16 v[204:207], v[16:19], a[200:203], v[204:207]\n"
    "v_mfma_f32_16x16x32_bf16 v[236:239], v[16:19], a[204:207], v[236:239]\n"
    "s_waitcnt lgkmcnt(4)\n"
    "v_mfma_f32_16x16x32_bf16 v[144:147], v[20:23], a[192:195], v[144:147]\n"
    "buffer_load_dwordx4 v7, s[48:51], s87 offen offset:1024 lds\n"
    "v_mfma_f32_16x16x32_bf16 v[176:179], v[20:23], a[196:199], v[176:179]\n"
    "ds_read_b64_tr_b16 v[32:33], v6 offset:32776\n"
    "v_mfma_f32_16x16x32_bf16 v[208:211], v[20:23], a[200:203], v[208:211]\n"
    "ds_read_b64_tr_b16 v[34:35], v6 offset:36872\n"
    "v_mfma_f32_16x16x32_bf16 v[240:243], v[20:23], a[204:207], v[240:243]\n"
    "s_waitcnt lgkmcnt(4)\n"
    "v_mfma_f32_16x16x32_bf16 v[148:151], v[24:27], a[192:195], v[148:151]\n"
    "ds_read_b64_tr_b16 v[36:37], v3 offset:40960\n"
    "v_mfma_f32_16x16x32_bf16 v[180:183], v[24:27], a[196:199], v[180:183]\n"
    "ds_read_b64_tr_b16 v[38:39], v3 offset:45056\n"
    "v_mfma_f32_16x16x32_bf16 v[212:215], v[24:27], a[200:203], v[212:215]\n"
    "v_mfma_f32_16x16x32_bf16 v[244:247], v[24:27], a[204:207], v[244:247]\n"
    "s_waitcnt lgkmcnt(4)\n"
    "v_mfma_f32_16x16x32_bf16 v[152:155], v[28:31], a[192:195], v[152:155]\n"
    "ds_read_b64_tr_b16 v[48:49], v3 offset:40968\n"
    "v_mfma_f32_16x16x32_bf16 v[184:187], v[28:31], a[196:199], v[184:187]\n"
    "s_add_u32 m0, s91, 16384\n"
    "s_add_u32 s87, s90, 1179648\n"
    "buffer_load_dwordx4 v7, s[44:47], s87 offen lds\n"
    "v_mfma_f32_16x16x32_bf16 v[216:219], v[28:31], a[200:203], v[216:219]\n"
    "ds_read_b64_tr_b16 v[50:51], v3 offset:45064\n"
    "v_mfma_f32_16x16x32_bf16 v[248:251], v[28:31], a[204:207], v[248:251]\n"
    "s_waitcnt lgkmcnt(4)\n"
    "v_mfma_f32_16x16x32_bf16 v[156:159], v[32:35], a[192:195], v[156:159]\n"
    "ds_read_b64_tr_b16 v[52:53], v4 offset:40960\n"
    "v_mfma_f32_16x16x32_bf16 v[188:191], v[32:35], a[196:199], v[188:191]\n"
    "ds_read_b64_tr_b16 v[54:55], v4 offset:45056\n"
    "v_mfma_f32_16x16x32_bf16 v[220:223], v[32:35], a[200:203], v[220:223]\n"
    "v_mfma_f32_16x16x32_bf16 v[252:255], v[32:35], a[204:207], v[252:255]\n"
    "s_waitcnt vmcnt(26) lgkmcnt(4)\n"
    "v_mfma_f32_16x16x32_bf16 v[128:131], v[36:39], a[208:211], v[128:131]\n"
    "s_waitcnt vmcnt(25)\n"
    "ds_read_b64_tr_b16 v[56:57], v4 offset:40968\n"
    "v_mfma_f32_16x16x32_bf16 v[160:163], v[36:39], a[212:215], v[160:163]\n"
    "s_waitcnt vmcnt(24)\n"
    "ds_read_b64_tr_b16 v[58:59], v4 offset:45064\n"
    "v_mfma_f32_16x16x32_bf16 v[192:195], v[36:39], a[216:219], v[192:195]\n"
    "s_waitcnt vmcnt(23)\n"
    "buffer_load_dwordx4 v7, s[48:51], s87 offen offset:1024 lds\n"
    "v_mfma_f32_16x16x32_bf16 v[224:227], v[36:39], a[220:223], v[224:227]\n"
    "s_waitcnt lgkmcnt(4)\n"
    "v_mfma_f32_16x16x32_bf16 v[132:135], v[48:51], a[208:211], v[132:135]\n"
    "ds_read_b64_tr_b16 v[16:17], v5 offset:40960\n"
    "v_mfma_f32_16x16x32_bf16 v[164:167], v[48:51], a[212:215], v[164:167]\n"
    "ds_read_b64_tr_b16 v[18:19], v5 offset:45056\n"
    "v_mfma_f32_16x16x32_bf16 v[196:199], v[48:51], a[216:219], v[196:199]\n"
    "v_mfma_f32_16x16x32_bf16 v[228:231], v[48:51], a[220:223], v[228:231]\n"
    "s_waitcnt lgkmcnt(4)\n"
    "v_mfma_f32_16x16x32_bf16 v[136:139], v[52:55], a[208:211], v[136:139]\n"
    "ds_read_b64_tr_b16 v[20:21], v5 offset:40968\n"
    "v_mfma_f32_16x16x32_bf16 v[168:171], v[52:55], a[212:215], v[168:171]\n"
    "ds_read_b64_tr_b16 v[22:23], v5 offset:45064\n"
    "v_mfma_f32_16x16x32_bf16 v[200:203], v[52:55], a[216:219], v[200:203]\n"
    "v_mfma_f32_16x16x32_bf16 v[232:235], v[52:55], a[220:223], v[232:235]\n"
    "s_waitcnt lgkmcnt(4)\n"
    "s_add_u32 m0, s91, 24576\n"
    "s_add_u32 s87, s90, 1769472\n"
    "buffer_load_dwordx4 v7, s[44:47], s87 offen lds\n"
    "v_mfma_f32_16x16x32_bf16 v[140:143], v[56:59], a[208:211], v[140:143]\n"
    "ds_read_b64_tr_b16 v[24:25], v6 offset:40960\n"
    "v_mfma_f32_16x16x32_bf16 v[172:175], v[56:59], a[212:215], v[172:175]\n"
    "ds_read_b64_tr_b16 v[26:27], v6 offset:45056\n"
    "v_mfma_f32_16x16x32_bf16 v[204:207], v[56:59], a[216:219], v[204:207]\n"
    "v_mfma_f32_16x16x32_bf16 v[236:239], v[56:59], a[220:223], v[236:239]\n"
    "s_waitcnt lgkmcnt(4)\n"
    "v_mfma_f32_16x16x32_bf16 v[144:147], v[16:19], a[208:211], v[144:147]\n"
    "ds_read_b64_tr_b16 v[28:29], v6 offset:40968\n"
    "v_mfma_f32_16x16x32_bf16 v[176:179], v[16:19], a[212:215], v[176:179]\n"
    "ds_read_b64_tr_b16 v[30:31], v6 offset:45064\n"
    "v_mfma_f32_16x16x32_bf16 v[208:211], v[16:19], a[216:219], v[208:211]\n"
    "v_mfma_f32_16x16x32_bf16 v[240:243], v[16:19], a[220:223], v[240:243]\n"
    "s_waitcnt lgkmcnt(4)\n"
    "v_mfma_f32_16x16x32_bf16 v[148:151], v[20:23], a[208:211], v[148:151]\n"
    "buffer_load_dwordx4 v7, s[48:51], s87 offen offset:1024 lds\n"
    "v_mfma_f32_16x16x32_bf16 v[180:183], v[20:23], a[212:215], v[180:183]\n"
    "ds_read_b64_tr_b16 v[32:33], v3 offset:49152\n"
    "v_mfma_f32_16x16x32_bf16 v[212:215], v[20:23], a[216:219], v[212:215]\n"
    "ds_read_b64_tr_b16 v[34:35], v3 offset:53248\n"
    "v_mfma_f32_16x16x32_bf16 v[244:247], v[20:23], a[220:223], v[244:247]\n"
    "s_waitcnt lgkmcnt(4)\n"
    "v_mfma_f32_16x16x32_bf16 v[152:155], v[24:27], a[208:211], v[152:155]\n"
    "ds_read_b64_tr_b16 v[36:37], v3 offset:49160\n"
    "v_mfma_f32_16x16x32_bf16 v[184:187], v[24:27], a[212:215], v[184:187]\n"
    "ds_read_b64_tr_b16 v[38:39], v3 offset:53256\n"
    "v_mfma_f32_16x16x32_bf16 v[216:219], v[24:27], a[216:219], v[216:219]\n"
    "v_mfma_f32_16x16x32_bf16 v[248:251], v[24:27], a[220:223], v[248:251]\n"
    "s_waitcnt lgkmcnt(4)\n"
    "v_mfma_f32_16x16x32_bf16 v[156:159], v[28:31], a[208:211], v[156:159]\n"
    "ds_read_b64_tr_b16 v[48:49], v4 offset:49152\n"
    "v_mfma_f32_16x16x32_bf16 v[188:191], v[28:31], a[212:215], v[188:191]\n"
    "s_add_u32 m0, s91, 32768\n"
    "s_add_u32 s87, s90, 2359296\n"
    "buffer_load_dwordx4 v7, s[44:47], s87 offen lds\n"
    "v_mfma_f32_16x16x32_bf16 v[220:223], v[28:31], a[216:219], v[220:223]\n"
    "ds_read_b64_tr_b16 v[50:51], v4 offset:53248\n"
    "v_mfma_f32_16x16x32_bf16 v[252:255], v[28:31], a[220:223], v[252:255]\n"
    "s_waitcnt vmcnt(26) lgkmcnt(4)\n"
    "v_mfma_f32_16x16x32_bf16 v[128:131], v[32:35], a[224:227], v[128:131]\n"
    "s_waitcnt vmcnt(25)\n"
    "ds_read_b64_tr_b16 v[52:53], v4 offset:49160\n"
    "v_mfma_f32_16x16x32_bf16 v[160:163], v[32:35], a[228:231], v[160:163]\n"
    "s_waitcnt vmcnt(24)\n"
    "ds_read_b64_tr_b16 v[54:55], v4 offset:53256\n"
    "v_mfma_f32_16x16x32_bf16 v[192:195], v[32:35], a[232:235], v[192:195]\n"
    "s_waitcnt vmcnt(23)\n"
    "v_mfma_f32_16x16x32_bf16 v[224:227], v[32:35], a[236:239], v[224:227]\n"
    "s_waitcnt lgkmcnt(4)\n"
    "v_mfma_f32_16x16x32_bf16 v[132:135], v[36:39], a[224:227], v[132:135]\n"
    "ds_read_b64_tr_b16 v[56:57], v5 offset:49152\n"
    "v_mfma_f32_16x16x32_bf16 v[164:167], v[36:39], a[228:231], v[164:167]\n"
    "ds_read_b64_tr_b16 v[58:59], v5 offset:53248\n"
    "v_mfma_f32_16x16x32_bf16 v[196:199], v[36:39], a[232:235], v[196:199]\n"
    "buffer_load_dwordx4 v7, s[48:51], s87 offen offset:1024 lds\n"
    "v_mfma_f32_16x16x32_bf16 v[228:231], v[36:39], a[236:239], v[228:231]\n"
    "s_waitcnt lgkmcnt(4)\n"
    "v_mfma_f32_16x16x32_bf16 v[136:139], v[48:51], a[224:227], v[136:139]\n"
    "ds_read_b64_tr_b16 v[16:17], v5 offset:49160\n"
    "v_mfma_f32_16x16x32_bf16 v[168:171], v[48:51], a[228:231], v[168:171]\n"
    "ds_read_b64_tr_b16 v[18:19], v5 offset:53256\n"
    "v_mfma_f32_16x16x32_bf16 v[200:203], v[48:51], a[232:235], v[200:203]\n"
    "v_mfma_f32_16x16x32_bf16 v[232:235], v[48:51], a[236:239], v[232:235]\n"
    "s_waitcnt lgkmcnt(4)\n"
    "v_mfma_f32_16x16x32_bf16 v[140:143], v[52:55], a[224:227], v[140:143]\n"
    "ds_read_b64_tr_b16 v[20:21], v6 offset:49152\n"
    "v_mfma_f32_16x16x32_bf16 v[172:175], v[52:55], a[228:231], v[172:175]\n"
    "ds_read_b64_tr_b16 v[22:23], v6 offset:53248\n"
    "v_mfma_f32_16x16x32_bf16 v[204:207], v[52:55], a[232:235], v[204:207]\n"
    "v_mfma_f32_16x16x32_bf16 v[236:239], v[52:55], a[236:239], v[236:239]\n"
    "s_waitcnt lgkmcnt(4)\n"
    "s_add_u32 m0, s91, 40960\n"
    "s_add_u32 s87, s90, 2949120\n"
    "buffer_load_dwordx4 v7, s[44:47], s87 offen lds\n"
    "v_mfma_f32_16x16x32_bf16 v[144:147], v[56:59], a[224:227], v[144:147]\n"
    "ds_read_b64_tr_b16 v[24:25], v6 offset:49160\n"
    "v_mfma_f32_16x16x32_bf16 v[176:179], v[56:59], a[228:231], v[176:179]\n"
    "ds_read_b64_tr_b16 v[26:27], v6 offset:53256\n"
    "v_mfma_f32_16x16x32_bf16 v[208:211], v[56:59], a[232:235], v[208:211]\n"
    "v_mfma_f32_16x16x32_bf16 v[240:243], v[56:59], a[236:239], v[240:243]\n"
    "s_waitcnt lgkmcnt(4)\n"
    "v_mfma_f32_16x16x32_bf16 v[148:151], v[16:19], a[224:227], v[148:151]\n"
    "ds_read_b64_tr_b16 v[28:29], v3 offset:57344\n"
    "v_mfma_f32_16x16x32_bf16 v[180:183], v[16:19], a[228:231], v[180:183]\n"
    "ds_read_b64_tr_b16 v[30:31], v3 offset:61440\n"
    "v_mfma_f32_16x16x32_bf16 v[212:215], v[16:19], a[232:235], v[212:215]\n"
    "v_mfma_f32_16x16x32_bf16 v[244:247], v[16:19], a[236:239], v[244:247]\n"
    "s_waitcnt lgkmcnt(4)\n"
    "v_mfma_f32_16x16x32_bf16 v[152:155], v[20:23], a[224:227], v[152:155]\n"
    "buffer_load_dwordx4 v7, s[48:51], s87 offen offset:1024 lds\n"
    "v_mfma_f32_16x16x32_bf16 v[184:187], v[20:23], a[228:231], v[184:187]\n"
    "ds_read_b64_tr_b16 v[32:33], v3 offset:57352\n"
    "v_mfma_f32_16x16x32_bf16 v[216:219], v[20:23], a[232:235], v[216:219]\n"
    "ds_read_b64_tr_b16 v[34:35], v3 offset:61448\n"
    "v_add_u32 v3, 0x10000, v3\n"
    "v_mfma_f32_16x16x32_bf16 v[248:251], v[20:23], a[236:239], v[248:251]\n"
    "s_waitcnt lgkmcnt(4)\n"
    "v_mfma_f32_16x16x32_bf16 v[156:159], v[24:27], a[224:227], v[156:159]\n"
    "ds_read_b64_tr_b16 v[36:37], v4 offset:57344\n"
    "v_mfma_f32_16x16x32_bf16 v[188:191], v[24:27], a[228:231], v[188:191]\n"
    "ds_read_b64_tr_b16 v[38:39], v4 offset:61440\n"
    "v_mfma_f32_16x16x32_bf16 v[220:223], v[24:27], a[232:235], v[220:223]\n"
    "v_mfma_f32_16x16x32_bf16 v[252:255], v[24:27], a[236:239], v[252:255]\n"
    "s_waitcnt vmcnt(25) lgkmcnt(4)\n"
    "v_mfma_f32_16x16x32_bf16 v[128:131], v[28:31], a[240:243], v[128:131]\n"
    "s_waitcnt vmcnt(24)\n"
    "ds_read_b64_tr_b16 v[48:49], v4 offset:57352\n"
    "v_mfma_f32_16x16x32_bf16 v[160:163], v[28:31], a[244:247], v[160:163]\n"
    "s_waitcnt vmcnt(23)\n"
    "s_add_u32 m0, s91, 49152\n"
    "s_add_u32 s87, s90, 3538944\n"
    "buffer_load_dwordx4 v7, s[44:47], s87 offen lds\n"
    "v_mfma_f32_16x16x32_bf16 v[192:195], v[28:31], a[248:251], v[192:195]\n"
    "s_waitcnt vmcnt(23)\n"
    "ds_read_b64_tr_b16 v[50:51], v4 offset:61448\n"
    "v_add_u32 v4, 0x10000, v4\n"
    "v_mfma_f32_16x16x32_bf16 v[224:227], v[28:31], a[252:255], v[224:227]\n"
    "s_waitcnt lgkmcnt(4)\n"
    "v_mfma_f32_16x16x32_bf16 v[132:135], v[32:35], a[240:243], v[132:135]\n"
    "ds_read_b64_tr_b16 v[52:53], v5 offset:57344\n"
    "v_mfma_f32_16x16x32_bf16 v[164:167], v[32:35], a[244:247], v[164:167]\n"
    "ds_read_b64_tr_b16 v[54:55], v5 offset:61440\n"
    "v_mfma_f32_16x16x32_bf16 v[196:199], v[32:35], a[248:251], v[196:199]\n"
    "v_mfma_f32_16x16x32_bf16 v[228:231], v[32:35], a[252:255], v[228:231]\n"
    "s_waitcnt lgkmcnt(4)\n"
    "v_mfma_f32_16x16x32_bf16 v[136:139], v[36:39], a[240:243], v[136:139]\n"
    "ds_read_b64_tr_b16 v[56:57], v5 offset:57352\n"
    "v_mfma_f32_16x16x32_bf16 v[168:171], v[36:39], a[244:247], v[168:171]\n"
    "ds_read_b64_tr_b16 v[58:59], v5 offset:61448\n"
    "v_add_u32 v5, 0x10000, v5\n"
    "v_mfma_f32_16x16x32_bf16 v[200:203], v[36:39], a[248:251], v[200:203]\n"
    "buffer_load_dwordx4 v7, s[48:51], s87 offen offset:1024 lds\n"
    "v_mfma_f32_16x16x32_bf16 v[232:235], v[36:39], a[252:255], v[232:235]\n"
    "s_waitcnt lgkmcnt(4)\n"
    "v_mfma_f32_16x16x32_bf16 v[140:143], v[48:51], a[240:243], v[140:143]\n"
    "ds_read_b64_tr_b16 v[16:17], v6 offset:57344\n"
    "v_mfma_f32_16x16x32_bf16 v[172:175], v[48:51], a[244:247], v[172:175]\n"
    "ds_read_b64_tr_b16 v[18:19], v6 offset:61440\n"
    "v_mfma_f32_16x16x32_bf16 v[204:207], v[48:51], a[248:251], v[204:207]\n"
    "v_mfma_f32_16x16x32_bf16 v[236:239], v[48:51], a[252:255], v[236:239]\n"
    "s_waitcnt lgkmcnt(4)\n"
    "v_mfma_f32_16x16x32_bf16 v[144:147], v[52:55], a[240:243], v[144:147]\n"
    "ds_read_b64_tr_b16 v[20:21], v6 offset:57352\n"
    "v_mfma_f32_16x16x32_bf16 v[176:179], v[52:55], a[244:247], v[176:179]\n"
    "ds_read_b64_tr_b16 v[22:23], v6 offset:61448\n"
    "v_add_u32 v6, 0x10000, v6\n"
    "v_mfma_f32_16x16x32_bf16 v[208:211], v[52:55], a[248:251], v[208:211]\n"
    "v_mfma_f32_16x16x32_bf16 v[240:243], v[52:55], a[252:255], v[240:243]\n"
    "s_waitcnt lgkmcnt(4)\n"
    "s_add_u32 m0, s91, 57344\n"
    "s_add_u32 s87, s90, 4128768\n"
    "buffer_load_dwordx4 v7, s[44:47], s87 offen lds\n"
    "v_mfma_f32_16x16x32_bf16 v[148:151], v[56:59], a[240:243], v[148:151]\n"
    "ds_read_b64_tr_b16 v[24:25], v3 offset:0\n"
    "v_mfma_f32_16x16x32_bf16 v[180:183], v[56:59], a[244:247], v[180:183]\n"
    "ds_read_b64_tr_b16 v[26:27], v3 offset:4096\n"
    "v_mfma_f32_16x16x32_bf16 v[212:215], v[56:59], a[248:251], v[212:215]\n"
    "v_mfma_f32_16x16x32_bf16 v[244:247], v[56:59], a[252:255], v[244:247]\n"
    "s_waitcnt lgkmcnt(4)\n"
    "v_mfma_f32_16x16x32_bf16 v[152:155], v[16:19], a[240:243], v[152:155]\n"
    "ds_read_b64_tr_b16 v[28:29], v3 offset:8\n"
    "v_mfma_f32_16x16x32_bf16 v[184:187], v[16:19], a[244:247], v[184:187]\n"
    "ds_read_b64_tr_b16 v[30:31], v3 offset:4104\n"
    "v_mfma_f32_16x16x32_bf16 v[216:219], v[16:19], a[248:251], v[216:219]\n"
    "v_mfma_f32_16x16x32_bf16 v[248:251], v[16:19], a[252:255], v[248:251]\n"
    "s_waitcnt lgkmcnt(4)\n"
    "v_mfma_f32_16x16x32_bf16 v[156:159], v[20:23], a[240:243], v[156:159]\n"
    "buffer_load_dwordx4 v7, s[48:51], s87 offen offset:1024 lds\n"
    "v_mfma_f32_16x16x32_bf16 v[188:191], v[20:23], a[244:247], v[188:191]\n"
    "ds_read_b64_tr_b16 v[32:33], v4 offset:0\n"
    "v_mfma_f32_16x16x32_bf16 v[220:223], v[20:23], a[248:251], v[220:223]\n"
    "ds_read_b64_tr_b16 v[34:35], v4 offset:4096\n"
    "v_mfma_f32_16x16x32_bf16 v[252:255], v[20:23], a[252:255], v[252:255]\n"
    "s_waitcnt vmcnt(25) lgkmcnt(4)\n"
    "v_mfma_f32_16x16x32_bf16 v[128:131], v[24:27], v[96:99], v[128:131]\n"
    "s_waitcnt vmcnt(24)\n"
    "ds_read_b64_tr_b16 v[36:37], v4 offset:8\n"
    "v_mfma_f32_16x16x32_bf16 v[160:163], v[24:27], v[100:103], v[160:163]\n"
    "s_waitcnt vmcnt(23)\n"
    "ds_read_b64_tr_b16 v[38:39], v4 offset:4104\n"
    "v_mfma_f32_16x16x32_bf16 v[192:195], v[24:27], v[104:107], v[192:195]\n"
    "s_waitcnt vmcnt(22)\n"
    "v_mfma_f32_16x16x32_bf16 v[224:227], v[24:27], v[108:111], v[224:227]\n"
    "s_waitcnt lgkmcnt(4)\n"
    "v_mfma_f32_16x16x32_bf16 v[132:135], v[28:31], v[96:99], v[132:135]\n"
    "ds_read_b64_tr_b16 v[48:49], v5 offset:0\n"
    "v_mfma_f32_16x16x32_bf16 v[164:167], v[28:31], v[100:103], v[164:167]\n"
    "s_add_u32 m0, s91, 65536\n"
    "s_add_u32 s87, s90, 4718592\n"
    "buffer_load_dwordx4 v7, s[44:47], s87 offen lds\n"
    "v_mfma_f32_16x16x32_bf16 v[196:199], v[28:31], v[104:107], v[196:199]\n"
    "ds_read_b64_tr_b16 v[50:51], v5 offset:4096\n"
    "v_mfma_f32_16x16x32_bf16 v[228:231], v[28:31], v[108:111], v[228:231]\n"
    "s_waitcnt lgkmcnt(4)\n"
    "v_mfma_f32_16x16x32_bf16 v[136:139], v[32:35], v[96:99], v[136:139]\n"
    "ds_read_b64_tr_b16 v[52:53], v5 offset:8\n"
    "v_mfma_f32_16x16x32_bf16 v[168:171], v[32:35], v[100:103], v[168:171]\n"
    "ds_read_b64_tr_b16 v[54:55], v5 offset:4104\n"
    "v_mfma_f32_16x16x32_bf16 v[200:203], v[32:35], v[104:107], v[200:203]\n"
    "v_mfma_f32_16x16x32_bf16 v[232:235], v[32:35], v[108:111], v[232:235]\n"
    "s_waitcnt lgkmcnt(4)\n"
    "v_mfma_f32_16x16x32_bf16 v[140:143], v[36:39], v[96:99], v[140:143]\n"
    "ds_read_b64_tr_b16 v[56:57], v6 offset:0\n"
    "v_mfma_f32_16x16x32_bf16 v[172:175], v[36:39], v[100:103], v[172:175]\n"
    "ds_read_b64_tr_b16 v[58:59], v6 offset:4096\n"
    "v_mfma_f32_16x16x32_bf16 v[204:207], v[36:39], v[104:107], v[204:207]\n"
    "buffer_load_dwordx4 v7, s[48:51], s87 offen offset:1024 lds\n"
    "v_mfma_f32_16x16x32_bf16 v[236:239], v[36:39], v[108:111], v[236:239]\n"
    "s_waitcnt lgkmcnt(4)\n"
    "v_mfma_f32_16x16x32_bf16 v[144:147], v[48:51], v[96:99], v[144:147]\n"
    "ds_read_b64_tr_b16 v[16:17], v6 offset:8\n"
    "v_mfma_f32_16x16x32_bf16 v[176:179], v[48:51], v[100:103], v[176:179]\n"
    "ds_read_b64_tr_b16 v[18:19], v6 offset:4104\n"
    "v_mfma_f32_16x16x32_bf16 v[208:211], v[48:51], v[104:107], v[208:211]\n"
    "v_mfma_f32_16x16x32_bf16 v[240:243], v[48:51], v[108:111], v[240:243]\n"
    "s_waitcnt lgkmcnt(4)\n"
    "v_mfma_f32_16x16x32_bf16 v[148:151], v[52:55], v[96:99], v[148:151]\n"
    "ds_read_b64_tr_b16 v[20:21], v3 offset:8192\n"
    "v_mfma_f32_16x16x32_bf16 v[180:183], v[52:55], v[100:103], v[180:183]\n"
    "ds_read_b64_tr_b16 v[22:23], v3 offset:12288\n"
    "v_mfma_f32_16x16x32_bf16 v[212:215], v[52:55], v[104:107], v[212:215]\n"
    "v_mfma_f32_16x16x32_bf16 v[244:247], v[52:55], v[108:111], v[244:247]\n"
    "s_waitcnt lgkmcnt(4)\n"
    "v_mfma_f32_16x16x32_bf16 v[152:155], v[56:59], v[96:99], v[152:155]\n"
    "ds_read_b64_tr_b16 v[24:25], v3 offset:8200\n"
    "v_mfma_f32_16x16x32_bf16 v[184:187], v[56:59], v[100:103], v[184:187]\n"
    "ds_read_b64_tr_b16 v[26:27], v3 offset:12296\n"
    "v_mfma_f32_16x16x32_bf16 v[216:219], v[56:59], v[104:107], v[216:219]\n"
    "v_mfma_f32_16x16x32_bf16 v[248:251], v[56:59], v[108:111], v[248:251]\n"
    "s_waitcnt lgkmcnt(4)\n"
    "v_mfma_f32_16x16x32_bf16 v[156:159], v[16:19], v[96:99], v[156:159]\n"
    "buffer_load_dword v60, v62, s[32:35], s88 offen\n"
    "v_mfma_f32_16x16x32_bf16 v[188:191], v[16:19], v[100:103], v[188:191]\n"
    "ds_read_b64_tr_b16 v[28:29], v4 offset:8192\n"
    "v_mfma_f32_16x16x32_bf16 v[220:223], v[16:19], v[104:107], v[220:223]\n"
    "ds_read_b64_tr_b16 v[30:31], v4 offset:12288\n"
    "v_mfma_f32_16x16x32_bf16 v[252:255], v[16:19], v[108:111], v[252:255]\n"
    "s_waitcnt vmcnt(24) lgkmcnt(4)\n"
    "v_mfma_f32_16x16x32_bf16 v[128:131], v[20:23], v[112:115], v[128:131]\n"
    "s_waitcnt vmcnt(23)\n"
    "ds_read_b64_tr_b16 v[32:33], v4 offset:8200\n"
    "v_mfma_f32_16x16x32_bf16 v[160:163], v[20:23], v[116:119], v[160:163]\n"
    "s_waitcnt vmcnt(22)\n"
    "ds_read_b64_tr_b16 v[34:35], v4 offset:12296\n"
    "v_mfma_f32_16x16x32_bf16 v[192:195], v[20:23], v[120:123], v[192:195]\n"
    "s_waitcnt vmcnt(21)\n"
    "v_mfma_f32_16x16x32_bf16 v[224:227], v[20:23], v[124:127], v[224:227]\n"
    "s_waitcnt lgkmcnt(4)\n"
    "v_mfma_f32_16x16x32_bf16 v[132:135], v[24:27], v[112:115], v[132:135]\n"
    "ds_read_b64_tr_b16 v[36:37], v5 offset:8192\n"
    "v_mfma_f32_16x16x32_bf16 v[164:167], v[24:27], v[116:119], v[164:167]\n"
    "ds_read_b64_tr_b16 v[38:39], v5 offset:12288\n"
    "v_mfma_f32_16x16x32_bf16 v[196:199], v[24:27], v[120:123], v[196:199]\n"
    "v_mfma_f32_16x16x32_bf16 v[228:231], v[24:27], v[124:127], v[228:231]\n"
    "s_waitcnt lgkmcnt(4)\n"
    "v_mfma_f32_16x16x32_bf16 v[136:139], v[28:31], v[112:115], v[136:139]\n"
    "ds_read_b64_tr_b16 v[48:49], v5 offset:8200\n"
    "v_mfma_f32_16x16x32_bf16 v[168:171], v[28:31], v[116:119], v[168:171]\n"
    "ds_read_b64_tr_b16 v[50:51], v5 offset:12296\n"
    "v_mfma_f32_16x16x32_bf16 v[200:203], v[28:31], v[120:123], v[200:203]\n"
    "v_mfma_f32_16x16x32_bf16 v[232:235], v[28:31], v[124:127], v[232:235]\n"
    "s_waitcnt lgkmcnt(4)\n"
    "v_mfma_f32_16x16x32_bf16 v[140:143], v[32:35], v[112:115], v[140:143]\n"
    "ds_read_b64_tr_b16 v[52:53], v6 offset:8192\n"
    "v_mfma_f32_16x16x32_bf16 v[172:175], v[32:35], v[116:119], v[172:175]\n"
    "ds_read_b64_tr_b16 v[54:55], v6 offset:12288\n"
    "v_mfma_f32_16x16x32_bf16 v[204:207], v[32:35], v[120:123], v[204:207]\n"
    "v_mfma_f32_16x16x32_bf16 v[236:239], v[32:35], v[124:127], v[236:239]\n"
    "s_waitcnt lgkmcnt(4)\n"
    "v_mfma_f32_16x16x32_bf16 v[144:147], v[36:39], v[112:115], v[144:147]\n"
    "ds_read_b64_tr_b16 v[56:57], v6 offset:8200\n"
    "v_mfma_f32_16x16x32_bf16 v[176:179], v[36:39], v[116:119], v[176:179]\n"
    "ds_read_b64_tr_b16 v[58:59], v6 offset:12296\n"
    "v_mfma_f32_16x16x32_bf16 v[208:211], v[36:39], v[120:123], v[208:211]\n"
    "v_mfma_f32_16x16x32_bf16 v[240:243], v[36:39], v[124:127], v[240:243]\n"
    "s_waitcnt lgkmcnt(4)\n"
    "v_mfma_f32_16x16x32_bf16 v[148:151], v[48:51], v[112:115], v[148:151]\n"
    "v_mfma_f32_16x16x32_bf16 v[180:183], v[48:51], v[116:119], v[180:183]\n"
    "v_mfma_f32_16x16x32_bf16 v[212:215], v[48:51], v[120:123], v[212:215]\n"
    "v_mfma_f32_16x16x32_bf16 v[244:247], v[48:51], v[124:127], v[244:247]\n"
    "s_waitcnt lgkmcnt(2)\n"
    "v_mfma_f32_16x16x32_bf16 v[152:155], v[52:55], v[112:115], v[152:155]\n"
    "v_mfma_f32_16x16x32_bf16 v[184:187], v[52:55], v[116:119], v[184:187]\n"
    "s_waitcnt vmcnt(9)\n"
    "s_barrier\n"
    "ds_read_b64_tr_b16 v[16:17], v8 offset:0\n"
    "v_mfma_f32_16x16x32_bf16 v[216:219], v[52:55], v[120:123], v[216:219]\n"
    "s_add_u32 s91, s86, 73728\n"
    "s_add_u32 s90, s90, 64\n"
    "ds_read_b64_tr_b16 v[18:19], v8 offset:2048\n"
    "v_mfma_f32_16x16x32_bf16 v[248:251], v[52:55], v[124:127], v[248:251]\n"
    "s_waitcnt lgkmcnt(2)\n"
    "ds_read_b64_tr_b16 v[20:21], v8 offset:4096\n"
    "v_mfma_f32_16x16x32_bf16 v[156:159], v[56:59], v[112:115], v[156:159]\n"
    "ds_read_b64_tr_b16 v[22:23], v8 offset:6144\n"
    "v_mfma_f32_16x16x32_bf16 v[188:191], v[56:59], v[116:119], v[188:191]\n"
    "ds_read_b64_tr_b16 v[24:25], v8 offset:8192\n"
    "ds_read_b64_tr_b16 v[26:27], v8 offset:10240\n"
    "v_mfma_f32_16x16x32_bf16 v[220:223], v[56:59], v[120:123], v[220:223]\n"
    "v_mfma_f32_16x16x32_bf16 v[252:255], v[56:59], v[124:127], v[252:255]\n"
    "s_waitcnt lgkmcnt(4)\n"
    "v_mfma_f32_16x16x32_bf16 v[64:67], v[16:19], a[0:3], 0\n"
    "s_waitcnt lgkmcnt(2)\n"
    "ds_read_b64_tr_b16 v[28:29], v8 offset:1024\n"
    "v_mfma_f32_16x16x32_bf16 v[64:67], v[20:23], a[16:19], v[64:67]\n"
    "s_waitcnt lgkmcnt(1)\n"
    "ds_read_b64_tr_b16 v[30:31], v8 offset:3072\n"
    "v_mfma_f32_16x16x32_bf16 v[64:67], v[24:27], a[32:35], v[64:67]\n"
    "ds_read_b64_tr_b16 v[32:33], v8 offset:5120\n"
    "v_mfma_f32_16x16x32_bf16 v[68:71], v[16:19], a[4:7], 0\n"
    "ds_read_b64_tr_b16 v[34:35], v8 offset:7168\n"
    "v_mfma_f32_16x16x32_bf16 v[68:71], v[20:23], a[20:23], v[68:71]\n"
    "ds_read_b64_tr_b16 v[36:37], v8 offset:9216\n"
    "v_mul_f32 v128, 0x3e0293ee, v128\n"
    "v_mfma_f32_16x16x32_bf16 v[68:71], v[24:27], a[36:39], v[68:71]\n"
    "s_add_u32 m0, s91, 0\n"
    "s_add_u32 s87, s90, 0\n"
    "buffer_load_dwordx4 v7, s[44:47], s87 offen lds\n"
    "v_mul_f32 v129, 0x3e0293ee, v129\n"
    "v_mfma_f32_16x16x32_bf16 v[72:75], v[16:19], a[8:11], 0\n"
    "ds_read_b64_tr_b16 v[38:39], v8 offset:11264\n"
    "v_mul_f32 v130, 0x3e0293ee, v130\n"
    "v_mfma_f32_16x16x32_bf16 v[72:75], v[20:23], a[24:27], v[72:75]\n"
    "v_mul_f32 v131, 0x3e0293ee, v131\n"
    "v_mul_f32 v132, 0x3e0293ee, v132\n"
    "v_mfma_f32_16x16x32_bf16 v[72:75], v[24:27], a[40:43], v[72:75]\n"
    "v_mul_f32 v133, 0x3e0293ee, v133\n"
    "v_mul_f32 v134, 0x3e0293ee, v134\n"
    "v_mfma_f32_16x16x32_bf16 v[76:79], v[16:19], a[12:15], 0\n"
    "v_mul_f32 v135, 0x3e0293ee, v135\n"
    "v_cvt_pk_bf16_f32 v128, v128, v129\n"
    "v_mfma_f32_16x16x32_bf16 v[76:79], v[20:23], a[28:31], v[76:79]\n"
    "v_cvt_pk_bf16_f32 v129, v130, v131\n"
    "v_cvt_pk_bf16_f32 v130, v132, v133\n"
    "v_mfma_f32_16x16x32_bf16 v[76:79], v[24:27], a[44:47], v[76:79]\n"
    "s_waitcnt lgkmcnt(4)\n"
    "v_cvt_pk_bf16_f32 v131, v134, v135\n"
    "v_mul_f32 v136, 0x3e0293ee, v136\n"
    "v_mfma_f32_16x16x32_bf16 v[80:83], v[28:31], a[0:3], 0\n"
    "s_waitcnt lgkmcnt(2)\n"
    "ds_read_b64_tr_b16 v[48:49], v8 offset:12288\n"
    "v_mul_f32 v137, 0x3e0293ee, v137\n"
    "v_mfma_f32_16x16x32_bf16 v[80:83], v[32:35], a[16:19], v[80:83]\n"
    "s_waitcnt lgkmcnt(1)\n"
    "ds_read_b64_tr_b16 v[50:51], v8 offset:14336\n"
    "v_mul_f32 v138, 0x3e0293ee, v138\n"
    "v_mfma_f32_16x16x32_bf16 v[80:83], v[36:39], a[32:35], v[80:83]\n"
    "ds_read_b64_tr_b16 v[52:53], v8 offset:16384\n"
    "v_mul_f32 v139, 0x3e0293ee, v139\n"
    "v_mfma_f32_16x16x32_bf16 v[84:87], v[28:31], a[4:7], 0\n"
    "ds_read_b64_tr_b16 v[54:55], v8 offset:18432\n"
    "v_mul_f32 v140, 0x3e0293ee, v140\n"
    "v_mfma_f32_16x16x32_bf16 v[84:87], v[32:35], a[20:23], v[84:87]\n"
    "ds_read_b64_tr_b16 v[56:57], v8 offset:20480\n"
    "v_mul_f32 v141, 0x3e0293ee, v141\n"
    "v_mfma_f32_16x16x32_bf16 v[84:87], v[36:39], a[36:39], v[84:87]\n"
    "buffer_load_dwordx4 v7, s[48:51], s87 offen offset:1024 lds\n"
    "v_mul_f32 v142, 0x3e0293ee, v142\n"
    "v_mfma_f32_16x16x32_bf16 v[88:91], v[28:31], a[8:11], 0\n"
    "ds_read_b64_tr_b16 v[58:59], v8 offset:22528\n"
    "v_mul_f32 v143, 0x3e0293ee, v143\n"
    "v_mfma_f32_16x16x32_bf16 v[88:91], v[32:35], a[24:27], v[88:91]\n"
    "v_cvt_pk_bf16_f32 v132, v136, v137\n"
    "v_cvt_pk_bf16_f32 v133, v138, v139\n"
    "v_mfma_f32_16x16x32_bf16 v[88:91], v[36:39], a[40:43], v[88:91]\n"
    "v_cvt_pk_bf16_f32 v134, v140, v141\n"
    "v_cvt_pk_bf16_f32 v135, v142, v143\n"
    "v_mfma_f32_16x16x32_bf16 v[92:95], v[28:31], a[12:15], 0\n"
    "v_mul_f32 v144, 0x3e0293ee, v144\n"
    "v_mul_f32 v145, 0x3e0293ee, v145\n"
    "v_mfma_f32_16x16x32_bf16 v[92:95], v[32:35], a[28:31], v[92:95]\n"
    "v_mul_f32 v146, 0x3e0293ee, v146\n"
    "v_mul_f32 v147, 0x3e0293ee, v147\n"
    "v_mfma_f32_16x16x32_bf16 v[92:95], v[36:39], a[44:47], v[92:95]\n"
    "s_waitcnt lgkmcnt(4)\n"
    "v_mul_f32 v148, 0x3e0293ee, v148\n"
    "v_mul_f32 v149, 0x3e0293ee, v149\n"
    "v_mfma_f32_16x16x32_bf16 v[64:67], v[48:51], a[48:51], v[64:67]\n"
    "s_waitcnt lgkmcnt(2)\n"
    "ds_read_b64_tr_b16 v[16:17], v8 offset:13312\n"
    "v_mul_f32 v150, 0x3e0293ee, v150\n"
    "v_mfma_f32_16x16x32_bf16 v[64:67], v[52:55], a[64:67], v[64:67]\n"
    "s_waitcnt lgkmcnt(1)\n"
    "ds_read_b64_tr_b16 v[18:19], v8 offset:15360\n"
    "v_mul_f32 v151, 0x3e0293ee, v151\n"
    "v_mfma_f32_16x16x32_bf16 v[64:67], v[56:59], a[80:83], v[64:67]\n"
    "ds_read_b64_tr_b16 v[20:21], v8 offset:17408\n"
    "v_cvt_pk_bf16_f32 v136, v144, v145\n"
    "v_mfma_f32_16x16x32_bf16 v[68:71], v[48:51], a[52:55], v[68:71]\n"
    "ds_read_b64_tr_b16 v[22:23], v8 offset:19456\n"
    "v_cvt_pk_bf16_f32 v137, v146, v147\n"
    "v_mfma_f32_16x16x32_bf16 v[68:71], v[52:55], a[68:71], v[68:71]\n"
    "ds_read_b64_tr_b16 v[24:25], v8 offset:21504\n"
    "v_cvt_pk_bf16_f32 v138, v148, v149\n"
    "v_mfma_f32_16x16x32_bf16 v[68:71], v[56:59], a[84:87], v[68:71]\n"
    "s_add_u32 m0, s91, 8192\n"
    "s_add_u32 s87, s90, 589824\n"
    "buffer_load_dwordx4 v7, s[44:47], s87 offen lds\n"
    "v_cvt_pk_bf16_f32 v139, v150, v151\n"
    "v_mfma_f32_16x16x32_bf16 v[72:75], v[48:51], a[56:59], v[72:75]\n"
    "ds_read_b64_tr_b16 v[26:27], v8 offset:23552\n"
    "v_mul_f32 v152, 0x3e0293ee, v152\n"
    "v_mfma_f32_16x16x32_bf16 v[72:75], v[52:55], a[72:75], v[72:75]\n"
    "v_mul_f32 v153, 0x3e0293ee, v153\n"
    "v_mul_f32 v154, 0x3e0293ee, v154\n"
    "v_mfma_f32_16x16x32_bf16 v[72:75], v[56:59], a[88:91], v[72:75]\n"
    "v_mul_f32 v155, 0x3e0293ee, v155\n"
    "v_mul_f32 v156, 0x3e0293ee, v156\n"
    "v_mfma_f32_16x16x32_bf16 v[76:79], v[48:51], a[60:63], v[76:79]\n"
    "v_mul_f32 v157, 0x3e0293ee, v157\n"
    "v_mul_f32 v158, 0x3e0293ee, v158\n"
    "v_mfma_f32_16x16x32_bf16 v[76:79], v[52:55], a[76:79], v[76:79]\n"
    "v_mul_f32 v159, 0x3e0293ee, v159\n"
    "v_cvt_pk_bf16_f32 v140, v152, v153\n"
    "v_mfma_f32_16x16x32_bf16 v[76:79], v[56:59], a[92:95], v[76:79]\n"
    "s_waitcnt lgkmcnt(4)\n"
    "v_cvt_pk_bf16_f32 v141, v154, v155\n"
    "v_cvt_pk_bf16_f32 v142, v156, v157\n"
    "v_mfma_f32_16x16x32_bf16 v[80:83], v[16:19], a[48:51], v[80:83]\n"
    "s_waitcnt lgkmcnt(2)\n"
    "ds_read_b64_tr_b16 v[28:29], v8 offset:24576\n"
    "v_cvt_pk_bf16_f32 v143, v158, v159\n"
    "v_mfma_f32_16x16x32_bf16 v[80:83], v[20:23], a[64:67], v[80:83]\n"
    "s_waitcnt lgkmcnt(1)\n"
    "ds_read_b64_tr_b16 v[30:31], v8 offset:26624\n"
    "v_mul_f32 v160, 0x3e0293ee, v160\n"
    "v_mfma_f32_16x16x32_bf16 v[80:83], v[24:27], a[80:83], v[80:83]\n"
    "ds_read_b64_tr_b16 v[32:33], v8 offset:28672\n"
    "v_mul_f32 v161, 0x3e0293ee, v161\n"
    "v_mfma_f32_16x16x32_bf16 v[84:87], v[16:19], a[52:55], v[84:87]\n"
    "ds_read_b64_tr_b16 v[34:35], v8 offset:30720\n"
    "v_mul_f32 v162, 0x3e0293ee, v162\n"
    "v_mfma_f32_16x16x32_bf16 v[84:87], v[20:23], a[68:71], v[84:87]\n"
    "ds_read_b64_tr_b16 v[36:37], v8 offset:32768\n"
    "v_mul_f32 v163, 0x3e0293ee, v163\n"
    "v_mfma_f32_16x16x32_bf16 v[84:87], v[24:27], a[84:87], v[84:87]\n"
    "buffer_load_dwordx4 v7, s[48:51], s87 offen offset:1024 lds\n"
    "v_mul_f32 v164, 0x3e0293ee, v164\n"
    "v_mfma_f32_16x16x32_bf16 v[88:91], v[16:19], a[56:59], v[88:91]\n"
    "ds_read_b64_tr_b16 v[38:39], v8 offset:34816\n"
    "v_mul_f32 v165, 0x3e0293ee, v165\n"
    "v_mfma_f32_16x16x32_bf16 v[88:91], v[20:23], a[72:75], v[88:91]\n"
    "v_mul_f32 v166, 0x3e0293ee, v166\n"
    "v_mul_f32 v167, 0x3e0293ee, v167\n"
    "v_mfma_f32_16x16x32_bf16 v[88:91], v[24:27], a[88:91], v[88:91]\n"
    "v_cvt_pk_bf16_f32 v144, v160, v161\n"
    "v_cvt_pk_bf16_f32 v145, v162, v163\n"
    "v_mfma_f32_16x16x32_bf16 v[92:95], v[16:19], a[60:63], v[92:95]\n"
    "v_cvt_pk_bf16_f32 v146, v164, v165\n"
    "v_cvt_pk_bf16_f32 v147, v166, v167\n"
    "v_mfma_f32_16x16x32_bf16 v[92:95], v[20:23], a[76:79], v[92:95]\n"
    "v_mul_f32 v168, 0x3e0293ee, v168\n"
    "v_mul_f32 v169, 0x3e0293ee, v169\n"
    "v_mfma_f32_16x16x32_bf16 v[92:95], v[24:27], a[92:95], v[92:95]\n"
    "s_waitcnt lgkmcnt(4)\n"
    "v_mul_f32 v170, 0x3e0293ee, v170\n"
    "v_mul_f32 v171, 0x3e0293ee, v171\n"
    "v_mfma_f32_16x16x32_bf16 v[64:67], v[28:31], a[96:99], v[64:67]\n"
    "s_waitcnt lgkmcnt(2)\n"
    "ds_read_b64_tr_b16 v[48:49], v8 offset:25600\n"
    "v_mul_f32 v172, 0x3e0293ee, v172\n"
    "v_mfma_f32_16x16x32_bf16 v[64:67], v[32:35], a[112:115], v[64:67]\n"
    "s_waitcnt lgkmcnt(1)\n"
    "ds_read_b64_tr_b16 v[50:51], v8 offset:27648\n"
    "v_mul_f32 v173, 0x3e0293ee, v173\n"
    "v_mfma_f32_16x16x32_bf16 v[64:67], v[36:39], a[128:131], v[64:67]\n"
    "ds_read_b64_tr_b16 v[52:53], v8 offset:29696\n"
    "v_mul_f32 v174, 0x3e0293ee, v174\n"
    "v_mfma_f32_16x16x32_bf16 v[68:71], v[28:31], a[100:103], v[68:71]\n"
    "ds_read_b64_tr_b16 v[54:55], v8 offset:31744\n"
    "v_mul_f32 v175, 0x3e0293ee, v175\n"
    "v_mfma_f32_16x16x32_bf16 v[68:71], v[32:35], a[116:119], v[68:71]\n"
    "ds_read_b64_tr_b16 v[56:57], v8 offset:33792\n"
    "v_cvt_pk_bf16_f32 v148, v168, v169\n"
    "v_mfma_f32_16x16x32_bf16 v[68:71], v[36:39], a[132:135], v[68:71]\n"
    "s_add_u32 m0, s91, 16384\n"
    "s_add_u32 s87, s90, 1179648\n"
    "buffer_load_dwordx4 v7, s[44:47], s87 offen lds\n"
    "v_cvt_pk_bf16_f32 v149, v170, v171\n"
    "v_mfma_f32_16x16x32_bf16 v[72:75], v[28:31], a[104:107], v[72:75]\n"
    "ds_read_b64_tr_b16 v[58:59], v8 offset:35840\n"
    "v_xor_b32 v8, 32, v8\n"
    "v_mfma_f32_16x16x32_bf16 v[72:75], v[32:35], a[120:123], v[72:75]\n"
    "v_cvt_pk_bf16_f32 v150, v172, v173\n"
    "v_cvt_pk_bf16_f32 v151, v174, v175\n"
    "v_mfma_f32_16x16x32_bf16 v[72:75], v[36:39], a[136:139], v[72:75]\n"
    "v_mul_f32 v176, 0x3e0293ee, v176\n"
    "v_mul_f32 v177, 0x3e0293ee, v177\n"
    "v_mfma_f32_16x16x32_bf16 v[76:79], v[28:31], a[108:111], v[76:79]\n"
    "v_mul_f32 v178, 0x3e0293ee, v178\n"
    "v_mul_f32 v179, 0x3e0293ee, v179\n"
    "v_mfma_f32_16x16x32_bf16 v[76:79], v[32:35], a[124:127], v[76:79]\n"
    "v_mul_f32 v180, 0x3e0293ee, v180\n"
    "v_mul_f32 v181, 0x3e0293ee, v181\n"
    "v_mfma_f32_16x16x32_bf16 v[76:79], v[36:39], a[140:143], v[76:79]\n"
    "s_waitcnt lgkmcnt(4)\n"
    "v_mul_f32 v182, 0x3e0293ee, v182\n"
    "v_mul_f32 v183, 0x3e0293ee, v183\n"
    "v_mfma_f32_16x16x32_bf16 v[80:83], v[48:51], a[96:99], v[80:83]\n"
    "s_waitcnt vmcnt(6) lgkmcnt(2)\n"
    "s_barrier\n"
    "ds_read_b64_tr_b16 v[16:17], v9 offset:0\n"
    "v_cvt_pk_bf16_f32 v152, v176, v177\n"
    "v_mfma_f32_16x16x32_bf16 v[80:83], v[52:55], a[112:115], v[80:83]\n"
    "s_waitcnt lgkmcnt(1)\n"
    "ds_read_b64_tr_b16 v[18:19], v9 offset:2048\n"
    "v_cvt_pk_bf16_f32 v153, v178, v179\n"
    "v_mfma_f32_16x16x32_bf16 v[80:83], v[56:59], a[128:131], v[80:83]\n"
    "ds_read_b64_tr_b16 v[20:21], v9 offset:4096\n"
    "v_cvt_pk_bf16_f32 v154, v180, v181\n"
    "v_mfma_f32_16x16x32_bf16 v[84:87], v[48:51], a[100:103], v[84:87]\n"
    "ds_read_b64_tr_b16 v[22:23], v9 offset:6144\n"
    "v_cvt_pk_bf16_f32 v155, v182, v183\n"
    "v_mfma_f32_16x16x32_bf16 v[84:87], v[52:55], a[116:119], v[84:87]\n"
    "ds_read_b64_tr_b16 v[24:25], v9 offset:8192\n"
    "v_mul_f32 v184, 0x3e0293ee, v184\n"
    "v_mfma_f32_16x16x32_bf16 v[84:87], v[56:59], a[132:135], v[84:87]\n"
    "buffer_load_dwordx4 v7, s[48:51], s87 offen offset:1024 lds\n"
    "v_mul_f32 v185, 0x3e0293ee, v185\n"
    "v_mfma_f32_16x16x32_bf16 v[88:91], v[48:51], a[104:107], v[88:91]\n"
    "ds_read_b64_tr_b16 v[26:27], v9 offset:10240\n"
    "v_mul_f32 v186, 0x3e0293ee, v186\n"
    "v_mfma_f32_16x16x32_bf16 v[88:91], v[52:55], a[120:123], v[88:91]\n"
    "v_mul_f32 v187, 0x3e0293ee, v187\n"
    "v_mul_f32 v188, 0x3e0293ee, v188\n"
    "v_mfma_f32_16x16x32_bf16 v[88:91], v[56:59], a[136:139], v[88:91]\n"
    "v_mul_f32 v189, 0x3e0293ee, v189\n"
    "v_mul_f32 v190, 0x3e0293ee, v190\n"
    "v_mfma_f32_16x16x32_bf16 v[92:95], v[48:51], a[108:111], v[92:95]\n"
    "v_mul_f32 v191, 0x3e0293ee, v191\n"
    "v_cvt_pk_bf16_f32 v156, v184, v185\n"
    "v_mfma_f32_16x16x32_bf16 v[92:95], v[52:55], a[124:127], v[92:95]\n"
    "v_cvt_pk_bf16_f32 v157, v186, v187\n"
    "v_cvt_pk_bf16_f32 v158, v188, v189\n"
    "v_mfma_f32_16x16x32_bf16 v[92:95], v[56:59], a[140:143], v[92:95]\n"
    "s_waitcnt lgkmcnt(4)\n"
    "v_cvt_pk_bf16_f32 v159, v190, v191\n"
    "v_mul_f32 v192, 0x3e0293ee, v192\n"
    "v_mfma_f32_16x16x32_bf16 v[64:67], v[16:19], a[144:147], v[64:67]\n"
    "s_waitcnt lgkmcnt(2)\n"
    "ds_read_b64_tr_b16 v[28:29], v9 offset:1024\n"
    "v_mul_f32 v193, 0x3e0293ee, v193\n"
    "v_mfma_f32_16x16x32_bf16 v[64:67], v[20:23], a[160:163], v[64:67]\n"
    "s_waitcnt lgkmcnt(1)\n"
    "ds_read_b64_tr_b16 v[30:31], v9 offset:3072\n"
    "v_mul_f32 v194, 0x3e0293ee, v194\n"
    "v_mfma_f32_16x16x32_bf16 v[64:67], v[24:27], a[176:179], v[64:67]\n"
    "ds_read_b64_tr_b16 v[32:33], v9 offset:5120\n"
    "v_mul_f32 v195, 0x3e0293ee, v195\n"
    "v_mfma_f32_16x16x32_bf16 v[68:71], v[16:19], a[148:151], v[68:71]\n"
    "ds_read_b64_tr_b16 v[34:35], v9 offset:7168\n"
    "v_mul_f32 v196, 0x3e0293ee, v196\n"
    "v_mfma_f32_16x16x32_bf16 v[68:71], v[20:23], a[164:167], v[68:71]\n"
    "ds_read_b64_tr_b16 v[36:37], v9 offset:9216\n"
    "v_mul_f32 v197, 0x3e0293ee, v197\n"
    "v_mfma_f32_16x16x32_bf16 v[68:71], v[24:27], a[180:183], v[68:71]\n"
    "s_add_u32 m0, s91, 24576\n"
    "s_add_u32 s87, s90, 1769472\n"
    "buffer_load_dwordx4 v7, s[44:47], s87 offen lds\n"
    "v_mul_f32 v198, 0x3e0293ee, v198\n"
    "v_mfma_f32_16x16x32_bf16 v[72:75], v[16:19], a[152:155], v[72:75]\n"
    "ds_read_b64_tr_b16 v[38:39], v9 offset:11264\n"
    "v_mul_f32 v199, 0x3e0293ee, v199\n"
    "v_mfma_f32_16x16x32_bf16 v[72:75], v[20:23], a[168:171], v[72:75]\n"
    "v_cvt_pk_bf16_f32 v160, v192, v193\n"
    "v_cvt_pk_bf16_f32 v161, v194, v195\n"
    "v_mfma_f32_16x16x32_bf16 v[72:75], v[24:27], a[184:187], v[72:75]\n"
    "v_cvt_pk_bf16_f32 v162, v196, v197\n"
    "v_cvt_pk_bf16_f32 v163, v198, v199\n"
    "v_mfma_f32_16x16x32_bf16 v[76:79], v[16:19], a[156:159], v[76:79]\n"
    "v_mul_f32 v200, 0x3e0293ee, v200\n"
    "v_mul_f32 v201, 0x3e0293ee, v201\n"
    "v_mfma_f32_16x16x32_bf16 v[76:79], v[20:23], a[172:175], v[76:79]\n"
    "v_mul_f32 v202, 0x3e0293ee, v202\n"
    "v_mul_f32 v203, 0x3e0293ee, v203\n"
    "v_mfma_f32_16x16x32_bf16 v[76:79], v[24:27], a[188:191], v[76:79]\n"
    "s_waitcnt lgkmcnt(4)\n"
    "v_mul_f32 v204, 0x3e0293ee, v204\n"
    "v_mul_f32 v205, 0x3e0293ee, v205\n"
    "v_mfma_f32_16x16x32_bf16 v[80:83], v[28:31], a[144:147], v[80:83]\n"
    "s_waitcnt lgkmcnt(2)\n"
    "ds_read_b64_tr_b16 v[48:49], v9 offset:12288\n"
    "v_mul_f32 v206, 0x3e0293ee, v206\n"
    "v_mfma_f32_16x16x32_bf16 v[80:83], v[32:35], a[160:163], v[80:83]\n"
    "s_waitcnt lgkmcnt(1)\n"
    "ds_read_b64_tr_b16 v[50:51], v9 offset:14336\n"
    "v_mul_f32 v207, 0x3e0293ee, v207\n"
    "v_mfma_f32_16x16x32_bf16 v[80:83], v[36:39], a[176:179], v[80:83]\n"
    "ds_read_b64_tr_b16 v[52:53], v9 offset:16384\n"
    "v_cvt_pk_bf16_f32 v164, v200, v201\n"
    "v_mfma_f32_16x16x32_bf16 v[84:87], v[28:31], a[148:151], v[84:87]\n"
    "ds_read_b64_tr_b16 v[54:55], v9 offset:18432\n"
    "v_cvt_pk_bf16_f32 v165, v202, v203\n"
    "v_mfma_f32_16x16x32_bf16 v[84:87], v[32:35], a[164:167], v[84:87]\n"
    "ds_read_b64_tr_b16 v[56:57], v9 offset:20480\n"
    "v_cvt_pk_bf16_f32 v166, v204, v205\n"
    "v_mfma_f32_16x16x32_bf16 v[84:87], v[36:39], a[180:183], v[84:87]\n"
    "buffer_load_dwordx4 v7, s[48:51], s87 offen offset:1024 lds\n"
    "v_cvt_pk_bf16_f32 v167, v206, v207\n"
    "v_mfma_f32_16x16x32_bf16 v[88:91], v[28:31], a[152:155], v[88:91]\n"
    "ds_read_b64_tr_b16 v[58:59], v9 offset:22528\n"
    "v_mul_f32 v208, 0x3e0293ee, v208\n"
    "v_mfma_f32_16x16x32_bf16 v[88:91], v[32:35], a[168:171], v[88:91]\n"
    "v_mul_f32 v209, 0x3e0293ee, v209\n"
    "v_mul_f32 v210, 0x3e0293ee, v210\n"
    "v_mfma_f32_16x16x32_bf16 v[88:91], v[36:39], a[184:187], v[88:91]\n"
    "v_mul_f32 v211, 0x3e0293ee, v211\n"
    "v_mul_f32 v212, 0x3e0293ee, v212\n"
    "v_mfma_f32_16x16x32_bf16 v[92:95], v[28:31], a[156:159], v[92:95]\n"
    "v_mul_f32 v213, 0x3e0293ee, v213\n"
    "v_mul_f32 v214, 0x3e0293ee, v214\n"
    "v_mfma_f32_16x16x32_bf16 v[92:95], v[32:35], a[172:175], v[92:95]\n"
    "v_mul_f32 v215, 0x3e0293ee, v215\n"
    "v_cvt_pk_bf16_f32 v168, v208, v209\n"
    "v_mfma_f32_16x16x32_bf16 v[92:95], v[36:39], a[188:191], v[92:95]\n"
    "s_waitcnt lgkmcnt(4)\n"
    "v_cvt_pk_bf16_f32 v169, v210, v211\n"
    "v_cvt_pk_bf16_f32 v170, v212, v213\n"
    "v_mfma_f32_16x16x32_bf16 v[64:67], v[48:51], a[192:195], v[64:67]\n"
    "s_waitcnt lgkmcnt(2)\n"
    "ds_read_b64_tr_b16 v[16:17], v9 offset:13312\n"
    "v_cvt_pk_bf16_f32 v171, v214, v215\n"
    "v_mfma_f32_16x16x32_bf16 v[64:67], v[52:55], a[208:211], v[64:67]\n"
    "s_waitcnt lgkmcnt(1)\n"
    "ds_read_b64_tr_b16 v[18:19], v9 offset:15360\n"
    "v_mul_f32 v216, 0x3e0293ee, v216\n"
    "v_mfma_f32_16x16x32_bf16 v[64:67], v[56:59], a[224:227], v[64:67]\n"
    "ds_read_b64_tr_b16 v[20:21], v9 offset:17408\n"
    "v_mul_f32 v217, 0x3e0293ee, v217\n"
    "v_mfma_f32_16x16x32_bf16 v[68:71], v[48:51], a[196:199], v[68:71]\n"
    "ds_read_b64_tr_b16 v[22:23], v9 offset:19456\n"
    "v_mul_f32 v218, 0x3e0293ee, v218\n"
    "v_mfma_f32_16x16x32_bf16 v[68:71], v[52:55], a[212:215], v[68:71]\n"
    "ds_read_b64_tr_b16 v[24:25], v9 offset:21504\n"
    "v_mul_f32 v219, 0x3e0293ee, v219\n"
    "v_mfma_f32_16x16x32_bf16 v[68:71], v[56:59], a[228:231], v[68:71]\n"
    "s_add_u32 m0, s91, 32768\n"
    "s_add_u32 s87, s90, 2359296\n"
    "buffer_load_dwordx4 v7, s[44:47], s87 offen lds\n"
    "v_mul_f32 v220, 0x3e0293ee, v220\n"
    "v_mfma_f32_16x16x32_bf16 v[72:75], v[48:51], a[200:203], v[72:75]\n"
    "ds_read_b64_tr_b16 v[26:27], v9 offset:23552\n"
    "v_mul_f32 v221, 0x3e0293ee, v221\n"
    "v_mfma_f32_16x16x32_bf16 v[72:75], v[52:55], a[216:219], v[72:75]\n"
    "v_mul_f32 v222, 0x3e0293ee, v222\n"
    "v_mul_f32 v223, 0x3e0293ee, v223\n"
    "v_mfma_f32_16x16x32_bf16 v[72:75], v[56:59], a[232:235], v[72:75]\n"
    "v_cvt_pk_bf16_f32 v172, v216, v217\n"
    "v_cvt_pk_bf16_f32 v173, v218, v219\n"
    "v_mfma_f32_16x16x32_bf16 v[76:79], v[48:51], a[204:207], v[76:79]\n"
    "v_cvt_pk_bf16_f32 v174, v220, v221\n"
    "v_cvt_pk_bf16_f32 v175, v222, v223\n"
    "v_mfma_f32_16x16x32_bf16 v[76:79], v[52:55], a[220:223], v[76:79]\n"
    "v_mul_f32 v224, 0x3e0293ee, v224\n"
    "v_mul_f32 v225, 0x3e0293ee, v225\n"
    "v_mfma_f32_16x16x32_bf16 v[76:79], v[56:59], a[236:239], v[76:79]\n"
    "s_waitcnt lgkmcnt(4)\n"
    "v_mul_f32 v226, 0x3e0293ee, v226\n"
    "v_mul_f32 v227, 0x3e0293ee, v227\n"
    "v_mfma_f32_16x16x32_bf16 v[80:83], v[16:19], a[192:195], v[80:83]\n"
    "s_waitcnt lgkmcnt(2)\n"
    "ds_read_b64_tr_b16 v[28:29], v9 offset:24576\n"
    "v_mul_f32 v228, 0x3e0293ee, v228\n"
    "v_mfma_f32_16x16x32_bf16 v[80:83], v[20:23], a[208:211], v[80:83]\n"
    "s_waitcnt lgkmcnt(1)\n"
    "ds_read_b64_tr_b16 v[30:31], v9 offset:26624\n"
    "v_mul_f32 v229, 0x3e0293ee, v229\n"
    "v_mfma_f32_16x16x32_bf16 v[80:83], v[24:27], a[224:227], v[80:83]\n"
    "ds_read_b64_tr_b16 v[32:33], v9 offset:28672\n"
    "v_mul_f32 v230, 0x3e0293ee, v230\n"
    "v_mfma_f32_16x16x32_bf16 v[84:87], v[16:19], a[196:199], v[84:87]\n"
    "ds_read_b64_tr_b16 v[34:35], v9 offset:30720\n"
    "v_mul_f32 v231, 0x3e0293ee, v231\n"
    "v_mfma_f32_16x16x32_bf16 v[84:87], v[20:23], a[212:215], v[84:87]\n"
    "ds_read_b64_tr_b16 v[36:37], v9 offset:32768\n"
    "v_cvt_pk_bf16_f32 v176, v224, v225\n"
    "v_mfma_f32_16x16x32_bf16 v[84:87], v[24:27], a[228:231], v[84:87]\n"
    "buffer_load_dwordx4 v7, s[48:51], s87 offen offset:1024 lds\n"
    "v_cvt_pk_bf16_f32 v177, v226, v227\n"
    "v_mfma_f32_16x16x32_bf16 v[88:91], v[16:19], a[200:203], v[88:91]\n"
    "ds_read_b64_tr_b16 v[38:39], v9 offset:34816\n"
    "v_cvt_pk_bf16_f32 v178, v228, v229\n"
    "v_mfma_f32_16x16x32_bf16 v[88:91], v[20:23], a[216:219], v[88:91]\n"
    "v_cvt_pk_bf16_f32 v179, v230, v231\n"
    "v_mul_f32 v232, 0x3e0293ee, v232\n"
    "v_mfma_f32_16x16x32_bf16 v[88:91], v[24:27], a[232:235], v[88:91]\n"
    "v_mul_f32 v233, 0x3e0293ee, v233\n"
    "v_mul_f32 v234, 0x3e0293ee, v234\n"
    "v_mfma_f32_16x16x32_bf16 v[92:95], v[16:19], a[204:207], v[92:95]\n"
    "v_mul_f32 v235, 0x3e0293ee, v235\n"
    "v_mul_f32 v236, 0x3e0293ee, v236\n"
    "v_mfma_f32_16x16x32_bf16 v[92:95], v[20:23], a[220:223], v[92:95]\n"
    "v_mul_f32 v237, 0x3e0293ee, v237\n"
    "v_mul_f32 v238, 0x3e0293ee, v238\n"
    "v_mfma_f32_16x16x32_bf16 v[92:95], v[24:27], a[236:239], v[92:95]\n"
    "s_waitcnt lgkmcnt(4)\n"
    "v_mul_f32 v239, 0x3e0293ee, v239\n"
    "v_cvt_pk_bf16_f32 v180, v232, v233\n"
    "v_mfma_f32_16x16x32_bf16 v[64:67], v[28:31], a[240:243], v[64:67]\n"
    "s_waitcnt lgkmcnt(2)\n"
    "ds_read_b64_tr_b16 v[48:49], v9 offset:25600\n"
    "v_cvt_pk_bf16_f32 v181, v234, v235\n"
    "v_mfma_f32_16x16x32_bf16 v[64:67], v[32:35], v[96:99], v[64:67]\n"
    "s_waitcnt lgkmcnt(1)\n"
    "ds_read_b64_tr_b16 v[50:51], v9 offset:27648\n"
    "v_cvt_pk_bf16_f32 v182, v236, v237\n"
    "v_mfma_f32_16x16x32_bf16 v[64:67], v[36:39], v[112:115], v[64:67]\n"
    "ds_read_b64_tr_b16 v[52:53], v9 offset:29696\n"
    "v_cvt_pk_bf16_f32 v183, v238, v239\n"
    "v_mfma_f32_16x16x32_bf16 v[68:71], v[28:31], a[244:247], v[68:71]\n"
    "ds_read_b64_tr_b16 v[54:55], v9 offset:31744\n"
    "v_mul_f32 v240, 0x3e0293ee, v240\n"
    "v_mfma_f32_16x16x32_bf16 v[68:71], v[32:35], v[100:103], v[68:71]\n"
    "ds_read_b64_tr_b16 v[56:57], v9 offset:33792\n"
    "v_mul_f32 v241, 0x3e0293ee, v241\n"
    "v_mfma_f32_16x16x32_bf16 v[68:71], v[36:39], v[116:119], v[68:71]\n"
    "s_add_u32 m0, s91, 40960\n"
    "s_add_u32 s87, s90, 2949120\n"
    "buffer_load_dwordx4 v7, s[44:47], s87 offen lds\n"
    "v_mul_f32 v242, 0x3e0293ee, v242\n"
    "v_mfma_f32_16x16x32_bf16 v[72:75], v[28:31], a[248:251], v[72:75]\n"
    "ds_read_b64_tr_b16 v[58:59], v9 offset:35840\n"
    "v_xor_b32 v9, 32, v9\n"
    "v_mfma_f32_16x16x32_bf16 v[72:75], v[32:35], v[104:107], v[72:75]\n"
    "v_mul_f32 v243, 0x3e0293ee, v243\n"
    "v_mul_f32 v244, 0x3e0293ee, v244\n"
    "v_mfma_f32_16x16x32_bf16 v[72:75], v[36:39], v[120:123], v[72:75]\n"
    "v_mul_f32 v245, 0x3e0293ee, v245\n"
    "v_mul_f32 v246, 0x3e0293ee, v246\n"
    "v_mfma_f32_16x16x32_bf16 v[76:79], v[28:31], a[252:255], v[76:79]\n"
    "v_mul_f32 v247, 0x3e0293ee, v247\n"
    "v_cvt_pk_bf16_f32 v184, v240, v241\n"
    "v_mfma_f32_16x16x32_bf16 v[76:79], v[32:35], v[108:111], v[76:79]\n"
    "v_cvt_pk_bf16_f32 v185, v242, v243\n"
    "v_cvt_pk_bf16_f32 v186, v244, v245\n"
    "v_mfma_f32_16x16x32_bf16 v[76:79], v[36:39], v[124:127], v[76:79]\n"
    "s_waitcnt lgkmcnt(4)\n"
    "v_cvt_pk_bf16_f32 v187, v246, v247\n"
    "v_mul_f32 v248, 0x3e0293ee, v248\n"
    "v_mfma_f32_16x16x32_bf16 v[80:83], v[48:51], a[240:243], v[80:83]\n"
    "s_waitcnt lgkmcnt(2)\n"
    "ds_read_b64_tr_b16 v[16:17], v8 offset:0\n"
    "v_mul_f32 v249, 0x3e0293ee, v249\n"
    "v_mfma_f32_16x16x32_bf16 v[80:83], v[52:55], v[96:99], v[80:83]\n"
    "s_waitcnt lgkmcnt(1)\n"
    "ds_read_b64_tr_b16 v[18:19], v8 offset:2048\n"
    "v_mul_f32 v250, 0x3e0293ee, v250\n"
    "v_mfma_f32_16x16x32_bf16 v[80:83], v[56:59], v[112:115], v[80:83]\n"
    "ds_read_b64_tr_b16 v[20:21], v8 offset:4096\n"
    "v_mul_f32 v251, 0x3e0293ee, v251\n"
    "v_mfma_f32_16x16x32_bf16 v[84:87], v[48:51], a[244:247], v[84:87]\n"
    "ds_read_b64_tr_b16 v[22:23], v8 offset:6144\n"
    "v_mul_f32 v252, 0x3e0293ee, v252\n"
    "v_mfma_f32_16x16x32_bf16 v[84:87], v[52:55], v[100:103], v[84:87]\n"
    "buffer_load_dword v60, v63, s[32:35], s88 offen\n"
    "v_mul_f32 v253, 0x3e0293ee, v253\n"
    "v_mfma_f32_16x16x32_bf16 v[84:87], v[56:59], v[116:119], v[84:87]\n"
    "s_mov_b64 s[32:33], s[100:101]\n"
    "s_mov_b32 s34, 2048\n"
    "buffer_load_dwordx4 v7, s[48:51], s87 offen offset:1024 lds\n"
    "v_mul_f32 v254, 0x3e0293ee, v254\n"
    "v_mfma_f32_16x16x32_bf16 v[88:91], v[48:51], a[248:251], v[88:91]\n"
    "ds_read_b64_tr_b16 v[24:25], v8 offset:8192\n"
    "v_mul_f32 v255, 0x3e0293ee, v255\n"
    "v_mfma_f32_16x16x32_bf16 v[88:91], v[52:55], v[104:107], v[88:91]\n"
    "s_and_b32 s84, s81, 1\n"
    "s_mul_i32 s84, s84, 288\n"
    "ds_read_b64_tr_b16 v[26:27], v8 offset:10240\n"
    "v_cvt_pk_bf16_f32 v188, v248, v249\n"
    "v_mfma_f32_16x16x32_bf16 v[88:91], v[56:59], v[120:123], v[88:91]\n"
    "v_cvt_pk_bf16_f32 v189, v250, v251\n"
    "v_cvt_pk_bf16_f32 v190, v252, v253\n"
    "v_mfma_f32_16x16x32_bf16 v[92:95], v[48:51], a[252:255], v[92:95]\n"
    "s_mov_b32 s88, 0\n"
    "v_cvt_pk_bf16_f32 v191, v254, v255\n"
    "v_mfma_f32_16x16x32_bf16 v[92:95], v[52:55], v[108:111], v[92:95]\n"
    "s_mov_b32 s85, 73728\n"
    "v_mfma_f32_16x16x32_bf16 v[92:95], v[56:59], v[124:127], v[92:95]\n"
    "L_P1%=:\n"
    "s_waitcnt lgkmcnt(4)\n"
    "v_mfma_f32_16x16x32_bf16 v[192:195], v[16:19], a[0:3], 0\n"
    "s_waitcnt lgkmcnt(2)\n"
    "ds_read_b64_tr_b16 v[28:29], v8 offset:1024\n"
    "v_mfma_f32_16x16x32_bf16 v[192:195], v[20:23], a[16:19], v[192:195]\n"
    "s_waitcnt lgkmcnt(1)\n"
    "ds_read_b64_tr_b16 v[30:31], v8 offset:3072\n"
    "v_mfma_f32_16x16x32_bf16 v[192:195], v[24:27], a[32:35], v[192:195]\n"
    "ds_read_b64_tr_b16 v[32:33], v8 offset:5120\n"
    "v_mfma_f32_16x16x32_bf16 v[196:199], v[16:19], a[4:7], 0\n"
    "ds_read_b64_tr_b16 v[34:35], v8 offset:7168\n"
    "v_mfma_f32_16x16x32_bf16 v[196:199], v[20:23], a[20:23], v[196:199]\n"
    "ds_read_b64_tr_b16 v[36:37], v8 offset:9216\n"
    "v_mul_f32 v224, v64, v64\n"
    "v_mfma_f32_16x16x32_bf16 v[196:199], v[24:27], a[36:39], v[196:199]\n"
    "s_add_u32 m0, s91, 49152\n"
    "s_add_u32 s87, s90, 3538944\n"
    "buffer_load_dwordx4 v7, s[44:47], s87 offen lds\n"
    "v_mul_f32 v225, v65, v65\n"
    "v_mfma_f32_16x16x32_bf16 v[200:203], v[16:19], a[8:11], 0\n"
    "ds_read_b64_tr_b16 v[38:39], v8 offset:11264\n"
    "v_fmaak_f32 v224, v224, v2, 0xc0135761\n"
    "v_mfma_f32_16x16x32_bf16 v[200:203], v[20:23], a[24:27], v[200:203]\n"
    "v_fmaak_f32 v225, v225, v2, 0xc0135761\n"
    "v_mul_f32 v224, v224, v64\n"
    "v_mfma_f32_16x16x32_bf16 v[200:203], v[24:27], a[40:43], v[200:203]\n"
    "v_exp_f32 v224, v224\n"
    "v_mfma_f32_16x16x32_bf16 v[204:207], v[16:19], a[12:15], 0\n"
    "v_mul_f32 v225, v225, v65\n"
    "v_add_f32 v224, 1.0, v224\n"
    "v_mfma_f32_16x16x32_bf16 v[204:207], v[20:23], a[28:31], v[204:207]\n"
    "v_exp_f32 v225, v225\n"
    "v_mfma_f32_16x16x32_bf16 v[204:207], v[24:27], a[44:47], v[204:207]\n"
    "s_waitcnt lgkmcnt(4)\n"
    "v_rcp_f32 v224, v224\n"
    "v_mfma_f32_16x16x32_bf16 v[208:211], v[28:31], a[0:3], 0\n"
    "s_waitcnt lgkmcnt(2)\n"
    "ds_read_b64_tr_b16 v[48:49], v8 offset:12288\n"
    "v_add_f32 v225, 1.0, v225\n"
    "v_mfma_f32_16x16x32_bf16 v[208:211], v[32:35], a[16:19], v[208:211]\n"
    "s_waitcnt lgkmcnt(1)\n"
    "ds_read_b64_tr_b16 v[50:51], v8 offset:14336\n"
    "v_mul_f32 v224, v224, v64\n"
    "v_mfma_f32_16x16x32_bf16 v[208:211], v[36:39], a[32:35], v[208:211]\n"
    "ds_read_b64_tr_b16 v[52:53], v8 offset:16384\n"
    "v_mul_f32 v224, v224, v80\n"
    "v_mfma_f32_16x16x32_bf16 v[212:215], v[28:31], a[4:7], 0\n"
    "ds_read_b64_tr_b16 v[54:55], v8 offset:18432\n"
    "v_mul_f32 v226, v66, v66\n"
    "v_mfma_f32_16x16x32_bf16 v[212:215], v[32:35], a[20:23], v[212:215]\n"
    "ds_read_b64_tr_b16 v[56:57], v8 offset:20480\n"
    "v_mul_f32 v227, v67, v67\n"
    "v_mfma_f32_16x16x32_bf16 v[212:215], v[36:39], a[36:39], v[212:215]\n"
    "buffer_load_dwordx4 v7, s[48:51], s87 offen offset:1024 lds\n"
    "v_fmaak_f32 v226, v226, v2, 0xc0135761\n"
    "v_mfma_f32_16x16x32_bf16 v[216:219], v[28:31], a[8:11], 0\n"
    "ds_read_b64_tr_b16 v[58:59], v8 offset:22528\n"
    "v_fmaak_f32 v227, v227, v2, 0xc0135761\n"
    "v_mfma_f32_16x16x32_bf16 v[216:219], v[32:35], a[24:27], v[216:219]\n"
    "v_rcp_f32 v225, v225\n"
    "v_mfma_f32_16x16x32_bf16 v[216:219], v[36:39], a[40:43], v[216:219]\n"
    "v_mul_f32 v225, v225, v65\n"
    "v_mul_f32 v225, v225, v81\n"
    "v_mfma_f32_16x16x32_bf16 v[220:223], v[28:31], a[12:15], 0\n"
    "v_cvt_pk_bf16_f32 v240, v224, v225\n"
    "v_mul_f32 v226, v226, v66\n"
    "v_mfma_f32_16x16x32_bf16 v[220:223], v[32:35], a[28:31], v[220:223]\n"
    "v_exp_f32 v226, v226\n"
    "v_mfma_f32_16x16x32_bf16 v[220:223], v[36:39], a[44:47], v[220:223]\n"
    "s_waitcnt lgkmcnt(4)\n"
    "v_mul_f32 v227, v227, v67\n"
    "v_add_f32 v226, 1.0, v226\n"
    "v_mfma_f32_16x16x32_bf16 v[192:195], v[48:51], a[48:51], v[192:195]\n"
    "s_waitcnt lgkmcnt(2)\n"
    "ds_read_b64_tr_b16 v[16:17], v8 offset:13312\n"
    "v_mul_f32 v228, v68, v68\n"
    "v_mfma_f32_16x16x32_bf16 v[192:195], v[52:55], a[64:67], v[192:195]\n"
    "s_waitcnt lgkmcnt(1)\n"
    "ds_read_b64_tr_b16 v[18:19], v8 offset:15360\n"
    "v_mul_f32 v229, v69, v69\n"
    "v_mfma_f32_16x16x32_bf16 v[192:195], v[56:59], a[80:83], v[192:195]\n"
    "ds_read_b64_tr_b16 v[20:21], v8 offset:17408\n"
    "v_fmaak_f32 v228, v228, v2, 0xc0135761\n"
    "v_mfma_f32_16x16x32_bf16 v[196:199], v[48:51], a[52:55], v[196:199]\n"
    "ds_read_b64_tr_b16 v[22:23], v8 offset:19456\n"
    "v_fmaak_f32 v229, v229, v2, 0xc0135761\n"
    "v_mfma_f32_16x16x32_bf16 v[196:199], v[52:55], a[68:71], v[196:199]\n"
    "ds_read_b64_tr_b16 v[24:25], v8 offset:21504\n"
    "v_mul_f32 v228, v228, v68\n"
    "v_mfma_f32_16x16x32_bf16 v[196:199], v[56:59], a[84:87], v[196:199]\n"
    "s_add_u32 m0, s91, 57344\n"
    "s_add_u32 s87, s90, 4128768\n"
    "buffer_load_dwordx4 v7, s[44:47], s87 offen lds\n"
    "v_mul_f32 v229, v229, v69\n"
    "v_mfma_f32_16x16x32_bf16 v[200:203], v[48:51], a[56:59], v[200:203]\n"
    "ds_read_b64_tr_b16 v[26:27], v8 offset:23552\n"
    "v_mul_f32 v230, v70, v70\n"
    "v_mfma_f32_16x16x32_bf16 v[200:203], v[52:55], a[72:75], v[200:203]\n"
    "v_exp_f32 v227, v227\n"
    "v_mfma_f32_16x16x32_bf16 v[200:203], v[56:59], a[88:91], v[200:203]\n"
    "v_rcp_f32 v226, v226\n"
    "v_mfma_f32_16x16x32_bf16 v[204:207], v[48:51], a[60:63], v[204:207]\n"
    "v_exp_f32 v228, v228\n"
    "v_mfma_f32_16x16x32_bf16 v[204:207], v[52:55], a[76:79], v[204:207]\n"
    "v_exp_f32 v229, v229\n"
    "v_mfma_f32_16x16x32_bf16 v[204:207], v[56:59], a[92:95], v[204:207]\n"
    "s_waitcnt lgkmcnt(4)\n"
    "v_add_f32 v227, 1.0, v227\n"
    "v_mul_f32 v226, v226, v66\n"
    "v_mfma_f32_16x16x32_bf16 v[208:211], v[16:19], a[48:51], v[208:211]\n"
    "s_waitcnt lgkmcnt(2)\n"
    "ds_read_b64_tr_b16 v[28:29], v8 offset:24576\n"
    "v_mul_f32 v226, v226, v82\n"
    "v_mfma_f32_16x16x32_bf16 v[208:211], v[20:23], a[64:67], v[208:211]\n"
    "s_waitcnt lgkmcnt(1)\n"
    "ds_read_b64_tr_b16 v[30:31], v8 offset:26624\n"
    "v_add_f32 v228, 1.0, v228\n"
    "v_mfma_f32_16x16x32_bf16 v[208:211], v[24:27], a[80:83], v[208:211]\n"
    "ds_read_b64_tr_b16 v[32:33], v8 offset:28672\n"
    "v_add_f32 v229, 1.0, v229\n"
    "v_mfma_f32_16x16x32_bf16 v[212:215], v[16:19], a[52:55], v[212:215]\n"
    "ds_read_b64_tr_b16 v[34:35], v8 offset:30720\n"
    "v_mul_f32 v231, v71, v71\n"
    "v_mfma_f32_16x16x32_bf16 v[212:215], v[20:23], a[68:71], v[212:215]\n"
    "ds_read_b64_tr_b16 v[36:37], v8 offset:32768\n"
    "v_fmaak_f32 v230, v230, v2, 0xc0135761\n"
    "v_mfma_f32_16x16x32_bf16 v[212:215], v[24:27], a[84:87], v[212:215]\n"
    "buffer_load_dwordx4 v7, s[48:51], s87 offen offset:1024 lds\n"
    "v_fmaak_f32 v231, v231, v2, 0xc0135761\n"
    "v_mfma_f32_16x16x32_bf16 v[216:219], v[16:19], a[56:59], v[216:219]\n"
    "ds_read_b64_tr_b16 v[38:39], v8 offset:34816\n"
    "v_mul_f32 v230, v230, v70\n"
    "v_mfma_f32_16x16x32_bf16 v[216:219], v[20:23], a[72:75], v[216:219]\n"
    "v_rcp_f32 v227, v227\n"
    "v_mfma_f32_16x16x32_bf16 v[216:219], v[24:27], a[88:91], v[216:219]\n"
    "v_rcp_f32 v228, v228\n"
    "v_mfma_f32_16x16x32_bf16 v[220:223], v[16:19], a[60:63], v[220:223]\n"
    "v_rcp_f32 v229, v229\n"
    "v_mfma_f32_16x16x32_bf16 v[220:223], v[20:23], a[76:79], v[220:223]\n"
    "v_exp_f32 v230, v230\n"
    "v_mfma_f32_16x16x32_bf16 v[220:223], v[24:27], a[92:95], v[220:223]\n"
    "s_waitcnt lgkmcnt(4)\n"
    "v_mul_f32 v227, v227, v67\n"
    "v_mul_f32 v227, v227, v83\n"
    "v_mfma_f32_16x16x32_bf16 v[192:195], v[28:31], a[96:99], v[192:195]\n"
    "s_waitcnt lgkmcnt(2)\n"
    "ds_read_b64_tr_b16 v[48:49], v8 offset:25600\n"
    "v_cvt_pk_bf16_f32 v241, v226, v227\n"
    "v_mfma_f32_16x16x32_bf16 v[192:195], v[32:35], a[112:115], v[192:195]\n"
    "s_waitcnt lgkmcnt(1)\n"
    "ds_read_b64_tr_b16 v[50:51], v8 offset:27648\n"
    "v_mul_f32 v228, v228, v68\n"
    "v_mfma_f32_16x16x32_bf16 v[192:195], v[36:39], a[128:131], v[192:195]\n"
    "ds_read_b64_tr_b16 v[52:53], v8 offset:29696\n"
    "v_mul_f32 v229, v229, v69\n"
    "v_mfma_f32_16x16x32_bf16 v[196:199], v[28:31], a[100:103], v[196:199]\n"
    "ds_read_b64_tr_b16 v[54:55], v8 offset:31744\n"
    "v_mul_f32 v228, v228, v84\n"
    "v_mfma_f32_16x16x32_bf16 v[196:199], v[32:35], a[116:119], v[196:199]\n"
    "ds_read_b64_tr_b16 v[56:57], v8 offset:33792\n"
    "v_mul_f32 v229, v229, v85\n"
    "v_mfma_f32_16x16x32_bf16 v[196:199], v[36:39], a[132:135], v[196:199]\n"
    "s_add_u32 m0, s91, 65536\n"
    "s_add_u32 s87, s90, 4718592\n"
    "buffer_load_dwordx4 v7, s[44:47], s87 offen lds\n"
    "v_cvt_pk_bf16_f32 v244, v228, v229\n"
    "v_mfma_f32_16x16x32_bf16 v[200:203], v[28:31], a[104:107], v[200:203]\n"
    "ds_read_b64_tr_b16 v[58:59], v8 offset:35840\n"
    "v_xor_b32 v8, 32, v8\n"
    "v_mfma_f32_16x16x32_bf16 v[200:203], v[32:35], a[120:123], v[200:203]\n"
    "v_add_u32 v8, s85, v8\n"
    "v_mul_f32 v231, v231, v71\n"
    "v_mfma_f32_16x16x32_bf16 v[200:203], v[36:39], a[136:139], v[200:203]\n"
    "v_exp_f32 v231, v231\n"
    "v_mfma_f32_16x16x32_bf16 v[204:207], v[28:31], a[108:111], v[204:207]\n"
    "v_add_f32 v230, 1.0, v230\n"
    "v_add_f32 v231, 1.0, v231\n"
    "v_mfma_f32_16x16x32_bf16 v[204:207], v[32:35], a[124:127], v[204:207]\n"
    "v_rcp_f32 v230, v230\n"
    "v_mfma_f32_16x16x32_bf16 v[204:207], v[36:39], a[140:143], v[204:207]\n"
    "s_waitcnt lgkmcnt(4)\n"
    "v_rcp_f32 v231, v231\n"
    "v_mfma_f32_16x16x32_bf16 v[208:211], v[48:51], a[96:99], v[208:211]\n"
    "s_waitcnt lgkmcnt(2)\n"
    "ds_read_b64_tr_b16 v[16:17], v9 offset:0\n"
    "v_mul_f32 v230, v230, v70\n"
    "v_mfma_f32_16x16x32_bf16 v[208:211], v[52:55], a[112:115], v[208:211]\n"
    "s_waitcnt lgkmcnt(1)\n"
    "ds_read_b64_tr_b16 v[18:19], v9 offset:2048\n"
    "v_mul_f32 v231, v231, v71\n"
    "v_mfma_f32_16x16x32_bf16 v[208:211], v[56:59], a[128:131], v[208:211]\n"
    "ds_read_b64_tr_b16 v[20:21], v9 offset:4096\n"
    "v_mul_f32 v230, v230, v86\n"
    "v_mfma_f32_16x16x32_bf16 v[212:215], v[48:51], a[100:103], v[212:215]\n"
    "ds_read_b64_tr_b16 v[22:23], v9 offset:6144\n"
    "v_mul_f32 v231, v231, v87\n"
    "v_mfma_f32_16x16x32_bf16 v[212:215], v[52:55], a[116:119], v[212:215]\n"
    "ds_read_b64_tr_b16 v[24:25], v9 offset:8192\n"
    "v_cvt_pk_bf16_f32 v245, v230, v231\n"
    "v_mfma_f32_16x16x32_bf16 v[212:215], v[56:59], a[132:135], v[212:215]\n"
    "buffer_load_dwordx4 v7, s[48:51], s87 offen offset:1024 lds\n"
    "v_mul_f32 v224, v72, v72\n"
    "v_mfma_f32_16x16x32_bf16 v[216:219], v[48:51], a[104:107], v[216:219]\n"
    "ds_read_b64_tr_b16 v[26:27], v9 offset:10240\n"
    "v_mul_f32 v225, v73, v73\n"
    "v_mfma_f32_16x16x32_bf16 v[216:219], v[52:55], a[120:123], v[216:219]\n"
    "v_fmaak_f32 v224, v224, v2, 0xc0135761\n"
    "v_fmaak_f32 v225, v225, v2, 0xc0135761\n"
    "v_mfma_f32_16x16x32_bf16 v[216:219], v[56:59], a[136:139], v[216:219]\n"
    "v_mul_f32 v224, v224, v72\n"
    "v_mul_f32 v225, v225, v73\n"
    "v_mfma_f32_16x16x32_bf16 v[220:223], v[48:51], a[108:111], v[220:223]\n"
    "v_exp_f32 v224, v224\n"
    "v_mfma_f32_16x16x32_bf16 v[220:223], v[52:55], a[124:127], v[220:223]\n"
    "s_cmp_eq_u32 s88, 1\n"
    "s_cselect_b32 s83, 0, 0x40000000\n"
    "v_exp_f32 v225, v225\n"
    "v_mfma_f32_16x16x32_bf16 v[220:223], v[56:59], a[140:143], v[220:223]\n"
    "s_waitcnt lgkmcnt(4)\n"
    "buffer_load_dword v60, v61, s[32:35], s83 offen\n"
    "v_add_f32 v224, 1.0, v224\n"
    "v_mfma_f32_16x16x32_bf16 v[192:195], v[16:19], a[144:147], v[192:195]\n"
    "s_waitcnt lgkmcnt(2)\n"
    "ds_read_b64_tr_b16 v[28:29], v9 offset:1024\n"
    "v_add_f32 v225, 1.0, v225\n"
    "v_mfma_f32_16x16x32_bf16 v[192:195], v[20:23], a[160:163], v[192:195]\n"
    "s_waitcnt lgkmcnt(1)\n"
    "ds_read_b64_tr_b16 v[30:31], v9 offset:3072\n"
    "v_mul_f32 v226, v74, v74\n"
    "v_mfma_f32_16x16x32_bf16 v[192:195], v[24:27], a[176:179], v[192:195]\n"
    "ds_read_b64_tr_b16 v[32:33], v9 offset:5120\n"
    "v_mul_f32 v227, v75, v75\n"
    "v_mfma_f32_16x16x32_bf16 v[196:199], v[16:19], a[148:151], v[196:199]\n"
    "ds_read_b64_tr_b16 v[34:35], v9 offset:7168\n"
    "v_fmaak_f32 v226, v226, v2, 0xc0135761\n"
    "v_mfma_f32_16x16x32_bf16 v[196:199], v[20:23], a[164:167], v[196:199]\n"
    "ds_read_b64_tr_b16 v[36:37], v9 offset:9216\n"
    "v_fmaak_f32 v227, v227, v2, 0xc0135761\n"
    "v_mfma_f32_16x16x32_bf16 v[196:199], v[24:27], a[180:183], v[196:199]\n"
    "ds_read_b64_tr_b16 v[38:39], v9 offset:11264\n"
    "v_mul_f32 v226, v226, v74\n"
    "v_mfma_f32_16x16x32_bf16 v[200:203], v[16:19], a[152:155], v[200:203]\n"
    "v_rcp_f32 v224, v224\n"
    "v_mfma_f32_16x16x32_bf16 v[200:203], v[20:23], a[168:171], v[200:203]\n"
    "v_rcp_f32 v225, v225\n"
    "v_mfma_f32_16x16x32_bf16 v[200:203], v[24:27], a[184:187], v[200:203]\n"
    "v_exp_f32 v226, v226\n"
    "v_mfma_f32_16x16x32_bf16 v[204:207], v[16:19], a[156:159], v[204:207]\n"
    "v_mul_f32 v224, v224, v72\n"
    "v_mul_f32 v225, v225, v73\n"
    "v_mfma_f32_16x16x32_bf16 v[204:207], v[20:23], a[172:175], v[204:207]\n"
    "v_mul_f32 v224, v224, v88\n"
    "v_mul_f32 v225, v225, v89\n"
    "v_mfma_f32_16x16x32_bf16 v[204:207], v[24:27], a[188:191], v[204:207]\n"
    "s_waitcnt lgkmcnt(4)\n"
    "v_cvt_pk_bf16_f32 v248, v224, v225\n"
    "v_mul_f32 v227, v227, v75\n"
    "v_mfma_f32_16x16x32_bf16 v[208:211], v[28:31], a[144:147], v[208:211]\n"
    "s_waitcnt lgkmcnt(2)\n"
    "ds_read_b64_tr_b16 v[48:49], v9 offset:12288\n"
    "v_add_f32 v226, 1.0, v226\n"
    "v_mfma_f32_16x16x32_bf16 v[208:211], v[32:35], a[160:163], v[208:211]\n"
    "s_waitcnt lgkmcnt(1)\n"
    "ds_read_b64_tr_b16 v[50:51], v9 offset:14336\n"
    "v_mul_f32 v228, v76, v76\n"
    "v_mfma_f32_16x16x32_bf16 v[208:211], v[36:39], a[176:179], v[208:211]\n"
    "ds_read_b64_tr_b16 v[52:53], v9 offset:16384\n"
    "v_mul_f32 v229, v77, v77\n"
    "v_mfma_f32_16x16x32_bf16 v[212:215], v[28:31], a[148:151], v[212:215]\n"
    "ds_read_b64_tr_b16 v[54:55], v9 offset:18432\n"
    "v_fmaak_f32 v228, v228, v2, 0xc0135761\n"
    "v_mfma_f32_16x16x32_bf16 v[212:215], v[32:35], a[164:167], v[212:215]\n"
    "ds_read_b64_tr_b16 v[56:57], v9 offset:20480\n"
    "v_fmaak_f32 v229, v229, v2, 0xc0135761\n"
    "v_mfma_f32_16x16x32_bf16 v[212:215], v[36:39], a[180:183], v[212:215]\n"
    "ds_read_b64_tr_b16 v[58:59], v9 offset:22528\n"
    "v_mul_f32 v228, v228, v76\n"
    "v_mfma_f32_16x16x32_bf16 v[216:219], v[28:31], a[152:155], v[216:219]\n"
    "v_exp_f32 v227, v227\n"
    "v_mfma_f32_16x16x32_bf16 v[216:219], v[32:35], a[168:171], v[216:219]\n"
    "v_rcp_f32 v226, v226\n"
    "v_mfma_f32_16x16x32_bf16 v[216:219], v[36:39], a[184:187], v[216:219]\n"
    "v_exp_f32 v228, v228\n"
    "v_mfma_f32_16x16x32_bf16 v[220:223], v[28:31], a[156:159], v[220:223]\n"
    "v_add_f32 v227, 1.0, v227\n"
    "v_mul_f32 v226, v226, v74\n"
    "v_mfma_f32_16x16x32_bf16 v[220:223], v[32:35], a[172:175], v[220:223]\n"
    "v_rcp_f32 v227, v227\n"
    "v_mfma_f32_16x16x32_bf16 v[220:223], v[36:39], a[188:191], v[220:223]\n"
    "s_waitcnt lgkmcnt(4)\n"
    "v_mul_f32 v227, v227, v75\n"
    "v_mul_f32 v226, v226, v90\n"
    "v_mfma_f32_16x16x32_bf16 v[192:195], v[48:51], a[192:195], v[192:195]\n"
    "s_waitcnt lgkmcnt(2)\n"
    "ds_read_b64_tr_b16 v[16:17], v9 offset:13312\n"
    "v_mul_f32 v227, v227, v91\n"
    "v_mfma_f32_16x16x32_bf16 v[192:195], v[52:55], a[208:211], v[192:195]\n"
    "s_waitcnt lgkmcnt(1)\n"
    "ds_read_b64_tr_b16 v[18:19], v9 offset:15360\n"
    "v_cvt_pk_bf16_f32 v249, v226, v227\n"
    "v_mfma_f32_16x16x32_bf16 v[192:195], v[56:59], a[224:227], v[192:195]\n"
    "ds_read_b64_tr_b16 v[20:21], v9 offset:17408\n"
    "v_mul_f32 v229, v229, v77\n"
    "v_mfma_f32_16x16x32_bf16 v[196:199], v[48:51], a[196:199], v[196:199]\n"
    "ds_read_b64_tr_b16 v[22:23], v9 offset:19456\n"
    "v_add_f32 v228, 1.0, v228\n"
    "v_mfma_f32_16x16x32_bf16 v[196:199], v[52:55], a[212:215], v[196:199]\n"
    "ds_read_b64_tr_b16 v[24:25], v9 offset:21504\n"
    "v_mul_f32 v230, v78, v78\n"
    "v_mfma_f32_16x16x32_bf16 v[196:199], v[56:59], a[228:231], v[196:199]\n"
    "ds_read_b64_tr_b16 v[26:27], v9 offset:23552\n"
    "v_mul_f32 v231, v79, v79\n"
    "v_mfma_f32_16x16x32_bf16 v[200:203], v[48:51], a[200:203], v[200:203]\n"
    "v_exp_f32 v229, v229\n"
    "v_mfma_f32_16x16x32_bf16 v[200:203], v[52:55], a[216:219], v[200:203]\n"
    "v_rcp_f32 v228, v228\n"
    "v_mfma_f32_16x16x32_bf16 v[200:203], v[56:59], a[232:235], v[200:203]\n"
    "v_add_f32 v229, 1.0, v229\n"
    "v_mul_f32 v228, v228, v76\n"
    "v_mfma_f32_16x16x32_bf16 v[204:207], v[48:51], a[204:207], v[204:207]\n"
    "v_rcp_f32 v229, v229\n"
    "v_mfma_f32_16x16x32_bf16 v[204:207], v[52:55], a[220:223], v[204:207]\n"
    "v_mul_f32 v229, v229, v77\n"
    "v_mul_f32 v228, v228, v92\n"
    "v_mfma_f32_16x16x32_bf16 v[204:207], v[56:59], a[236:239], v[204:207]\n"
    "s_waitcnt lgkmcnt(4)\n"
    "v_mul_f32 v229, v229, v93\n"
    "v_cvt_pk_bf16_f32 v252, v228, v229\n"
    "v_mfma_f32_16x16x32_bf16 v[208:211], v[16:19], a[192:195], v[208:211]\n"
    "s_waitcnt lgkmcnt(2)\n"
    "ds_read_b64_tr_b16 v[28:29], v9 offset:24576\n"
    "v_fmaak_f32 v230, v230, v2, 0xc0135761\n"
    "v_mfma_f32_16x16x32_bf16 v[208:211], v[20:23], a[208:211], v[208:211]\n"
    "s_waitcnt lgkmcnt(1)\n"
    "ds_read_b64_tr_b16 v[30:31], v9 offset:26624\n"
    "v_fmaak_f32 v231, v231, v2, 0xc0135761\n"
    "v_mfma_f32_16x16x32_bf16 v[208:211], v[24:27], a[224:227], v[208:211]\n"
    "ds_read_b64_tr_b16 v[32:33], v9 offset:28672\n"
    "v_mul_f32 v230, v230, v78\n"
    "v_mfma_f32_16x16x32_bf16 v[212:215], v[16:19], a[196:199], v[212:215]\n"
    "ds_read_b64_tr_b16 v[34:35], v9 offset:30720\n"
    "v_mul_f32 v231, v231, v79\n"
    "v_mfma_f32_16x16x32_bf16 v[212:215], v[20:23], a[212:215], v[212:215]\n"
    "ds_read_b64_tr_b16 v[36:37], v9 offset:32768\n"
    "v_bfe_u32 v13, v0, 4, 4\n"
    "v_mfma_f32_16x16x32_bf16 v[212:215], v[24:27], a[228:231], v[212:215]\n"
    "ds_read_b64_tr_b16 v[38:39], v9 offset:34816\n"
    "v_lshrrev_b32 v4, 8, v0\n"
    "v_mfma_f32_16x16x32_bf16 v[216:219], v[16:19], a[200:203], v[216:219]\n"
    "v_exp_f32 v230, v230\n"
    "v_mfma_f32_16x16x32_bf16 v[216:219], v[20:23], a[216:219], v[216:219]\n"
    "v_exp_f32 v231, v231\n"
    "v_mfma_f32_16x16x32_bf16 v[216:219], v[24:27], a[232:235], v[216:219]\n"
    "v_add_f32 v230, 1.0, v230\n"
    "v_add_f32 v231, 1.0, v231\n"
    "v_mfma_f32_16x16x32_bf16 v[220:223], v[16:19], a[204:207], v[220:223]\n"
    "v_rcp_f32 v230, v230\n"
    "v_mfma_f32_16x16x32_bf16 v[220:223], v[20:23], a[220:223], v[220:223]\n"
    "v_rcp_f32 v231, v231\n"
    "v_mfma_f32_16x16x32_bf16 v[220:223], v[24:27], a[236:239], v[220:223]\n"
    "s_waitcnt lgkmcnt(4)\n"
    "v_mul_f32 v230, v230, v78\n"
    "v_mul_f32 v231, v231, v79\n"
    "v_mfma_f32_16x16x32_bf16 v[192:195], v[28:31], a[240:243], v[192:195]\n"
    "s_waitcnt lgkmcnt(2)\n"
    "ds_read_b64_tr_b16 v[48:49], v9 offset:25600\n"
    "v_mul_f32 v230, v230, v94\n"
    "v_mfma_f32_16x16x32_bf16 v[192:195], v[32:35], v[96:99], v[192:195]\n"
    "s_waitcnt lgkmcnt(1)\n"
    "ds_read_b64_tr_b16 v[50:51], v9 offset:27648\n"
    "v_mul_f32 v231, v231, v95\n"
    "v_mfma_f32_16x16x32_bf16 v[192:195], v[36:39], v[112:115], v[192:195]\n"
    "ds_read_b64_tr_b16 v[52:53], v9 offset:29696\n"
    "v_cvt_pk_bf16_f32 v253, v230, v231\n"
    "v_mfma_f32_16x16x32_bf16 v[196:199], v[28:31], a[244:247], v[196:199]\n"
    "ds_read_b64_tr_b16 v[54:55], v9 offset:31744\n"
    "v_xor_b32 v4, v4, v13\n"
    "v_mfma_f32_16x16x32_bf16 v[196:199], v[32:35], v[100:103], v[196:199]\n"
    "ds_read_b64_tr_b16 v[56:57], v9 offset:33792\n"
    "v_lshlrev_b32 v4, 4, v4\n"
    "v_mfma_f32_16x16x32_bf16 v[196:199], v[36:39], v[116:119], v[196:199]\n"
    "ds_read_b64_tr_b16 v[58:59], v9 offset:35840\n"
    "v_xor_b32 v9, 32, v9\n"
    "v_mfma_f32_16x16x32_bf16 v[200:203], v[28:31], a[248:251], v[200:203]\n"
    "v_add_u32 v9, s85, v9\n"
    "v_lshl_add_u32 v4, v13, 8, v4\n"
    "v_mfma_f32_16x16x32_bf16 v[200:203], v[32:35], v[104:107], v[200:203]\n"
    "v_add_u32 v3, 98304, v4\n"
    "v_lshrrev_b32 v13, 6, v0\n"
    "v_mfma_f32_16x16x32_bf16 v[200:203], v[36:39], v[120:123], v[200:203]\n"
    "v_bfe_u32 v6, v0, 1, 5\n"
    "v_lshl_add_u32 v6, v13, 8, v6\n"
    "v_mfma_f32_16x16x32_bf16 v[204:207], v[28:31], a[252:255], v[204:207]\n"
    "v_and_b32 v13, 7, v13\n"
    "v_lshl_add_u32 v6, v13, 5, v6\n"
    "v_mfma_f32_16x16x32_bf16 v[204:207], v[32:35], v[108:111], v[204:207]\n"
    "v_add_u32 v5, 98304, v6\n"
    "v_mfma_f32_16x16x32_bf16 v[204:207], v[36:39], v[124:127], v[204:207]\n"
    "s_waitcnt lgkmcnt(4)\n"
    "v_mfma_f32_16x16x32_bf16 v[208:211], v[48:51], a[240:243], v[208:211]\n"
    "s_waitcnt lgkmcnt(2)\n"
    "v_mfma_f32_16x16x32_bf16 v[208:211], v[52:55], v[96:99], v[208:211]\n"
    "s_waitcnt lgkmcnt(0)\n"
    "v_mfma_f32_16x16x32_bf16 v[208:211], v[56:59], v[112:115], v[208:211]\n"
    "v_mfma_f32_16x16x32_bf16 v[212:215], v[48:51], a[244:247], v[212:215]\n"
    "v_mfma_f32_16x16x32_bf16 v[212:215], v[52:55], v[100:103], v[212:215]\n"
    "v_mfma_f32_16x16x32_bf16 v[212:215], v[56:59], v[116:119], v[212:215]\n"
    "s_waitcnt vmcnt(1)\n"
    "s_barrier\n"
    "ds_read_b64_tr_b16 v[16:17], v8 offset:0\n"
    "v_mfma_f32_16x16x32_bf16 v[216:219], v[48:51], a[248:251], v[216:219]\n"
    "ds_read_b64_tr_b16 v[18:19], v8 offset:2048\n"
    "v_mfma_f32_16x16x32_bf16 v[216:219], v[52:55], v[104:107], v[216:219]\n"
    "ds_read_b64_tr_b16 v[20:21], v8 offset:4096\n"
    "v_mfma_f32_16x16x32_bf16 v[216:219], v[56:59], v[120:123], v[216:219]\n"
    "ds_read_b64_tr_b16 v[22:23], v8 offset:6144\n"
    "v_mfma_f32_16x16x32_bf16 v[220:223], v[48:51], a[252:255], v[220:223]\n"
    "ds_read_b64_tr_b16 v[24:25], v8 offset:8192\n"
    "ds_read_b64_tr_b16 v[26:27], v8 offset:10240\n"
    "v_mfma_f32_16x16x32_bf16 v[220:223], v[52:55], v[108:111], v[220:223]\n"
    "v_mfma_f32_16x16x32_bf16 v[220:223], v[56:59], v[124:127], v[220:223]\n"
    "s_cmp_eq_u32 s88, 3\n"
    "s_cbranch_scc1 L_P1X%=\n"
    "s_waitcnt lgkmcnt(4)\n"
    "v_mfma_f32_16x16x32_bf16 v[64:67], v[16:19], a[0:3], 0\n"
    "s_waitcnt lgkmcnt(2)\n"
    "s_sub_u32 s91, s91, s85\n"
    "s_sub_u32 s85, 0, s85\n"
    "s_add_u32 s90, s90, 64\n"
    "ds_read_b64_tr_b16 v[28:29], v8 offset:1024\n"
    "v_mfma_f32_16x16x32_bf16 v[64:67], v[20:23], a[16:19], v[64:67]\n"
    "s_waitcnt lgkmcnt(1)\n"
    "s_cmp_eq_u32 s88, 2\n"
    "s_cselect_b32 s83, s84, 0\n"
    "s_cselect_b32 s87, 0x40000000, 0\n"
    "ds_read_b64_tr_b16 v[30:31], v8 offset:3072\n"
    "v_mfma_f32_16x16x32_bf16 v[64:67], v[24:27], a[32:35], v[64:67]\n"
    "s_sub_u32 s90, s90, s83\n"
    "ds_read_b64_tr_b16 v[32:33], v8 offset:5120\n"
    "v_bfe_i32 v12, v7, 5, 1\n"
    "v_and_b32 v12, s87, v12\n"
    "v_mfma_f32_16x16x32_bf16 v[68:71], v[16:19], a[4:7], 0\n"
    "ds_read_b64_tr_b16 v[34:35], v8 offset:7168\n"
    "v_or_b32 v7, v7, v12\n"
    "v_mfma_f32_16x16x32_bf16 v[68:71], v[20:23], a[20:23], v[68:71]\n"
    "ds_read_b64_tr_b16 v[36:37], v8 offset:9216\n"
    "v_mul_f32 v224, v192, v192\n"
    "v_mfma_f32_16x16x32_bf16 v[68:71], v[24:27], a[36:39], v[68:71]\n"
    "s_add_u32 m0, s91, 0\n"
    "s_add_u32 s87, s90, 0\n"
    "ds_read_b64_tr_b16 v[38:39], v8 offset:11264\n"
    "buffer_load_dwordx4 v7, s[44:47], s87 offen lds\n"
    "v_mfma_f32_16x16x32_bf16 v[72:75], v[16:19], a[8:11], 0\n"
    "v_mul_f32 v225, v193, v193\n"
    "v_fmaak_f32 v224, v224, v2, 0xc0135761\n"
    "v_mfma_f32_16x16x32_bf16 v[72:75], v[20:23], a[24:27], v[72:75]\n"
    "v_fmaak_f32 v225, v225, v2, 0xc0135761\n"
    "v_mul_f32 v224, v224, v192\n"
    "v_mfma_f32_16x16x32_bf16 v[72:75], v[24:27], a[40:43], v[72:75]\n"
    "v_exp_f32 v224, v224\n"
    "v_mfma_f32_16x16x32_bf16 v[76:79], v[16:19], a[12:15], 0\n"
    "v_mul_f32 v225, v225, v193\n"
    "v_add_f32 v224, 1.0, v224\n"
    "v_mfma_f32_16x16x32_bf16 v[76:79], v[20:23], a[28:31], v[76:79]\n"
    "v_exp_f32 v225, v225\n"
    "v_mfma_f32_16x16x32_bf16 v[76:79], v[24:27], a[44:47], v[76:79]\n"
    "s_waitcnt lgkmcnt(4)\n"
    "v_rcp_f32 v224, v224\n"
    "v_mfma_f32_16x16x32_bf16 v[80:83], v[28:31], a[0:3], 0\n"
    "s_waitcnt lgkmcnt(2)\n"
    "ds_read_b64_tr_b16 v[48:49], v8 offset:12288\n"
    "v_add_f32 v225, 1.0, v225\n"
    "v_mfma_f32_16x16x32_bf16 v[80:83], v[32:35], a[16:19], v[80:83]\n"
    "s_waitcnt lgkmcnt(1)\n"
    "ds_read_b64_tr_b16 v[50:51], v8 offset:14336\n"
    "v_mul_f32 v224, v224, v192\n"
    "v_mfma_f32_16x16x32_bf16 v[80:83], v[36:39], a[32:35], v[80:83]\n"
    "ds_read_b64_tr_b16 v[52:53], v8 offset:16384\n"
    "v_mul_f32 v224, v224, v208\n"
    "v_mfma_f32_16x16x32_bf16 v[84:87], v[28:31], a[4:7], 0\n"
    "ds_read_b64_tr_b16 v[54:55], v8 offset:18432\n"
    "v_mul_f32 v226, v194, v194\n"
    "v_mfma_f32_16x16x32_bf16 v[84:87], v[32:35], a[20:23], v[84:87]\n"
    "ds_read_b64_tr_b16 v[56:57], v8 offset:20480\n"
    "v_mul_f32 v227, v195, v195\n"
    "v_mfma_f32_16x16x32_bf16 v[84:87], v[36:39], a[36:39], v[84:87]\n"
    "ds_read_b64_tr_b16 v[58:59], v8 offset:22528\n"
    "buffer_load_dwordx4 v7, s[48:51], s87 offen offset:1024 lds\n"
    "v_mfma_f32_16x16x32_bf16 v[88:91], v[28:31], a[8:11], 0\n"
    "v_rcp_f32 v225, v225\n"
    "v_mfma_f32_16x16x32_bf16 v[88:91], v[32:35], a[24:27], v[88:91]\n"
    "v_mul_f32 v225, v225, v193\n"
    "v_mul_f32 v225, v225, v209\n"
    "v_mfma_f32_16x16x32_bf16 v[88:91], v[36:39], a[40:43], v[88:91]\n"
    "v_cvt_pk_bf16_f32 v242, v224, v225\n"
    "v_fmaak_f32 v226, v226, v2, 0xc0135761\n"
    "v_mfma_f32_16x16x32_bf16 v[92:95], v[28:31], a[12:15], 0\n"
    "v_fmaak_f32 v227, v227, v2, 0xc0135761\n"
    "v_mul_f32 v226, v226, v194\n"
    "v_mfma_f32_16x16x32_bf16 v[92:95], v[32:35], a[28:31], v[92:95]\n"
    "v_exp_f32 v226, v226\n"
    "v_mfma_f32_16x16x32_bf16 v[92:95], v[36:39], a[44:47], v[92:95]\n"
    "s_waitcnt lgkmcnt(4)\n"
    "v_mul_f32 v227, v227, v195\n"
    "v_add_f32 v226, 1.0, v226\n"
    "v_mfma_f32_16x16x32_bf16 v[64:67], v[48:51], a[48:51], v[64:67]\n"
    "s_waitcnt lgkmcnt(2)\n"
    "ds_read_b64_tr_b16 v[16:17], v8 offset:13312\n"
    "v_mul_f32 v228, v196, v196\n"
    "v_mfma_f32_16x16x32_bf16 v[64:67], v[52:55], a[64:67], v[64:67]\n"
    "s_waitcnt lgkmcnt(1)\n"
    "ds_read_b64_tr_b16 v[18:19], v8 offset:15360\n"
    "v_mul_f32 v229, v197, v197\n"
    "v_mfma_f32_16x16x32_bf16 v[64:67], v[56:59], a[80:83], v[64:67]\n"
    "ds_read_b64_tr_b16 v[20:21], v8 offset:17408\n"
    "v_fmaak_f32 v228, v228, v2, 0xc0135761\n"
    "v_mfma_f32_16x16x32_bf16 v[68:71], v[48:51], a[52:55], v[68:71]\n"
    "ds_read_b64_tr_b16 v[22:23], v8 offset:19456\n"
    "v_fmaak_f32 v229, v229, v2, 0xc0135761\n"
    "v_mfma_f32_16x16x32_bf16 v[68:71], v[52:55], a[68:71], v[68:71]\n"
    "ds_read_b64_tr_b16 v[24:25], v8 offset:21504\n"
    "v_mul_f32 v228, v228, v196\n"
    "v_mfma_f32_16x16x32_bf16 v[68:71], v[56:59], a[84:87], v[68:71]\n"
    "s_add_u32 m0, s91, 8192\n"
    "s_add_u32 s87, s90, 589824\n"
    "ds_read_b64_tr_b16 v[26:27], v8 offset:23552\n"
    "buffer_load_dwordx4 v7, s[44:47], s87 offen lds\n"
    "v_mfma_f32_16x16x32_bf16 v[72:75], v[48:51], a[56:59], v[72:75]\n"
    "v_exp_f32 v227, v227\n"
    "v_mfma_f32_16x16x32_bf16 v[72:75], v[52:55], a[72:75], v[72:75]\n"
    "v_rcp_f32 v226, v226\n"
    "v_mfma_f32_16x16x32_bf16 v[72:75], v[56:59], a[88:91], v[72:75]\n"
    "v_exp_f32 v228, v228\n"
    "v_mfma_f32_16x16x32_bf16 v[76:79], v[48:51], a[60:63], v[76:79]\n"
    "v_add_f32 v227, 1.0, v227\n"
    "v_mul_f32 v226, v226, v194\n"
    "v_mfma_f32_16x16x32_bf16 v[76:79], v[52:55], a[76:79], v[76:79]\n"
    "v_rcp_f32 v227, v227\n"
    "v_mfma_f32_16x16x32_bf16 v[76:79], v[56:59], a[92:95], v[76:79]\n"
    "s_waitcnt lgkmcnt(4)\n"
    "v_mul_f32 v227, v227, v195\n"
    "v_mul_f32 v226, v226, v210\n"
    "v_mfma_f32_16x16x32_bf16 v[80:83], v[16:19], a[48:51], v[80:83]\n"
    "s_waitcnt lgkmcnt(2)\n"
    "ds_read_b64_tr_b16 v[28:29], v8 offset:24576\n"
    "v_mul_f32 v227, v227, v211\n"
    "v_mfma_f32_16x16x32_bf16 v[80:83], v[20:23], a[64:67], v[80:83]\n"
    "s_waitcnt lgkmcnt(1)\n"
    "ds_read_b64_tr_b16 v[30:31], v8 offset:26624\n"
    "v_cvt_pk_bf16_f32 v243, v226, v227\n"
    "v_mfma_f32_16x16x32_bf16 v[80:83], v[24:27], a[80:83], v[80:83]\n"
    "ds_read_b64_tr_b16 v[32:33], v8 offset:28672\n"
    "buffer_store_dwordx4 v[240:243], v0, s[52:55], s89 offen offset:0 sc1\n"
    "v_mfma_f32_16x16x32_bf16 v[84:87], v[16:19], a[52:55], v[84:87]\n"
    "ds_read_b64_tr_b16 v[34:35], v8 offset:30720\n"
    "v_mul_f32 v229, v229, v197\n"
    "v_mfma_f32_16x16x32_bf16 v[84:87], v[20:23], a[68:71], v[84:87]\n"
    "ds_read_b64_tr_b16 v[36:37], v8 offset:32768\n"
    "v_add_f32 v228, 1.0, v228\n"
    "v_mfma_f32_16x16x32_bf16 v[84:87], v[24:27], a[84:87], v[84:87]\n"
    "ds_read_b64_tr_b16 v[38:39], v8 offset:34816\n"
    "buffer_load_dwordx4 v7, s[48:51], s87 offen offset:1024 lds\n"
    "v_mfma_f32_16x16x32_bf16 v[88:91], v[16:19], a[56:59], v[88:91]\n"
    "v_exp_f32 v229, v229\n"
    "v_mfma_f32_16x16x32_bf16 v[88:91], v[20:23], a[72:75], v[88:91]\n"
    "v_rcp_f32 v228, v228\n"
    "v_mfma_f32_16x16x32_bf16 v[88:91], v[24:27], a[88:91], v[88:91]\n"
    "v_add_f32 v229, 1.0, v229\n"
    "v_mul_f32 v228, v228, v196\n"
    "v_mfma_f32_16x16x32_bf16 v[92:95], v[16:19], a[60:63], v[92:95]\n"
    "v_rcp_f32 v229, v229\n"
    "v_mfma_f32_16x16x32_bf16 v[92:95], v[20:23], a[76:79], v[92:95]\n"
    "v_mul_f32 v229, v229, v197\n"
    "v_mul_f32 v228, v228, v212\n"
    "v_mfma_f32_16x16x32_bf16 v[92:95], v[24:27], a[92:95], v[92:95]\n"
    "s_waitcnt lgkmcnt(4)\n"
    "v_mul_f32 v229, v229, v213\n"
    "v_cvt_pk_bf16_f32 v246, v228, v229\n"
    "v_mfma_f32_16x16x32_bf16 v[64:67], v[28:31], a[96:99], v[64:67]\n"
    "s_waitcnt lgkmcnt(2)\n"
    "ds_read_b64_tr_b16 v[48:49], v8 offset:25600\n"
    "v_mul_f32 v230, v198, v198\n"
    "v_mfma_f32_16x16x32_bf16 v[64:67], v[32:35], a[112:115], v[64:67]\n"
    "s_waitcnt lgkmcnt(1)\n"
    "ds_read_b64_tr_b16 v[50:51], v8 offset:27648\n"
    "v_mul_f32 v231, v199, v199\n"
    "v_mfma_f32_16x16x32_bf16 v[64:67], v[36:39], a[128:131], v[64:67]\n"
    "ds_read_b64_tr_b16 v[52:53], v8 offset:29696\n"
    "v_fmaak_f32 v230, v230, v2, 0xc0135761\n"
    "v_mfma_f32_16x16x32_bf16 v[68:71], v[28:31], a[100:103], v[68:71]\n"
    "ds_read_b64_tr_b16 v[54:55], v8 offset:31744\n"
    "v_fmaak_f32 v231, v231, v2, 0xc0135761\n"
    "v_mfma_f32_16x16x32_bf16 v[68:71], v[32:35], a[116:119], v[68:71]\n"
    "ds_read_b64_tr_b16 v[56:57], v8 offset:33792\n"
    "v_mul_f32 v230, v230, v198\n"
    "v_mfma_f32_16x16x32_bf16 v[68:71], v[36:39], a[132:135], v[68:71]\n"
    "s_add_u32 m0, s91, 16384\n"
    "s_add_u32 s87, s90, 1179648\n"
    "ds_read_b64_tr_b16 v[58:59], v8 offset:35840\n"
    "buffer_load_dwordx4 v7, s[44:47], s87 offen lds\n"
    "v_xor_b32 v8, 32, v8\n"
    "v_mfma_f32_16x16x32_bf16 v[72:75], v[28:31], a[104:107], v[72:75]\n"
    "v_exp_f32 v230, v230\n"
    "v_mfma_f32_16x16x32_bf16 v[72:75], v[32:35], a[120:123], v[72:75]\n"
    "v_mul_f32 v231, v231, v199\n"
    "v_add_f32 v230, 1.0, v230\n"
    "v_mfma_f32_16x16x32_bf16 v[72:75], v[36:39], a[136:139], v[72:75]\n"
    "v_exp_f32 v231, v231\n"
    "v_mfma_f32_16x16x32_bf16 v[76:79], v[28:31], a[108:111], v[76:79]\n"
    "v_rcp_f32 v230, v230\n"
    "v_mfma_f32_16x16x32_bf16 v[76:79], v[32:35], a[124:127], v[76:79]\n"
    "v_add_f32 v231, 1.0, v231\n"
    "v_mul_f32 v230, v230, v198\n"
    "v_mfma_f32_16x16x32_bf16 v[76:79], v[36:39], a[140:143], v[76:79]\n"
    "s_waitcnt lgkmcnt(4)\n"
    "v_rcp_f32 v231, v231\n"
    "v_mfma_f32_16x16x32_bf16 v[80:83], v[48:51], a[96:99], v[80:83]\n"
    "s_waitcnt lgkmcnt(2)\n"
    "ds_read_b64_tr_b16 v[16:17], v9 offset:0\n"
    "v_mul_f32 v231, v231, v199\n"
    "v_mfma_f32_16x16x32_bf16 v[80:83], v[52:55], a[112:115], v[80:83]\n"
    "s_waitcnt lgkmcnt(1)\n"
    "ds_read_b64_tr_b16 v[18:19], v9 offset:2048\n"
    "v_mul_f32 v230, v230, v214\n"
    "v_mfma_f32_16x16x32_bf16 v[80:83], v[56:59], a[128:131], v[80:83]\n"
    "ds_read_b64_tr_b16 v[20:21], v9 offset:4096\n"
    "v_mul_f32 v231, v231, v215\n"
    "v_mfma_f32_16x16x32_bf16 v[84:87], v[48:51], a[100:103], v[84:87]\n"
    "ds_read_b64_tr_b16 v[22:23], v9 offset:6144\n"
    "v_cvt_pk_bf16_f32 v247, v230, v231\n"
    "v_mfma_f32_16x16x32_bf16 v[84:87], v[52:55], a[116:119], v[84:87]\n"
    "ds_read_b64_tr_b16 v[24:25], v9 offset:8192\n"
    "buffer_store_dwordx4 v[244:247], v0, s[52:55], s89 offen offset:1024 sc1\n"
    "v_mfma_f32_16x16x32_bf16 v[84:87], v[56:59], a[132:135], v[84:87]\n"
    "ds_read_b64_tr_b16 v[26:27], v9 offset:10240\n"
    "buffer_load_dwordx4 v7, s[48:51], s87 offen offset:1024 lds\n"
    "v_mfma_f32_16x16x32_bf16 v[88:91], v[48:51], a[104:107], v[88:91]\n"
    "v_mul_f32 v224, v200, v200\n"
    "v_mul_f32 v225, v201, v201\n"
    "v_mfma_f32_16x16x32_bf16 v[88:91], v[52:55], a[120:123], v[88:91]\n"
    "v_fmaak_f32 v224, v224, v2, 0xc0135761\n"
    "v_fmaak_f32 v225, v225, v2, 0xc0135761\n"
    "v_mfma_f32_16x16x32_bf16 v[88:91], v[56:59], a[136:139], v[88:91]\n"
    "v_mul_f32 v224, v224, v200\n"
    "v_mul_f32 v225, v225, v201\n"
    "v_mfma_f32_16x16x32_bf16 v[92:95], v[48:51], a[108:111], v[92:95]\n"
    "v_exp_f32 v224, v224\n"
    "v_mfma_f32_16x16x32_bf16 v[92:95], v[52:55], a[124:127], v[92:95]\n"
    "v_exp_f32 v225, v225\n"
    "v_mfma_f32_16x16x32_bf16 v[92:95], v[56:59], a[140:143], v[92:95]\n"
    "s_waitcnt lgkmcnt(4)\n"
    "v_add_f32 v224, 1.0, v224\n"
    "v_add_f32 v225, 1.0, v225\n"
    "v_mfma_f32_16x16x32_bf16 v[64:67], v[16:19], a[144:147], v[64:67]\n"
    "s_waitcnt lgkmcnt(2)\n"
    "ds_read_b64_tr_b16 v[28:29], v9 offset:1024\n"
    "v_mul_f32 v226, v202, v202\n"
    "v_mfma_f32_16x16x32_bf16 v[64:67], v[20:23], a[160:163], v[64:67]\n"
    "s_waitcnt lgkmcnt(1)\n"
    "ds_read_b64_tr_b16 v[30:31], v9 offset:3072\n"
    "v_mul_f32 v227, v203, v203\n"
    "v_mfma_f32_16x16x32_bf16 v[64:67], v[24:27], a[176:179], v[64:67]\n"
    "ds_read_b64_tr_b16 v[32:33], v9 offset:5120\n"
    "v_fmaak_f32 v226, v226, v2, 0xc0135761\n"
    "v_mfma_f32_16x16x32_bf16 v[68:71], v[16:19], a[148:151], v[68:71]\n"
    "ds_read_b64_tr_b16 v[34:35], v9 offset:7168\n"
    "v_fmaak_f32 v227, v227, v2, 0xc0135761\n"
    "v_mfma_f32_16x16x32_bf16 v[68:71], v[20:23], a[164:167], v[68:71]\n"
    "ds_read_b64_tr_b16 v[36:37], v9 offset:9216\n"
    "v_mul_f32 v226, v226, v202\n"
    "v_mfma_f32_16x16x32_bf16 v[68:71], v[24:27], a[180:183], v[68:71]\n"
    "s_add_u32 m0, s91, 24576\n"
    "s_add_u32 s87, s90, 1769472\n"
    "ds_read_b64_tr_b16 v[38:39], v9 offset:11264\n"
    "buffer_load_dwordx4 v7, s[44:47], s87 offen lds\n"
    "v_mfma_f32_16x16x32_bf16 v[72:75], v[16:19], a[152:155], v[72:75]\n"
    "v_rcp_f32 v224, v224\n"
    "v_mfma_f32_16x16x32_bf16 v[72:75], v[20:23], a[168:171], v[72:75]\n"
    "v_rcp_f32 v225, v225\n"
    "v_mfma_f32_16x16x32_bf16 v[72:75], v[24:27], a[184:187], v[72:75]\n"
    "v_exp_f32 v226, v226\n"
    "v_mfma_f32_16x16x32_bf16 v[76:79], v[16:19], a[156:159], v[76:79]\n"
    "v_mul_f32 v224, v224, v200\n"
    "v_mul_f32 v225, v225, v201\n"
    "v_mfma_f32_16x16x32_bf16 v[76:79], v[20:23], a[172:175], v[76:79]\n"
    "v_mul_f32 v224, v224, v216\n"
    "v_mul_f32 v225, v225, v217\n"
    "v_mfma_f32_16x16x32_bf16 v[76:79], v[24:27], a[188:191], v[76:79]\n"
    "s_waitcnt lgkmcnt(4)\n"
    "v_cvt_pk_bf16_f32 v250, v224, v225\n"
    "v_mul_f32 v227, v227, v203\n"
    "v_mfma_f32_16x16x32_bf16 v[80:83], v[28:31], a[144:147], v[80:83]\n"
    "s_waitcnt lgkmcnt(2)\n"
    "ds_read_b64_tr_b16 v[48:49], v9 offset:12288\n"
    "v_add_f32 v226, 1.0, v226\n"
    "v_mfma_f32_16x16x32_bf16 v[80:83], v[32:35], a[160:163], v[80:83]\n"
    "s_waitcnt lgkmcnt(1)\n"
    "ds_read_b64_tr_b16 v[50:51], v9 offset:14336\n"
    "v_mul_f32 v228, v204, v204\n"
    "v_mfma_f32_16x16x32_bf16 v[80:83], v[36:39], a[176:179], v[80:83]\n"
    "ds_read_b64_tr_b16 v[52:53], v9 offset:16384\n"
    "v_mul_f32 v229, v205, v205\n"
    "v_mfma_f32_16x16x32_bf16 v[84:87], v[28:31], a[148:151], v[84:87]\n"
    "ds_read_b64_tr_b16 v[54:55], v9 offset:18432\n"
    "v_fmaak_f32 v228, v228, v2, 0xc0135761\n"
    "v_mfma_f32_16x16x32_bf16 v[84:87], v[32:35], a[164:167], v[84:87]\n"
    "ds_read_b64_tr_b16 v[56:57], v9 offset:20480\n"
    "v_fmaak_f32 v229, v229, v2, 0xc0135761\n"
    "v_mfma_f32_16x16x32_bf16 v[84:87], v[36:39], a[180:183], v[84:87]\n"
    "ds_read_b64_tr_b16 v[58:59], v9 offset:22528\n"
    "buffer_load_dwordx4 v7, s[48:51], s87 offen offset:1024 lds\n"
    "v_mfma_f32_16x16x32_bf16 v[88:91], v[28:31], a[152:155], v[88:91]\n"
    "v_exp_f32 v227, v227\n"
    "v_mfma_f32_16x16x32_bf16 v[88:91], v[32:35], a[168:171], v[88:91]\n"
    "v_rcp_f32 v226, v226\n"
    "v_mfma_f32_16x16x32_bf16 v[88:91], v[36:39], a[184:187], v[88:91]\n"
    "v_add_f32 v227, 1.0, v227\n"
    "v_mul_f32 v226, v226, v202\n"
    "v_mfma_f32_16x16x32_bf16 v[92:95], v[28:31], a[156:159], v[92:95]\n"
    "v_rcp_f32 v227, v227\n"
    "v_mfma_f32_16x16x32_bf16 v[92:95], v[32:35], a[172:175], v[92:95]\n"
    "v_mul_f32 v227, v227, v203\n"
    "v_mul_f32 v226, v226, v218\n"
    "v_mfma_f32_16x16x32_bf16 v[92:95], v[36:39], a[188:191], v[92:95]\n"
    "s_waitcnt lgkmcnt(4)\n"
    "v_mul_f32 v227, v227, v219\n"
    "v_cvt_pk_bf16_f32 v251, v226, v227\n"
    "v_mfma_f32_16x16x32_bf16 v[64:67], v[48:51], a[192:195], v[64:67]\n"
    "s_waitcnt lgkmcnt(2)\n"
    "ds_read_b64_tr_b16 v[16:17], v9 offset:13312\n"
    "buffer_store_dwordx4 v[248:251], v0, s[52:55], s89 offen offset:2048 sc1\n"
    "v_mfma_f32_16x16x32_bf16 v[64:67], v[52:55], a[208:211], v[64:67]\n"
    "s_waitcnt lgkmcnt(1)\n"
    "ds_read_b64_tr_b16 v[18:19], v9 offset:15360\n"
    "v_mul_f32 v228, v228, v204\n"
    "v_mfma_f32_16x16x32_bf16 v[64:67], v[56:59], a[224:227], v[64:67]\n"
    "ds_read_b64_tr_b16 v[20:21], v9 offset:17408\n"
    "v_mul_f32 v229, v229, v205\n"
    "v_mfma_f32_16x16x32_bf16 v[68:71], v[48:51], a[196:199], v[68:71]\n"
    "ds_read_b64_tr_b16 v[22:23], v9 offset:19456\n"
    "v_mul_f32 v230, v206, v206\n"
    "v_mfma_f32_16x16x32_bf16 v[68:71], v[52:55], a[212:215], v[68:71]\n"
    "ds_read_b64_tr_b16 v[24:25], v9 offset:21504\n"
    "v_mul_f32 v231, v207, v207\n"
    "v_mfma_f32_16x16x32_bf16 v[68:71], v[56:59], a[228:231], v[68:71]\n"
    "s_add_u32 m0, s91, 32768\n"
    "s_add_u32 s87, s90, 2359296\n"
    "ds_read_b64_tr_b16 v[26:27], v9 offset:23552\n"
    "buffer_load_dwordx4 v7, s[44:47], s87 offen lds\n"
    "v_mfma_f32_16x16x32_bf16 v[72:75], v[48:51], a[200:203], v[72:75]\n"
    "v_exp_f32 v228, v228\n"
    "v_mfma_f32_16x16x32_bf16 v[72:75], v[52:55], a[216:219], v[72:75]\n"
    "v_exp_f32 v229, v229\n"
    "v_mfma_f32_16x16x32_bf16 v[72:75], v[56:59], a[232:235], v[72:75]\n"
    "v_add_f32 v228, 1.0, v228\n"
    "v_add_f32 v229, 1.0, v229\n"
    "v_mfma_f32_16x16x32_bf16 v[76:79], v[48:51], a[204:207], v[76:79]\n"
    "v_rcp_f32 v228, v228\n"
    "v_mfma_f32_16x16x32_bf16 v[76:79], v[52:55], a[220:223], v[76:79]\n"
    "v_rcp_f32 v229, v229\n"
    "v_mfma_f32_16x16x32_bf16 v[76:79], v[56:59], a[236:239], v[76:79]\n"
    "s_waitcnt lgkmcnt(4)\n"
    "v_mul_f32 v228, v228, v204\n"
    "v_mul_f32 v229, v229, v205\n"
    "v_mfma_f32_16x16x32_bf16 v[80:83], v[16:19], a[192:195], v[80:83]\n"
    "s_waitcnt lgkmcnt(2)\n"
    "ds_read_b64_tr_b16 v[28:29], v9 offset:24576\n"
    "v_mul_f32 v228, v228, v220\n"
    "v_mfma_f32_16x16x32_bf16 v[80:83], v[20:23], a[208:211], v[80:83]\n"
    "s_waitcnt lgkmcnt(1)\n"
    "ds_read_b64_tr_b16 v[30:31], v9 offset:26624\n"
    "v_mul_f32 v229, v229, v221\n"
    "v_mfma_f32_16x16x32_bf16 v[80:83], v[24:27], a[224:227], v[80:83]\n"
    "ds_read_b64_tr_b16 v[32:33], v9 offset:28672\n"
    "v_cvt_pk_bf16_f32 v254, v228, v229\n"
    "v_mfma_f32_16x16x32_bf16 v[84:87], v[16:19], a[196:199], v[84:87]\n"
    "ds_read_b64_tr_b16 v[34:35], v9 offset:30720\n"
    "v_fmaak_f32 v230, v230, v2, 0xc0135761\n"
    "v_mfma_f32_16x16x32_bf16 v[84:87], v[20:23], a[212:215], v[84:87]\n"
    "ds_read_b64_tr_b16 v[36:37], v9 offset:32768\n"
    "v_fmaak_f32 v231, v231, v2, 0xc0135761\n"
    "v_mfma_f32_16x16x32_bf16 v[84:87], v[24:27], a[228:231], v[84:87]\n"
    "ds_read_b64_tr_b16 v[38:39], v9 offset:34816\n"
    "buffer_load_dwordx4 v7, s[48:51], s87 offen offset:1024 lds\n"
    "v_mfma_f32_16x16x32_bf16 v[88:91], v[16:19], a[200:203], v[88:91]\n"
    "v_mul_f32 v230, v230, v206\n"
    "v_mul_f32 v231, v231, v207\n"
    "v_mfma_f32_16x16x32_bf16 v[88:91], v[20:23], a[216:219], v[88:91]\n"
    "v_exp_f32 v230, v230\n"
    "v_mfma_f32_16x16x32_bf16 v[88:91], v[24:27], a[232:235], v[88:91]\n"
    "v_exp_f32 v231, v231\n"
    "v_mfma_f32_16x16x32_bf16 v[92:95], v[16:19], a[204:207], v[92:95]\n"
    "v_add_f32 v230, 1.0, v230\n"
    "v_add_f32 v231, 1.0, v231\n"
    "v_mfma_f32_16x16x32_bf16 v[92:95], v[20:23], a[220:223], v[92:95]\n"
    "v_rcp_f32 v230, v230\n"
    "v_mfma_f32_16x16x32_bf16 v[92:95], v[24:27], a[236:239], v[92:95]\n"
    "s_waitcnt lgkmcnt(4)\n"
    "v_rcp_f32 v231, v231\n"
    "v_mfma_f32_16x16x32_bf16 v[64:67], v[28:31], a[240:243], v[64:67]\n"
    "s_waitcnt lgkmcnt(2)\n"
    "ds_read_b64_tr_b16 v[48:49], v9 offset:25600\n"
    "v_mul_f32 v230, v230, v206\n"
    "v_mfma_f32_16x16x32_bf16 v[64:67], v[32:35], v[96:99], v[64:67]\n"
    "s_waitcnt lgkmcnt(1)\n"
    "ds_read_b64_tr_b16 v[50:51], v9 offset:27648\n"
    "v_mul_f32 v231, v231, v207\n"
    "v_mfma_f32_16x16x32_bf16 v[64:67], v[36:39], v[112:115], v[64:67]\n"
    "ds_read_b64_tr_b16 v[52:53], v9 offset:29696\n"
    "v_mul_f32 v230, v230, v222\n"
    "v_mfma_f32_16x16x32_bf16 v[68:71], v[28:31], a[244:247], v[68:71]\n"
    "ds_read_b64_tr_b16 v[54:55], v9 offset:31744\n"
    "v_mul_f32 v231, v231, v223\n"
    "v_mfma_f32_16x16x32_bf16 v[68:71], v[32:35], v[100:103], v[68:71]\n"
    "ds_read_b64_tr_b16 v[56:57], v9 offset:33792\n"
    "v_cvt_pk_bf16_f32 v255, v230, v231\n"
    "v_mfma_f32_16x16x32_bf16 v[68:71], v[36:39], v[116:119], v[68:71]\n"
    "s_add_u32 m0, s91, 40960\n"
    "s_add_u32 s87, s90, 2949120\n"
    "ds_read_b64_tr_b16 v[58:59], v9 offset:35840\n"
    "buffer_load_dwordx4 v7, s[44:47], s87 offen lds\n"
    "v_xor_b32 v9, 32, v9\n"
    "v_mfma_f32_16x16x32_bf16 v[72:75], v[28:31], a[248:251], v[72:75]\n"
    "buffer_store_dwordx4 v[252:255], v0, s[52:55], s89 offen offset:3072 sc1\n"
    "v_mfma_f32_16x16x32_bf16 v[72:75], v[32:35], v[104:107], v[72:75]\n"
    "v_mfma_f32_16x16x32_bf16 v[72:75], v[36:39], v[120:123], v[72:75]\n"
    "v_mfma_f32_16x16x32_bf16 v[76:79], v[28:31], a[252:255], v[76:79]\n"
    "v_mfma_f32_16x16x32_bf16 v[76:79], v[32:35], v[108:111], v[76:79]\n"
    "v_mfma_f32_16x16x32_bf16 v[76:79], v[36:39], v[124:127], v[76:79]\n"
    "s_waitcnt lgkmcnt(4)\n"
    "v_mfma_f32_16x16x32_bf16 v[80:83], v[48:51], a[240:243], v[80:83]\n"
    "s_waitcnt lgkmcnt(2)\n"
    "ds_read_b64_tr_b16 v[16:17], v8 offset:0\n"
    "v_mfma_f32_16x16x32_bf16 v[80:83], v[52:55], v[96:99], v[80:83]\n"
    "s_waitcnt lgkmcnt(1)\n"
    "ds_read_b64_tr_b16 v[18:19], v8 offset:2048\n"
    "v_mfma_f32_16x16x32_bf16 v[80:83], v[56:59], v[112:115], v[80:83]\n"
    "ds_read_b64_tr_b16 v[20:21], v8 offset:4096\n"
    "v_mfma_f32_16x16x32_bf16 v[84:87], v[48:51], a[244:247], v[84:87]\n"
    "ds_read_b64_tr_b16 v[22:23], v8 offset:6144\n"
    "v_mfma_f32_16x16x32_bf16 v[84:87], v[52:55], v[100:103], v[84:87]\n"
    "ds_read_b64_tr_b16 v[24:25], v8 offset:8192\n"
    "v_mfma_f32_16x16x32_bf16 v[84:87], v[56:59], v[116:119], v[84:87]\n"
    "ds_read_b64_tr_b16 v[26:27], v8 offset:10240\n"
    "buffer_load_dwordx4 v7, s[48:51], s87 offen offset:1024 lds\n"
    "v_mfma_f32_16x16x32_bf16 v[88:91], v[48:51], a[248:251], v[88:91]\n"
    "v_mfma_f32_16x16x32_bf16 v[88:91], v[52:55], v[104:107], v[88:91]\n"
    "v_mfma_f32_16x16x32_bf16 v[88:91], v[56:59], v[120:123], v[88:91]\n"
    "v_mfma_f32_16x16x32_bf16 v[92:95], v[48:51], a[252:255], v[92:95]\n"
    "v_mfma_f32_16x16x32_bf16 v[92:95], v[52:55], v[108:111], v[92:95]\n"
    "s_add_u32 s88, s88, 1\n"
    "v_mfma_f32_16x16x32_bf16 v[92:95], v[56:59], v[124:127], v[92:95]\n"
    "s_add_u32 s89, s89, 4096\n"
    "s_branch L_P1%=\n"
    "L_P1X%=:\n"
    "s_waitcnt lgkmcnt(4)\n"
    "v_mfma_f32_16x16x32_bf16 v[64:67], v[16:19], a[0:3], 0\n"
    "s_waitcnt vmcnt(0) lgkmcnt(2)\n"
    "ds_read_b64_tr_b16 v[28:29], v8 offset:1024\n"
    "v_xor_b32 v60, 0, v3\n"
    "v_xor_b32 v61, 64, v3\n"
    "v_mfma_f32_16x16x32_bf16 v[64:67], v[20:23], a[16:19], v[64:67]\n"
    "s_waitcnt lgkmcnt(1)\n"
    "s_lshl_b32 s86, s80, 12\n"
    "s_add_u32 m0, s86, 98304\n"
    "s_nop 0\n"
    "ds_read_b64_tr_b16 v[30:31], v8 offset:3072\n"
    "buffer_load_dwordx4 v40, s[56:59], s92 offen lds\n"
    "v_xor_b32 v62, 128, v3\n"
    "v_mfma_f32_16x16x32_bf16 v[64:67], v[24:27], a[32:35], v[64:67]\n"
    "ds_read_b64_tr_b16 v[32:33], v8 offset:5120\n"
    "v_xor_b32 v63, 192, v3\n"
    "v_and_b32 v12, 48, v0\n"
    "v_mfma_f32_16x16x32_bf16 v[68:71], v[16:19], a[4:7], 0\n"
    "ds_read_b64_tr_b16 v[34:35], v8 offset:7168\n"
    "v_cmp_eq_u32 vcc, 0, v12\n"
    "v_mov_b32 v13, 0x3f803f80\n"
    "v_mfma_f32_16x16x32_bf16 v[68:71], v[20:23], a[20:23], v[68:71]\n"
    "ds_read_b64_tr_b16 v[36:37], v8 offset:9216\n"
    "v_mul_f32 v224, v192, v192\n"
    "v_cndmask_b32 v12, 0, v13, vcc\n"
    "v_mfma_f32_16x16x32_bf16 v[68:71], v[24:27], a[36:39], v[68:71]\n"
    "ds_read_b64_tr_b16 v[38:39], v8 offset:11264\n"
    "buffer_load_dwordx4 v41, s[56:59], s92 offen offset:1024 lds\n"
    "v_mfma_f32_16x16x32_bf16 v[72:75], v[16:19], a[8:11], 0\n"
    "v_mul_f32 v225, v193, v193\n"
    "v_fmaak_f32 v224, v224, v2, 0xc0135761\n"
    "v_mfma_f32_16x16x32_bf16 v[72:75], v[20:23], a[24:27], v[72:75]\n"
    "v_fmaak_f32 v225, v225, v2, 0xc0135761\n"
    "v_mul_f32 v224, v224, v192\n"
    "v_mfma_f32_16x16x32_bf16 v[72:75], v[24:27], a[40:43], v[72:75]\n"
    "v_exp_f32 v224, v224\n"
    "v_mfma_f32_16x16x32_bf16 v[76:79], v[16:19], a[12:15], 0\n"
    "buffer_load_dwordx4 v42, s[56:59], s92 offen offset:2048 lds\n"
    "v_mul_f32 v225, v225, v193\n"
    "v_mfma_f32_16x16x32_bf16 v[76:79], v[20:23], a[28:31], v[76:79]\n"
    "v_exp_f32 v225, v225\n"
    "v_mfma_f32_16x16x32_bf16 v[76:79], v[24:27], a[44:47], v[76:79]\n"
    "s_waitcnt lgkmcnt(4)\n"
    "v_add_f32 v224, 1.0, v224\n"
    "v_add_f32 v225, 1.0, v225\n"
    "v_mfma_f32_16x16x32_bf16 v[80:83], v[28:31], a[0:3], 0\n"
    "s_waitcnt lgkmcnt(2)\n"
    "ds_read_b64_tr_b16 v[48:49], v8 offset:12288\n"
    "v_mul_f32 v226, v194, v194\n"
    "v_mfma_f32_16x16x32_bf16 v[80:83], v[32:35], a[16:19], v[80:83]\n"
    "s_waitcnt lgkmcnt(1)\n"
    "ds_read_b64_tr_b16 v[50:51], v8 offset:14336\n"
    "buffer_load_dwordx4 v43, s[56:59], s92 offen offset:3072 lds\n"
    "v_mfma_f32_16x16x32_bf16 v[80:83], v[36:39], a[32:35], v[80:83]\n"
    "ds_read_b64_tr_b16 v[52:53], v8 offset:16384\n"
    "v_mul_f32 v227, v195, v195\n"
    "v_mfma_f32_16x16x32_bf16 v[84:87], v[28:31], a[4:7], 0\n"
    "ds_read_b64_tr_b16 v[54:55], v8 offset:18432\n"
    "v_fmaak_f32 v226, v226, v2, 0xc0135761\n"
    "v_mfma_f32_16x16x32_bf16 v[84:87], v[32:35], a[20:23], v[84:87]\n"
    "ds_read_b64_tr_b16 v[56:57], v8 offset:20480\n"
    "v_fmaak_f32 v227, v227, v2, 0xc0135761\n"
    "v_mfma_f32_16x16x32_bf16 v[84:87], v[36:39], a[36:39], v[84:87]\n"
    "s_add_u32 m0, s86, 114688\n"
    "s_nop 0\n"
    "ds_read_b64_tr_b16 v[58:59], v8 offset:22528\n"
    "buffer_load_dwordx4 v44, s[60:63], s92 offen lds\n"
    "v_mfma_f32_16x16x32_bf16 v[88:91], v[28:31], a[8:11], 0\n"
    "v_rcp_f32 v224, v224\n"
    "v_mfma_f32_16x16x32_bf16 v[88:91], v[32:35], a[24:27], v[88:91]\n"
    "v_rcp_f32 v225, v225\n"
    "v_mfma_f32_16x16x32_bf16 v[88:91], v[36:39], a[40:43], v[88:91]\n"
    "v_mul_f32 v224, v224, v192\n"
    "v_mul_f32 v225, v225, v193\n"
    "v_mfma_f32_16x16x32_bf16 v[92:95], v[28:31], a[12:15], 0\n"
    "buffer_load_dwordx4 v45, s[60:63], s92 offen offset:1024 lds\n"
    "v_mul_f32 v224, v224, v208\n"
    "v_mfma_f32_16x16x32_bf16 v[92:95], v[32:35], a[28:31], v[92:95]\n"
    "v_mul_f32 v225, v225, v209\n"
    "v_cvt_pk_bf16_f32 v242, v224, v225\n"
    "v_mfma_f32_16x16x32_bf16 v[92:95], v[36:39], a[44:47], v[92:95]\n"
    "s_waitcnt lgkmcnt(4)\n"
    "v_mul_f32 v226, v226, v194\n"
    "v_mul_f32 v227, v227, v195\n"
    "v_mfma_f32_16x16x32_bf16 v[64:67], v[48:51], a[48:51], v[64:67]\n"
    "s_waitcnt lgkmcnt(2)\n"
    "ds_read_b64_tr_b16 v[16:17], v8 offset:13312\n"
    "v_mul_f32 v228, v196, v196\n"
    "v_mfma_f32_16x16x32_bf16 v[64:67], v[52:55], a[64:67], v[64:67]\n"
    "s_waitcnt lgkmcnt(1)\n"
    "ds_read_b64_tr_b16 v[18:19], v8 offset:15360\n"
    "buffer_load_dwordx4 v44, s[60:63], s92 offen offset:2048 lds\n"
    "v_mfma_f32_16x16x32_bf16 v[64:67], v[56:59], a[80:83], v[64:67]\n"
    "ds_read_b64_tr_b16 v[20:21], v8 offset:17408\n"
    "v_mul_f32 v229, v197, v197\n"
    "v_mfma_f32_16x16x32_bf16 v[68:71], v[48:51], a[52:55], v[68:71]\n"
    "ds_read_b64_tr_b16 v[22:23], v8 offset:19456\n"
    "v_fmaak_f32 v228, v228, v2, 0xc0135761\n"
    "v_mfma_f32_16x16x32_bf16 v[68:71], v[52:55], a[68:71], v[68:71]\n"
    "ds_read_b64_tr_b16 v[24:25], v8 offset:21504\n"
    "v_fmaak_f32 v229, v229, v2, 0xc0135761\n"
    "v_mfma_f32_16x16x32_bf16 v[68:71], v[56:59], a[84:87], v[68:71]\n"
    "ds_read_b64_tr_b16 v[26:27], v8 offset:23552\n"
    "buffer_load_dwordx4 v45, s[60:63], s92 offen offset:3072 lds\n"
    "s_add_u32 s92, s92, 16384\n"
    "v_mfma_f32_16x16x32_bf16 v[72:75], v[48:51], a[56:59], v[72:75]\n"
    "v_exp_f32 v226, v226\n"
    "v_mfma_f32_16x16x32_bf16 v[72:75], v[52:55], a[72:75], v[72:75]\n"
    "v_exp_f32 v227, v227\n"
    "v_mfma_f32_16x16x32_bf16 v[72:75], v[56:59], a[88:91], v[72:75]\n"
    "v_add_f32 v226, 1.0, v226\n"
    "v_add_f32 v227, 1.0, v227\n"
    "v_mfma_f32_16x16x32_bf16 v[76:79], v[48:51], a[60:63], v[76:79]\n"
    "s_add_u32 m0, s86, 131072\n"
    "s_nop 0\n"
    "buffer_load_dwordx4 v40, s[56:59], s92 offen lds\n"
    "v_mul_f32 v228, v228, v196\n"
    "v_mfma_f32_16x16x32_bf16 v[76:79], v[52:55], a[76:79], v[76:79]\n"
    "v_rcp_f32 v226, v226\n"
    "v_mfma_f32_16x16x32_bf16 v[76:79], v[56:59], a[92:95], v[76:79]\n"
    "s_waitcnt lgkmcnt(4)\n"
    "v_rcp_f32 v227, v227\n"
    "v_mfma_f32_16x16x32_bf16 v[80:83], v[16:19], a[48:51], v[80:83]\n"
    "s_waitcnt lgkmcnt(2)\n"
    "ds_read_b64_tr_b16 v[28:29], v8 offset:24576\n"
    "v_mul_f32 v226, v226, v194\n"
    "v_mfma_f32_16x16x32_bf16 v[80:83], v[20:23], a[64:67], v[80:83]\n"
    "s_waitcnt lgkmcnt(1)\n"
    "ds_read_b64_tr_b16 v[30:31], v8 offset:26624\n"
    "buffer_load_dwordx4 v41, s[56:59], s92 offen offset:1024 lds\n"
    "v_mfma_f32_16x16x32_bf16 v[80:83], v[24:27], a[80:83], v[80:83]\n"
    "ds_read_b64_tr_b16 v[32:33], v8 offset:28672\n"
    "v_mul_f32 v227, v227, v195\n"
    "v_mfma_f32_16x16x32_bf16 v[84:87], v[16:19], a[52:55], v[84:87]\n"
    "ds_read_b64_tr_b16 v[34:35], v8 offset:30720\n"
    "v_mul_f32 v226, v226, v210\n"
    "v_mfma_f32_16x16x32_bf16 v[84:87], v[20:23], a[68:71], v[84:87]\n"
    "ds_read_b64_tr_b16 v[36:37], v8 offset:32768\n"
    "v_mul_f32 v227, v227, v211\n"
    "v_mfma_f32_16x16x32_bf16 v[84:87], v[24:27], a[84:87], v[84:87]\n"
    "ds_read_b64_tr_b16 v[38:39], v8 offset:34816\n"
    "buffer_load_dwordx4 v42, s[56:59], s92 offen offset:2048 lds\n"
    "v_mfma_f32_16x16x32_bf16 v[88:91], v[16:19], a[56:59], v[88:91]\n"
    "v_exp_f32 v228, v228\n"
    "v_mfma_f32_16x16x32_bf16 v[88:91], v[20:23], a[72:75], v[88:91]\n"
    "v_cvt_pk_bf16_f32 v243, v226, v227\n"
    "v_mul_f32 v229, v229, v197\n"
    "v_mfma_f32_16x16x32_bf16 v[88:91], v[24:27], a[88:91], v[88:91]\n"
    "v_exp_f32 v229, v229\n"
    "v_mfma_f32_16x16x32_bf16 v[92:95], v[16:19], a[60:63], v[92:95]\n"
    "buffer_load_dwordx4 v43, s[56:59], s92 offen offset:3072 lds\n"
    "buffer_store_dwordx4 v[240:243], v0, s[52:55], s89 offen offset:0 sc1\n"
    "v_mfma_f32_16x16x32_bf16 v[92:95], v[20:23], a[76:79], v[92:95]\n"
    "v_add_f32 v228, 1.0, v228\n"
    "v_add_f32 v229, 1.0, v229\n"
    "v_mfma_f32_16x16x32_bf16 v[92:95], v[24:27], a[92:95], v[92:95]\n"
    "s_waitcnt lgkmcnt(4)\n"
    "v_rcp_f32 v228, v228\n"
    "v_mfma_f32_16x16x32_bf16 v[64:67], v[28:31], a[96:99], v[64:67]\n"
    "s_waitcnt lgkmcnt(2)\n"
    "ds_read_b64_tr_b16 v[48:49], v8 offset:25600\n"
    "v_mul_f32 v228, v228, v196\n"
    "v_mfma_f32_16x16x32_bf16 v[64:67], v[32:35], a[112:115], v[64:67]\n"
    "s_waitcnt lgkmcnt(1)\n"
    "s_add_u32 m0, s86, 147456\n"
    "s_nop 0\n"
    "ds_read_b64_tr_b16 v[50:51], v8 offset:27648\n"
    "buffer_load_dwordx4 v44, s[60:63], s92 offen lds\n"
    "v_mfma_f32_16x16x32_bf16 v[64:67], v[36:39], a[128:131], v[64:67]\n"
    "ds_read_b64_tr_b16 v[52:53], v8 offset:29696\n"
    "v_mul_f32 v228, v228, v212\n"
    "v_accvgpr_write_b32 a0, v128\n"
    "v_mfma_f32_16x16x32_bf16 v[68:71], v[28:31], a[100:103], v[68:71]\n"
    "ds_read_b64_tr_b16 v[54:55], v8 offset:31744\n"
    "v_mul_f32 v230, v198, v198\n"
    "v_accvgpr_write_b32 a1, v129\n"
    "v_mfma_f32_16x16x32_bf16 v[68:71], v[32:35], a[116:119], v[68:71]\n"
    "ds_read_b64_tr_b16 v[56:57], v8 offset:33792\n"
    "v_mul_f32 v231, v199, v199\n"
    "v_accvgpr_write_b32 a2, v130\n"
    "v_mfma_f32_16x16x32_bf16 v[68:71], v[36:39], a[132:135], v[68:71]\n"
    "ds_read_b64_tr_b16 v[58:59], v8 offset:35840\n"
    "buffer_load_dwordx4 v45, s[60:63], s92 offen offset:1024 lds\n"
    "v_xor_b32 v8, 32, v8\n"
    "v_mfma_f32_16x16x32_bf16 v[72:75], v[28:31], a[104:107], v[72:75]\n"
    "v_rcp_f32 v229, v229\n"
    "v_accvgpr_write_b32 a3, v131\n"
    "v_mfma_f32_16x16x32_bf16 v[72:75], v[32:35], a[120:123], v[72:75]\n"
    "v_mul_f32 v229, v229, v197\n"
    "v_mul_f32 v229, v229, v213\n"
    "v_accvgpr_write_b32 a4, v132\n"
    "v_mfma_f32_16x16x32_bf16 v[72:75], v[36:39], a[136:139], v[72:75]\n"
    "v_cvt_pk_bf16_f32 v246, v228, v229\n"
    "v_fmaak_f32 v230, v230, v2, 0xc0135761\n"
    "v_accvgpr_write_b32 a5, v133\n"
    "v_mfma_f32_16x16x32_bf16 v[76:79], v[28:31], a[108:111], v[76:79]\n"
    "buffer_load_dwordx4 v44, s[60:63], s92 offen offset:2048 lds\n"
    "v_fmaak_f32 v231, v231, v2, 0xc0135761\n"
    "v_accvgpr_write_b32 a6, v134\n"
    "v_mfma_f32_16x16x32_bf16 v[76:79], v[32:35], a[124:127], v[76:79]\n"
    "v_mul_f32 v230, v230, v198\n"
    "v_mul_f32 v231, v231, v199\n"
    "v_accvgpr_write_b32 a7, v135\n"
    "v_mfma_f32_16x16x32_bf16 v[76:79], v[36:39], a[140:143], v[76:79]\n"
    "s_waitcnt lgkmcnt(4)\n"
    "v_exp_f32 v230, v230\n"
    "v_accvgpr_write_b32 a8, v136\n"
    "v_mfma_f32_16x16x32_bf16 v[80:83], v[48:51], a[96:99], v[80:83]\n"
    "s_waitcnt lgkmcnt(2)\n"
    "ds_read_b64_tr_b16 v[16:17], v9 offset:0\n"
    "v_add_f32 v230, 1.0, v230\n"
    "v_accvgpr_write_b32 a9, v137\n"
    "v_mfma_f32_16x16x32_bf16 v[80:83], v[52:55], a[112:115], v[80:83]\n"
    "s_waitcnt lgkmcnt(1)\n"
    "ds_read_b64_tr_b16 v[18:19], v9 offset:2048\n"
    "buffer_load_dwordx4 v45, s[60:63], s92 offen offset:3072 lds\n"
    "s_add_u32 s92, s92, 16384\n"
    "v_accvgpr_write_b32 a10, v138\n"
    "v_mfma_f32_16x16x32_bf16 v[80:83], v[56:59], a[128:131], v[80:83]\n"
    "ds_read_b64_tr_b16 v[20:21], v9 offset:4096\n"
    "v_mul_f32 v224, v200, v200\n"
    "v_accvgpr_write_b32 a11, v139\n"
    "v_mfma_f32_16x16x32_bf16 v[84:87], v[48:51], a[100:103], v[84:87]\n"
    "ds_read_b64_tr_b16 v[22:23], v9 offset:6144\n"
    "v_mul_f32 v225, v201, v201\n"
    "v_accvgpr_write_b32 a12, v140\n"
    "v_mfma_f32_16x16x32_bf16 v[84:87], v[52:55], a[116:119], v[84:87]\n"
    "ds_read_b64_tr_b16 v[24:25], v9 offset:8192\n"
    "v_fmaak_f32 v224, v224, v2, 0xc0135761\n"
    "v_accvgpr_write_b32 a13, v141\n"
    "v_mfma_f32_16x16x32_bf16 v[84:87], v[56:59], a[132:135], v[84:87]\n"
    "ds_read_b64_tr_b16 v[26:27], v9 offset:10240\n"
    "v_fmaak_f32 v225, v225, v2, 0xc0135761\n"
    "v_accvgpr_write_b32 a14, v142\n"
    "v_mfma_f32_16x16x32_bf16 v[88:91], v[48:51], a[104:107], v[88:91]\n"
    "v_exp_f32 v231, v231\n"
    "v_accvgpr_write_b32 a15, v143\n"
    "v_mfma_f32_16x16x32_bf16 v[88:91], v[52:55], a[120:123], v[88:91]\n"
    "v_rcp_f32 v230, v230\n"
    "v_accvgpr_write_b32 a16, v144\n"
    "v_mfma_f32_16x16x32_bf16 v[88:91], v[56:59], a[136:139], v[88:91]\n"
    "v_add_f32 v231, 1.0, v231\n"
    "v_mul_f32 v230, v230, v198\n"
    "v_accvgpr_write_b32 a17, v145\n"
    "v_mfma_f32_16x16x32_bf16 v[92:95], v[48:51], a[108:111], v[92:95]\n"
    "v_rcp_f32 v231, v231\n"
    "v_accvgpr_write_b32 a18, v146\n"
    "v_mfma_f32_16x16x32_bf16 v[92:95], v[52:55], a[124:127], v[92:95]\n"
    "s_getpc_b64 s[32:33]\n"
    "L_PD%=:\n"
    "v_mul_f32 v231, v231, v199\n"
    "v_mul_f32 v230, v230, v214\n"
    "v_accvgpr_write_b32 a19, v147\n"
    "v_mfma_f32_16x16x32_bf16 v[92:95], v[56:59], a[140:143], v[92:95]\n"
    "s_waitcnt lgkmcnt(4)\n"
    "s_lshr_b32 s83, %1, 3\n"
    "v_mul_f32 v231, v231, v215\n"
    "v_cvt_pk_bf16_f32 v247, v230, v231\n"
    "v_accvgpr_write_b32 a20, v148\n"
    "v_mfma_f32_16x16x32_bf16 v[64:67], v[16:19], a[144:147], v[64:67]\n"
    "s_waitcnt lgkmcnt(2)\n"
    "s_lshl_b32 s83, s83, 2\n"
    "ds_read_b64_tr_b16 v[28:29], v9 offset:1024\n"
    "buffer_store_dwordx4 v[244:247], v0, s[52:55], s89 offen offset:1024 sc1\n"
    "v_accvgpr_write_b32 a21, v149\n"
    "v_mfma_f32_16x16x32_bf16 v[64:67], v[20:23], a[160:163], v[64:67]\n"
    "s_waitcnt lgkmcnt(1)\n"
    "s_add_u32 s83, s83, s80\n"
    "ds_read_b64_tr_b16 v[30:31], v9 offset:3072\n"
    "v_mul_f32 v224, v224, v200\n"
    "v_accvgpr_write_b32 a22, v150\n"
    "v_mfma_f32_16x16x32_bf16 v[64:67], v[24:27], a[176:179], v[64:67]\n"
    "s_lshl_b32 s83, s83, 10\n"
    "ds_read_b64_tr_b16 v[32:33], v9 offset:5120\n"
    "v_mul_f32 v225, v225, v201\n"
    "v_accvgpr_write_b32 a23, v151\n"
    "v_mfma_f32_16x16x32_bf16 v[68:71], v[16:19], a[148:151], v[68:71]\n"
    "s_cmp_lt_u32 s83, L_END%=-L_LAST%=-1023\n"
    "ds_read_b64_tr_b16 v[34:35], v9 offset:7168\n"
    "v_mul_f32 v226, v202, v202\n"
    "v_accvgpr_write_b32 a24, v152\n"
    "v_mfma_f32_16x16x32_bf16 v[68:71], v[20:23], a[164:167], v[68:71]\n"
    "s_cselect_b32 s34, 1024, 0\n"
    "ds_read_b64_tr_b16 v[36:37], v9 offset:9216\n"
    "v_mul_f32 v227, v203, v203\n"
    "v_accvgpr_write_b32 a25, v153\n"
    "v_mfma_f32_16x16x32_bf16 v[68:71], v[24:27], a[180:183], v[68:71]\n"
    "s_add_u32 s83, s83, L_LAST%=-L_PD%=\n"
    "ds_read_b64_tr_b16 v[38:39], v9 offset:11264\n"
    "v_fmaak_f32 v226, v226, v2, 0xc0135761\n"
    "v_accvgpr_write_b32 a26, v154\n"
    "v_mfma_f32_16x16x32_bf16 v[72:75], v[16:19], a[152:155], v[72:75]\n"
    "s_add_u32 s32, s32, s83\n"
    "v_exp_f32 v224, v224\n"
    "v_accvgpr_write_b32 a27, v155\n"
    "v_mfma_f32_16x16x32_bf16 v[72:75], v[20:23], a[168:171], v[72:75]\n"
    "s_addc_u32 s33, s33, 0\n"
    "v_exp_f32 v225, v225\n"
    "v_accvgpr_write_b32 a28, v156\n"
    "v_mfma_f32_16x16x32_bf16 v[72:75], v[24:27], a[184:187], v[72:75]\n"
    "v_add_f32 v224, 1.0, v224\n"
    "v_add_f32 v225, 1.0, v225\n"
    "v_accvgpr_write_b32 a29, v157\n"
    "v_mfma_f32_16x16x32_bf16 v[76:79], v[16:19], a[156:159], v[76:79]\n"
    "v_rcp_f32 v224, v224\n"
    "v_accvgpr_write_b32 a30, v158\n"
    "v_mfma_f32_16x16x32_bf16 v[76:79], v[20:23], a[172:175], v[76:79]\n"
    "v_rcp_f32 v225, v225\n"
    "v_accvgpr_write_b32 a31, v159\n"
    "v_mfma_f32_16x16x32_bf16 v[76:79], v[24:27], a[188:191], v[76:79]\n"
    "s_waitcnt lgkmcnt(4)\n"
    "v_mul_f32 v224, v224, v200\n"
    "v_mul_f32 v225, v225, v201\n"
    "v_accvgpr_write_b32 a32, v160\n"
    "v_mfma_f32_16x16x32_bf16 v[80:83], v[28:31], a[144:147], v[80:83]\n"
    "s_waitcnt lgkmcnt(2)\n"
    "ds_read_b64_tr_b16 v[48:49], v9 offset:12288\n"
    "v_mul_f32 v224, v224, v216\n"
    "v_accvgpr_write_b32 a33, v161\n"
    "v_mfma_f32_16x16x32_bf16 v[80:83], v[32:35], a[160:163], v[80:83]\n"
    "s_waitcnt lgkmcnt(1)\n"
    "ds_read_b64_tr_b16 v[50:51], v9 offset:14336\n"
    "v_mul_f32 v225, v225, v217\n"
    "v_accvgpr_write_b32 a34, v162\n"
    "v_mfma_f32_16x16x32_bf16 v[80:83], v[36:39], a[176:179], v[80:83]\n"
    "ds_read_b64_tr_b16 v[52:53], v9 offset:16384\n"
    "v_cvt_pk_bf16_f32 v250, v224, v225\n"
    "v_accvgpr_write_b32 a35, v163\n"
    "v_mfma_f32_16x16x32_bf16 v[84:87], v[28:31], a[148:151], v[84:87]\n"
    "ds_read_b64_tr_b16 v[54:55], v9 offset:18432\n"
    "v_fmaak_f32 v227, v227, v2, 0xc0135761\n"
    "v_accvgpr_write_b32 a36, v164\n"
    "v_mfma_f32_16x16x32_bf16 v[84:87], v[32:35], a[164:167], v[84:87]\n"
    "ds_read_b64_tr_b16 v[56:57], v9 offset:20480\n"
    "v_mul_f32 v226, v226, v202\n"
    "v_accvgpr_write_b32 a37, v165\n"
    "v_mfma_f32_16x16x32_bf16 v[84:87], v[36:39], a[180:183], v[84:87]\n"
    "ds_read_b64_tr_b16 v[58:59], v9 offset:22528\n"
    "v_mul_f32 v227, v227, v203\n"
    "v_accvgpr_write_b32 a38, v166\n"
    "v_mfma_f32_16x16x32_bf16 v[88:91], v[28:31], a[152:155], v[88:91]\n"
    "v_exp_f32 v226, v226\n"
    "v_accvgpr_write_b32 a39, v167\n"
    "v_mfma_f32_16x16x32_bf16 v[88:91], v[32:35], a[168:171], v[88:91]\n"
    "v_exp_f32 v227, v227\n"
    "v_accvgpr_write_b32 a40, v168\n"
    "v_mfma_f32_16x16x32_bf16 v[88:91], v[36:39], a[184:187], v[88:91]\n"
    "v_add_f32 v226, 1.0, v226\n"
    "v_add_f32 v227, 1.0, v227\n"
    "v_accvgpr_write_b32 a41, v169\n"
    "v_mfma_f32_16x16x32_bf16 v[92:95], v[28:31], a[156:159], v[92:95]\n"
    "v_rcp_f32 v226, v226\n"
    "v_accvgpr_write_b32 a42, v170\n"
    "v_mfma_f32_16x16x32_bf16 v[92:95], v[32:35], a[172:175], v[92:95]\n"
    "v_rcp_f32 v227, v227\n"
    "v_accvgpr_write_b32 a43, v171\n"
    "v_mfma_f32_16x16x32_bf16 v[92:95], v[36:39], a[188:191], v[92:95]\n"
    "s_waitcnt lgkmcnt(4)\n"
    "v_mul_f32 v226, v226, v202\n"
    "v_mul_f32 v227, v227, v203\n"
    "v_accvgpr_write_b32 a44, v172\n"
    "v_mfma_f32_16x16x32_bf16 v[64:67], v[48:51], a[192:195], v[64:67]\n"
    "s_waitcnt lgkmcnt(2)\n"
    "ds_read_b64_tr_b16 v[16:17], v9 offset:13312\n"
    "v_mul_f32 v226, v226, v218\n"
    "v_accvgpr_write_b32 a45, v173\n"
    "v_mfma_f32_16x16x32_bf16 v[64:67], v[52:55], a[208:211], v[64:67]\n"
    "s_waitcnt lgkmcnt(1)\n"
    "ds_read_b64_tr_b16 v[18:19], v9 offset:15360\n"
    "v_mul_f32 v227, v227, v219\n"
    "v_accvgpr_write_b32 a46, v174\n"
    "v_mfma_f32_16x16x32_bf16 v[64:67], v[56:59], a[224:227], v[64:67]\n"
    "ds_read_b64_tr_b16 v[20:21], v9 offset:17408\n"
    "v_cvt_pk_bf16_f32 v251, v226, v227\n"
    "v_accvgpr_write_b32 a47, v175\n"
    "v_mfma_f32_16x16x32_bf16 v[68:71], v[48:51], a[196:199], v[68:71]\n"
    "ds_read_b64_tr_b16 v[22:23], v9 offset:19456\n"
    "buffer_store_dwordx4 v[248:251], v0, s[52:55], s89 offen offset:2048 sc1\n"
    "v_accvgpr_write_b32 a48, v176\n"
    "v_mfma_f32_16x16x32_bf16 v[68:71], v[52:55], a[212:215], v[68:71]\n"
    "ds_read_b64_tr_b16 v[24:25], v9 offset:21504\n"
    "v_mul_f32 v228, v204, v204\n"
    "v_accvgpr_write_b32 a49, v177\n"
    "v_mfma_f32_16x16x32_bf16 v[68:71], v[56:59], a[228:231], v[68:71]\n"
    "ds_read_b64_tr_b16 v[26:27], v9 offset:23552\n"
    "v_mul_f32 v229, v205, v205\n"
    "v_accvgpr_write_b32 a50, v178\n"
    "v_mfma_f32_16x16x32_bf16 v[72:75], v[48:51], a[200:203], v[72:75]\n"
    "v_fmaak_f32 v228, v228, v2, 0xc0135761\n"
    "v_fmaak_f32 v229, v229, v2, 0xc0135761\n"
    "v_accvgpr_write_b32 a51, v179\n"
    "v_mfma_f32_16x16x32_bf16 v[72:75], v[52:55], a[216:219], v[72:75]\n"
    "v_mul_f32 v228, v228, v204\n"
    "v_mul_f32 v229, v229, v205\n"
    "v_accvgpr_write_b32 a52, v180\n"
    "v_mfma_f32_16x16x32_bf16 v[72:75], v[56:59], a[232:235], v[72:75]\n"
    "v_exp_f32 v228, v228\n"
    "v_accvgpr_write_b32 a53, v181\n"
    "v_mfma_f32_16x16x32_bf16 v[76:79], v[48:51], a[204:207], v[76:79]\n"
    "v_exp_f32 v229, v229\n"
    "v_accvgpr_write_b32 a54, v182\n"
    "v_mfma_f32_16x16x32_bf16 v[76:79], v[52:55], a[220:223], v[76:79]\n"
    "v_add_f32 v228, 1.0, v228\n"
    "v_add_f32 v229, 1.0, v229\n"
    "v_accvgpr_write_b32 a55, v183\n"
    "v_mfma_f32_16x16x32_bf16 v[76:79], v[56:59], a[236:239], v[76:79]\n"
    "s_waitcnt lgkmcnt(4)\n"
    "v_rcp_f32 v228, v228\n"
    "v_accvgpr_write_b32 a56, v184\n"
    "v_mfma_f32_16x16x32_bf16 v[80:83], v[16:19], a[192:195], v[80:83]\n"
    "s_waitcnt lgkmcnt(2)\n"
    "ds_read_b64_tr_b16 v[28:29], v9 offset:24576\n"
    "v_mul_f32 v228, v228, v204\n"
    "v_accvgpr_write_b32 a57, v185\n"
    "v_mfma_f32_16x16x32_bf16 v[80:83], v[20:23], a[208:211], v[80:83]\n"
    "s_waitcnt lgkmcnt(1)\n"
    "ds_read_b64_tr_b16 v[30:31], v9 offset:26624\n"
    "v_mul_f32 v228, v228, v220\n"
    "v_accvgpr_write_b32 a58, v186\n"
    "v_mfma_f32_16x16x32_bf16 v[80:83], v[24:27], a[224:227], v[80:83]\n"
    "ds_read_b64_tr_b16 v[32:33], v9 offset:28672\n"
    "v_mul_f32 v230, v206, v206\n"
    "v_accvgpr_write_b32 a59, v187\n"
    "v_mfma_f32_16x16x32_bf16 v[84:87], v[16:19], a[196:199], v[84:87]\n"
    "ds_read_b64_tr_b16 v[34:35], v9 offset:30720\n"
    "v_mul_f32 v231, v207, v207\n"
    "v_accvgpr_write_b32 a60, v188\n"
    "v_mfma_f32_16x16x32_bf16 v[84:87], v[20:23], a[212:215], v[84:87]\n"
    "ds_read_b64_tr_b16 v[36:37], v9 offset:32768\n"
    "v_fmaak_f32 v230, v230, v2, 0xc0135761\n"
    "v_accvgpr_write_b32 a61, v189\n"
    "v_mfma_f32_16x16x32_bf16 v[84:87], v[24:27], a[228:231], v[84:87]\n"
    "ds_read_b64_tr_b16 v[38:39], v9 offset:34816\n"
    "v_fmaak_f32 v231, v231, v2, 0xc0135761\n"
    "v_accvgpr_write_b32 a62, v190\n"
    "v_mfma_f32_16x16x32_bf16 v[88:91], v[16:19], a[200:203], v[88:91]\n"
    "v_rcp_f32 v229, v229\n"
    "v_accvgpr_write_b32 a63, v191\n"
    "v_mfma_f32_16x16x32_bf16 v[88:91], v[20:23], a[216:219], v[88:91]\n"
    "v_mul_f32 v229, v229, v205\n"
    "v_mul_f32 v229, v229, v221\n"
    "v_accvgpr_write_b32 a144, v12\n"
    "v_mfma_f32_16x16x32_bf16 v[88:91], v[24:27], a[232:235], v[88:91]\n"
    "v_cvt_pk_bf16_f32 v254, v228, v229\n"
    "v_mul_f32 v230, v230, v206\n"
    "v_accvgpr_write_b32 a145, v12\n"
    "v_mfma_f32_16x16x32_bf16 v[92:95], v[16:19], a[204:207], v[92:95]\n"
    "v_exp_f32 v230, v230\n"
    "v_accvgpr_write_b32 a146, v12\n"
    "v_mfma_f32_16x16x32_bf16 v[92:95], v[20:23], a[220:223], v[92:95]\n"
    "v_mul_f32 v231, v231, v207\n"
    "v_add_f32 v230, 1.0, v230\n"
    "v_accvgpr_write_b32 a147, v12\n"
    "v_mfma_f32_16x16x32_bf16 v[92:95], v[24:27], a[236:239], v[92:95]\n"
    "s_waitcnt lgkmcnt(4)\n"
    "v_exp_f32 v231, v231\n"
    "v_accvgpr_write_b32 a148, 0\n"
    "v_mfma_f32_16x16x32_bf16 v[64:67], v[28:31], a[240:243], v[64:67]\n"
    "s_waitcnt lgkmcnt(2)\n"
    "ds_read_b64_tr_b16 v[48:49], v9 offset:25600\n"
    "v_add_f32 v231, 1.0, v231\n"
    "v_accvgpr_write_b32 a149, 0\n"
    "v_mfma_f32_16x16x32_bf16 v[64:67], v[32:35], v[96:99], v[64:67]\n"
    "s_waitcnt lgkmcnt(1)\n"
    "ds_read_b64_tr_b16 v[50:51], v9 offset:27648\n"
    "v_accvgpr_write_b32 a150, 0\n"
    "v_accvgpr_write_b32 a151, 0\n"
    "v_mfma_f32_16x16x32_bf16 v[64:67], v[36:39], v[112:115], v[64:67]\n"
    "ds_read_b64_tr_b16 v[52:53], v9 offset:29696\n"
    "v_mfma_f32_16x16x32_bf16 v[68:71], v[28:31], a[244:247], v[68:71]\n"
    "ds_read_b64_tr_b16 v[54:55], v9 offset:31744\n"
    "v_mfma_f32_16x16x32_bf16 v[68:71], v[32:35], v[100:103], v[68:71]\n"
    "ds_read_b64_tr_b16 v[56:57], v9 offset:33792\n"
    "v_mfma_f32_16x16x32_bf16 v[68:71], v[36:39], v[116:119], v[68:71]\n"
    "ds_read_b64_tr_b16 v[58:59], v9 offset:35840\n"
    "v_xor_b32 v9, 32, v9\n"
    "v_mfma_f32_16x16x32_bf16 v[72:75], v[28:31], a[248:251], v[72:75]\n"
    "v_rcp_f32 v230, v230\n"
    "v_mfma_f32_16x16x32_bf16 v[72:75], v[32:35], v[104:107], v[72:75]\n"
    "v_rcp_f32 v231, v231\n"
    "v_mfma_f32_16x16x32_bf16 v[72:75], v[36:39], v[120:123], v[72:75]\n"
    "v_mul_f32 v230, v230, v206\n"
    "v_mul_f32 v231, v231, v207\n"
    "v_mfma_f32_16x16x32_bf16 v[76:79], v[28:31], a[252:255], v[76:79]\n"
    "v_mul_f32 v230, v230, v222\n"
    "v_mul_f32 v231, v231, v223\n"
    "v_mfma_f32_16x16x32_bf16 v[76:79], v[32:35], v[108:111], v[76:79]\n"
    "s_waitcnt vmcnt(11) lgkmcnt(4)\n"
    "s_barrier\n"
    "v_cvt_pk_bf16_f32 v255, v230, v231\n"
    "v_mfma_f32_16x16x32_bf16 v[76:79], v[36:39], v[124:127], v[76:79]\n"
    "s_waitcnt lgkmcnt(4)\n"
    "ds_read_b128 a[64:67], v60 offset:0\n"
    "buffer_store_dwordx4 v[252:255], v0, s[52:55], s89 offen offset:3072 sc1\n"
    "v_mfma_f32_16x16x32_bf16 v[80:83], v[48:51], a[240:243], v[80:83]\n"
    "s_waitcnt lgkmcnt(3)\n"
    "ds_read_b128 a[68:71], v61 offset:0\n"
    "v_mfma_f32_16x16x32_bf16 v[80:83], v[52:55], v[96:99], v[80:83]\n"
    "s_waitcnt lgkmcnt(2)\n"
    "ds_read_b128 a[72:75], v62 offset:0\n"
    "v_mfma_f32_16x16x32_bf16 v[80:83], v[56:59], v[112:115], v[80:83]\n"
    "ds_read_b128 a[76:79], v63 offset:0\n"
    "v_mfma_f32_16x16x32_bf16 v[84:87], v[48:51], a[244:247], v[84:87]\n"
    "ds_read_b128 a[80:83], v60 offset:4096\n"
    "v_mfma_f32_16x16x32_bf16 v[84:87], v[52:55], v[100:103], v[84:87]\n"
    "ds_read_b128 a[84:87], v61 offset:4096\n"
    "v_mfma_f32_16x16x32_bf16 v[84:87], v[56:59], v[116:119], v[84:87]\n"
    "ds_read_b128 a[88:91], v62 offset:4096\n"
    "v_mfma_f32_16x16x32_bf16 v[88:91], v[48:51], a[248:251], v[88:91]\n"
    "ds_read_b128 a[92:95], v63 offset:4096\n"
    "v_mfma_f32_16x16x32_bf16 v[88:91], v[52:55], v[104:107], v[88:91]\n"
    "v_mfma_f32_16x16x32_bf16 v[88:91], v[56:59], v[120:123], v[88:91]\n"
    "v_mfma_f32_16x16x32_bf16 v[92:95], v[48:51], a[252:255], v[92:95]\n"
    "v_mfma_f32_16x16x32_bf16 v[92:95], v[52:55], v[108:111], v[92:95]\n"
    "v_mfma_f32_16x16x32_bf16 v[92:95], v[56:59], v[124:127], v[92:95]\n"
    "s_waitcnt lgkmcnt(7)\n"
    "s_add_u32 s89, s89, 4096\n"
    "v_mfma_f32_16x16x32_bf16 v[96:99], a[64:67], a[0:3], 0\n"
    "s_add_u32 m0, s86, 32768\n"
    "v_xor_b32 v16, 0, v3\n"
    "v_xor_b32 v17, 64, v3\n"
    "v_xor_b32 v18, 128, v3\n"
    "v_xor_b32 v19, 192, v3\n"
    "v_xor_b32 v20, 0, v4\n"
    "v_xor_b32 v21, 64, v4\n"
    "v_xor_b32 v22, 128, v4\n"
    "s_waitcnt lgkmcnt(6)\n"
    "v_mfma_f32_16x16x32_bf16 v[96:99], a[68:71], a[4:7], v[96:99]\n"
    "s_waitcnt lgkmcnt(5)\n"
    "buffer_load_dwordx4 v40, s[56:59], s92 offen lds\n"
    "v_xor_b32 v23, 192, v4\n"
    "v_xor_b32 v24, 0, v5\n"
    "v_xor_b32 v25, 32, v5\n"
    "v_xor_b32 v26, 64, v5\n"
    "v_xor_b32 v27, 96, v5\n"
    "v_xor_b32 v28, 128, v5\n"
    "v_mfma_f32_16x16x32_bf16 v[96:99], a[72:75], a[8:11], v[96:99]\n"
    "s_waitcnt lgkmcnt(4)\n"
    "v_xor_b32 v29, 160, v5\n"
    "v_xor_b32 v30, 192, v5\n"
    "v_xor_b32 v31, 224, v5\n"
    "v_xor_b32 v32, 0, v6\n"
    "v_xor_b32 v33, 32, v6\n"
    "v_xor_b32 v34, 64, v6\n"
    "v_xor_b32 v35, 96, v6\n"
    "v_mfma_f32_16x16x32_bf16 v[96:99], a[76:79], a[12:15], v[96:99]\n"
    "buffer_load_dwordx4 v41, s[56:59], s92 offen offset:1024 lds\n"
    "v_xor_b32 v36, 128, v6\n"
    "v_xor_b32 v37, 160, v6\n"
    "v_xor_b32 v38, 192, v6\n"
    "v_xor_b32 v39, 224, v6\n"
    "v_mul_f32 v224, v64, v64\n"
    "v_mul_f32 v225, v65, v65\n"
    "v_mfma_f32_16x16x32_bf16 v[104:107], a[64:67], a[16:19], 0\n"
    "v_fmaak_f32 v224, v224, v2, 0xc0135761\n"
    "v_fmaak_f32 v225, v225, v2, 0xc0135761\n"
    "v_mul_f32 v224, v224, v64\n"
    "v_exp_f32 v224, v224\n"
    "v_mul_f32 v225, v225, v65\n"
    "v_add_f32 v224, 1.0, v224\n"
    "v_mfma_f32_16x16x32_bf16 v[104:107], a[68:71], a[20:23], v[104:107]\n"
    "buffer_load_dwordx4 v42, s[56:59], s92 offen offset:2048 lds\n"
    "v_exp_f32 v225, v225\n"
    "v_rcp_f32 v224, v224\n"
    "v_add_f32 v225, 1.0, v225\n"
    "v_mul_f32 v224, v224, v64\n"
    "v_mfma_f32_16x16x32_bf16 v[104:107], a[72:75], a[24:27], v[104:107]\n"
    "v_rcp_f32 v225, v225\n"
    "s_nop 0\n"
    "v_mul_f32 v225, v225, v65\n"
    "v_mul_f32 v224, v224, v80\n"
    "v_mul_f32 v225, v225, v81\n"
    "v_cvt_pk_bf16_f32 v240, v224, v225\n"
    "v_mul_f32 v226, v66, v66\n"
    "v_mfma_f32_16x16x32_bf16 v[104:107], a[76:79], a[28:31], v[104:107]\n"
    "buffer_load_dwordx4 v43, s[56:59], s92 offen offset:3072 lds\n"
    "v_mul_f32 v227, v67, v67\n"
    "v_fmaak_f32 v226, v226, v2, 0xc0135761\n"
    "v_fmaak_f32 v227, v227, v2, 0xc0135761\n"
    "v_mul_f32 v226, v226, v66\n"
    "v_exp_f32 v226, v226\n"
    "v_mfma_f32_16x16x32_bf16 v[112:115], a[64:67], a[32:35], 0\n"
    "s_add_u32 m0, s86, 49152\n"
    "v_mul_f32 v227, v227, v67\n"
    "v_exp_f32 v227, v227\n"
    "v_add_f32 v226, 1.0, v226\n"
    "v_rcp_f32 v226, v226\n"
    "v_add_f32 v227, 1.0, v227\n"
    "v_mfma_f32_16x16x32_bf16 v[112:115], a[68:71], a[36:39], v[112:115]\n"
    "buffer_load_dwordx4 v44, s[60:63], s92 offen lds\n"
    "v_rcp_f32 v227, v227\n"
    "v_mul_f32 v226, v226, v66\n"
    "v_mul_f32 v227, v227, v67\n"
    "v_mul_f32 v226, v226, v82\n"
    "v_mul_f32 v227, v227, v83\n"
    "v_mfma_f32_16x16x32_bf16 v[112:115], a[72:75], a[40:43], v[112:115]\n"
    "v_cvt_pk_bf16_f32 v241, v226, v227\n"
    "v_mul_f32 v228, v68, v68\n"
    "v_mul_f32 v229, v69, v69\n"
    "v_fmaak_f32 v228, v228, v2, 0xc0135761\n"
    "v_fmaak_f32 v229, v229, v2, 0xc0135761\n"
    "v_mul_f32 v228, v228, v68\n"
    "v_mul_f32 v229, v229, v69\n"
    "v_mfma_f32_16x16x32_bf16 v[112:115], a[76:79], a[44:47], v[112:115]\n"
    "buffer_load_dwordx4 v45, s[60:63], s92 offen offset:1024 lds\n"
    "v_exp_f32 v228, v228\n"
    "v_exp_f32 v229, v229\n"
    "v_add_f32 v228, 1.0, v228\n"
    "v_add_f32 v229, 1.0, v229\n"
    "v_mfma_f32_16x16x32_bf16 v[120:123], a[64:67], a[48:51], 0\n"
    "v_rcp_f32 v228, v228\n"
    "v_rcp_f32 v229, v229\n"
    "v_mul_f32 v228, v228, v68\n"
    "v_mul_f32 v229, v229, v69\n"
    "v_mul_f32 v228, v228, v84\n"
    "v_mfma_f32_16x16x32_bf16 v[120:123], a[68:71], a[52:55], v[120:123]\n"
    "buffer_load_dwordx4 v44, s[60:63], s92 offen offset:2048 lds\n"
    "v_mul_f32 v229, v229, v85\n"
    "v_cvt_pk_bf16_f32 v244, v228, v229\n"
    "v_mul_f32 v230, v70, v70\n"
    "v_mul_f32 v231, v71, v71\n"
    "v_fmaak_f32 v230, v230, v2, 0xc0135761\n"
    "v_fmaak_f32 v231, v231, v2, 0xc0135761\n"
    "v_mfma_f32_16x16x32_bf16 v[120:123], a[72:75], a[56:59], v[120:123]\n"
    "v_mul_f32 v230, v230, v70\n"
    "v_exp_f32 v230, v230\n"
    "v_mul_f32 v231, v231, v71\n"
    "v_exp_f32 v231, v231\n"
    "v_add_f32 v230, 1.0, v230\n"
    "v_mfma_f32_16x16x32_bf16 v[120:123], a[76:79], a[60:63], v[120:123]\n"
    "s_waitcnt lgkmcnt(3)\n"
    "buffer_load_dwordx4 v45, s[60:63], s92 offen offset:3072 lds\n"
    "s_add_u32 s92, s92, 16384\n"
    "v_rcp_f32 v230, v230\n"
    "v_add_f32 v231, 1.0, v231\n"
    "v_rcp_f32 v231, v231\n"
    "v_mul_f32 v230, v230, v70\n"
    "v_mfma_f32_16x16x32_bf16 v[100:103], a[80:83], a[0:3], 0\n"
    "s_waitcnt lgkmcnt(2)\n"
    "v_mul_f32 v231, v231, v71\n"
    "v_mul_f32 v230, v230, v86\n"
    "v_mul_f32 v231, v231, v87\n"
    "v_cvt_pk_bf16_f32 v245, v230, v231\n"
    "v_mul_f32 v224, v72, v72\n"
    "v_mul_f32 v225, v73, v73\n"
    "v_fmaak_f32 v224, v224, v2, 0xc0135761\n"
    "v_mfma_f32_16x16x32_bf16 v[100:103], a[84:87], a[4:7], v[100:103]\n"
    "s_and_b32 s83, s81, 1\n"
    "s_mul_i32 s83, s83, 19968\n"
    "s_sub_u32 s89, s89, s83\n"
    "v_fmaak_f32 v225, v225, v2, 0xc0135761\n"
    "v_mul_f32 v224, v224, v72\n"
    "v_exp_f32 v224, v224\n"
    "v_mul_f32 v225, v225, v73\n"
    "v_exp_f32 v225, v225\n"
    "s_waitcnt lgkmcnt(1)\n"
    "v_mfma_f32_16x16x32_bf16 v[100:103], a[88:91], a[8:11], v[100:103]\n"
    "s_waitcnt lgkmcnt(0)\n"
    "ds_read_b128 a[64:67], v60 offset:8192\n"
    "v_add_f32 v224, 1.0, v224\n"
    "v_rcp_f32 v224, v224\n"
    "v_add_f32 v225, 1.0, v225\n"
    "v_rcp_f32 v225, v225\n"
    "v_mfma_f32_16x16x32_bf16 v[100:103], a[92:95], a[12:15], v[100:103]\n"
    "v_mul_f32 v224, v224, v72\n"
    "v_mul_f32 v225, v225, v73\n"
    "v_mul_f32 v224, v224, v88\n"
    "v_mul_f32 v225, v225, v89\n"
    "v_cvt_pk_bf16_f32 v248, v224, v225\n"
    "v_mul_f32 v226, v74, v74\n"
    "v_mul_f32 v227, v75, v75\n"
    "v_mfma_f32_16x16x32_bf16 v[108:111], a[80:83], a[16:19], 0\n"
    "ds_read_b128 a[68:71], v61 offset:8192\n"
    "v_fmaak_f32 v226, v226, v2, 0xc0135761\n"
    "v_fmaak_f32 v227, v227, v2, 0xc0135761\n"
    "v_mul_f32 v226, v226, v74\n"
    "v_exp_f32 v226, v226\n"
    "v_mul_f32 v227, v227, v75\n"
    "v_mfma_f32_16x16x32_bf16 v[108:111], a[84:87], a[20:23], v[108:111]\n"
    "v_exp_f32 v227, v227\n"
    "v_add_f32 v226, 1.0, v226\n"
    "v_rcp_f32 v226, v226\n"
    "v_add_f32 v227, 1.0, v227\n"
    "v_mul_f32 v226, v226, v74\n"
    "v_mfma_f32_16x16x32_bf16 v[108:111], a[88:91], a[24:27], v[108:111]\n"
    "ds_read_b128 a[72:75], v62 offset:8192\n"
    "v_rcp_f32 v227, v227\n"
    "s_nop 0\n"
    "v_mul_f32 v227, v227, v75\n"
    "v_mul_f32 v226, v226, v90\n"
    "v_mul_f32 v227, v227, v91\n"
    "v_cvt_pk_bf16_f32 v249, v226, v227\n"
    "v_mfma_f32_16x16x32_bf16 v[108:111], a[92:95], a[28:31], v[108:111]\n"
    "v_mul_f32 v228, v76, v76\n"
    "v_mul_f32 v229, v77, v77\n"
    "v_fmaak_f32 v228, v228, v2, 0xc0135761\n"
    "v_fmaak_f32 v229, v229, v2, 0xc0135761\n"
    "v_mul_f32 v228, v228, v76\n"
    "v_exp_f32 v228, v228\n"
    "v_mfma_f32_16x16x32_bf16 v[116:119], a[80:83], a[32:35], 0\n"
    "ds_read_b128 a[76:79], v63 offset:8192\n"
    "v_mul_f32 v229, v229, v77\n"
    "v_exp_f32 v229, v229\n"
    "v_add_f32 v228, 1.0, v228\n"
    "v_rcp_f32 v228, v228\n"
    "v_mfma_f32_16x16x32_bf16 v[116:119], a[84:87], a[36:39], v[116:119]\n"
    "v_add_f32 v229, 1.0, v229\n"
    "v_rcp_f32 v229, v229\n"
    "v_mul_f32 v228, v228, v76\n"
    "v_mul_f32 v229, v229, v77\n"
    "v_mul_f32 v228, v228, v92\n"
    "v_mul_f32 v229, v229, v93\n"
    "v_mfma_f32_16x16x32_bf16 v[116:119], a[88:91], a[40:43], v[116:119]\n"
    "v_cvt_pk_bf16_f32 v252, v228, v229\n"
    "v_mul_f32 v230, v78, v78\n"
    "v_mul_f32 v231, v79, v79\n"
    "v_fmaak_f32 v230, v230, v2, 0xc0135761\n"
    "v_fmaak_f32 v231, v231, v2, 0xc0135761\n"
    "v_mul_f32 v230, v230, v78\n"
    "v_mul_f32 v231, v231, v79\n"
    "v_mfma_f32_16x16x32_bf16 v[116:119], a[92:95], a[44:47], v[116:119]\n"
    "v_exp_f32 v230, v230\n"
    "v_exp_f32 v231, v231\n"
    "v_add_f32 v230, 1.0, v230\n"
    "v_rcp_f32 v230, v230\n"
    "v_mfma_f32_16x16x32_bf16 v[124:127], a[80:83], a[48:51], 0\n"
    "v_add_f32 v231, 1.0, v231\n"
    "v_rcp_f32 v231, v231\n"
    "v_mul_f32 v230, v230, v78\n"
    "v_mul_f32 v231, v231, v79\n"
    "v_mul_f32 v230, v230, v94\n"
    "v_mul_f32 v231, v231, v95\n"
    "v_mfma_f32_16x16x32_bf16 v[124:127], a[84:87], a[52:55], v[124:127]\n"
    "v_cvt_pk_bf16_f32 v253, v230, v231\n"
    "v_mfma_f32_16x16x32_bf16 v[124:127], a[88:91], a[56:59], v[124:127]\n"
    "v_mfma_f32_16x16x32_bf16 v[124:127], a[92:95], a[60:63], v[124:127]\n"
    "ds_read_b128 a[80:83], v60 offset:12288\n"
    "ds_read_b128 a[84:87], v61 offset:12288\n"
    "ds_read_b128 a[88:91], v62 offset:12288\n"
    "ds_read_b128 a[92:95], v63 offset:12288\n"
    "buffer_store_dwordx2 v[240:241], v10, s[52:55], s89 offen offset:0 sc1\n"
    "buffer_store_dwordx2 v[244:245], v10, s[52:55], s89 offen offset:1024 sc1\n"
    "buffer_store_dwordx2 v[248:249], v10, s[52:55], s89 offen offset:2048 sc1\n"
    "buffer_store_dwordx2 v[252:253], v10, s[52:55], s89 offen offset:3072 sc1\n"
    "v_max3_f32 v48, v96, v97, v98\n"
    "v_max3_f32 v49, v99, v100, v101\n"
    "v_max3_f32 v48, v48, v102, v103\n"
    "v_max_f32 v48, v48, v49\n"
    "v_max3_f32 v52, v104, v105, v106\n"
    "v_max3_f32 v53, v107, v108, v109\n"
    "v_max3_f32 v52, v52, v110, v111\n"
    "v_max_f32 v52, v52, v53\n"
    "v_max3_f32 v56, v112, v113, v114\n"
    "v_max3_f32 v57, v115, v116, v117\n"
    "v_max3_f32 v56, v56, v118, v119\n"
    "v_max_f32 v56, v56, v57\n"
    "v_max3_f32 v60, v120, v121, v122\n"
    "v_max3_f32 v61, v123, v124, v125\n"
    "v_max3_f32 v60, v60, v126, v127\n"
    "v_max_f32 v60, v60, v61\n"
    "v_mov_b32 v49, v48\n"
    "v_mov_b32 v53, v52\n"
    "v_mov_b32 v57, v56\n"
    "v_mov_b32 v61, v60\n"
    "v_permlane16_swap_b32 v48, v49\n"
    "v_permlane16_swap_b32 v52, v53\n"
    "v_permlane16_swap_b32 v56, v57\n"
    "v_permlane16_swap_b32 v60, v61\n"
    "v_max_f32 v48, v48, v49\n"
    "v_max_f32 v52, v52, v53\n"
    "v_max_f32 v56, v56, v57\n"
    "v_max_f32 v60, v60, v61\n"
    "v_mov_b32 v49, v48\n"
    "v_mov_b32 v53, v52\n"
    "v_mov_b32 v57, v56\n"
    "v_mov_b32 v61, v60\n"
    "v_permlane32_swap_b32 v48, v49\n"
    "v_permlane32_swap_b32 v52, v53\n"
    "v_permlane32_swap_b32 v56, v57\n"
    "v_permlane32_swap_b32 v60, v61\n"
    "v_max_f32 v48, v48, v49\n"
    "v_max_f32 v52, v52, v53\n"
    "v_max_f32 v56, v56, v57\n"
    "v_max_f32 v60, v60, v61\n"
    "v_sub_f32 v48, 0xc2b00000, v48\n"
    "v_mov_b32 v49, v48\n"
    "v_mov_b32 v50, v48\n"
    "v_mov_b32 v51, v48\n"
    "v_sub_f32 v52, 0xc2b00000, v52\n"
    "v_mov_b32 v53, v52\n"
    "v_mov_b32 v54, v52\n"
    "v_mov_b32 v55, v52\n"
    "v_sub_f32 v56, 0xc2b00000, v56\n"
    "v_mov_b32 v57, v56\n"
    "v_mov_b32 v58, v56\n"
    "v_mov_b32 v59, v56\n"
    "v_sub_f32 v60, 0xc2b00000, v60\n"
    "v_mov_b32 v61, v60\n"
    "v_mov_b32 v62, v60\n"
    "v_mov_b32 v63, v60\n"
    "v_lshlrev_b32 v12, 3, v0\n"
    "L_PRO2%=:\n"
    "s_mov_b32 s88, 7\n"
    "v_mfma_f32_32x32x16_bf16 v[128:143], a[148:151], a[148:151], 0\n"
    "v_add_f32 v96, v96, v48\n"
    "v_add_f32 v97, v97, v48\n"
    "v_add_f32 v98, v98, v48\n"
    "v_add_f32 v99, v99, v48\n"
    "v_add_f32 v100, v100, v48\n"
    "v_add_f32 v101, v101, v48\n"
    "v_mfma_f32_32x32x16_bf16 v[144:159], a[148:151], a[148:151], 0\n"
    "v_add_f32 v102, v102, v48\n"
    "v_add_f32 v103, v103, v48\n"
    "v_add_f32 v104, v104, v52\n"
    "v_add_f32 v105, v105, v52\n"
    "v_add_f32 v106, v106, v52\n"
    "v_add_f32 v107, v107, v52\n"
    "v_mfma_f32_32x32x16_bf16 v[160:175], a[148:151], a[148:151], 0\n"
    "v_add_f32 v108, v108, v52\n"
    "v_add_f32 v109, v109, v52\n"
    "v_add_f32 v110, v110, v52\n"
    "v_add_f32 v111, v111, v52\n"
    "v_add_f32 v112, v112, v56\n"
    "v_add_f32 v113, v113, v56\n"
    "v_mfma_f32_32x32x16_bf16 v[176:191], a[148:151], a[148:151], 0\n"
    "v_add_f32 v114, v114, v56\n"
    "v_add_f32 v115, v115, v56\n"
    "v_add_f32 v116, v116, v56\n"
    "v_add_f32 v117, v117, v56\n"
    "v_add_f32 v118, v118, v56\n"
    "v_add_f32 v119, v119, v56\n"
    "v_mfma_f32_32x32x16_bf16 v[192:207], a[148:151], a[148:151], 0\n"
    "v_add_f32 v120, v120, v60\n"
    "v_add_f32 v121, v121, v60\n"
    "v_add_f32 v122, v122, v60\n"
    "v_add_f32 v123, v123, v60\n"
    "v_add_f32 v124, v124, v60\n"
    "v_add_f32 v125, v125, v60\n"
    "v_mfma_f32_32x32x16_bf16 v[208:223], a[148:151], a[148:151], 0\n"
    "v_add_f32 v126, v126, v60\n"
    "v_add_f32 v127, v127, v60\n"
    "v_exp_f32 v96, v96\n"
    "v_exp_f32 v97, v97\n"
    "v_mfma_f32_32x32x16_bf16 v[224:239], a[148:151], a[148:151], 0\n"
    "v_exp_f32 v98, v98\n"
    "v_cvt_pk_bf16_f32 v96, v96, v97\n"
    "v_exp_f32 v99, v99\n"
    "v_mfma_f32_32x32x16_bf16 v[240:255], a[148:151], a[148:151], 0\n"
    "v_exp_f32 v104, v104\n"
    "v_cvt_pk_bf16_f32 v97, v98, v99\n"
    "v_exp_f32 v105, v105\n"
    "v_mfma_f32_32x32x16_bf16 a[128:143], a[148:151], a[148:151], 0\n"
    "v_exp_f32 v106, v106\n"
    "v_cvt_pk_bf16_f32 v104, v104, v105\n"
    "v_exp_f32 v107, v107\n"
    "v_exp_f32 v112, v112\n"
    "v_cvt_pk_bf16_f32 v105, v106, v107\n"
    "v_exp_f32 v113, v113\n"
    "v_exp_f32 v114, v114\n"
    "v_cvt_pk_bf16_f32 v112, v112, v113\n"
    "v_exp_f32 v115, v115\n"
    "v_exp_f32 v120, v120\n"
    "v_cvt_pk_bf16_f32 v113, v114, v115\n"
    "v_exp_f32 v121, v121\n"
    "v_exp_f32 v122, v122\n"
    "v_cvt_pk_bf16_f32 v120, v120, v121\n"
    "v_exp_f32 v123, v123\n"
    "v_exp_f32 v100, v100\n"
    "v_cvt_pk_bf16_f32 v121, v122, v123\n"
    "v_exp_f32 v101, v101\n"
    "v_exp_f32 v102, v102\n"
    "v_cvt_pk_bf16_f32 v98, v100, v101\n"
    "v_exp_f32 v103, v103\n"
    "v_exp_f32 v108, v108\n"
    "v_cvt_pk_bf16_f32 v99, v102, v103\n"
    "v_exp_f32 v109, v109\n"
    "v_exp_f32 v110, v110\n"
    "v_cvt_pk_bf16_f32 v106, v108, v109\n"
    "v_exp_f32 v111, v111\n"
    "v_exp_f32 v116, v116\n"
    "v_cvt_pk_bf16_f32 v107, v110, v111\n"
    "v_exp_f32 v117, v117\n"
    "v_exp_f32 v118, v118\n"
    "v_cvt_pk_bf16_f32 v114, v116, v117\n"
    ".p2align 6\n"
    "L_A%=:\n"
    "s_waitcnt lgkmcnt(7)\n"
    "v_mfma_f32_16x16x32_bf16 v[64:67], a[64:67], a[0:3], v[48:51]\n"
    "s_waitcnt lgkmcnt(6)\n"
    "v_exp_f32 v119, v119\n"
    "v_mfma_f32_16x16x32_bf16 v[64:67], a[68:71], a[4:7], v[64:67]\n"
    "s_waitcnt lgkmcnt(5)\n"
    "ds_read_b64_tr_b16 a[96:97], v24 offset:16384\n"
    "v_cvt_pk_bf16_f32 v115, v118, v119\n"
    "v_mfma_f32_16x16x32_bf16 v[64:67], a[72:75], a[8:11], v[64:67]\n"
    "s_waitcnt lgkmcnt(5)\n"
    "v_exp_f32 v124, v124\n"
    "v_mfma_f32_16x16x32_bf16 v[64:67], a[76:79], a[12:15], v[64:67]\n"
    "ds_read_b64_tr_b16 a[98:99], v24 offset:20480\n"
    "v_mfma_f32_16x16x32_bf16 v[72:75], a[64:67], a[16:19], v[52:55]\n"
    "v_exp_f32 v125, v125\n"
    "v_mfma_f32_16x16x32_bf16 v[72:75], a[68:71], a[20:23], v[72:75]\n"
    "ds_read_b64_tr_b16 a[100:101], v25 offset:16384\n"
    "v_cvt_pk_bf16_f32 v122, v124, v125\n"
    "v_mfma_f32_16x16x32_bf16 v[72:75], a[72:75], a[24:27], v[72:75]\n"
    "v_exp_f32 v126, v126\n"
    "v_mfma_f32_16x16x32_bf16 v[72:75], a[76:79], a[28:31], v[72:75]\n"
    "ds_read_b64_tr_b16 a[102:103], v25 offset:20480\n"
    "v_mfma_f32_16x16x32_bf16 v[80:83], a[64:67], a[32:35], v[56:59]\n"
    "v_exp_f32 v127, v127\n"
    "v_mfma_f32_16x16x32_bf16 v[80:83], a[68:71], a[36:39], v[80:83]\n"
    "ds_read_b64_tr_b16 a[104:105], v26 offset:16384\n"
    "v_cvt_pk_bf16_f32 v123, v126, v127\n"
    "v_mfma_f32_16x16x32_bf16 v[80:83], a[72:75], a[40:43], v[80:83]\n"
    "v_exp_f32 v64, v64\n"
    "v_mfma_f32_16x16x32_bf16 v[80:83], a[76:79], a[44:47], v[80:83]\n"
    "ds_read_b64_tr_b16 a[106:107], v26 offset:20480\n"
    "v_mfma_f32_16x16x32_bf16 v[88:91], a[64:67], a[48:51], v[60:63]\n"
    "v_exp_f32 v65, v65\n"
    "v_mfma_f32_16x16x32_bf16 v[88:91], a[68:71], a[52:55], v[88:91]\n"
    "ds_read_b64_tr_b16 a[108:109], v27 offset:16384\n"
    "v_cvt_pk_bf16_f32 v64, v64, v65\n"
    "v_mfma_f32_16x16x32_bf16 v[88:91], a[72:75], a[56:59], v[88:91]\n"
    "v_exp_f32 v66, v66\n"
    "v_mfma_f32_16x16x32_bf16 v[88:91], a[76:79], a[60:63], v[88:91]\n"
    "s_waitcnt lgkmcnt(10)\n"
    "ds_read_b64_tr_b16 a[110:111], v27 offset:20480\n"
    "v_mfma_f32_16x16x32_bf16 v[68:71], a[80:83], a[0:3], v[48:51]\n"
    "s_waitcnt lgkmcnt(10)\n"
    "v_exp_f32 v67, v67\n"
    "v_mfma_f32_16x16x32_bf16 v[68:71], a[84:87], a[4:7], v[68:71]\n"
    "s_waitcnt lgkmcnt(9)\n"
    "ds_read_b64_tr_b16 a[112:113], v28 offset:16384\n"
    "v_cvt_pk_bf16_f32 v65, v66, v67\n"
    "v_mfma_f32_16x16x32_bf16 v[68:71], a[88:91], a[8:11], v[68:71]\n"
    "s_waitcnt lgkmcnt(9)\n"
    "v_exp_f32 v72, v72\n"
    "v_mfma_f32_16x16x32_bf16 v[68:71], a[92:95], a[12:15], v[68:71]\n"
    "ds_read_b64_tr_b16 a[114:115], v28 offset:20480\n"
    "v_mfma_f32_16x16x32_bf16 v[76:79], a[80:83], a[16:19], v[52:55]\n"
    "v_exp_f32 v73, v73\n"
    "v_mfma_f32_16x16x32_bf16 v[76:79], a[84:87], a[20:23], v[76:79]\n"
    "ds_read_b64_tr_b16 a[116:117], v29 offset:16384\n"
    "v_cvt_pk_bf16_f32 v72, v72, v73\n"
    "v_mfma_f32_16x16x32_bf16 v[76:79], a[88:91], a[24:27], v[76:79]\n"
    "v_exp_f32 v74, v74\n"
    "v_mfma_f32_16x16x32_bf16 v[76:79], a[92:95], a[28:31], v[76:79]\n"
    "ds_read_b64_tr_b16 a[118:119], v29 offset:20480\n"
    "v_mfma_f32_16x16x32_bf16 v[84:87], a[80:83], a[32:35], v[56:59]\n"
    "v_exp_f32 v75, v75\n"
    "v_mfma_f32_16x16x32_bf16 v[84:87], a[84:87], a[36:39], v[84:87]\n"
    "ds_read_b64_tr_b16 a[120:121], v30 offset:16384\n"
    "v_cvt_pk_bf16_f32 v73, v74, v75\n"
    "v_mfma_f32_16x16x32_bf16 v[84:87], a[88:91], a[40:43], v[84:87]\n"
    "v_exp_f32 v80, v80\n"
    "v_mfma_f32_16x16x32_bf16 v[84:87], a[92:95], a[44:47], v[84:87]\n"
    "ds_read_b64_tr_b16 a[122:123], v30 offset:20480\n"
    "v_mfma_f32_16x16x32_bf16 v[92:95], a[80:83], a[48:51], v[60:63]\n"
    "v_exp_f32 v81, v81\n"
    "v_mfma_f32_16x16x32_bf16 v[92:95], a[84:87], a[52:55], v[92:95]\n"
    "ds_read_b64_tr_b16 a[124:125], v31 offset:16384\n"
    "v_cvt_pk_bf16_f32 v80, v80, v81\n"
    "v_mfma_f32_16x16x32_bf16 v[92:95], a[88:91], a[56:59], v[92:95]\n"
    "v_exp_f32 v82, v82\n"
    "v_mfma_f32_16x16x32_bf16 v[92:95], a[92:95], a[60:63], v[92:95]\n"
    "ds_read_b64_tr_b16 a[126:127], v31 offset:20480\n"
    "v_mfma_f32_16x16x32_bf16 a[128:131], a[144:147], v[96:99], a[128:131]\n"
    "v_exp_f32 v83, v83\n"
    "v_mfma_f32_16x16x32_bf16 a[132:135], a[144:147], v[104:107], a[132:135]\n"
    "v_cvt_pk_bf16_f32 v81, v82, v83\n"
    "v_mfma_f32_16x16x32_bf16 a[136:139], a[144:147], v[112:115], a[136:139]\n"
    "v_exp_f32 v88, v88\n"
    "v_mfma_f32_16x16x32_bf16 a[140:143], a[144:147], v[120:123], a[140:143]\n"
    "s_waitcnt lgkmcnt(14)\n"
    "v_mfma_f32_16x16x32_bf16 v[128:131], a[96:99], v[96:99], v[128:131]\n"
    "v_exp_f32 v89, v89\n"
    "v_mfma_f32_16x16x32_bf16 v[160:163], a[96:99], v[104:107], v[160:163]\n"
    "s_waitcnt vmcnt(8)\n"
    "s_barrier\n"
    "v_cvt_pk_bf16_f32 v88, v88, v89\n"
    "v_mfma_f32_16x16x32_bf16 v[192:195], a[96:99], v[112:115], v[192:195]\n"
    "v_exp_f32 v90, v90\n"
    "v_mfma_f32_16x16x32_bf16 v[224:227], a[96:99], v[120:123], v[224:227]\n"
    "s_waitcnt lgkmcnt(12)\n"
    "v_mfma_f32_16x16x32_bf16 v[132:135], a[100:103], v[96:99], v[132:135]\n"
    "v_exp_f32 v91, v91\n"
    "v_mfma_f32_16x16x32_bf16 v[164:167], a[100:103], v[104:107], v[164:167]\n"
    "ds_read_b128 a[64:67], v16 offset:32768\n"
    "v_cvt_pk_bf16_f32 v89, v90, v91\n"
    "v_mfma_f32_16x16x32_bf16 v[196:199], a[100:103], v[112:115], v[196:199]\n"
    "v_exp_f32 v68, v68\n"
    "v_mfma_f32_16x16x32_bf16 v[228:231], a[100:103], v[120:123], v[228:231]\n"
    "s_waitcnt lgkmcnt(11)\n"
    "ds_read_b128 a[68:71], v17 offset:32768\n"
    "v_mfma_f32_16x16x32_bf16 v[136:139], a[104:107], v[96:99], v[136:139]\n"
    "v_exp_f32 v69, v69\n"
    "v_mfma_f32_16x16x32_bf16 v[168:171], a[104:107], v[104:107], v[168:171]\n"
    "ds_read_b128 a[72:75], v18 offset:32768\n"
    "v_cvt_pk_bf16_f32 v66, v68, v69\n"
    "v_mfma_f32_16x16x32_bf16 v[200:203], a[104:107], v[112:115], v[200:203]\n"
    "v_exp_f32 v70, v70\n"
    "v_mfma_f32_16x16x32_bf16 v[232:235], a[104:107], v[120:123], v[232:235]\n"
    "s_waitcnt lgkmcnt(11)\n"
    "ds_read_b128 a[76:79], v19 offset:32768\n"
    "v_mfma_f32_16x16x32_bf16 v[140:143], a[108:111], v[96:99], v[140:143]\n"
    "v_exp_f32 v71, v71\n"
    "v_mfma_f32_16x16x32_bf16 v[172:175], a[108:111], v[104:107], v[172:175]\n"
    "ds_read_b128 a[80:83], v16 offset:36864\n"
    "v_cvt_pk_bf16_f32 v67, v70, v71\n"
    "v_mfma_f32_16x16x32_bf16 v[204:207], a[108:111], v[112:115], v[204:207]\n"
    "v_exp_f32 v76, v76\n"
    "v_mfma_f32_16x16x32_bf16 v[236:239], a[108:111], v[120:123], v[236:239]\n"
    "s_waitcnt lgkmcnt(11)\n"
    "ds_read_b128 a[84:87], v17 offset:36864\n"
    "v_mfma_f32_16x16x32_bf16 v[144:147], a[112:115], v[96:99], v[144:147]\n"
    "v_exp_f32 v77, v77\n"
    "v_mfma_f32_16x16x32_bf16 v[176:179], a[112:115], v[104:107], v[176:179]\n"
    "ds_read_b128 a[88:91], v18 offset:36864\n"
    "v_cvt_pk_bf16_f32 v74, v76, v77\n"
    "v_mfma_f32_16x16x32_bf16 v[208:211], a[112:115], v[112:115], v[208:211]\n"
    "v_exp_f32 v78, v78\n"
    "v_mfma_f32_16x16x32_bf16 v[240:243], a[112:115], v[120:123], v[240:243]\n"
    "s_waitcnt lgkmcnt(11)\n"
    "ds_read_b128 a[92:95], v19 offset:36864\n"
    "v_mfma_f32_16x16x32_bf16 v[148:151], a[116:119], v[96:99], v[148:151]\n"
    "s_add_u32 m0, s86, 0\n"
    "v_exp_f32 v79, v79\n"
    "v_mfma_f32_16x16x32_bf16 v[180:183], a[116:119], v[104:107], v[180:183]\n"
    "buffer_load_dwordx4 v40, s[56:59], s92 offen lds\n"
    "v_cvt_pk_bf16_f32 v75, v78, v79\n"
    "v_mfma_f32_16x16x32_bf16 v[212:215], a[116:119], v[112:115], v[212:215]\n"
    "v_exp_f32 v84, v84\n"
    "v_mfma_f32_16x16x32_bf16 v[244:247], a[116:119], v[120:123], v[244:247]\n"
    "s_waitcnt lgkmcnt(10)\n"
    "buffer_load_dwordx4 v41, s[56:59], s92 offen offset:1024 lds\n"
    "v_mfma_f32_16x16x32_bf16 v[152:155], a[120:123], v[96:99], v[152:155]\n"
    "v_exp_f32 v85, v85\n"
    "v_mfma_f32_16x16x32_bf16 v[184:187], a[120:123], v[104:107], v[184:187]\n"
    "buffer_load_dwordx4 v42, s[56:59], s92 offen offset:2048 lds\n"
    "v_cvt_pk_bf16_f32 v82, v84, v85\n"
    "v_mfma_f32_16x16x32_bf16 v[216:219], a[120:123], v[112:115], v[216:219]\n"
    "s_add_u32 s83, s39, 0\n"
    "v_exp_f32 v86, v86\n"
    "v_mfma_f32_16x16x32_bf16 v[248:251], a[120:123], v[120:123], v[248:251]\n"
    "s_waitcnt lgkmcnt(8)\n"
    "s_cmp_eq_u32 s88, 2\n"
    "s_cselect_b32 s83, s83, 0x40000000\n"
    "buffer_load_dwordx4 v43, s[56:59], s92 offen offset:3072 lds\n"
    "v_mfma_f32_16x16x32_bf16 v[156:159], a[124:127], v[96:99], v[156:159]\n"
    "buffer_load_dword a152, v12, s[40:43], s83 offen\n"
    "v_mfma_f32_16x16x32_bf16 v[188:191], a[124:127], v[104:107], v[188:191]\n"
    "v_mfma_f32_16x16x32_bf16 v[220:223], a[124:127], v[112:115], v[220:223]\n"
    "v_mfma_f32_16x16x32_bf16 v[252:255], a[124:127], v[120:123], v[252:255]\n"
    "s_waitcnt lgkmcnt(7)\n"
    "v_mfma_f32_16x16x32_bf16 v[96:99], a[64:67], a[0:3], v[48:51]\n"
    "s_waitcnt lgkmcnt(6)\n"
    "v_exp_f32 v87, v87\n"
    "v_mfma_f32_16x16x32_bf16 v[96:99], a[68:71], a[4:7], v[96:99]\n"
    "s_waitcnt lgkmcnt(5)\n"
    "ds_read_b64_tr_b16 a[96:97], v24 offset:24576\n"
    "v_cvt_pk_bf16_f32 v83, v86, v87\n"
    "v_mfma_f32_16x16x32_bf16 v[96:99], a[72:75], a[8:11], v[96:99]\n"
    "s_waitcnt lgkmcnt(5)\n"
    "v_exp_f32 v92, v92\n"
    "v_mfma_f32_16x16x32_bf16 v[96:99], a[76:79], a[12:15], v[96:99]\n"
    "ds_read_b64_tr_b16 a[98:99], v24 offset:28672\n"
    "v_mfma_f32_16x16x32_bf16 v[104:107], a[64:67], a[16:19], v[52:55]\n"
    "v_exp_f32 v93, v93\n"
    "v_mfma_f32_16x16x32_bf16 v[104:107], a[68:71], a[20:23], v[104:107]\n"
    "ds_read_b64_tr_b16 a[100:101], v25 offset:24576\n"
    "v_cvt_pk_bf16_f32 v90, v92, v93\n"
    "v_mfma_f32_16x16x32_bf16 v[104:107], a[72:75], a[24:27], v[104:107]\n"
    "v_exp_f32 v94, v94\n"
    "v_mfma_f32_16x16x32_bf16 v[104:107], a[76:79], a[28:31], v[104:107]\n"
    "ds_read_b64_tr_b16 a[102:103], v25 offset:28672\n"
    "v_mfma_f32_16x16x32_bf16 v[112:115], a[64:67], a[32:35], v[56:59]\n"
    "v_exp_f32 v95, v95\n"
    "v_mfma_f32_16x16x32_bf16 v[112:115], a[68:71], a[36:39], v[112:115]\n"
    "ds_read_b64_tr_b16 a[104:105], v26 offset:24576\n"
    "v_cvt_pk_bf16_f32 v91, v94, v95\n"
    "v_mfma_f32_16x16x32_bf16 v[112:115], a[72:75], a[40:43], v[112:115]\n"
    "v_exp_f32 v96, v96\n"
    "v_mfma_f32_16x16x32_bf16 v[112:115], a[76:79], a[44:47], v[112:115]\n"
    "ds_read_b64_tr_b16 a[106:107], v26 offset:28672\n"
    "v_mfma_f32_16x16x32_bf16 v[120:123], a[64:67], a[48:51], v[60:63]\n"
    "v_exp_f32 v97, v97\n"
    "v_mfma_f32_16x16x32_bf16 v[120:123], a[68:71], a[52:55], v[120:123]\n"
    "ds_read_b64_tr_b16 a[108:109], v27 offset:24576\n"
    "v_cvt_pk_bf16_f32 v96, v96, v97\n"
    "v_mfma_f32_16x16x32_bf16 v[120:123], a[72:75], a[56:59], v[120:123]\n"
    "v_exp_f32 v98, v98\n"
    "v_mfma_f32_16x16x32_bf16 v[120:123], a[76:79], a[60:63], v[120:123]\n"
    "s_waitcnt lgkmcnt(10)\n"
    "ds_read_b64_tr_b16 a[110:111], v27 offset:28672\n"
    "v_mfma_f32_16x16x32_bf16 v[100:103], a[80:83], a[0:3], v[48:51]\n"
    "s_waitcnt lgkmcnt(10)\n"
    "v_exp_f32 v99, v99\n"
    "v_mfma_f32_16x16x32_bf16 v[100:103], a[84:87], a[4:7], v[100:103]\n"
    "s_waitcnt lgkmcnt(9)\n"
    "ds_read_b64_tr_b16 a[112:113], v28 offset:24576\n"
    "v_cvt_pk_bf16_f32 v97, v98, v99\n"
    "v_mfma_f32_16x16x32_bf16 v[100:103], a[88:91], a[8:11], v[100:103]\n"
    "s_waitcnt lgkmcnt(9)\n"
    "v_exp_f32 v104, v104\n"
    "v_mfma_f32_16x16x32_bf16 v[100:103], a[92:95], a[12:15], v[100:103]\n"
    "ds_read_b64_tr_b16 a[114:115], v28 offset:28672\n"
    "v_mfma_f32_16x16x32_bf16 v[108:111], a[80:83], a[16:19], v[52:55]\n"
    "v_exp_f32 v105, v105\n"
    "v_mfma_f32_16x16x32_bf16 v[108:111], a[84:87], a[20:23], v[108:111]\n"
    "ds_read_b64_tr_b16 a[116:117], v29 offset:24576\n"
    "v_cvt_pk_bf16_f32 v104, v104, v105\n"
    "v_mfma_f32_16x16x32_bf16 v[108:111], a[88:91], a[24:27], v[108:111]\n"
    "v_exp_f32 v106, v106\n"
    "v_mfma_f32_16x16x32_bf16 v[108:111], a[92:95], a[28:31], v[108:111]\n"
    "ds_read_b64_tr_b16 a[118:119], v29 offset:28672\n"
    "v_mfma_f32_16x16x32_bf16 v[116:119], a[80:83], a[32:35], v[56:59]\n"
    "v_exp_f32 v107, v107\n"
    "v_mfma_f32_16x16x32_bf16 v[116:119], a[84:87], a[36:39], v[116:119]\n"
    "ds_read_b64_tr_b16 a[120:121], v30 offset:24576\n"
    "v_cvt_pk_bf16_f32 v105, v106, v107\n"
    "v_mfma_f32_16x16x32_bf16 v[116:119], a[88:91], a[40:43], v[116:119]\n"
    "v_exp_f32 v112, v112\n"
    "v_mfma_f32_16x16x32_bf16 v[116:119], a[92:95], a[44:47], v[116:119]\n"
    "ds_read_b64_tr_b16 a[122:123], v30 offset:28672\n"
    "v_mfma_f32_16x16x32_bf16 v[124:127], a[80:83], a[48:51], v[60:63]\n"
    "v_exp_f32 v113, v113\n"
    "v_mfma_f32_16x16x32_bf16 v[124:127], a[84:87], a[52:55], v[124:127]\n"
    "ds_read_b64_tr_b16 a[124:125], v31 offset:24576\n"
    "v_cvt_pk_bf16_f32 v112, v112, v113\n"
    "v_mfma_f32_16x16x32_bf16 v[124:127], a[88:91], a[56:59], v[124:127]\n"
    "v_exp_f32 v114, v114\n"
    "v_mfma_f32_16x16x32_bf16 v[124:127], a[92:95], a[60:63], v[124:127]\n"
    "ds_read_b64_tr_b16 a[126:127], v31 offset:28672\n"
    "v_mfma_f32_16x16x32_bf16 a[128:131], a[144:147], v[64:67], a[128:131]\n"
    "v_exp_f32 v115, v115\n"
    "v_mfma_f32_16x16x32_bf16 a[132:135], a[144:147], v[72:75], a[132:135]\n"
    "v_cvt_pk_bf16_f32 v113, v114, v115\n"
    "v_mfma_f32_16x16x32_bf16 a[136:139], a[144:147], v[80:83], a[136:139]\n"
    "v_exp_f32 v120, v120\n"
    "v_mfma_f32_16x16x32_bf16 a[140:143], a[144:147], v[88:91], a[140:143]\n"
    "s_waitcnt lgkmcnt(14)\n"
    "v_mfma_f32_16x16x32_bf16 v[128:131], a[96:99], v[64:67], v[128:131]\n"
    "v_exp_f32 v121, v121\n"
    "v_mfma_f32_16x16x32_bf16 v[160:163], a[96:99], v[72:75], v[160:163]\n"
    "v_cvt_pk_bf16_f32 v120, v120, v121\n"
    "v_mfma_f32_16x16x32_bf16 v[192:195], a[96:99], v[80:83], v[192:195]\n"
    "v_exp_f32 v122, v122\n"
    "v_mfma_f32_16x16x32_bf16 v[224:227], a[96:99], v[88:91], v[224:227]\n"
    "s_waitcnt lgkmcnt(12)\n"
    "v_mfma_f32_16x16x32_bf16 v[132:135], a[100:103], v[64:67], v[132:135]\n"
    "v_exp_f32 v123, v123\n"
    "v_mfma_f32_16x16x32_bf16 v[164:167], a[100:103], v[72:75], v[164:167]\n"
    "ds_read_b128 a[64:67], v16 offset:40960\n"
    "v_cvt_pk_bf16_f32 v121, v122, v123\n"
    "v_mfma_f32_16x16x32_bf16 v[196:199], a[100:103], v[80:83], v[196:199]\n"
    "v_exp_f32 v100, v100\n"
    "v_mfma_f32_16x16x32_bf16 v[228:231], a[100:103], v[88:91], v[228:231]\n"
    "s_waitcnt lgkmcnt(11)\n"
    "ds_read_b128 a[68:71], v17 offset:40960\n"
    "v_mfma_f32_16x16x32_bf16 v[136:139], a[104:107], v[64:67], v[136:139]\n"
    "v_exp_f32 v101, v101\n"
    "v_mfma_f32_16x16x32_bf16 v[168:171], a[104:107], v[72:75], v[168:171]\n"
    "ds_read_b128 a[72:75], v18 offset:40960\n"
    "v_cvt_pk_bf16_f32 v98, v100, v101\n"
    "v_mfma_f32_16x16x32_bf16 v[200:203], a[104:107], v[80:83], v[200:203]\n"
    "v_exp_f32 v102, v102\n"
    "v_mfma_f32_16x16x32_bf16 v[232:235], a[104:107], v[88:91], v[232:235]\n"
    "s_waitcnt lgkmcnt(11)\n"
    "ds_read_b128 a[76:79], v19 offset:40960\n"
    "v_mfma_f32_16x16x32_bf16 v[140:143], a[108:111], v[64:67], v[140:143]\n"
    "v_exp_f32 v103, v103\n"
    "v_mfma_f32_16x16x32_bf16 v[172:175], a[108:111], v[72:75], v[172:175]\n"
    "ds_read_b128 a[80:83], v16 offset:45056\n"
    "v_cvt_pk_bf16_f32 v99, v102, v103\n"
    "v_mfma_f32_16x16x32_bf16 v[204:207], a[108:111], v[80:83], v[204:207]\n"
    "v_exp_f32 v108, v108\n"
    "v_mfma_f32_16x16x32_bf16 v[236:239], a[108:111], v[88:91], v[236:239]\n"
    "s_waitcnt lgkmcnt(11)\n"
    "ds_read_b128 a[84:87], v17 offset:45056\n"
    "v_mfma_f32_16x16x32_bf16 v[144:147], a[112:115], v[64:67], v[144:147]\n"
    "v_exp_f32 v109, v109\n"
    "v_mfma_f32_16x16x32_bf16 v[176:179], a[112:115], v[72:75], v[176:179]\n"
    "ds_read_b128 a[88:91], v18 offset:45056\n"
    "v_cvt_pk_bf16_f32 v106, v108, v109\n"
    "v_mfma_f32_16x16x32_bf16 v[208:211], a[112:115], v[80:83], v[208:211]\n"
    "v_exp_f32 v110, v110\n"
    "v_mfma_f32_16x16x32_bf16 v[240:243], a[112:115], v[88:91], v[240:243]\n"
    "s_waitcnt lgkmcnt(11)\n"
    "ds_read_b128 a[92:95], v19 offset:45056\n"
    "v_mfma_f32_16x16x32_bf16 v[148:151], a[116:119], v[64:67], v[148:151]\n"
    "s_add_u32 m0, s86, 16384\n"
    "v_exp_f32 v111, v111\n"
    "v_mfma_f32_16x16x32_bf16 v[180:183], a[116:119], v[72:75], v[180:183]\n"
    "buffer_load_dwordx4 v44, s[60:63], s92 offen lds\n"
    "v_cvt_pk_bf16_f32 v107, v110, v111\n"
    "v_mfma_f32_16x16x32_bf16 v[212:215], a[116:119], v[80:83], v[212:215]\n"
    "v_exp_f32 v116, v116\n"
    "v_mfma_f32_16x16x32_bf16 v[244:247], a[116:119], v[88:91], v[244:247]\n"
    "s_waitcnt lgkmcnt(10)\n"
    "buffer_load_dwordx4 v45, s[60:63], s92 offen offset:1024 lds\n"
    "v_mfma_f32_16x16x32_bf16 v[152:155], a[120:123], v[64:67], v[152:155]\n"
    "v_exp_f32 v117, v117\n"
    "v_mfma_f32_16x16x32_bf16 v[184:187], a[120:123], v[72:75], v[184:187]\n"
    "buffer_load_dwordx4 v44, s[60:63], s92 offen offset:2048 lds\n"
    "v_cvt_pk_bf16_f32 v114, v116, v117\n"
    "v_mfma_f32_16x16x32_bf16 v[216:219], a[120:123], v[80:83], v[216:219]\n"
    "v_exp_f32 v118, v118\n"
    "v_mfma_f32_16x16x32_bf16 v[248:251], a[120:123], v[88:91], v[248:251]\n"
    "s_waitcnt lgkmcnt(8)\n"
    "s_cmp_eq_u32 s88, 1\n"
    "s_cselect_b32 s83, 0, 0x40000000\n"
    "buffer_load_dwordx4 v45, s[60:63], s92 offen offset:3072 lds\n"
    "s_add_u32 s92, s92, 16384\n"
    "v_mfma_f32_16x16x32_bf16 v[156:159], a[124:127], v[64:67], v[156:159]\n"
    "buffer_load_dword a152, v12, s[32:35], s83 offen\n"
    "v_mfma_f32_16x16x32_bf16 v[188:191], a[124:127], v[72:75], v[188:191]\n"
    "v_mfma_f32_16x16x32_bf16 v[220:223], a[124:127], v[80:83], v[220:223]\n"
    "v_mfma_f32_16x16x32_bf16 v[252:255], a[124:127], v[88:91], v[252:255]\n"
    "s_waitcnt lgkmcnt(7)\n"
    "v_mfma_f32_16x16x32_bf16 v[64:67], a[64:67], a[0:3], v[48:51]\n"
    "s_waitcnt lgkmcnt(6)\n"
    "v_exp_f32 v119, v119\n"
    "v_mfma_f32_16x16x32_bf16 v[64:67], a[68:71], a[4:7], v[64:67]\n"
    "s_waitcnt lgkmcnt(5)\n"
    "ds_read_b64_tr_b16 a[96:97], v24 offset:49152\n"
    "v_cvt_pk_bf16_f32 v115, v118, v119\n"
    "v_mfma_f32_16x16x32_bf16 v[64:67], a[72:75], a[8:11], v[64:67]\n"
    "s_waitcnt lgkmcnt(5)\n"
    "v_exp_f32 v124, v124\n"
    "v_mfma_f32_16x16x32_bf16 v[64:67], a[76:79], a[12:15], v[64:67]\n"
    "ds_read_b64_tr_b16 a[98:99], v24 offset:53248\n"
    "v_mfma_f32_16x16x32_bf16 v[72:75], a[64:67], a[16:19], v[52:55]\n"
    "v_exp_f32 v125, v125\n"
    "v_mfma_f32_16x16x32_bf16 v[72:75], a[68:71], a[20:23], v[72:75]\n"
    "ds_read_b64_tr_b16 a[100:101], v25 offset:49152\n"
    "v_cvt_pk_bf16_f32 v122, v124, v125\n"
    "v_mfma_f32_16x16x32_bf16 v[72:75], a[72:75], a[24:27], v[72:75]\n"
    "v_exp_f32 v126, v126\n"
    "v_mfma_f32_16x16x32_bf16 v[72:75], a[76:79], a[28:31], v[72:75]\n"
    "ds_read_b64_tr_b16 a[102:103], v25 offset:53248\n"
    "v_mfma_f32_16x16x32_bf16 v[80:83], a[64:67], a[32:35], v[56:59]\n"
    "v_exp_f32 v127, v127\n"
    "v_mfma_f32_16x16x32_bf16 v[80:83], a[68:71], a[36:39], v[80:83]\n"
    "ds_read_b64_tr_b16 a[104:105], v26 offset:49152\n"
    "v_cvt_pk_bf16_f32 v123, v126, v127\n"
    "v_mfma_f32_16x16x32_bf16 v[80:83], a[72:75], a[40:43], v[80:83]\n"
    "v_exp_f32 v64, v64\n"
    "v_mfma_f32_16x16x32_bf16 v[80:83], a[76:79], a[44:47], v[80:83]\n"
    "ds_read_b64_tr_b16 a[106:107], v26 offset:53248\n"
    "v_mfma_f32_16x16x32_bf16 v[88:91], a[64:67], a[48:51], v[60:63]\n"
    "v_exp_f32 v65, v65\n"
    "v_mfma_f32_16x16x32_bf16 v[88:91], a[68:71], a[52:55], v[88:91]\n"
    "ds_read_b64_tr_b16 a[108:109], v27 offset:49152\n"
    "v_cvt_pk_bf16_f32 v64, v64, v65\n"
    "v_mfma_f32_16x16x32_bf16 v[88:91], a[72:75], a[56:59], v[88:91]\n"
    "v_exp_f32 v66, v66\n"
    "v_mfma_f32_16x16x32_bf16 v[88:91], a[76:79], a[60:63], v[88:91]\n"
    "s_waitcnt lgkmcnt(10)\n"
    "ds_read_b64_tr_b16 a[110:111], v27 offset:53248\n"
    "v_mfma_f32_16x16x32_bf16 v[68:71], a[80:83], a[0:3], v[48:51]\n"
    "s_waitcnt lgkmcnt(10)\n"
    "v_exp_f32 v67, v67\n"
    "v_mfma_f32_16x16x32_bf16 v[68:71], a[84:87], a[4:7], v[68:71]\n"
    "s_waitcnt lgkmcnt(9)\n"
    "ds_read_b64_tr_b16 a[112:113], v28 offset:49152\n"
    "v_cvt_pk_bf16_f32 v65, v66, v67\n"
    "v_mfma_f32_16x16x32_bf16 v[68:71], a[88:91], a[8:11], v[68:71]\n"
    "s_waitcnt lgkmcnt(9)\n"
    "v_exp_f32 v72, v72\n"
    "v_mfma_f32_16x16x32_bf16 v[68:71], a[92:95], a[12:15], v[68:71]\n"
    "ds_read_b64_tr_b16 a[114:115], v28 offset:53248\n"
    "v_mfma_f32_16x16x32_bf16 v[76:79], a[80:83], a[16:19], v[52:55]\n"
    "v_exp_f32 v73, v73\n"
    "v_mfma_f32_16x16x32_bf16 v[76:79], a[84:87], a[20:23], v[76:79]\n"
    "ds_read_b64_tr_b16 a[116:117], v29 offset:49152\n"
    "v_cvt_pk_bf16_f32 v72, v72, v73\n"
    "v_mfma_f32_16x16x32_bf16 v[76:79], a[88:91], a[24:27], v[76:79]\n"
    "v_exp_f32 v74, v74\n"
    "v_mfma_f32_16x16x32_bf16 v[76:79], a[92:95], a[28:31], v[76:79]\n"
    "ds_read_b64_tr_b16 a[118:119], v29 offset:53248\n"
    "v_mfma_f32_16x16x32_bf16 v[84:87], a[80:83], a[32:35], v[56:59]\n"
    "v_exp_f32 v75, v75\n"
    "v_mfma_f32_16x16x32_bf16 v[84:87], a[84:87], a[36:39], v[84:87]\n"
    "ds_read_b64_tr_b16 a[120:121], v30 offset:49152\n"
    "v_cvt_pk_bf16_f32 v73, v74, v75\n"
    "v_mfma_f32_16x16x32_bf16 v[84:87], a[88:91], a[40:43], v[84:87]\n"
    "v_exp_f32 v80, v80\n"
    "v_mfma_f32_16x16x32_bf16 v[84:87], a[92:95], a[44:47], v[84:87]\n"
    "ds_read_b64_tr_b16 a[122:123], v30 offset:53248\n"
    "v_mfma_f32_16x16x32_bf16 v[92:95], a[80:83], a[48:51], v[60:63]\n"
    "v_exp_f32 v81, v81\n"
    "v_mfma_f32_16x16x32_bf16 v[92:95], a[84:87], a[52:55], v[92:95]\n"
    "ds_read_b64_tr_b16 a[124:125], v31 offset:49152\n"
    "v_cvt_pk_bf16_f32 v80, v80, v81\n"
    "v_mfma_f32_16x16x32_bf16 v[92:95], a[88:91], a[56:59], v[92:95]\n"
    "v_exp_f32 v82, v82\n"
    "v_mfma_f32_16x16x32_bf16 v[92:95], a[92:95], a[60:63], v[92:95]\n"
    "ds_read_b64_tr_b16 a[126:127], v31 offset:53248\n"
    "v_mfma_f32_16x16x32_bf16 a[128:131], a[144:147], v[96:99], a[128:131]\n"
    "v_exp_f32 v83, v83\n"
    "v_mfma_f32_16x16x32_bf16 a[132:135], a[144:147], v[104:107], a[132:135]\n"
    "v_cvt_pk_bf16_f32 v81, v82, v83\n"
    "v_mfma_f32_16x16x32_bf16 a[136:139], a[144:147], v[112:115], a[136:139]\n"
    "v_exp_f32 v88, v88\n"
    "v_mfma_f32_16x16x32_bf16 a[140:143], a[144:147], v[120:123], a[140:143]\n"
    "s_waitcnt lgkmcnt(14)\n"
    "v_mfma_f32_16x16x32_bf16 v[128:131], a[96:99], v[96:99], v[128:131]\n"
    "v_exp_f32 v89, v89\n"
    "v_mfma_f32_16x16x32_bf16 v[160:163], a[96:99], v[104:107], v[160:163]\n"
    "s_waitcnt vmcnt(10)\n"
    "s_barrier\n"
    "v_cvt_pk_bf16_f32 v88, v88, v89\n"
    "v_mfma_f32_16x16x32_bf16 v[192:195], a[96:99], v[112:115], v[192:195]\n"
    "v_exp_f32 v90, v90\n"
    "v_mfma_f32_16x16x32_bf16 v[224:227], a[96:99], v[120:123], v[224:227]\n"
    "s_waitcnt lgkmcnt(12)\n"
    "v_mfma_f32_16x16x32_bf16 v[132:135], a[100:103], v[96:99], v[132:135]\n"
    "v_exp_f32 v91, v91\n"
    "v_mfma_f32_16x16x32_bf16 v[164:167], a[100:103], v[104:107], v[164:167]\n"
    "ds_read_b128 a[64:67], v20 offset:32768\n"
    "v_cvt_pk_bf16_f32 v89, v90, v91\n"
    "v_mfma_f32_16x16x32_bf16 v[196:199], a[100:103], v[112:115], v[196:199]\n"
    "v_exp_f32 v68, v68\n"
    "v_mfma_f32_16x16x32_bf16 v[228:231], a[100:103], v[120:123], v[228:231]\n"
    "s_waitcnt lgkmcnt(11)\n"
    "ds_read_b128 a[68:71], v21 offset:32768\n"
    "v_mfma_f32_16x16x32_bf16 v[136:139], a[104:107], v[96:99], v[136:139]\n"
    "v_exp_f32 v69, v69\n"
    "v_mfma_f32_16x16x32_bf16 v[168:171], a[104:107], v[104:107], v[168:171]\n"
    "ds_read_b128 a[72:75], v22 offset:32768\n"
    "v_cvt_pk_bf16_f32 v66, v68, v69\n"
    "v_mfma_f32_16x16x32_bf16 v[200:203], a[104:107], v[112:115], v[200:203]\n"
    "v_exp_f32 v70, v70\n"
    "v_mfma_f32_16x16x32_bf16 v[232:235], a[104:107], v[120:123], v[232:235]\n"
    "s_waitcnt lgkmcnt(11)\n"
    "ds_read_b128 a[76:79], v23 offset:32768\n"
    "v_mfma_f32_16x16x32_bf16 v[140:143], a[108:111], v[96:99], v[140:143]\n"
    "v_exp_f32 v71, v71\n"
    "v_mfma_f32_16x16x32_bf16 v[172:175], a[108:111], v[104:107], v[172:175]\n"
    "ds_read_b128 a[80:83], v20 offset:36864\n"
    "v_cvt_pk_bf16_f32 v67, v70, v71\n"
    "v_mfma_f32_16x16x32_bf16 v[204:207], a[108:111], v[112:115], v[204:207]\n"
    "v_exp_f32 v76, v76\n"
    "v_mfma_f32_16x16x32_bf16 v[236:239], a[108:111], v[120:123], v[236:239]\n"
    "s_waitcnt lgkmcnt(11)\n"
    "ds_read_b128 a[84:87], v21 offset:36864\n"
    "v_mfma_f32_16x16x32_bf16 v[144:147], a[112:115], v[96:99], v[144:147]\n"
    "v_exp_f32 v77, v77\n"
    "v_mfma_f32_16x16x32_bf16 v[176:179], a[112:115], v[104:107], v[176:179]\n"
    "ds_read_b128 a[88:91], v22 offset:36864\n"
    "v_cvt_pk_bf16_f32 v74, v76, v77\n"
    "v_mfma_f32_16x16x32_bf16 v[208:211], a[112:115], v[112:115], v[208:211]\n"
    "v_exp_f32 v78, v78\n"
    "v_mfma_f32_16x16x32_bf16 v[240:243], a[112:115], v[120:123], v[240:243]\n"
    "s_waitcnt lgkmcnt(11)\n"
    "ds_read_b128 a[92:95], v23 offset:36864\n"
    "v_mfma_f32_16x16x32_bf16 v[148:151], a[116:119], v[96:99], v[148:151]\n"
    "s_add_u32 m0, s86, 98304\n"
    "v_exp_f32 v79, v79\n"
    "v_mfma_f32_16x16x32_bf16 v[180:183], a[116:119], v[104:107], v[180:183]\n"
    "buffer_load_dwordx4 v40, s[56:59], s92 offen lds\n"
    "v_cvt_pk_bf16_f32 v75, v78, v79\n"
    "v_mfma_f32_16x16x32_bf16 v[212:215], a[116:119], v[112:115], v[212:215]\n"
    "v_exp_f32 v84, v84\n"
    "v_mfma_f32_16x16x32_bf16 v[244:247], a[116:119], v[120:123], v[244:247]\n"
    "s_waitcnt lgkmcnt(10)\n"
    "buffer_load_dwordx4 v41, s[56:59], s92 offen offset:1024 lds\n"
    "v_mfma_f32_16x16x32_bf16 v[152:155], a[120:123], v[96:99], v[152:155]\n"
    "v_exp_f32 v85, v85\n"
    "v_mfma_f32_16x16x32_bf16 v[184:187], a[120:123], v[104:107], v[184:187]\n"
    "buffer_load_dwordx4 v42, s[56:59], s92 offen offset:2048 lds\n"
    "v_cvt_pk_bf16_f32 v82, v84, v85\n"
    "v_mfma_f32_16x16x32_bf16 v[216:219], a[120:123], v[112:115], v[216:219]\n"
    "v_exp_f32 v86, v86\n"
    "v_mfma_f32_16x16x32_bf16 v[248:251], a[120:123], v[120:123], v[248:251]\n"
    "s_waitcnt lgkmcnt(8)\n"
    "buffer_load_dwordx4 v43, s[56:59], s92 offen offset:3072 lds\n"
    "v_mfma_f32_16x16x32_bf16 v[156:159], a[124:127], v[96:99], v[156:159]\n"
    "v_mfma_f32_16x16x32_bf16 v[188:191], a[124:127], v[104:107], v[188:191]\n"
    "v_mfma_f32_16x16x32_bf16 v[220:223], a[124:127], v[112:115], v[220:223]\n"
    "v_mfma_f32_16x16x32_bf16 v[252:255], a[124:127], v[120:123], v[252:255]\n"
    "s_waitcnt lgkmcnt(7)\n"
    "v_mfma_f32_16x16x32_bf16 v[96:99], a[64:67], a[0:3], v[48:51]\n"
    "s_waitcnt lgkmcnt(6)\n"
    "v_exp_f32 v87, v87\n"
    "v_mfma_f32_16x16x32_bf16 v[96:99], a[68:71], a[4:7], v[96:99]\n"
    "s_waitcnt lgkmcnt(5)\n"
    "ds_read_b64_tr_b16 a[96:97], v24 offset:57344\n"
    "v_cvt_pk_bf16_f32 v83, v86, v87\n"
    "v_mfma_f32_16x16x32_bf16 v[96:99], a[72:75], a[8:11], v[96:99]\n"
    "s_waitcnt lgkmcnt(5)\n"
    "v_exp_f32 v92, v92\n"
    "v_mfma_f32_16x16x32_bf16 v[96:99], a[76:79], a[12:15], v[96:99]\n"
    "ds_read_b64_tr_b16 a[98:99], v24 offset:61440\n"
    "v_mfma_f32_16x16x32_bf16 v[104:107], a[64:67], a[16:19], v[52:55]\n"
    "v_exp_f32 v93, v93\n"
    "v_mfma_f32_16x16x32_bf16 v[104:107], a[68:71], a[20:23], v[104:107]\n"
    "ds_read_b64_tr_b16 a[100:101], v25 offset:57344\n"
    "v_cvt_pk_bf16_f32 v90, v92, v93\n"
    "v_mfma_f32_16x16x32_bf16 v[104:107], a[72:75], a[24:27], v[104:107]\n"
    "v_exp_f32 v94, v94\n"
    "v_mfma_f32_16x16x32_bf16 v[104:107], a[76:79], a[28:31], v[104:107]\n"
    "ds_read_b64_tr_b16 a[102:103], v25 offset:61440\n"
    "v_mfma_f32_16x16x32_bf16 v[112:115], a[64:67], a[32:35], v[56:59]\n"
    "v_exp_f32 v95, v95\n"
    "v_mfma_f32_16x16x32_bf16 v[112:115], a[68:71], a[36:39], v[112:115]\n"
    "ds_read_b64_tr_b16 a[104:105], v26 offset:57344\n"
    "v_cvt_pk_bf16_f32 v91, v94, v95\n"
    "v_mfma_f32_16x16x32_bf16 v[112:115], a[72:75], a[40:43], v[112:115]\n"
    "v_exp_f32 v96, v96\n"
    "v_mfma_f32_16x16x32_bf16 v[112:115], a[76:79], a[44:47], v[112:115]\n"
    "ds_read_b64_tr_b16 a[106:107], v26 offset:61440\n"
    "v_mfma_f32_16x16x32_bf16 v[120:123], a[64:67], a[48:51], v[60:63]\n"
    "v_exp_f32 v97, v97\n"
    "v_mfma_f32_16x16x32_bf16 v[120:123], a[68:71], a[52:55], v[120:123]\n"
    "ds_read_b64_tr_b16 a[108:109], v27 offset:57344\n"
    "v_cvt_pk_bf16_f32 v96, v96, v97\n"
    "v_mfma_f32_16x16x32_bf16 v[120:123], a[72:75], a[56:59], v[120:123]\n"
    "v_exp_f32 v98, v98\n"
    "v_mfma_f32_16x16x32_bf16 v[120:123], a[76:79], a[60:63], v[120:123]\n"
    "s_waitcnt lgkmcnt(10)\n"
    "ds_read_b64_tr_b16 a[110:111], v27 offset:61440\n"
    "v_mfma_f32_16x16x32_bf16 v[100:103], a[80:83], a[0:3], v[48:51]\n"
    "s_waitcnt lgkmcnt(10)\n"
    "v_exp_f32 v99, v99\n"
    "v_mfma_f32_16x16x32_bf16 v[100:103], a[84:87], a[4:7], v[100:103]\n"
    "s_waitcnt lgkmcnt(9)\n"
    "ds_read_b64_tr_b16 a[112:113], v28 offset:57344\n"
    "v_cvt_pk_bf16_f32 v97, v98, v99\n"
    "v_mfma_f32_16x16x32_bf16 v[100:103], a[88:91], a[8:11], v[100:103]\n"
    "s_waitcnt lgkmcnt(9)\n"
    "v_exp_f32 v104, v104\n"
    "v_mfma_f32_16x16x32_bf16 v[100:103], a[92:95], a[12:15], v[100:103]\n"
    "ds_read_b64_tr_b16 a[114:115], v28 offset:61440\n"
    "v_mfma_f32_16x16x32_bf16 v[108:111], a[80:83], a[16:19], v[52:55]\n"
    "v_exp_f32 v105, v105\n"
    "v_mfma_f32_16x16x32_bf16 v[108:111], a[84:87], a[20:23], v[108:111]\n"
    "ds_read_b64_tr_b16 a[116:117], v29 offset:57344\n"
    "v_cvt_pk_bf16_f32 v104, v104, v105\n"
    "v_mfma_f32_16x16x32_bf16 v[108:111], a[88:91], a[24:27], v[108:111]\n"
    "v_exp_f32 v106, v106\n"
    "v_mfma_f32_16x16x32_bf16 v[108:111], a[92:95], a[28:31], v[108:111]\n"
    "ds_read_b64_tr_b16 a[118:119], v29 offset:61440\n"
    "v_mfma_f32_16x16x32_bf16 v[116:119], a[80:83], a[32:35], v[56:59]\n"
    "v_exp_f32 v107, v107\n"
    "v_mfma_f32_16x16x32_bf16 v[116:119], a[84:87], a[36:39], v[116:119]\n"
    "ds_read_b64_tr_b16 a[120:121], v30 offset:57344\n"
    "v_cvt_pk_bf16_f32 v105, v106, v107\n"
    "v_mfma_f32_16x16x32_bf16 v[116:119], a[88:91], a[40:43], v[116:119]\n"
    "v_exp_f32 v112, v112\n"
    "v_mfma_f32_16x16x32_bf16 v[116:119], a[92:95], a[44:47], v[116:119]\n"
    "ds_read_b64_tr_b16 a[122:123], v30 offset:61440\n"
    "v_mfma_f32_16x16x32_bf16 v[124:127], a[80:83], a[48:51], v[60:63]\n"
    "v_exp_f32 v113, v113\n"
    "v_mfma_f32_16x16x32_bf16 v[124:127], a[84:87], a[52:55], v[124:127]\n"
    "ds_read_b64_tr_b16 a[124:125], v31 offset:57344\n"
    "v_cvt_pk_bf16_f32 v112, v112, v113\n"
    "v_mfma_f32_16x16x32_bf16 v[124:127], a[88:91], a[56:59], v[124:127]\n"
    "v_exp_f32 v114, v114\n"
    "v_mfma_f32_16x16x32_bf16 v[124:127], a[92:95], a[60:63], v[124:127]\n"
    "ds_read_b64_tr_b16 a[126:127], v31 offset:61440\n"
    "v_mfma_f32_16x16x32_bf16 a[128:131], a[144:147], v[64:67], a[128:131]\n"
    "v_exp_f32 v115, v115\n"
    "v_mfma_f32_16x16x32_bf16 a[132:135], a[144:147], v[72:75], a[132:135]\n"
    "v_cvt_pk_bf16_f32 v113, v114, v115\n"
    "v_mfma_f32_16x16x32_bf16 a[136:139], a[144:147], v[80:83], a[136:139]\n"
    "v_exp_f32 v120, v120\n"
    "v_mfma_f32_16x16x32_bf16 a[140:143], a[144:147], v[88:91], a[140:143]\n"
    "s_waitcnt lgkmcnt(14)\n"
    "v_mfma_f32_16x16x32_bf16 v[128:131], a[96:99], v[64:67], v[128:131]\n"
    "v_exp_f32 v121, v121\n"
    "v_mfma_f32_16x16x32_bf16 v[160:163], a[96:99], v[72:75], v[160:163]\n"
    "v_cvt_pk_bf16_f32 v120, v120, v121\n"
    "v_mfma_f32_16x16x32_bf16 v[192:195], a[96:99], v[80:83], v[192:195]\n"
    "v_exp_f32 v122, v122\n"
    "v_mfma_f32_16x16x32_bf16 v[224:227], a[96:99], v[88:91], v[224:227]\n"
    "s_waitcnt lgkmcnt(12)\n"
    "v_mfma_f32_16x16x32_bf16 v[132:135], a[100:103], v[64:67], v[132:135]\n"
    "v_exp_f32 v123, v123\n"
    "v_mfma_f32_16x16x32_bf16 v[164:167], a[100:103], v[72:75], v[164:167]\n"
    "ds_read_b128 a[64:67], v20 offset:40960\n"
    "v_cvt_pk_bf16_f32 v121, v122, v123\n"
    "v_mfma_f32_16x16x32_bf16 v[196:199], a[100:103], v[80:83], v[196:199]\n"
    "v_exp_f32 v100, v100\n"
    "v_mfma_f32_16x16x32_bf16 v[228:231], a[100:103], v[88:91], v[228:231]\n"
    "s_waitcnt lgkmcnt(11)\n"
    "ds_read_b128 a[68:71], v21 offset:40960\n"
    "v_mfma_f32_16x16x32_bf16 v[136:139], a[104:107], v[64:67], v[136:139]\n"
    "v_exp_f32 v101, v101\n"
    "v_mfma_f32_16x16x32_bf16 v[168:171], a[104:107], v[72:75], v[168:171]\n"
    "ds_read_b128 a[72:75], v22 offset:40960\n"
    "v_cvt_pk_bf16_f32 v98, v100, v101\n"
    "v_mfma_f32_16x16x32_bf16 v[200:203], a[104:107], v[80:83], v[200:203]\n"
    "v_exp_f32 v102, v102\n"
    "v_mfma_f32_16x16x32_bf16 v[232:235], a[104:107], v[88:91], v[232:235]\n"
    "s_waitcnt lgkmcnt(11)\n"
    "ds_read_b128 a[76:79], v23 offset:40960\n"
    "v_mfma_f32_16x16x32_bf16 v[140:143], a[108:111], v[64:67], v[140:143]\n"
    "v_exp_f32 v103, v103\n"
    "v_mfma_f32_16x16x32_bf16 v[172:175], a[108:111], v[72:75], v[172:175]\n"
    "ds_read_b128 a[80:83], v20 offset:45056\n"
    "v_cvt_pk_bf16_f32 v99, v102, v103\n"
    "v_mfma_f32_16x16x32_bf16 v[204:207], a[108:111], v[80:83], v[204:207]\n"
    "v_exp_f32 v108, v108\n"
    "v_mfma_f32_16x16x32_bf16 v[236:239], a[108:111], v[88:91], v[236:239]\n"
    "s_waitcnt lgkmcnt(11)\n"
    "ds_read_b128 a[84:87], v21 offset:45056\n"
    "v_mfma_f32_16x16x32_bf16 v[144:147], a[112:115], v[64:67], v[144:147]\n"
    "v_exp_f32 v109, v109\n"
    "v_mfma_f32_16x16x32_bf16 v[176:179], a[112:115], v[72:75], v[176:179]\n"
    "ds_read_b128 a[88:91], v22 offset:45056\n"
    "v_cvt_pk_bf16_f32 v106, v108, v109\n"
    "v_mfma_f32_16x16x32_bf16 v[208:211], a[112:115], v[80:83], v[208:211]\n"
    "v_exp_f32 v110, v110\n"
    "v_mfma_f32_16x16x32_bf16 v[240:243], a[112:115], v[88:91], v[240:243]\n"
    "s_waitcnt lgkmcnt(11)\n"
    "ds_read_b128 a[92:95], v23 offset:45056\n"
    "v_mfma_f32_16x16x32_bf16 v[148:151], a[116:119], v[64:67], v[148:151]\n"
    "s_add_u32 m0, s86, 114688\n"
    "v_exp_f32 v111, v111\n"
    "v_mfma_f32_16x16x32_bf16 v[180:183], a[116:119], v[72:75], v[180:183]\n"
    "buffer_load_dwordx4 v44, s[60:63], s92 offen lds\n"
    "v_cvt_pk_bf16_f32 v107, v110, v111\n"
    "v_mfma_f32_16x16x32_bf16 v[212:215], a[116:119], v[80:83], v[212:215]\n"
    "v_exp_f32 v116, v116\n"
    "v_mfma_f32_16x16x32_bf16 v[244:247], a[116:119], v[88:91], v[244:247]\n"
    "s_waitcnt lgkmcnt(10)\n"
    "buffer_load_dwordx4 v45, s[60:63], s92 offen offset:1024 lds\n"
    "v_mfma_f32_16x16x32_bf16 v[152:155], a[120:123], v[64:67], v[152:155]\n"
    "v_exp_f32 v117, v117\n"
    "v_mfma_f32_16x16x32_bf16 v[184:187], a[120:123], v[72:75], v[184:187]\n"
    "buffer_load_dwordx4 v44, s[60:63], s92 offen offset:2048 lds\n"
    "v_cvt_pk_bf16_f32 v114, v116, v117\n"
    "v_mfma_f32_16x16x32_bf16 v[216:219], a[120:123], v[80:83], v[216:219]\n"
    "s_add_u32 s83, s39, 8192\n"
    "v_exp_f32 v118, v118\n"
    "v_mfma_f32_16x16x32_bf16 v[248:251], a[120:123], v[88:91], v[248:251]\n"
    "s_waitcnt lgkmcnt(8)\n"
    "s_cmp_eq_u32 s88, 2\n"
    "s_cselect_b32 s83, s83, 0x40000000\n"
    "buffer_load_dwordx4 v45, s[60:63], s92 offen offset:3072 lds\n"
    "s_add_u32 s92, s92, 16384\n"
    "v_mfma_f32_16x16x32_bf16 v[156:159], a[124:127], v[64:67], v[156:159]\n"
    "buffer_load_dword a152, v12, s[40:43], s83 offen\n"
    "v_mfma_f32_16x16x32_bf16 v[188:191], a[124:127], v[72:75], v[188:191]\n"
    "v_mfma_f32_16x16x32_bf16 v[220:223], a[124:127], v[80:83], v[220:223]\n"
    "v_mfma_f32_16x16x32_bf16 v[252:255], a[124:127], v[88:91], v[252:255]\n"
    "s_waitcnt lgkmcnt(7)\n"
    "v_mfma_f32_16x16x32_bf16 v[64:67], a[64:67], a[0:3], v[48:51]\n"
    "s_waitcnt lgkmcnt(6)\n"
    "v_exp_f32 v119, v119\n"
    "v_mfma_f32_16x16x32_bf16 v[64:67], a[68:71], a[4:7], v[64:67]\n"
    "s_waitcnt lgkmcnt(5)\n"
    "ds_read_b64_tr_b16 a[96:97], v32 offset:49152\n"
    "v_cvt_pk_bf16_f32 v115, v118, v119\n"
    "v_mfma_f32_16x16x32_bf16 v[64:67], a[72:75], a[8:11], v[64:67]\n"
    "s_waitcnt lgkmcnt(5)\n"
    "v_exp_f32 v124, v124\n"
    "v_mfma_f32_16x16x32_bf16 v[64:67], a[76:79], a[12:15], v[64:67]\n"
    "ds_read_b64_tr_b16 a[98:99], v32 offset:53248\n"
    "v_mfma_f32_16x16x32_bf16 v[72:75], a[64:67], a[16:19], v[52:55]\n"
    "v_exp_f32 v125, v125\n"
    "v_mfma_f32_16x16x32_bf16 v[72:75], a[68:71], a[20:23], v[72:75]\n"
    "ds_read_b64_tr_b16 a[100:101], v33 offset:49152\n"
    "v_cvt_pk_bf16_f32 v122, v124, v125\n"
    "v_mfma_f32_16x16x32_bf16 v[72:75], a[72:75], a[24:27], v[72:75]\n"
    "v_exp_f32 v126, v126\n"
    "v_mfma_f32_16x16x32_bf16 v[72:75], a[76:79], a[28:31], v[72:75]\n"
    "ds_read_b64_tr_b16 a[102:103], v33 offset:53248\n"
    "v_mfma_f32_16x16x32_bf16 v[80:83], a[64:67], a[32:35], v[56:59]\n"
    "v_exp_f32 v127, v127\n"
    "v_mfma_f32_16x16x32_bf16 v[80:83], a[68:71], a[36:39], v[80:83]\n"
    "ds_read_b64_tr_b16 a[104:105], v34 offset:49152\n"
    "v_cvt_pk_bf16_f32 v123, v126, v127\n"
    "v_mfma_f32_16x16x32_bf16 v[80:83], a[72:75], a[40:43], v[80:83]\n"
    "v_exp_f32 v64, v64\n"
    "v_mfma_f32_16x16x32_bf16 v[80:83], a[76:79], a[44:47], v[80:83]\n"
    "ds_read_b64_tr_b16 a[106:107], v34 offset:53248\n"
    "v_mfma_f32_16x16x32_bf16 v[88:91], a[64:67], a[48:51], v[60:63]\n"
    "v_exp_f32 v65, v65\n"
    "v_mfma_f32_16x16x32_bf16 v[88:91], a[68:71], a[52:55], v[88:91]\n"
    "ds_read_b64_tr_b16 a[108:109], v35 offset:49152\n"
    "v_cvt_pk_bf16_f32 v64, v64, v65\n"
    "v_mfma_f32_16x16x32_bf16 v[88:91], a[72:75], a[56:59], v[88:91]\n"
    "v_exp_f32 v66, v66\n"
    "v_mfma_f32_16x16x32_bf16 v[88:91], a[76:79], a[60:63], v[88:91]\n"
    "s_waitcnt lgkmcnt(10)\n"
    "ds_read_b64_tr_b16 a[110:111], v35 offset:53248\n"
    "v_mfma_f32_16x16x32_bf16 v[68:71], a[80:83], a[0:3], v[48:51]\n"
    "s_waitcnt lgkmcnt(10)\n"
    "v_exp_f32 v67, v67\n"
    "v_mfma_f32_16x16x32_bf16 v[68:71], a[84:87], a[4:7], v[68:71]\n"
    "s_waitcnt lgkmcnt(9)\n"
    "ds_read_b64_tr_b16 a[112:113], v36 offset:49152\n"
    "v_cvt_pk_bf16_f32 v65, v66, v67\n"
    "v_mfma_f32_16x16x32_bf16 v[68:71], a[88:91], a[8:11], v[68:71]\n"
    "s_waitcnt lgkmcnt(9)\n"
    "v_exp_f32 v72, v72\n"
    "v_mfma_f32_16x16x32_bf16 v[68:71], a[92:95], a[12:15], v[68:71]\n"
    "ds_read_b64_tr_b16 a[114:115], v36 offset:53248\n"
    "v_mfma_f32_16x16x32_bf16 v[76:79], a[80:83], a[16:19], v[52:55]\n"
    "v_exp_f32 v73, v73\n"
    "v_mfma_f32_16x16x32_bf16 v[76:79], a[84:87], a[20:23], v[76:79]\n"
    "ds_read_b64_tr_b16 a[116:117], v37 offset:49152\n"
    "v_cvt_pk_bf16_f32 v72, v72, v73\n"
    "v_mfma_f32_16x16x32_bf16 v[76:79], a[88:91], a[24:27], v[76:79]\n"
    "v_exp_f32 v74, v74\n"
    "v_mfma_f32_16x16x32_bf16 v[76:79], a[92:95], a[28:31], v[76:79]\n"
    "ds_read_b64_tr_b16 a[118:119], v37 offset:53248\n"
    "v_mfma_f32_16x16x32_bf16 v[84:87], a[80:83], a[32:35], v[56:59]\n"
    "v_exp_f32 v75, v75\n"
    "v_mfma_f32_16x16x32_bf16 v[84:87], a[84:87], a[36:39], v[84:87]\n"
    "ds_read_b64_tr_b16 a[120:121], v38 offset:49152\n"
    "v_cvt_pk_bf16_f32 v73, v74, v75\n"
    "v_mfma_f32_16x16x32_bf16 v[84:87], a[88:91], a[40:43], v[84:87]\n"
    "v_exp_f32 v80, v80\n"
    "v_mfma_f32_16x16x32_bf16 v[84:87], a[92:95], a[44:47], v[84:87]\n"
    "ds_read_b64_tr_b16 a[122:123], v38 offset:53248\n"
    "v_mfma_f32_16x16x32_bf16 v[92:95], a[80:83], a[48:51], v[60:63]\n"
    "v_exp_f32 v81, v81\n"
    "v_mfma_f32_16x16x32_bf16 v[92:95], a[84:87], a[52:55], v[92:95]\n"
    "ds_read_b64_tr_b16 a[124:125], v39 offset:49152\n"
    "v_cvt_pk_bf16_f32 v80, v80, v81\n"
    "v_mfma_f32_16x16x32_bf16 v[92:95], a[88:91], a[56:59], v[92:95]\n"
    "v_exp_f32 v82, v82\n"
    "v_mfma_f32_16x16x32_bf16 v[92:95], a[92:95], a[60:63], v[92:95]\n"
    "ds_read_b64_tr_b16 a[126:127], v39 offset:53248\n"
    "v_mfma_f32_16x16x32_bf16 a[128:131], a[144:147], v[96:99], a[128:131]\n"
    "v_exp_f32 v83, v83\n"
    "v_mfma_f32_16x16x32_bf16 a[132:135], a[144:147], v[104:107], a[132:135]\n"
    "v_cvt_pk_bf16_f32 v81, v82, v83\n"
    "v_mfma_f32_16x16x32_bf16 a[136:139], a[144:147], v[112:115], a[136:139]\n"
    "v_exp_f32 v88, v88\n"
    "v_mfma_f32_16x16x32_bf16 a[140:143], a[144:147], v[120:123], a[140:143]\n"
    "s_waitcnt lgkmcnt(14)\n"
    "v_mfma_f32_16x16x32_bf16 v[128:131], a[96:99], v[96:99], v[128:131]\n"
    "v_exp_f32 v89, v89\n"
    "v_mfma_f32_16x16x32_bf16 v[160:163], a[96:99], v[104:107], v[160:163]\n"
    "s_waitcnt vmcnt(10)\n"
    "s_barrier\n"
    "v_cvt_pk_bf16_f32 v88, v88, v89\n"
    "v_mfma_f32_16x16x32_bf16 v[192:195], a[96:99], v[112:115], v[192:195]\n"
    "v_exp_f32 v90, v90\n"
    "v_mfma_f32_16x16x32_bf16 v[224:227], a[96:99], v[120:123], v[224:227]\n"
    "s_waitcnt lgkmcnt(12)\n"
    "v_mfma_f32_16x16x32_bf16 v[132:135], a[100:103], v[96:99], v[132:135]\n"
    "v_exp_f32 v91, v91\n"
    "v_mfma_f32_16x16x32_bf16 v[164:167], a[100:103], v[104:107], v[164:167]\n"
    "ds_read_b128 a[64:67], v20 offset:0\n"
    "v_cvt_pk_bf16_f32 v89, v90, v91\n"
    "v_mfma_f32_16x16x32_bf16 v[196:199], a[100:103], v[112:115], v[196:199]\n"
    "v_exp_f32 v68, v68\n"
    "v_mfma_f32_16x16x32_bf16 v[228:231], a[100:103], v[120:123], v[228:231]\n"
    "s_waitcnt lgkmcnt(11)\n"
    "ds_read_b128 a[68:71], v21 offset:0\n"
    "v_mfma_f32_16x16x32_bf16 v[136:139], a[104:107], v[96:99], v[136:139]\n"
    "v_exp_f32 v69, v69\n"
    "v_mfma_f32_16x16x32_bf16 v[168:171], a[104:107], v[104:107], v[168:171]\n"
    "ds_read_b128 a[72:75], v22 offset:0\n"
    "v_cvt_pk_bf16_f32 v66, v68, v69\n"
    "v_mfma_f32_16x16x32_bf16 v[200:203], a[104:107], v[112:115], v[200:203]\n"
    "v_exp_f32 v70, v70\n"
    "v_mfma_f32_16x16x32_bf16 v[232:235], a[104:107], v[120:123], v[232:235]\n"
    "s_waitcnt lgkmcnt(11)\n"
    "ds_read_b128 a[76:79], v23 offset:0\n"
    "v_mfma_f32_16x16x32_bf16 v[140:143], a[108:111], v[96:99], v[140:143]\n"
    "v_exp_f32 v71, v71\n"
    "v_mfma_f32_16x16x32_bf16 v[172:175], a[108:111], v[104:107], v[172:175]\n"
    "ds_read_b128 a[80:83], v20 offset:4096\n"
    "v_cvt_pk_bf16_f32 v67, v70, v71\n"
    "v_mfma_f32_16x16x32_bf16 v[204:207], a[108:111], v[112:115], v[204:207]\n"
    "v_exp_f32 v76, v76\n"
    "v_mfma_f32_16x16x32_bf16 v[236:239], a[108:111], v[120:123], v[236:239]\n"
    "s_waitcnt lgkmcnt(11)\n"
    "ds_read_b128 a[84:87], v21 offset:4096\n"
    "v_mfma_f32_16x16x32_bf16 v[144:147], a[112:115], v[96:99], v[144:147]\n"
    "v_exp_f32 v77, v77\n"
    "v_mfma_f32_16x16x32_bf16 v[176:179], a[112:115], v[104:107], v[176:179]\n"
    "ds_read_b128 a[88:91], v22 offset:4096\n"
    "v_cvt_pk_bf16_f32 v74, v76, v77\n"
    "v_mfma_f32_16x16x32_bf16 v[208:211], a[112:115], v[112:115], v[208:211]\n"
    "v_exp_f32 v78, v78\n"
    "v_mfma_f32_16x16x32_bf16 v[240:243], a[112:115], v[120:123], v[240:243]\n"
    "s_waitcnt lgkmcnt(11)\n"
    "ds_read_b128 a[92:95], v23 offset:4096\n"
    "v_mfma_f32_16x16x32_bf16 v[148:151], a[116:119], v[96:99], v[148:151]\n"
    "s_add_u32 m0, s86, 131072\n"
    "v_exp_f32 v79, v79\n"
    "v_mfma_f32_16x16x32_bf16 v[180:183], a[116:119], v[104:107], v[180:183]\n"
    "buffer_load_dwordx4 v40, s[56:59], s92 offen lds\n"
    "v_cvt_pk_bf16_f32 v75, v78, v79\n"
    "v_mfma_f32_16x16x32_bf16 v[212:215], a[116:119], v[112:115], v[212:215]\n"
    "v_exp_f32 v84, v84\n"
    "v_mfma_f32_16x16x32_bf16 v[244:247], a[116:119], v[120:123], v[244:247]\n"
    "s_waitcnt lgkmcnt(10)\n"
    "buffer_load_dwordx4 v41, s[56:59], s92 offen offset:1024 lds\n"
    "v_mfma_f32_16x16x32_bf16 v[152:155], a[120:123], v[96:99], v[152:155]\n"
    "v_exp_f32 v85, v85\n"
    "v_mfma_f32_16x16x32_bf16 v[184:187], a[120:123], v[104:107], v[184:187]\n"
    "buffer_load_dwordx4 v42, s[56:59], s92 offen offset:2048 lds\n"
    "v_cvt_pk_bf16_f32 v82, v84, v85\n"
    "v_mfma_f32_16x16x32_bf16 v[216:219], a[120:123], v[112:115], v[216:219]\n"
    "v_exp_f32 v86, v86\n"
    "v_mfma_f32_16x16x32_bf16 v[248:251], a[120:123], v[120:123], v[248:251]\n"
    "s_waitcnt lgkmcnt(8)\n"
    "buffer_load_dwordx4 v43, s[56:59], s92 offen offset:3072 lds\n"
    "v_mfma_f32_16x16x32_bf16 v[156:159], a[124:127], v[96:99], v[156:159]\n"
    "buffer_store_dwordx2 v[46:47], v11, s[76:79], 0 offen sc1\n"
    "v_mfma_f32_16x16x32_bf16 v[188:191], a[124:127], v[104:107], v[188:191]\n"
    "v_mfma_f32_16x16x32_bf16 v[220:223], a[124:127], v[112:115], v[220:223]\n"
    "v_mfma_f32_16x16x32_bf16 v[252:255], a[124:127], v[120:123], v[252:255]\n"
    "s_waitcnt lgkmcnt(7)\n"
    "v_mfma_f32_16x16x32_bf16 v[96:99], a[64:67], a[0:3], v[48:51]\n"
    "s_waitcnt lgkmcnt(6)\n"
    "v_exp_f32 v87, v87\n"
    "v_mfma_f32_16x16x32_bf16 v[96:99], a[68:71], a[4:7], v[96:99]\n"
    "s_waitcnt lgkmcnt(5)\n"
    "ds_read_b64_tr_b16 a[96:97], v32 offset:57344\n"
    "v_cvt_pk_bf16_f32 v83, v86, v87\n"
    "v_mfma_f32_16x16x32_bf16 v[96:99], a[72:75], a[8:11], v[96:99]\n"
    "s_waitcnt lgkmcnt(5)\n"
    "v_exp_f32 v92, v92\n"
    "v_mfma_f32_16x16x32_bf16 v[96:99], a[76:79], a[12:15], v[96:99]\n"
    "ds_read_b64_tr_b16 a[98:99], v32 offset:61440\n"
    "v_mfma_f32_16x16x32_bf16 v[104:107], a[64:67], a[16:19], v[52:55]\n"
    "v_exp_f32 v93, v93\n"
    "v_mfma_f32_16x16x32_bf16 v[104:107], a[68:71], a[20:23], v[104:107]\n"
    "ds_read_b64_tr_b16 a[100:101], v33 offset:57344\n"
    "v_cvt_pk_bf16_f32 v90, v92, v93\n"
    "v_mfma_f32_16x16x32_bf16 v[104:107], a[72:75], a[24:27], v[104:107]\n"
    "v_exp_f32 v94, v94\n"
    "v_mfma_f32_16x16x32_bf16 v[104:107], a[76:79], a[28:31], v[104:107]\n"
    "ds_read_b64_tr_b16 a[102:103], v33 offset:61440\n"
    "v_mfma_f32_16x16x32_bf16 v[112:115], a[64:67], a[32:35], v[56:59]\n"
    "v_exp_f32 v95, v95\n"
    "v_mfma_f32_16x16x32_bf16 v[112:115], a[68:71], a[36:39], v[112:115]\n"
    "ds_read_b64_tr_b16 a[104:105], v34 offset:57344\n"
    "v_cvt_pk_bf16_f32 v91, v94, v95\n"
    "v_mfma_f32_16x16x32_bf16 v[112:115], a[72:75], a[40:43], v[112:115]\n"
    "v_exp_f32 v96, v96\n"
    "v_mfma_f32_16x16x32_bf16 v[112:115], a[76:79], a[44:47], v[112:115]\n"
    "ds_read_b64_tr_b16 a[106:107], v34 offset:61440\n"
    "v_mfma_f32_16x16x32_bf16 v[120:123], a[64:67], a[48:51], v[60:63]\n"
    "v_exp_f32 v97, v97\n"
    "v_mfma_f32_16x16x32_bf16 v[120:123], a[68:71], a[52:55], v[120:123]\n"
    "ds_read_b64_tr_b16 a[108:109], v35 offset:57344\n"
    "v_cvt_pk_bf16_f32 v96, v96, v97\n"
    "v_mfma_f32_16x16x32_bf16 v[120:123], a[72:75], a[56:59], v[120:123]\n"
    "v_exp_f32 v98, v98\n"
    "v_mfma_f32_16x16x32_bf16 v[120:123], a[76:79], a[60:63], v[120:123]\n"
    "s_waitcnt lgkmcnt(10)\n"
    "ds_read_b64_tr_b16 a[110:111], v35 offset:61440\n"
    "v_mfma_f32_16x16x32_bf16 v[100:103], a[80:83], a[0:3], v[48:51]\n"
    "s_waitcnt lgkmcnt(10)\n"
    "v_exp_f32 v99, v99\n"
    "v_mfma_f32_16x16x32_bf16 v[100:103], a[84:87], a[4:7], v[100:103]\n"
    "s_waitcnt lgkmcnt(9)\n"
    "ds_read_b64_tr_b16 a[112:113], v36 offset:57344\n"
    "v_cvt_pk_bf16_f32 v97, v98, v99\n"
    "v_mfma_f32_16x16x32_bf16 v[100:103], a[88:91], a[8:11], v[100:103]\n"
    "s_waitcnt lgkmcnt(9)\n"
    "v_exp_f32 v104, v104\n"
    "v_mfma_f32_16x16x32_bf16 v[100:103], a[92:95], a[12:15], v[100:103]\n"
    "ds_read_b64_tr_b16 a[114:115], v36 offset:61440\n"
    "v_mfma_f32_16x16x32_bf16 v[108:111], a[80:83], a[16:19], v[52:55]\n"
    "v_exp_f32 v105, v105\n"
    "v_mfma_f32_16x16x32_bf16 v[108:111], a[84:87], a[20:23], v[108:111]\n"
    "ds_read_b64_tr_b16 a[116:117], v37 offset:57344\n"
    "v_cvt_pk_bf16_f32 v104, v104, v105\n"
    "v_mfma_f32_16x16x32_bf16 v[108:111], a[88:91], a[24:27], v[108:111]\n"
    "v_exp_f32 v106, v106\n"
    "v_mfma_f32_16x16x32_bf16 v[108:111], a[92:95], a[28:31], v[108:111]\n"
    "ds_read_b64_tr_b16 a[118:119], v37 offset:61440\n"
    "v_mfma_f32_16x16x32_bf16 v[116:119], a[80:83], a[32:35], v[56:59]\n"
    "v_exp_f32 v107, v107\n"
    "v_mfma_f32_16x16x32_bf16 v[116:119], a[84:87], a[36:39], v[116:119]\n"
    "ds_read_b64_tr_b16 a[120:121], v38 offset:57344\n"
    "v_cvt_pk_bf16_f32 v105, v106, v107\n"
    "v_mfma_f32_16x16x32_bf16 v[116:119], a[88:91], a[40:43], v[116:119]\n"
    "v_exp_f32 v112, v112\n"
    "v_mfma_f32_16x16x32_bf16 v[116:119], a[92:95], a[44:47], v[116:119]\n"
    "ds_read_b64_tr_b16 a[122:123], v38 offset:61440\n"
    "v_mfma_f32_16x16x32_bf16 v[124:127], a[80:83], a[48:51], v[60:63]\n"
    "v_exp_f32 v113, v113\n"
    "v_mfma_f32_16x16x32_bf16 v[124:127], a[84:87], a[52:55], v[124:127]\n"
    "ds_read_b64_tr_b16 a[124:125], v39 offset:57344\n"
    "v_cvt_pk_bf16_f32 v112, v112, v113\n"
    "v_mfma_f32_16x16x32_bf16 v[124:127], a[88:91], a[56:59], v[124:127]\n"
    "v_exp_f32 v114, v114\n"
    "v_mfma_f32_16x16x32_bf16 v[124:127], a[92:95], a[60:63], v[124:127]\n"
    "ds_read_b64_tr_b16 a[126:127], v39 offset:61440\n"
    "v_mfma_f32_16x16x32_bf16 a[128:131], a[144:147], v[64:67], a[128:131]\n"
    "v_exp_f32 v115, v115\n"
    "v_mfma_f32_16x16x32_bf16 a[132:135], a[144:147], v[72:75], a[132:135]\n"
    "v_cvt_pk_bf16_f32 v113, v114, v115\n"
    "v_mfma_f32_16x16x32_bf16 a[136:139], a[144:147], v[80:83], a[136:139]\n"
    "v_exp_f32 v120, v120\n"
    "v_mfma_f32_16x16x32_bf16 a[140:143], a[144:147], v[88:91], a[140:143]\n"
    "s_waitcnt lgkmcnt(14)\n"
    "v_mfma_f32_16x16x32_bf16 v[128:131], a[96:99], v[64:67], v[128:131]\n"
    "v_exp_f32 v121, v121\n"
    "v_mfma_f32_16x16x32_bf16 v[160:163], a[96:99], v[72:75], v[160:163]\n"
    "v_cvt_pk_bf16_f32 v120, v120, v121\n"
    "v_mfma_f32_16x16x32_bf16 v[192:195], a[96:99], v[80:83], v[192:195]\n"
    "v_exp_f32 v122, v122\n"
    "v_mfma_f32_16x16x32_bf16 v[224:227], a[96:99], v[88:91], v[224:227]\n"
    "s_waitcnt lgkmcnt(12)\n"
    "v_mfma_f32_16x16x32_bf16 v[132:135], a[100:103], v[64:67], v[132:135]\n"
    "v_exp_f32 v123, v123\n"
    "v_mfma_f32_16x16x32_bf16 v[164:167], a[100:103], v[72:75], v[164:167]\n"
    "ds_read_b128 a[64:67], v20 offset:8192\n"
    "v_cvt_pk_bf16_f32 v121, v122, v123\n"
    "v_mfma_f32_16x16x32_bf16 v[196:199], a[100:103], v[80:83], v[196:199]\n"
    "v_exp_f32 v100, v100\n"
    "v_mfma_f32_16x16x32_bf16 v[228:231], a[100:103], v[88:91], v[228:231]\n"
    "s_waitcnt lgkmcnt(11)\n"
    "ds_read_b128 a[68:71], v21 offset:8192\n"
    "v_mfma_f32_16x16x32_bf16 v[136:139], a[104:107], v[64:67], v[136:139]\n"
    "v_exp_f32 v101, v101\n"
    "v_mfma_f32_16x16x32_bf16 v[168:171], a[104:107], v[72:75], v[168:171]\n"
    "ds_read_b128 a[72:75], v22 offset:8192\n"
    "v_cvt_pk_bf16_f32 v98, v100, v101\n"
    "v_mfma_f32_16x16x32_bf16 v[200:203], a[104:107], v[80:83], v[200:203]\n"
    "v_exp_f32 v102, v102\n"
    "v_mfma_f32_16x16x32_bf16 v[232:235], a[104:107], v[88:91], v[232:235]\n"
    "s_waitcnt lgkmcnt(11)\n"
    "ds_read_b128 a[76:79], v23 offset:8192\n"
    "v_mfma_f32_16x16x32_bf16 v[140:143], a[108:111], v[64:67], v[140:143]\n"
    "v_exp_f32 v103, v103\n"
    "v_mfma_f32_16x16x32_bf16 v[172:175], a[108:111], v[72:75], v[172:175]\n"
    "ds_read_b128 a[80:83], v20 offset:12288\n"
    "v_cvt_pk_bf16_f32 v99, v102, v103\n"
    "v_mfma_f32_16x16x32_bf16 v[204:207], a[108:111], v[80:83], v[204:207]\n"
    "v_exp_f32 v108, v108\n"
    "v_mfma_f32_16x16x32_bf16 v[236:239], a[108:111], v[88:91], v[236:239]\n"
    "s_waitcnt lgkmcnt(11)\n"
    "ds_read_b128 a[84:87], v21 offset:12288\n"
    "v_mfma_f32_16x16x32_bf16 v[144:147], a[112:115], v[64:67], v[144:147]\n"
    "v_exp_f32 v109, v109\n"
    "v_mfma_f32_16x16x32_bf16 v[176:179], a[112:115], v[72:75], v[176:179]\n"
    "ds_read_b128 a[88:91], v22 offset:12288\n"
    "v_cvt_pk_bf16_f32 v106, v108, v109\n"
    "v_mfma_f32_16x16x32_bf16 v[208:211], a[112:115], v[80:83], v[208:211]\n"
    "v_exp_f32 v110, v110\n"
    "v_mfma_f32_16x16x32_bf16 v[240:243], a[112:115], v[88:91], v[240:243]\n"
    "s_waitcnt lgkmcnt(11)\n"
    "ds_read_b128 a[92:95], v23 offset:12288\n"
    "v_mfma_f32_16x16x32_bf16 v[148:151], a[116:119], v[64:67], v[148:151]\n"
    "s_add_u32 m0, s86, 147456\n"
    "v_exp_f32 v111, v111\n"
    "v_mfma_f32_16x16x32_bf16 v[180:183], a[116:119], v[72:75], v[180:183]\n"
    "buffer_load_dwordx4 v44, s[60:63], s92 offen lds\n"
    "v_cvt_pk_bf16_f32 v107, v110, v111\n"
    "v_mfma_f32_16x16x32_bf16 v[212:215], a[116:119], v[80:83], v[212:215]\n"
    "v_exp_f32 v116, v116\n"
    "v_mfma_f32_16x16x32_bf16 v[244:247], a[116:119], v[88:91], v[244:247]\n"
    "s_waitcnt lgkmcnt(10)\n"
    "buffer_load_dwordx4 v45, s[60:63], s92 offen offset:1024 lds\n"
    "v_mfma_f32_16x16x32_bf16 v[152:155], a[120:123], v[64:67], v[152:155]\n"
    "v_exp_f32 v117, v117\n"
    "v_mfma_f32_16x16x32_bf16 v[184:187], a[120:123], v[72:75], v[184:187]\n"
    "buffer_load_dwordx4 v44, s[60:63], s92 offen offset:2048 lds\n"
    "v_cvt_pk_bf16_f32 v114, v116, v117\n"
    "v_mfma_f32_16x16x32_bf16 v[216:219], a[120:123], v[80:83], v[216:219]\n"
    "v_exp_f32 v118, v118\n"
    "v_mfma_f32_16x16x32_bf16 v[248:251], a[120:123], v[88:91], v[248:251]\n"
    "s_waitcnt lgkmcnt(8)\n"
    "buffer_load_dwordx4 v45, s[60:63], s92 offen offset:3072 lds\n"
    "s_add_u32 s92, s92, 16384\n"
    "v_mfma_f32_16x16x32_bf16 v[156:159], a[124:127], v[64:67], v[156:159]\n"
    "v_mfma_f32_16x16x32_bf16 v[188:191], a[124:127], v[72:75], v[188:191]\n"
    "v_mfma_f32_16x16x32_bf16 v[220:223], a[124:127], v[80:83], v[220:223]\n"
    "v_mfma_f32_16x16x32_bf16 v[252:255], a[124:127], v[88:91], v[252:255]\n"
    "s_waitcnt lgkmcnt(7)\n"
    "v_mfma_f32_16x16x32_bf16 v[64:67], a[64:67], a[0:3], v[48:51]\n"
    "s_waitcnt lgkmcnt(6)\n"
    "v_exp_f32 v119, v119\n"
    "v_mfma_f32_16x16x32_bf16 v[64:67], a[68:71], a[4:7], v[64:67]\n"
    "s_waitcnt lgkmcnt(5)\n"
    "ds_read_b64_tr_b16 a[96:97], v32 offset:16384\n"
    "v_cvt_pk_bf16_f32 v115, v118, v119\n"
    "v_mfma_f32_16x16x32_bf16 v[64:67], a[72:75], a[8:11], v[64:67]\n"
    "s_waitcnt lgkmcnt(5)\n"
    "v_exp_f32 v124, v124\n"
    "v_mfma_f32_16x16x32_bf16 v[64:67], a[76:79], a[12:15], v[64:67]\n"
    "ds_read_b64_tr_b16 a[98:99], v32 offset:20480\n"
    "v_mfma_f32_16x16x32_bf16 v[72:75], a[64:67], a[16:19], v[52:55]\n"
    "v_exp_f32 v125, v125\n"
    "v_mfma_f32_16x16x32_bf16 v[72:75], a[68:71], a[20:23], v[72:75]\n"
    "ds_read_b64_tr_b16 a[100:101], v33 offset:16384\n"
    "v_cvt_pk_bf16_f32 v122, v124, v125\n"
    "v_mfma_f32_16x16x32_bf16 v[72:75], a[72:75], a[24:27], v[72:75]\n"
    "v_exp_f32 v126, v126\n"
    "v_mfma_f32_16x16x32_bf16 v[72:75], a[76:79], a[28:31], v[72:75]\n"
    "ds_read_b64_tr_b16 a[102:103], v33 offset:20480\n"
    "v_mfma_f32_16x16x32_bf16 v[80:83], a[64:67], a[32:35], v[56:59]\n"
    "v_exp_f32 v127, v127\n"
    "v_mfma_f32_16x16x32_bf16 v[80:83], a[68:71], a[36:39], v[80:83]\n"
    "ds_read_b64_tr_b16 a[104:105], v34 offset:16384\n"
    "v_cvt_pk_bf16_f32 v123, v126, v127\n"
    "v_mfma_f32_16x16x32_bf16 v[80:83], a[72:75], a[40:43], v[80:83]\n"
    "v_exp_f32 v64, v64\n"
    "v_mfma_f32_16x16x32_bf16 v[80:83], a[76:79], a[44:47], v[80:83]\n"
    "ds_read_b64_tr_b16 a[106:107], v34 offset:20480\n"
    "v_mfma_f32_16x16x32_bf16 v[88:91], a[64:67], a[48:51], v[60:63]\n"
    "v_exp_f32 v65, v65\n"
    "v_mfma_f32_16x16x32_bf16 v[88:91], a[68:71], a[52:55], v[88:91]\n"
    "ds_read_b64_tr_b16 a[108:109], v35 offset:16384\n"
    "v_cvt_pk_bf16_f32 v64, v64, v65\n"
    "v_mfma_f32_16x16x32_bf16 v[88:91], a[72:75], a[56:59], v[88:91]\n"
    "v_exp_f32 v66, v66\n"
    "v_mfma_f32_16x16x32_bf16 v[88:91], a[76:79], a[60:63], v[88:91]\n"
    "s_waitcnt lgkmcnt(10)\n"
    "ds_read_b64_tr_b16 a[110:111], v35 offset:20480\n"
    "v_mfma_f32_16x16x32_bf16 v[68:71], a[80:83], a[0:3], v[48:51]\n"
    "s_waitcnt lgkmcnt(10)\n"
    "v_exp_f32 v67, v67\n"
    "v_mfma_f32_16x16x32_bf16 v[68:71], a[84:87], a[4:7], v[68:71]\n"
    "s_waitcnt lgkmcnt(9)\n"
    "ds_read_b64_tr_b16 a[112:113], v36 offset:16384\n"
    "v_cvt_pk_bf16_f32 v65, v66, v67\n"
    "v_mfma_f32_16x16x32_bf16 v[68:71], a[88:91], a[8:11], v[68:71]\n"
    "s_waitcnt lgkmcnt(9)\n"
    "v_exp_f32 v72, v72\n"
    "v_mfma_f32_16x16x32_bf16 v[68:71], a[92:95], a[12:15], v[68:71]\n"
    "ds_read_b64_tr_b16 a[114:115], v36 offset:20480\n"
    "v_mfma_f32_16x16x32_bf16 v[76:79], a[80:83], a[16:19], v[52:55]\n"
    "v_exp_f32 v73, v73\n"
    "v_mfma_f32_16x16x32_bf16 v[76:79], a[84:87], a[20:23], v[76:79]\n"
    "ds_read_b64_tr_b16 a[116:117], v37 offset:16384\n"
    "v_cvt_pk_bf16_f32 v72, v72, v73\n"
    "v_mfma_f32_16x16x32_bf16 v[76:79], a[88:91], a[24:27], v[76:79]\n"
    "v_exp_f32 v74, v74\n"
    "v_mfma_f32_16x16x32_bf16 v[76:79], a[92:95], a[28:31], v[76:79]\n"
    "ds_read_b64_tr_b16 a[118:119], v37 offset:20480\n"
    "v_mfma_f32_16x16x32_bf16 v[84:87], a[80:83], a[32:35], v[56:59]\n"
    "v_exp_f32 v75, v75\n"
    "v_mfma_f32_16x16x32_bf16 v[84:87], a[84:87], a[36:39], v[84:87]\n"
    "ds_read_b64_tr_b16 a[120:121], v38 offset:16384\n"
    "v_cvt_pk_bf16_f32 v73, v74, v75\n"
    "v_mfma_f32_16x16x32_bf16 v[84:87], a[88:91], a[40:43], v[84:87]\n"
    "v_exp_f32 v80, v80\n"
    "v_mfma_f32_16x16x32_bf16 v[84:87], a[92:95], a[44:47], v[84:87]\n"
    "ds_read_b64_tr_b16 a[122:123], v38 offset:20480\n"
    "v_mfma_f32_16x16x32_bf16 v[92:95], a[80:83], a[48:51], v[60:63]\n"
    "v_exp_f32 v81, v81\n"
    "v_mfma_f32_16x16x32_bf16 v[92:95], a[84:87], a[52:55], v[92:95]\n"
    "ds_read_b64_tr_b16 a[124:125], v39 offset:16384\n"
    "v_cvt_pk_bf16_f32 v80, v80, v81\n"
    "v_mfma_f32_16x16x32_bf16 v[92:95], a[88:91], a[56:59], v[92:95]\n"
    "v_exp_f32 v82, v82\n"
    "v_mfma_f32_16x16x32_bf16 v[92:95], a[92:95], a[60:63], v[92:95]\n"
    "ds_read_b64_tr_b16 a[126:127], v39 offset:20480\n"
    "v_mfma_f32_16x16x32_bf16 a[128:131], a[144:147], v[96:99], a[128:131]\n"
    "v_exp_f32 v83, v83\n"
    "v_mfma_f32_16x16x32_bf16 a[132:135], a[144:147], v[104:107], a[132:135]\n"
    "v_cvt_pk_bf16_f32 v81, v82, v83\n"
    "v_mfma_f32_16x16x32_bf16 a[136:139], a[144:147], v[112:115], a[136:139]\n"
    "v_exp_f32 v88, v88\n"
    "v_mfma_f32_16x16x32_bf16 a[140:143], a[144:147], v[120:123], a[140:143]\n"
    "s_waitcnt lgkmcnt(14)\n"
    "v_mfma_f32_16x16x32_bf16 v[128:131], a[96:99], v[96:99], v[128:131]\n"
    "v_exp_f32 v89, v89\n"
    "v_mfma_f32_16x16x32_bf16 v[160:163], a[96:99], v[104:107], v[160:163]\n"
    "s_waitcnt vmcnt(10)\n"
    "s_barrier\n"
    "v_cvt_pk_bf16_f32 v88, v88, v89\n"
    "v_mfma_f32_16x16x32_bf16 v[192:195], a[96:99], v[112:115], v[192:195]\n"
    "v_exp_f32 v90, v90\n"
    "v_mfma_f32_16x16x32_bf16 v[224:227], a[96:99], v[120:123], v[224:227]\n"
    "s_waitcnt lgkmcnt(12)\n"
    "v_mfma_f32_16x16x32_bf16 v[132:135], a[100:103], v[96:99], v[132:135]\n"
    "v_exp_f32 v91, v91\n"
    "v_mfma_f32_16x16x32_bf16 v[164:167], a[100:103], v[104:107], v[164:167]\n"
    "ds_read_b128 a[64:67], v16 offset:0\n"
    "v_cvt_pk_bf16_f32 v89, v90, v91\n"
    "v_mfma_f32_16x16x32_bf16 v[196:199], a[100:103], v[112:115], v[196:199]\n"
    "v_exp_f32 v68, v68\n"
    "v_mfma_f32_16x16x32_bf16 v[228:231], a[100:103], v[120:123], v[228:231]\n"
    "s_waitcnt lgkmcnt(11)\n"
    "ds_read_b128 a[68:71], v17 offset:0\n"
    "v_mfma_f32_16x16x32_bf16 v[136:139], a[104:107], v[96:99], v[136:139]\n"
    "v_exp_f32 v69, v69\n"
    "v_mfma_f32_16x16x32_bf16 v[168:171], a[104:107], v[104:107], v[168:171]\n"
    "ds_read_b128 a[72:75], v18 offset:0\n"
    "v_cvt_pk_bf16_f32 v66, v68, v69\n"
    "v_mfma_f32_16x16x32_bf16 v[200:203], a[104:107], v[112:115], v[200:203]\n"
    "v_exp_f32 v70, v70\n"
    "v_mfma_f32_16x16x32_bf16 v[232:235], a[104:107], v[120:123], v[232:235]\n"
    "s_waitcnt lgkmcnt(11)\n"
    "ds_read_b128 a[76:79], v19 offset:0\n"
    "v_mfma_f32_16x16x32_bf16 v[140:143], a[108:111], v[96:99], v[140:143]\n"
    "v_exp_f32 v71, v71\n"
    "v_mfma_f32_16x16x32_bf16 v[172:175], a[108:111], v[104:107], v[172:175]\n"
    "ds_read_b128 a[80:83], v16 offset:4096\n"
    "v_cvt_pk_bf16_f32 v67, v70, v71\n"
    "v_mfma_f32_16x16x32_bf16 v[204:207], a[108:111], v[112:115], v[204:207]\n"
    "v_exp_f32 v76, v76\n"
    "v_mfma_f32_16x16x32_bf16 v[236:239], a[108:111], v[120:123], v[236:239]\n"
    "s_waitcnt lgkmcnt(11)\n"
    "ds_read_b128 a[84:87], v17 offset:4096\n"
    "v_mfma_f32_16x16x32_bf16 v[144:147], a[112:115], v[96:99], v[144:147]\n"
    "v_exp_f32 v77, v77\n"
    "v_mfma_f32_16x16x32_bf16 v[176:179], a[112:115], v[104:107], v[176:179]\n"
    "ds_read_b128 a[88:91], v18 offset:4096\n"
    "v_cvt_pk_bf16_f32 v74, v76, v77\n"
    "v_mfma_f32_16x16x32_bf16 v[208:211], a[112:115], v[112:115], v[208:211]\n"
    "v_exp_f32 v78, v78\n"
    "v_mfma_f32_16x16x32_bf16 v[240:243], a[112:115], v[120:123], v[240:243]\n"
    "s_waitcnt lgkmcnt(11)\n"
    "ds_read_b128 a[92:95], v19 offset:4096\n"
    "v_mfma_f32_16x16x32_bf16 v[148:151], a[116:119], v[96:99], v[148:151]\n"
    "s_add_u32 m0, s86, 32768\n"
    "v_exp_f32 v79, v79\n"
    "v_mfma_f32_16x16x32_bf16 v[180:183], a[116:119], v[104:107], v[180:183]\n"
    "buffer_load_dwordx4 v40, s[56:59], s92 offen lds\n"
    "v_cvt_pk_bf16_f32 v75, v78, v79\n"
    "v_mfma_f32_16x16x32_bf16 v[212:215], a[116:119], v[112:115], v[212:215]\n"
    "v_exp_f32 v84, v84\n"
    "v_mfma_f32_16x16x32_bf16 v[244:247], a[116:119], v[120:123], v[244:247]\n"
    "s_waitcnt lgkmcnt(10)\n"
    "buffer_load_dwordx4 v41, s[56:59], s92 offen offset:1024 lds\n"
    "v_mfma_f32_16x16x32_bf16 v[152:155], a[120:123], v[96:99], v[152:155]\n"
    "v_exp_f32 v85, v85\n"
    "v_mfma_f32_16x16x32_bf16 v[184:187], a[120:123], v[104:107], v[184:187]\n"
    "buffer_load_dwordx4 v42, s[56:59], s92 offen offset:2048 lds\n"
    "v_cvt_pk_bf16_f32 v82, v84, v85\n"
    "v_mfma_f32_16x16x32_bf16 v[216:219], a[120:123], v[112:115], v[216:219]\n"
    "v_exp_f32 v86, v86\n"
    "v_mfma_f32_16x16x32_bf16 v[248:251], a[120:123], v[120:123], v[248:251]\n"
    "s_waitcnt lgkmcnt(8)\n"
    "buffer_load_dwordx4 v43, s[56:59], s92 offen offset:3072 lds\n"
    "v_mfma_f32_16x16x32_bf16 v[156:159], a[124:127], v[96:99], v[156:159]\n"
    "v_mfma_f32_16x16x32_bf16 v[188:191], a[124:127], v[104:107], v[188:191]\n"
    "v_mfma_f32_16x16x32_bf16 v[220:223], a[124:127], v[112:115], v[220:223]\n"
    "v_mfma_f32_16x16x32_bf16 v[252:255], a[124:127], v[120:123], v[252:255]\n"
    "s_waitcnt lgkmcnt(7)\n"
    "v_mfma_f32_16x16x32_bf16 v[96:99], a[64:67], a[0:3], v[48:51]\n"
    "s_waitcnt lgkmcnt(6)\n"
    "v_exp_f32 v87, v87\n"
    "v_mfma_f32_16x16x32_bf16 v[96:99], a[68:71], a[4:7], v[96:99]\n"
    "s_waitcnt lgkmcnt(5)\n"
    "ds_read_b64_tr_b16 a[96:97], v32 offset:24576\n"
    "v_cvt_pk_bf16_f32 v83, v86, v87\n"
    "v_mfma_f32_16x16x32_bf16 v[96:99], a[72:75], a[8:11], v[96:99]\n"
    "s_waitcnt lgkmcnt(5)\n"
    "v_exp_f32 v92, v92\n"
    "v_mfma_f32_16x16x32_bf16 v[96:99], a[76:79], a[12:15], v[96:99]\n"
    "ds_read_b64_tr_b16 a[98:99], v32 offset:28672\n"
    "v_mfma_f32_16x16x32_bf16 v[104:107], a[64:67], a[16:19], v[52:55]\n"
    "v_exp_f32 v93, v93\n"
    "v_mfma_f32_16x16x32_bf16 v[104:107], a[68:71], a[20:23], v[104:107]\n"
    "ds_read_b64_tr_b16 a[100:101], v33 offset:24576\n"
    "v_cvt_pk_bf16_f32 v90, v92, v93\n"
    "v_mfma_f32_16x16x32_bf16 v[104:107], a[72:75], a[24:27], v[104:107]\n"
    "v_exp_f32 v94, v94\n"
    "v_mfma_f32_16x16x32_bf16 v[104:107], a[76:79], a[28:31], v[104:107]\n"
    "ds_read_b64_tr_b16 a[102:103], v33 offset:28672\n"
    "v_mfma_f32_16x16x32_bf16 v[112:115], a[64:67], a[32:35], v[56:59]\n"
    "v_exp_f32 v95, v95\n"
    "v_mfma_f32_16x16x32_bf16 v[112:115], a[68:71], a[36:39], v[112:115]\n"
    "ds_read_b64_tr_b16 a[104:105], v34 offset:24576\n"
    "v_cvt_pk_bf16_f32 v91, v94, v95\n"
    "v_mfma_f32_16x16x32_bf16 v[112:115], a[72:75], a[40:43], v[112:115]\n"
    "v_exp_f32 v96, v96\n"
    "v_mfma_f32_16x16x32_bf16 v[112:115], a[76:79], a[44:47], v[112:115]\n"
    "ds_read_b64_tr_b16 a[106:107], v34 offset:28672\n"
    "v_mfma_f32_16x16x32_bf16 v[120:123], a[64:67], a[48:51], v[60:63]\n"
    "v_exp_f32 v97, v97\n"
    "v_mfma_f32_16x16x32_bf16 v[120:123], a[68:71], a[52:55], v[120:123]\n"
    "ds_read_b64_tr_b16 a[108:109], v35 offset:24576\n"
    "v_cvt_pk_bf16_f32 v96, v96, v97\n"
    "v_mfma_f32_16x16x32_bf16 v[120:123], a[72:75], a[56:59], v[120:123]\n"
    "v_exp_f32 v98, v98\n"
    "v_mfma_f32_16x16x32_bf16 v[120:123], a[76:79], a[60:63], v[120:123]\n"
    "s_waitcnt lgkmcnt(10)\n"
    "ds_read_b64_tr_b16 a[110:111], v35 offset:28672\n"
    "v_mfma_f32_16x16x32_bf16 v[100:103], a[80:83], a[0:3], v[48:51]\n"
    "s_waitcnt lgkmcnt(10)\n"
    "v_exp_f32 v99, v99\n"
    "v_mfma_f32_16x16x32_bf16 v[100:103], a[84:87], a[4:7], v[100:103]\n"
    "s_waitcnt lgkmcnt(9)\n"
    "ds_read_b64_tr_b16 a[112:113], v36 offset:24576\n"
    "v_cvt_pk_bf16_f32 v97, v98, v99\n"
    "v_mfma_f32_16x16x32_bf16 v[100:103], a[88:91], a[8:11], v[100:103]\n"
    "s_waitcnt lgkmcnt(9)\n"
    "v_exp_f32 v104, v104\n"
    "v_mfma_f32_16x16x32_bf16 v[100:103], a[92:95], a[12:15], v[100:103]\n"
    "ds_read_b64_tr_b16 a[114:115], v36 offset:28672\n"
    "v_mfma_f32_16x16x32_bf16 v[108:111], a[80:83], a[16:19], v[52:55]\n"
    "v_exp_f32 v105, v105\n"
    "v_mfma_f32_16x16x32_bf16 v[108:111], a[84:87], a[20:23], v[108:111]\n"
    "ds_read_b64_tr_b16 a[116:117], v37 offset:24576\n"
    "v_cvt_pk_bf16_f32 v104, v104, v105\n"
    "v_mfma_f32_16x16x32_bf16 v[108:111], a[88:91], a[24:27], v[108:111]\n"
    "v_exp_f32 v106, v106\n"
    "v_mfma_f32_16x16x32_bf16 v[108:111], a[92:95], a[28:31], v[108:111]\n"
    "ds_read_b64_tr_b16 a[118:119], v37 offset:28672\n"
    "v_mfma_f32_16x16x32_bf16 v[116:119], a[80:83], a[32:35], v[56:59]\n"
    "v_exp_f32 v107, v107\n"
    "v_mfma_f32_16x16x32_bf16 v[116:119], a[84:87], a[36:39], v[116:119]\n"
    "ds_read_b64_tr_b16 a[120:121], v38 offset:24576\n"
    "v_cvt_pk_bf16_f32 v105, v106, v107\n"
    "v_mfma_f32_16x16x32_bf16 v[116:119], a[88:91], a[40:43], v[116:119]\n"
    "v_exp_f32 v112, v112\n"
    "v_mfma_f32_16x16x32_bf16 v[116:119], a[92:95], a[44:47], v[116:119]\n"
    "ds_read_b64_tr_b16 a[122:123], v38 offset:28672\n"
    "v_mfma_f32_16x16x32_bf16 v[124:127], a[80:83], a[48:51], v[60:63]\n"
    "v_exp_f32 v113, v113\n"
    "v_mfma_f32_16x16x32_bf16 v[124:127], a[84:87], a[52:55], v[124:127]\n"
    "ds_read_b64_tr_b16 a[124:125], v39 offset:24576\n"
    "v_cvt_pk_bf16_f32 v112, v112, v113\n"
    "v_mfma_f32_16x16x32_bf16 v[124:127], a[88:91], a[56:59], v[124:127]\n"
    "v_exp_f32 v114, v114\n"
    "v_mfma_f32_16x16x32_bf16 v[124:127], a[92:95], a[60:63], v[124:127]\n"
    "ds_read_b64_tr_b16 a[126:127], v39 offset:28672\n"
    "v_mfma_f32_16x16x32_bf16 a[128:131], a[144:147], v[64:67], a[128:131]\n"
    "v_exp_f32 v115, v115\n"
    "v_mfma_f32_16x16x32_bf16 a[132:135], a[144:147], v[72:75], a[132:135]\n"
    "v_cvt_pk_bf16_f32 v113, v114, v115\n"
    "v_mfma_f32_16x16x32_bf16 a[136:139], a[144:147], v[80:83], a[136:139]\n"
    "v_exp_f32 v120, v120\n"
    "v_mfma_f32_16x16x32_bf16 a[140:143], a[144:147], v[88:91], a[140:143]\n"
    "s_waitcnt lgkmcnt(14)\n"
    "v_mfma_f32_16x16x32_bf16 v[128:131], a[96:99], v[64:67], v[128:131]\n"
    "v_exp_f32 v121, v121\n"
    "v_mfma_f32_16x16x32_bf16 v[160:163], a[96:99], v[72:75], v[160:163]\n"
    "v_cvt_pk_bf16_f32 v120, v120, v121\n"
    "v_mfma_f32_16x16x32_bf16 v[192:195], a[96:99], v[80:83], v[192:195]\n"
    "v_exp_f32 v122, v122\n"
    "v_mfma_f32_16x16x32_bf16 v[224:227], a[96:99], v[88:91], v[224:227]\n"
    "s_waitcnt lgkmcnt(12)\n"
    "v_mfma_f32_16x16x32_bf16 v[132:135], a[100:103], v[64:67], v[132:135]\n"
    "v_exp_f32 v123, v123\n"
    "v_mfma_f32_16x16x32_bf16 v[164:167], a[100:103], v[72:75], v[164:167]\n"
    "ds_read_b128 a[64:67], v16 offset:8192\n"
    "v_cvt_pk_bf16_f32 v121, v122, v123\n"
    "v_mfma_f32_16x16x32_bf16 v[196:199], a[100:103], v[80:83], v[196:199]\n"
    "v_exp_f32 v100, v100\n"
    "v_mfma_f32_16x16x32_bf16 v[228:231], a[100:103], v[88:91], v[228:231]\n"
    "s_waitcnt lgkmcnt(11)\n"
    "ds_read_b128 a[68:71], v17 offset:8192\n"
    "v_mfma_f32_16x16x32_bf16 v[136:139], a[104:107], v[64:67], v[136:139]\n"
    "v_exp_f32 v101, v101\n"
    "v_mfma_f32_16x16x32_bf16 v[168:171], a[104:107], v[72:75], v[168:171]\n"
    "ds_read_b128 a[72:75], v18 offset:8192\n"
    "v_cvt_pk_bf16_f32 v98, v100, v101\n"
    "v_mfma_f32_16x16x32_bf16 v[200:203], a[104:107], v[80:83], v[200:203]\n"
    "v_exp_f32 v102, v102\n"
    "v_mfma_f32_16x16x32_bf16 v[232:235], a[104:107], v[88:91], v[232:235]\n"
    "s_waitcnt lgkmcnt(11)\n"
    "ds_read_b128 a[76:79], v19 offset:8192\n"
    "v_mfma_f32_16x16x32_bf16 v[140:143], a[108:111], v[64:67], v[140:143]\n"
    "v_exp_f32 v103, v103\n"
    "v_mfma_f32_16x16x32_bf16 v[172:175], a[108:111], v[72:75], v[172:175]\n"
    "ds_read_b128 a[80:83], v16 offset:12288\n"
    "v_cvt_pk_bf16_f32 v99, v102, v103\n"
    "v_mfma_f32_16x16x32_bf16 v[204:207], a[108:111], v[80:83], v[204:207]\n"
    "v_exp_f32 v108, v108\n"
    "v_mfma_f32_16x16x32_bf16 v[236:239], a[108:111], v[88:91], v[236:239]\n"
    "s_waitcnt lgkmcnt(11)\n"
    "ds_read_b128 a[84:87], v17 offset:12288\n"
    "v_mfma_f32_16x16x32_bf16 v[144:147], a[112:115], v[64:67], v[144:147]\n"
    "v_exp_f32 v109, v109\n"
    "v_mfma_f32_16x16x32_bf16 v[176:179], a[112:115], v[72:75], v[176:179]\n"
    "ds_read_b128 a[88:91], v18 offset:12288\n"
    "v_cvt_pk_bf16_f32 v106, v108, v109\n"
    "v_mfma_f32_16x16x32_bf16 v[208:211], a[112:115], v[80:83], v[208:211]\n"
    "v_exp_f32 v110, v110\n"
    "v_mfma_f32_16x16x32_bf16 v[240:243], a[112:115], v[88:91], v[240:243]\n"
    "s_waitcnt lgkmcnt(11)\n"
    "ds_read_b128 a[92:95], v19 offset:12288\n"
    "v_mfma_f32_16x16x32_bf16 v[148:151], a[116:119], v[64:67], v[148:151]\n"
    "s_add_u32 m0, s86, 49152\n"
    "v_exp_f32 v111, v111\n"
    "v_mfma_f32_16x16x32_bf16 v[180:183], a[116:119], v[72:75], v[180:183]\n"
    "buffer_load_dwordx4 v44, s[60:63], s92 offen lds\n"
    "v_cvt_pk_bf16_f32 v107, v110, v111\n"
    "v_mfma_f32_16x16x32_bf16 v[212:215], a[116:119], v[80:83], v[212:215]\n"
    "v_exp_f32 v116, v116\n"
    "v_mfma_f32_16x16x32_bf16 v[244:247], a[116:119], v[88:91], v[244:247]\n"
    "s_waitcnt lgkmcnt(10)\n"
    "buffer_load_dwordx4 v45, s[60:63], s92 offen offset:1024 lds\n"
    "v_mfma_f32_16x16x32_bf16 v[152:155], a[120:123], v[64:67], v[152:155]\n"
    "v_exp_f32 v117, v117\n"
    "v_mfma_f32_16x16x32_bf16 v[184:187], a[120:123], v[72:75], v[184:187]\n"
    "buffer_load_dwordx4 v44, s[60:63], s92 offen offset:2048 lds\n"
    "v_cvt_pk_bf16_f32 v114, v116, v117\n"
    "v_mfma_f32_16x16x32_bf16 v[216:219], a[120:123], v[80:83], v[216:219]\n"
    "v_exp_f32 v118, v118\n"
    "v_mfma_f32_16x16x32_bf16 v[248:251], a[120:123], v[88:91], v[248:251]\n"
    "s_waitcnt lgkmcnt(8)\n"
    "buffer_load_dwordx4 v45, s[60:63], s92 offen offset:3072 lds\n"
    "s_add_u32 s92, s92, 16384\n"
    "v_mfma_f32_16x16x32_bf16 v[156:159], a[124:127], v[64:67], v[156:159]\n"
    "v_mfma_f32_16x16x32_bf16 v[188:191], a[124:127], v[72:75], v[188:191]\n"
    "v_mfma_f32_16x16x32_bf16 v[220:223], a[124:127], v[80:83], v[220:223]\n"
    "v_mfma_f32_16x16x32_bf16 v[252:255], a[124:127], v[88:91], v[252:255]\n"
    "s_sub_u32 s88, s88, 1\n"
    "s_cmp_lg_u32 s88, 0\n"
    "s_cbranch_scc1 L_A%=\n"
    "L_LAST%=:\n"
    "s_waitcnt lgkmcnt(7)\n"
    "v_mfma_f32_16x16x32_bf16 v[64:67], a[64:67], a[0:3], v[48:51]\n"
    "s_waitcnt lgkmcnt(6)\n"
    "v_exp_f32 v119, v119\n"
    "v_mfma_f32_16x16x32_bf16 v[64:67], a[68:71], a[4:7], v[64:67]\n"
    "s_waitcnt lgkmcnt(5)\n"
    "ds_read_b64_tr_b16 a[96:97], v24 offset:16384\n"
    "v_cvt_pk_bf16_f32 v115, v118, v119\n"
    "v_mfma_f32_16x16x32_bf16 v[64:67], a[72:75], a[8:11], v[64:67]\n"
    "s_waitcnt lgkmcnt(5)\n"
    "v_exp_f32 v124, v124\n"
    "v_mfma_f32_16x16x32_bf16 v[64:67], a[76:79], a[12:15], v[64:67]\n"
    "ds_read_b64_tr_b16 a[98:99], v24 offset:20480\n"
    "v_add_u32 v2, 0, v0\n"
    "v_mfma_f32_16x16x32_bf16 v[72:75], a[64:67], a[16:19], v[52:55]\n"
    "v_exp_f32 v125, v125\n"
    "v_mfma_f32_16x16x32_bf16 v[72:75], a[68:71], a[20:23], v[72:75]\n"
    "ds_read_b64_tr_b16 a[100:101], v25 offset:16384\n"
    "v_cvt_pk_bf16_f32 v122, v124, v125\n"
    "v_mfma_f32_16x16x32_bf16 v[72:75], a[72:75], a[24:27], v[72:75]\n"
    "v_exp_f32 v126, v126\n"
    "v_mfma_f32_16x16x32_bf16 v[72:75], a[76:79], a[28:31], v[72:75]\n"
    "ds_read_b64_tr_b16 a[102:103], v25 offset:20480\n"
    "v_mul_u32_u24 v13, 58255, v2\n"
    "v_mfma_f32_16x16x32_bf16 v[80:83], a[64:67], a[32:35], v[56:59]\n"
    "v_exp_f32 v127, v127\n"
    "v_mfma_f32_16x16x32_bf16 v[80:83], a[68:71], a[36:39], v[80:83]\n"
    "ds_read_b64_tr_b16 a[104:105], v26 offset:16384\n"
    "v_cvt_pk_bf16_f32 v123, v126, v127\n"
    "v_mfma_f32_16x16x32_bf16 v[80:83], a[72:75], a[40:43], v[80:83]\n"
    "v_exp_f32 v64, v64\n"
    "v_mfma_f32_16x16x32_bf16 v[80:83], a[76:79], a[44:47], v[80:83]\n"
    "ds_read_b64_tr_b16 a[106:107], v26 offset:20480\n"
    "v_lshrrev_b32 v13, 24, v13\n"
    "v_mfma_f32_16x16x32_bf16 v[88:91], a[64:67], a[48:51], v[60:63]\n"
    "v_exp_f32 v65, v65\n"
    "v_mfma_f32_16x16x32_bf16 v[88:91], a[68:71], a[52:55], v[88:91]\n"
    "ds_read_b64_tr_b16 a[108:109], v27 offset:16384\n"
    "v_cvt_pk_bf16_f32 v64, v64, v65\n"
    "v_mfma_f32_16x16x32_bf16 v[88:91], a[72:75], a[56:59], v[88:91]\n"
    "v_exp_f32 v66, v66\n"
    "v_mfma_f32_16x16x32_bf16 v[88:91], a[76:79], a[60:63], v[88:91]\n"
    "s_waitcnt lgkmcnt(10)\n"
    "ds_read_b64_tr_b16 a[110:111], v27 offset:20480\n"
    "v_mul_u32_u24 v13, 864, v13\n"
    "v_mfma_f32_16x16x32_bf16 v[68:71], a[80:83], a[0:3], v[48:51]\n"
    "s_waitcnt lgkmcnt(10)\n"
    "v_exp_f32 v67, v67\n"
    "v_mfma_f32_16x16x32_bf16 v[68:71], a[84:87], a[4:7], v[68:71]\n"
    "s_waitcnt lgkmcnt(9)\n"
    "ds_read_b64_tr_b16 a[112:113], v28 offset:16384\n"
    "v_cvt_pk_bf16_f32 v65, v66, v67\n"
    "v_mfma_f32_16x16x32_bf16 v[68:71], a[88:91], a[8:11], v[68:71]\n"
    "s_waitcnt lgkmcnt(9)\n"
    "v_exp_f32 v72, v72\n"
    "v_mfma_f32_16x16x32_bf16 v[68:71], a[92:95], a[12:15], v[68:71]\n"
    "ds_read_b64_tr_b16 a[114:115], v28 offset:20480\n"
    "v_add_u32 v2, v2, v13\n"
    "v_mfma_f32_16x16x32_bf16 v[76:79], a[80:83], a[16:19], v[52:55]\n"
    "v_exp_f32 v73, v73\n"
    "v_mfma_f32_16x16x32_bf16 v[76:79], a[84:87], a[20:23], v[76:79]\n"
    "ds_read_b64_tr_b16 a[116:117], v29 offset:16384\n"
    "v_cvt_pk_bf16_f32 v72, v72, v73\n"
    "v_mfma_f32_16x16x32_bf16 v[76:79], a[88:91], a[24:27], v[76:79]\n"
    "v_exp_f32 v74, v74\n"
    "v_mfma_f32_16x16x32_bf16 v[76:79], a[92:95], a[28:31], v[76:79]\n"
    "ds_read_b64_tr_b16 a[118:119], v29 offset:20480\n"
    "v_add_u32 v3, 1024, v0\n"
    "v_mfma_f32_16x16x32_bf16 v[84:87], a[80:83], a[32:35], v[56:59]\n"
    "v_exp_f32 v75, v75\n"
    "v_mfma_f32_16x16x32_bf16 v[84:87], a[84:87], a[36:39], v[84:87]\n"
    "ds_read_b64_tr_b16 a[120:121], v30 offset:16384\n"
    "v_cvt_pk_bf16_f32 v73, v74, v75\n"
    "v_mfma_f32_16x16x32_bf16 v[84:87], a[88:91], a[40:43], v[84:87]\n"
    "v_exp_f32 v80, v80\n"
    "v_mfma_f32_16x16x32_bf16 v[84:87], a[92:95], a[44:47], v[84:87]\n"
    "ds_read_b64_tr_b16 a[122:123], v30 offset:20480\n"
    "v_mul_u32_u24 v13, 58255, v3\n"
    "v_mfma_f32_16x16x32_bf16 v[92:95], a[80:83], a[48:51], v[60:63]\n"
    "v_exp_f32 v81, v81\n"
    "v_mfma_f32_16x16x32_bf16 v[92:95], a[84:87], a[52:55], v[92:95]\n"
    "ds_read_b64_tr_b16 a[124:125], v31 offset:16384\n"
    "v_cvt_pk_bf16_f32 v80, v80, v81\n"
    "v_mfma_f32_16x16x32_bf16 v[92:95], a[88:91], a[56:59], v[92:95]\n"
    "v_exp_f32 v82, v82\n"
    "v_mfma_f32_16x16x32_bf16 v[92:95], a[92:95], a[60:63], v[92:95]\n"
    "ds_read_b64_tr_b16 a[126:127], v31 offset:20480\n"
    "v_lshrrev_b32 v13, 24, v13\n"
    "v_mfma_f32_16x16x32_bf16 a[128:131], a[144:147], v[96:99], a[128:131]\n"
    "s_add_u32 s38, s95, 2\n"
    "v_exp_f32 v83, v83\n"
    "v_mfma_f32_16x16x32_bf16 a[132:135], a[144:147], v[104:107], a[132:135]\n"
    "buffer_load_dwordx2 v[14:15], v1, s[76:79], s12 offen sc1\n"
    "v_cvt_pk_bf16_f32 v81, v82, v83\n"
    "v_mfma_f32_16x16x32_bf16 a[136:139], a[144:147], v[112:115], a[136:139]\n"
    "v_exp_f32 v88, v88\n"
    "v_mfma_f32_16x16x32_bf16 a[140:143], a[144:147], v[120:123], a[140:143]\n"
    "s_waitcnt lgkmcnt(14)\n"
    "v_mul_u32_u24 v13, 864, v13\n"
    "v_add_u32 v3, v3, v13\n"
    "v_mfma_f32_16x16x32_bf16 v[128:131], a[96:99], v[96:99], v[128:131]\n"
    "v_exp_f32 v89, v89\n"
    "v_mfma_f32_16x16x32_bf16 v[160:163], a[96:99], v[104:107], v[160:163]\n"
    "s_waitcnt vmcnt(9)\n"
    "s_barrier\n"
    "v_cvt_pk_bf16_f32 v88, v88, v89\n"
    "v_subrev_u32 v3, 1024, v3\n"
    "v_mfma_f32_16x16x32_bf16 v[192:195], a[96:99], v[112:115], v[192:195]\n"
    "v_exp_f32 v90, v90\n"
    "v_mfma_f32_16x16x32_bf16 v[224:227], a[96:99], v[120:123], v[224:227]\n"
    "s_waitcnt lgkmcnt(12)\n"
    "v_add_u32 v4, 2048, v0\n"
    "v_mul_u32_u24 v13, 58255, v4\n"
    "v_mfma_f32_16x16x32_bf16 v[132:135], a[100:103], v[96:99], v[132:135]\n"
    "v_exp_f32 v91, v91\n"
    "v_mfma_f32_16x16x32_bf16 v[164:167], a[100:103], v[104:107], v[164:167]\n"
    "ds_read_b128 a[64:67], v16 offset:32768\n"
    "v_cvt_pk_bf16_f32 v89, v90, v91\n"
    "v_mfma_f32_16x16x32_bf16 v[196:199], a[100:103], v[112:115], v[196:199]\n"
    "v_exp_f32 v68, v68\n"
    "v_mfma_f32_16x16x32_bf16 v[228:231], a[100:103], v[120:123], v[228:231]\n"
    "s_waitcnt lgkmcnt(11)\n"
    "ds_read_b128 a[68:71], v17 offset:32768\n"
    "v_lshrrev_b32 v13, 24, v13\n"
    "v_mfma_f32_16x16x32_bf16 v[136:139], a[104:107], v[96:99], v[136:139]\n"
    "v_exp_f32 v69, v69\n"
    "v_mfma_f32_16x16x32_bf16 v[168:171], a[104:107], v[104:107], v[168:171]\n"
    "ds_read_b128 a[72:75], v18 offset:32768\n"
    "v_cvt_pk_bf16_f32 v66, v68, v69\n"
    "v_mfma_f32_16x16x32_bf16 v[200:203], a[104:107], v[112:115], v[200:203]\n"
    "v_exp_f32 v70, v70\n"
    "v_mfma_f32_16x16x32_bf16 v[232:235], a[104:107], v[120:123], v[232:235]\n"
    "s_waitcnt lgkmcnt(11)\n"
    "ds_read_b128 a[76:79], v19 offset:32768\n"
    "v_mul_u32_u24 v13, 864, v13\n"
    "v_mfma_f32_16x16x32_bf16 v[140:143], a[108:111], v[96:99], v[140:143]\n"
    "v_exp_f32 v71, v71\n"
    "v_mfma_f32_16x16x32_bf16 v[172:175], a[108:111], v[104:107], v[172:175]\n"
    "ds_read_b128 a[80:83], v16 offset:36864\n"
    "v_cvt_pk_bf16_f32 v67, v70, v71\n"
    "v_mfma_f32_16x16x32_bf16 v[204:207], a[108:111], v[112:115], v[204:207]\n"
    "v_exp_f32 v76, v76\n"
    "v_mfma_f32_16x16x32_bf16 v[236:239], a[108:111], v[120:123], v[236:239]\n"
    "s_waitcnt lgkmcnt(11)\n"
    "ds_read_b128 a[84:87], v17 offset:36864\n"
    "v_add_u32 v4, v4, v13\n"
    "v_mfma_f32_16x16x32_bf16 v[144:147], a[112:115], v[96:99], v[144:147]\n"
    "v_exp_f32 v77, v77\n"
    "v_mfma_f32_16x16x32_bf16 v[176:179], a[112:115], v[104:107], v[176:179]\n"
    "ds_read_b128 a[88:91], v18 offset:36864\n"
    "v_cvt_pk_bf16_f32 v74, v76, v77\n"
    "v_mfma_f32_16x16x32_bf16 v[208:211], a[112:115], v[112:115], v[208:211]\n"
    "v_exp_f32 v78, v78\n"
    "v_mfma_f32_16x16x32_bf16 v[240:243], a[112:115], v[120:123], v[240:243]\n"
    "s_waitcnt lgkmcnt(11)\n"
    "ds_read_b128 a[92:95], v19 offset:36864\n"
    "v_subrev_u32 v4, 2048, v4\n"
    "v_mfma_f32_16x16x32_bf16 v[148:151], a[116:119], v[96:99], v[148:151]\n"
    "s_add_u32 m0, s86, 0\n"
    "v_exp_f32 v79, v79\n"
    "v_mfma_f32_16x16x32_bf16 v[180:183], a[116:119], v[104:107], v[180:183]\n"
    "buffer_load_dwordx4 v40, s[56:59], s92 offen lds\n"
    "v_cvt_pk_bf16_f32 v75, v78, v79\n"
    "v_mfma_f32_16x16x32_bf16 v[212:215], a[116:119], v[112:115], v[212:215]\n"
    "v_exp_f32 v84, v84\n"
    "v_mfma_f32_16x16x32_bf16 v[244:247], a[116:119], v[120:123], v[244:247]\n"
    "s_waitcnt lgkmcnt(10)\n"
    "buffer_load_dwordx4 v41, s[56:59], s92 offen offset:1024 lds\n"
    "v_mfma_f32_16x16x32_bf16 v[152:155], a[120:123], v[96:99], v[152:155]\n"
    "v_exp_f32 v85, v85\n"
    "v_mfma_f32_16x16x32_bf16 v[184:187], a[120:123], v[104:107], v[184:187]\n"
    "buffer_load_dwordx4 v42, s[56:59], s92 offen offset:2048 lds\n"
    "v_cvt_pk_bf16_f32 v82, v84, v85\n"
    "v_mfma_f32_16x16x32_bf16 v[216:219], a[120:123], v[112:115], v[216:219]\n"
    "v_exp_f32 v86, v86\n"
    "v_mfma_f32_16x16x32_bf16 v[248:251], a[120:123], v[120:123], v[248:251]\n"
    "s_waitcnt lgkmcnt(8)\n"
    "buffer_load_dwordx4 v43, s[56:59], s92 offen offset:3072 lds\n"
    "v_mfma_f32_16x16x32_bf16 v[156:159], a[124:127], v[96:99], v[156:159]\n"
    "v_mfma_f32_16x16x32_bf16 v[188:191], a[124:127], v[104:107], v[188:191]\n"
    "v_mfma_f32_16x16x32_bf16 v[220:223], a[124:127], v[112:115], v[220:223]\n"
    "v_mfma_f32_16x16x32_bf16 v[252:255], a[124:127], v[120:123], v[252:255]\n"
    "s_waitcnt lgkmcnt(7)\n"
    "v_mfma_f32_16x16x32_bf16 v[96:99], a[64:67], a[0:3], v[48:51]\n"
    "s_waitcnt lgkmcnt(6)\n"
    "v_exp_f32 v87, v87\n"
    "v_mfma_f32_16x16x32_bf16 v[96:99], a[68:71], a[4:7], v[96:99]\n"
    "s_waitcnt lgkmcnt(5)\n"
    "ds_read_b64_tr_b16 a[96:97], v24 offset:24576\n"
    "v_cvt_pk_bf16_f32 v83, v86, v87\n"
    "v_mfma_f32_16x16x32_bf16 v[96:99], a[72:75], a[8:11], v[96:99]\n"
    "s_waitcnt lgkmcnt(5)\n"
    "v_exp_f32 v92, v92\n"
    "v_mfma_f32_16x16x32_bf16 v[96:99], a[76:79], a[12:15], v[96:99]\n"
    "ds_read_b64_tr_b16 a[98:99], v24 offset:28672\n"
    "v_add_u32 v5, 3072, v0\n"
    "v_mfma_f32_16x16x32_bf16 v[104:107], a[64:67], a[16:19], v[52:55]\n"
    "v_exp_f32 v93, v93\n"
    "v_mfma_f32_16x16x32_bf16 v[104:107], a[68:71], a[20:23], v[104:107]\n"
    "ds_read_b64_tr_b16 a[100:101], v25 offset:24576\n"
    "v_cvt_pk_bf16_f32 v90, v92, v93\n"
    "v_mfma_f32_16x16x32_bf16 v[104:107], a[72:75], a[24:27], v[104:107]\n"
    "v_exp_f32 v94, v94\n"
    "v_mfma_f32_16x16x32_bf16 v[104:107], a[76:79], a[28:31], v[104:107]\n"
    "ds_read_b64_tr_b16 a[102:103], v25 offset:28672\n"
    "v_mul_u32_u24 v13, 58255, v5\n"
    "v_mfma_f32_16x16x32_bf16 v[112:115], a[64:67], a[32:35], v[56:59]\n"
    "v_exp_f32 v95, v95\n"
    "v_mfma_f32_16x16x32_bf16 v[112:115], a[68:71], a[36:39], v[112:115]\n"
    "ds_read_b64_tr_b16 a[104:105], v26 offset:24576\n"
    "v_cvt_pk_bf16_f32 v91, v94, v95\n"
    "v_mfma_f32_16x16x32_bf16 v[112:115], a[72:75], a[40:43], v[112:115]\n"
    "v_exp_f32 v96, v96\n"
    "v_mfma_f32_16x16x32_bf16 v[112:115], a[76:79], a[44:47], v[112:115]\n"
    "ds_read_b64_tr_b16 a[106:107], v26 offset:28672\n"
    "v_lshrrev_b32 v13, 24, v13\n"
    "v_mfma_f32_16x16x32_bf16 v[120:123], a[64:67], a[48:51], v[60:63]\n"
    "v_exp_f32 v97, v97\n"
    "v_mfma_f32_16x16x32_bf16 v[120:123], a[68:71], a[52:55], v[120:123]\n"
    "ds_read_b64_tr_b16 a[108:109], v27 offset:24576\n"
    "v_cvt_pk_bf16_f32 v96, v96, v97\n"
    "v_mfma_f32_16x16x32_bf16 v[120:123], a[72:75], a[56:59], v[120:123]\n"
    "v_exp_f32 v98, v98\n"
    "v_mfma_f32_16x16x32_bf16 v[120:123], a[76:79], a[60:63], v[120:123]\n"
    "s_waitcnt lgkmcnt(10)\n"
    "ds_read_b64_tr_b16 a[110:111], v27 offset:28672\n"
    "v_mul_u32_u24 v13, 864, v13\n"
    "v_mfma_f32_16x16x32_bf16 v[100:103], a[80:83], a[0:3], v[48:51]\n"
    "s_waitcnt lgkmcnt(10)\n"
    "v_exp_f32 v99, v99\n"
    "v_mfma_f32_16x16x32_bf16 v[100:103], a[84:87], a[4:7], v[100:103]\n"
    "s_waitcnt lgkmcnt(9)\n"
    "ds_read_b64_tr_b16 a[112:113], v28 offset:24576\n"
    "v_cvt_pk_bf16_f32 v97, v98, v99\n"
    "v_mfma_f32_16x16x32_bf16 v[100:103], a[88:91], a[8:11], v[100:103]\n"
    "s_waitcnt lgkmcnt(9)\n"
    "v_exp_f32 v104, v104\n"
    "v_mfma_f32_16x16x32_bf16 v[100:103], a[92:95], a[12:15], v[100:103]\n"
    "ds_read_b64_tr_b16 a[114:115], v28 offset:28672\n"
    "v_add_u32 v5, v5, v13\n"
    "v_mfma_f32_16x16x32_bf16 v[108:111], a[80:83], a[16:19], v[52:55]\n"
    "v_exp_f32 v105, v105\n"
    "v_mfma_f32_16x16x32_bf16 v[108:111], a[84:87], a[20:23], v[108:111]\n"
    "ds_read_b64_tr_b16 a[116:117], v29 offset:24576\n"
    "v_cvt_pk_bf16_f32 v104, v104, v105\n"
    "v_mfma_f32_16x16x32_bf16 v[108:111], a[88:91], a[24:27], v[108:111]\n"
    "v_exp_f32 v106, v106\n"
    "v_mfma_f32_16x16x32_bf16 v[108:111], a[92:95], a[28:31], v[108:111]\n"
    "ds_read_b64_tr_b16 a[118:119], v29 offset:28672\n"
    "v_subrev_u32 v5, 3072, v5\n"
    "v_mfma_f32_16x16x32_bf16 v[116:119], a[80:83], a[32:35], v[56:59]\n"
    "v_exp_f32 v107, v107\n"
    "v_mfma_f32_16x16x32_bf16 v[116:119], a[84:87], a[36:39], v[116:119]\n"
    "ds_read_b64_tr_b16 a[120:121], v30 offset:24576\n"
    "v_cvt_pk_bf16_f32 v105, v106, v107\n"
    "v_mfma_f32_16x16x32_bf16 v[116:119], a[88:91], a[40:43], v[116:119]\n"
    "v_exp_f32 v112, v112\n"
    "v_mfma_f32_16x16x32_bf16 v[116:119], a[92:95], a[44:47], v[116:119]\n"
    "ds_read_b64_tr_b16 a[122:123], v30 offset:28672\n"
    "v_add_u32 v6, 4096, v0\n"
    "v_mfma_f32_16x16x32_bf16 v[124:127], a[80:83], a[48:51], v[60:63]\n"
    "v_exp_f32 v113, v113\n"
    "v_mfma_f32_16x16x32_bf16 v[124:127], a[84:87], a[52:55], v[124:127]\n"
    "ds_read_b64_tr_b16 a[124:125], v31 offset:24576\n"
    "v_cvt_pk_bf16_f32 v112, v112, v113\n"
    "v_mfma_f32_16x16x32_bf16 v[124:127], a[88:91], a[56:59], v[124:127]\n"
    "v_exp_f32 v114, v114\n"
    "v_mfma_f32_16x16x32_bf16 v[124:127], a[92:95], a[60:63], v[124:127]\n"
    "ds_read_b64_tr_b16 a[126:127], v31 offset:28672\n"
    "v_mul_u32_u24 v13, 58255, v6\n"
    "v_mfma_f32_16x16x32_bf16 a[128:131], a[144:147], v[64:67], a[128:131]\n"
    "v_exp_f32 v115, v115\n"
    "v_mfma_f32_16x16x32_bf16 a[132:135], a[144:147], v[72:75], a[132:135]\n"
    "v_cvt_pk_bf16_f32 v113, v114, v115\n"
    "v_lshrrev_b32 v13, 24, v13\n"
    "v_mfma_f32_16x16x32_bf16 a[136:139], a[144:147], v[80:83], a[136:139]\n"
    "v_exp_f32 v120, v120\n"
    "v_mfma_f32_16x16x32_bf16 a[140:143], a[144:147], v[88:91], a[140:143]\n"
    "s_waitcnt vmcnt(4)\n"
    "v_subrev_u32 v14, s38, v14\n"
    "v_xor_b32 v15, v47, v15\n"
    "v_cmp_lt_u64 vcc, 15, v[14:15]\n"
    "s_nop 0\n"
    "s_cbranch_vccnz L_SP0_%=\n"
    "L_BK0_%=:\n"
    "s_waitcnt lgkmcnt(14)\n"
    "v_mfma_f32_16x16x32_bf16 v[128:131], a[96:99], v[64:67], v[128:131]\n"
    "v_exp_f32 v121, v121\n"
    "v_mfma_f32_16x16x32_bf16 v[160:163], a[96:99], v[72:75], v[160:163]\n"
    "v_cvt_pk_bf16_f32 v120, v120, v121\n"
    "v_mul_u32_u24 v13, 864, v13\n"
    "v_mfma_f32_16x16x32_bf16 v[192:195], a[96:99], v[80:83], v[192:195]\n"
    "v_exp_f32 v122, v122\n"
    "v_mfma_f32_16x16x32_bf16 v[224:227], a[96:99], v[88:91], v[224:227]\n"
    "s_waitcnt lgkmcnt(12)\n"
    "v_add_u32 v6, v6, v13\n"
    "v_add_u32 v7, 5120, v0\n"
    "v_mfma_f32_16x16x32_bf16 v[132:135], a[100:103], v[64:67], v[132:135]\n"
    "v_exp_f32 v123, v123\n"
    "v_mfma_f32_16x16x32_bf16 v[164:167], a[100:103], v[72:75], v[164:167]\n"
    "ds_read_b128 a[64:67], v16 offset:40960\n"
    "v_cvt_pk_bf16_f32 v121, v122, v123\n"
    "v_mfma_f32_16x16x32_bf16 v[196:199], a[100:103], v[80:83], v[196:199]\n"
    "v_exp_f32 v100, v100\n"
    "v_mfma_f32_16x16x32_bf16 v[228:231], a[100:103], v[88:91], v[228:231]\n"
    "s_waitcnt lgkmcnt(11)\n"
    "ds_read_b128 a[68:71], v17 offset:40960\n"
    "v_mul_u32_u24 v13, 58255, v7\n"
    "v_mfma_f32_16x16x32_bf16 v[136:139], a[104:107], v[64:67], v[136:139]\n"
    "v_exp_f32 v101, v101\n"
    "v_mfma_f32_16x16x32_bf16 v[168:171], a[104:107], v[72:75], v[168:171]\n"
    "ds_read_b128 a[72:75], v18 offset:40960\n"
    "v_cvt_pk_bf16_f32 v98, v100, v101\n"
    "v_mfma_f32_16x16x32_bf16 v[200:203], a[104:107], v[80:83], v[200:203]\n"
    "v_exp_f32 v102, v102\n"
    "v_mfma_f32_16x16x32_bf16 v[232:235], a[104:107], v[88:91], v[232:235]\n"
    "s_waitcnt lgkmcnt(11)\n"
    "ds_read_b128 a[76:79], v19 offset:40960\n"
    "v_lshrrev_b32 v13, 24, v13\n"
    "v_mfma_f32_16x16x32_bf16 v[140:143], a[108:111], v[64:67], v[140:143]\n"
    "v_exp_f32 v103, v103\n"
    "v_mfma_f32_16x16x32_bf16 v[172:175], a[108:111], v[72:75], v[172:175]\n"
    "ds_read_b128 a[80:83], v16 offset:45056\n"
    "v_cvt_pk_bf16_f32 v99, v102, v103\n"
    "v_mfma_f32_16x16x32_bf16 v[204:207], a[108:111], v[80:83], v[204:207]\n"
    "v_exp_f32 v108, v108\n"
    "v_mfma_f32_16x16x32_bf16 v[236:239], a[108:111], v[88:91], v[236:239]\n"
    "s_waitcnt lgkmcnt(11)\n"
    "ds_read_b128 a[84:87], v17 offset:45056\n"
    "v_mul_u32_u24 v13, 864, v13\n"
    "v_mfma_f32_16x16x32_bf16 v[144:147], a[112:115], v[64:67], v[144:147]\n"
    "v_exp_f32 v109, v109\n"
    "v_mfma_f32_16x16x32_bf16 v[176:179], a[112:115], v[72:75], v[176:179]\n"
    "ds_read_b128 a[88:91], v18 offset:45056\n"
    "v_cvt_pk_bf16_f32 v106, v108, v109\n"
    "v_mfma_f32_16x16x32_bf16 v[208:211], a[112:115], v[80:83], v[208:211]\n"
    "v_exp_f32 v110, v110\n"
    "v_mfma_f32_16x16x32_bf16 v[240:243], a[112:115], v[88:91], v[240:243]\n"
    "s_waitcnt lgkmcnt(11)\n"
    "ds_read_b128 a[92:95], v19 offset:45056\n"
    "v_add_u32 v7, v7, v13\n"
    "v_mfma_f32_16x16x32_bf16 v[148:151], a[116:119], v[64:67], v[148:151]\n"
    "s_add_u32 m0, s86, 16384\n"
    "v_exp_f32 v111, v111\n"
    "v_mfma_f32_16x16x32_bf16 v[180:183], a[116:119], v[72:75], v[180:183]\n"
    "buffer_load_dwordx4 v44, s[60:63], s92 offen lds\n"
    "v_cvt_pk_bf16_f32 v107, v110, v111\n"
    "v_mfma_f32_16x16x32_bf16 v[212:215], a[116:119], v[80:83], v[212:215]\n"
    "v_exp_f32 v116, v116\n"
    "v_mfma_f32_16x16x32_bf16 v[244:247], a[116:119], v[88:91], v[244:247]\n"
    "s_waitcnt lgkmcnt(10)\n"
    "buffer_load_dwordx4 v45, s[60:63], s92 offen offset:1024 lds\n"
    "v_mfma_f32_16x16x32_bf16 v[152:155], a[120:123], v[64:67], v[152:155]\n"
    "v_exp_f32 v117, v117\n"
    "v_mfma_f32_16x16x32_bf16 v[184:187], a[120:123], v[72:75], v[184:187]\n"
    "buffer_load_dwordx4 v44, s[60:63], s92 offen offset:2048 lds\n"
    "v_cvt_pk_bf16_f32 v114, v116, v117\n"
    "v_mfma_f32_16x16x32_bf16 v[216:219], a[120:123], v[80:83], v[216:219]\n"
    "v_exp_f32 v118, v118\n"
    "v_mfma_f32_16x16x32_bf16 v[248:251], a[120:123], v[88:91], v[248:251]\n"
    "s_waitcnt lgkmcnt(8)\n"
    "buffer_load_dwordx4 v45, s[60:63], s92 offen offset:3072 lds\n"
    "s_add_u32 s92, s92, 16384\n"
    "v_mfma_f32_16x16x32_bf16 v[156:159], a[124:127], v[64:67], v[156:159]\n"
    "s_lshr_b32 s83, s19, 1\n"
    "s_add_u32 s83, s83, 24576\n"
    "s_or_b32 s83, s83, s18\n"
    "v_mfma_f32_16x16x32_bf16 v[188:191], a[124:127], v[72:75], v[188:191]\n"
    "buffer_load_dword a152, v12, s[8:11], s83 offen sc1\n"
    "v_mfma_f32_16x16x32_bf16 v[220:223], a[124:127], v[80:83], v[220:223]\n"
    "v_mfma_f32_16x16x32_bf16 v[252:255], a[124:127], v[88:91], v[252:255]\n"
    "s_waitcnt lgkmcnt(7)\n"
    "v_mfma_f32_16x16x32_bf16 v[64:67], a[64:67], a[0:3], v[48:51]\n"
    "s_waitcnt lgkmcnt(6)\n"
    "v_exp_f32 v119, v119\n"
    "v_mfma_f32_16x16x32_bf16 v[64:67], a[68:71], a[4:7], v[64:67]\n"
    "s_waitcnt lgkmcnt(5)\n"
    "ds_read_b64_tr_b16 a[96:97], v24 offset:49152\n"
    "v_cvt_pk_bf16_f32 v115, v118, v119\n"
    "v_mfma_f32_16x16x32_bf16 v[64:67], a[72:75], a[8:11], v[64:67]\n"
    "s_waitcnt lgkmcnt(5)\n"
    "v_exp_f32 v124, v124\n"
    "v_mfma_f32_16x16x32_bf16 v[64:67], a[76:79], a[12:15], v[64:67]\n"
    "ds_read_b64_tr_b16 a[98:99], v24 offset:53248\n"
    "v_subrev_u32 v7, 1024, v7\n"
    "v_mfma_f32_16x16x32_bf16 v[72:75], a[64:67], a[16:19], v[52:55]\n"
    "v_exp_f32 v125, v125\n"
    "v_mfma_f32_16x16x32_bf16 v[72:75], a[68:71], a[20:23], v[72:75]\n"
    "ds_read_b64_tr_b16 a[100:101], v25 offset:49152\n"
    "v_cvt_pk_bf16_f32 v122, v124, v125\n"
    "v_mfma_f32_16x16x32_bf16 v[72:75], a[72:75], a[24:27], v[72:75]\n"
    "v_exp_f32 v126, v126\n"
    "v_mfma_f32_16x16x32_bf16 v[72:75], a[76:79], a[28:31], v[72:75]\n"
    "ds_read_b64_tr_b16 a[102:103], v25 offset:53248\n"
    "v_add_u32 v8, 6144, v0\n"
    "v_mfma_f32_16x16x32_bf16 v[80:83], a[64:67], a[32:35], v[56:59]\n"
    "v_exp_f32 v127, v127\n"
    "v_mfma_f32_16x16x32_bf16 v[80:83], a[68:71], a[36:39], v[80:83]\n"
    "ds_read_b64_tr_b16 a[104:105], v26 offset:49152\n"
    "v_cvt_pk_bf16_f32 v123, v126, v127\n"
    "v_mfma_f32_16x16x32_bf16 v[80:83], a[72:75], a[40:43], v[80:83]\n"
    "v_exp_f32 v64, v64\n"
    "v_mfma_f32_16x16x32_bf16 v[80:83], a[76:79], a[44:47], v[80:83]\n"
    "ds_read_b64_tr_b16 a[106:107], v26 offset:53248\n"
    "v_mul_u32_u24 v13, 58255, v8\n"
    "v_mfma_f32_16x16x32_bf16 v[88:91], a[64:67], a[48:51], v[60:63]\n"
    "v_exp_f32 v65, v65\n"
    "v_mfma_f32_16x16x32_bf16 v[88:91], a[68:71], a[52:55], v[88:91]\n"
    "ds_read_b64_tr_b16 a[108:109], v27 offset:49152\n"
    "v_cvt_pk_bf16_f32 v64, v64, v65\n"
    "v_mfma_f32_16x16x32_bf16 v[88:91], a[72:75], a[56:59], v[88:91]\n"
    "v_exp_f32 v66, v66\n"
    "v_mfma_f32_16x16x32_bf16 v[88:91], a[76:79], a[60:63], v[88:91]\n"
    "s_waitcnt lgkmcnt(10)\n"
    "ds_read_b64_tr_b16 a[110:111], v27 offset:53248\n"
    "v_lshrrev_b32 v13, 24, v13\n"
    "v_mfma_f32_16x16x32_bf16 v[68:71], a[80:83], a[0:3], v[48:51]\n"
    "s_waitcnt lgkmcnt(10)\n"
    "v_exp_f32 v67, v67\n"
    "v_mfma_f32_16x16x32_bf16 v[68:71], a[84:87], a[4:7], v[68:71]\n"
    "s_waitcnt lgkmcnt(9)\n"
    "ds_read_b64_tr_b16 a[112:113], v28 offset:49152\n"
    "v_cvt_pk_bf16_f32 v65, v66, v67\n"
    "v_mfma_f32_16x16x32_bf16 v[68:71], a[88:91], a[8:11], v[68:71]\n"
    "s_waitcnt lgkmcnt(9)\n"
    "v_exp_f32 v72, v72\n"
    "v_mfma_f32_16x16x32_bf16 v[68:71], a[92:95], a[12:15], v[68:71]\n"
    "ds_read_b64_tr_b16 a[114:115], v28 offset:53248\n"
    "v_mul_u32_u24 v13, 864, v13\n"
    "v_mfma_f32_16x16x32_bf16 v[76:79], a[80:83], a[16:19], v[52:55]\n"
    "v_exp_f32 v73, v73\n"
    "v_mfma_f32_16x16x32_bf16 v[76:79], a[84:87], a[20:23], v[76:79]\n"
    "ds_read_b64_tr_b16 a[116:117], v29 offset:49152\n"
    "v_cvt_pk_bf16_f32 v72, v72, v73\n"
    "v_mfma_f32_16x16x32_bf16 v[76:79], a[88:91], a[24:27], v[76:79]\n"
    "v_exp_f32 v74, v74\n"
    "v_mfma_f32_16x16x32_bf16 v[76:79], a[92:95], a[28:31], v[76:79]\n"
    "ds_read_b64_tr_b16 a[118:119], v29 offset:53248\n"
    "v_add_u32 v8, v8, v13\n"
    "v_mfma_f32_16x16x32_bf16 v[84:87], a[80:83], a[32:35], v[56:59]\n"
    "v_exp_f32 v75, v75\n"
    "v_mfma_f32_16x16x32_bf16 v[84:87], a[84:87], a[36:39], v[84:87]\n"
    "ds_read_b64_tr_b16 a[120:121], v30 offset:49152\n"
    "v_cvt_pk_bf16_f32 v73, v74, v75\n"
    "v_mfma_f32_16x16x32_bf16 v[84:87], a[88:91], a[40:43], v[84:87]\n"
    "v_exp_f32 v80, v80\n"
    "v_mfma_f32_16x16x32_bf16 v[84:87], a[92:95], a[44:47], v[84:87]\n"
    "ds_read_b64_tr_b16 a[122:123], v30 offset:53248\n"
    "v_subrev_u32 v8, 2048, v8\n"
    "v_mfma_f32_16x16x32_bf16 v[92:95], a[80:83], a[48:51], v[60:63]\n"
    "v_exp_f32 v81, v81\n"
    "v_mfma_f32_16x16x32_bf16 v[92:95], a[84:87], a[52:55], v[92:95]\n"
    "ds_read_b64_tr_b16 a[124:125], v31 offset:49152\n"
    "v_cvt_pk_bf16_f32 v80, v80, v81\n"
    "v_mfma_f32_16x16x32_bf16 v[92:95], a[88:91], a[56:59], v[92:95]\n"
    "v_exp_f32 v82, v82\n"
    "v_mfma_f32_16x16x32_bf16 v[92:95], a[92:95], a[60:63], v[92:95]\n"
    "ds_read_b64_tr_b16 a[126:127], v31 offset:53248\n"
    "v_add_u32 v9, 7168, v0\n"
    "v_mfma_f32_16x16x32_bf16 a[128:131], a[144:147], v[96:99], a[128:131]\n"
    "v_exp_f32 v83, v83\n"
    "v_mfma_f32_16x16x32_bf16 a[132:135], a[144:147], v[104:107], a[132:135]\n"
    "v_cvt_pk_bf16_f32 v81, v82, v83\n"
    "v_mul_u32_u24 v13, 58255, v9\n"
    "v_mfma_f32_16x16x32_bf16 a[136:139], a[144:147], v[112:115], a[136:139]\n"
    "v_exp_f32 v88, v88\n"
    "v_mfma_f32_16x16x32_bf16 a[140:143], a[144:147], v[120:123], a[140:143]\n"
    "s_waitcnt lgkmcnt(14)\n"
    "v_lshrrev_b32 v13, 24, v13\n"
    "v_mul_u32_u24 v13, 864, v13\n"
    "v_mfma_f32_16x16x32_bf16 v[128:131], a[96:99], v[96:99], v[128:131]\n"
    "v_exp_f32 v89, v89\n"
    "v_mfma_f32_16x16x32_bf16 v[160:163], a[96:99], v[104:107], v[160:163]\n"
    "s_barrier\n"
    "v_cvt_pk_bf16_f32 v88, v88, v89\n"
    "v_add_u32 v9, v9, v13\n"
    "v_mfma_f32_16x16x32_bf16 v[192:195], a[96:99], v[112:115], v[192:195]\n"
    "v_exp_f32 v90, v90\n"
    "v_mfma_f32_16x16x32_bf16 v[224:227], a[96:99], v[120:123], v[224:227]\n"
    "s_waitcnt lgkmcnt(12)\n"
    "v_subrev_u32 v9, 3072, v9\n"
    "v_add_u32 v10, 8192, v0\n"
    "v_mfma_f32_16x16x32_bf16 v[132:135], a[100:103], v[96:99], v[132:135]\n"
    "v_exp_f32 v91, v91\n"
    "v_mfma_f32_16x16x32_bf16 v[164:167], a[100:103], v[104:107], v[164:167]\n"
    "ds_read_b128 a[64:67], v20 offset:32768\n"
    "v_cvt_pk_bf16_f32 v89, v90, v91\n"
    "v_mfma_f32_16x16x32_bf16 v[196:199], a[100:103], v[112:115], v[196:199]\n"
    "v_exp_f32 v68, v68\n"
    "v_mfma_f32_16x16x32_bf16 v[228:231], a[100:103], v[120:123], v[228:231]\n"
    "s_waitcnt lgkmcnt(11)\n"
    "ds_read_b128 a[68:71], v21 offset:32768\n"
    "v_mul_u32_u24 v13, 58255, v10\n"
    "v_mfma_f32_16x16x32_bf16 v[136:139], a[104:107], v[96:99], v[136:139]\n"
    "v_exp_f32 v69, v69\n"
    "v_mfma_f32_16x16x32_bf16 v[168:171], a[104:107], v[104:107], v[168:171]\n"
    "ds_read_b128 a[72:75], v22 offset:32768\n"
    "v_cvt_pk_bf16_f32 v66, v68, v69\n"
    "v_mfma_f32_16x16x32_bf16 v[200:203], a[104:107], v[112:115], v[200:203]\n"
    "v_exp_f32 v70, v70\n"
    "v_mfma_f32_16x16x32_bf16 v[232:235], a[104:107], v[120:123], v[232:235]\n"
    "s_waitcnt lgkmcnt(11)\n"
    "ds_read_b128 a[76:79], v23 offset:32768\n"
    "v_lshrrev_b32 v13, 24, v13\n"
    "v_mfma_f32_16x16x32_bf16 v[140:143], a[108:111], v[96:99], v[140:143]\n"
    "v_exp_f32 v71, v71\n"
    "v_mfma_f32_16x16x32_bf16 v[172:175], a[108:111], v[104:107], v[172:175]\n"
    "ds_read_b128 a[80:83], v20 offset:36864\n"
    "v_cvt_pk_bf16_f32 v67, v70, v71\n"
    "v_mfma_f32_16x16x32_bf16 v[204:207], a[108:111], v[112:115], v[204:207]\n"
    "v_exp_f32 v76, v76\n"
    "v_mfma_f32_16x16x32_bf16 v[236:239], a[108:111], v[120:123], v[236:239]\n"
    "s_waitcnt lgkmcnt(11)\n"
    "ds_read_b128 a[84:87], v21 offset:36864\n"
    "v_mul_u32_u24 v13, 864, v13\n"
    "v_mfma_f32_16x16x32_bf16 v[144:147], a[112:115], v[96:99], v[144:147]\n"
    "v_exp_f32 v77, v77\n"
    "v_mfma_f32_16x16x32_bf16 v[176:179], a[112:115], v[104:107], v[176:179]\n"
    "ds_read_b128 a[88:91], v22 offset:36864\n"
    "v_cvt_pk_bf16_f32 v74, v76, v77\n"
    "v_mfma_f32_16x16x32_bf16 v[208:211], a[112:115], v[112:115], v[208:211]\n"
    "v_exp_f32 v78, v78\n"
    "v_mfma_f32_16x16x32_bf16 v[240:243], a[112:115], v[120:123], v[240:243]\n"
    "s_waitcnt lgkmcnt(11)\n"
    "ds_read_b128 a[92:95], v23 offset:36864\n"
    "v_add_u32 v10, v10, v13\n"
    "v_mfma_f32_16x16x32_bf16 v[148:151], a[116:119], v[96:99], v[148:151]\n"
    "v_exp_f32 v79, v79\n"
    "v_mfma_f32_16x16x32_bf16 v[180:183], a[116:119], v[104:107], v[180:183]\n"
    "v_cvt_pk_bf16_f32 v75, v78, v79\n"
    "v_lshrrev_b32 v13, 1, v0\n"
    "v_mfma_f32_16x16x32_bf16 v[212:215], a[116:119], v[112:115], v[212:215]\n"
    "s_add_u32 s92, s18, 0\n"
    "v_exp_f32 v84, v84\n"
    "v_mfma_f32_16x16x32_bf16 v[244:247], a[116:119], v[120:123], v[244:247]\n"
    "s_waitcnt lgkmcnt(10)\n"
    "buffer_load_dwordx4 a[172:175], v0, s[20:23], s92 offen offset:3072 sc1\n"
    "v_mfma_f32_16x16x32_bf16 v[152:155], a[120:123], v[96:99], v[152:155]\n"
    "v_exp_f32 v85, v85\n"
    "v_mfma_f32_16x16x32_bf16 v[184:187], a[120:123], v[104:107], v[184:187]\n"
    "buffer_load_dwordx4 a[168:171], v0, s[20:23], s92 offen offset:2048 sc1\n"
    "v_cvt_pk_bf16_f32 v82, v84, v85\n"
    "v_mfma_f32_16x16x32_bf16 v[216:219], a[120:123], v[112:115], v[216:219]\n"
    "v_exp_f32 v86, v86\n"
    "v_mfma_f32_16x16x32_bf16 v[248:251], a[120:123], v[120:123], v[248:251]\n"
    "s_waitcnt lgkmcnt(8)\n"
    "buffer_load_dwordx4 a[164:167], v0, s[20:23], s92 offen offset:1024 sc1\n"
    "v_mfma_f32_16x16x32_bf16 v[156:159], a[124:127], v[96:99], v[156:159]\n"
    "buffer_load_dwordx4 a[160:163], v0, s[20:23], s92 offen offset:0 sc1\n"
    "v_mfma_f32_16x16x32_bf16 v[188:191], a[124:127], v[104:107], v[188:191]\n"
    "s_add_u32 s92, s18, 4096\n"
    "v_mfma_f32_16x16x32_bf16 v[220:223], a[124:127], v[112:115], v[220:223]\n"
    "buffer_load_dwordx4 a[188:191], v0, s[20:23], s92 offen offset:3072 sc1\n"
    "v_mfma_f32_16x16x32_bf16 v[252:255], a[124:127], v[120:123], v[252:255]\n"
    "s_waitcnt lgkmcnt(7)\n"
    "buffer_load_dwordx4 a[184:187], v0, s[20:23], s92 offen offset:2048 sc1\n"
    "v_mfma_f32_16x16x32_bf16 v[96:99], a[64:67], a[0:3], v[48:51]\n"
    "s_waitcnt lgkmcnt(6)\n"
    "v_exp_f32 v87, v87\n"
    "v_mfma_f32_16x16x32_bf16 v[96:99], a[68:71], a[4:7], v[96:99]\n"
    "s_waitcnt lgkmcnt(5)\n"
    "ds_read_b64_tr_b16 a[96:97], v24 offset:57344\n"
    "v_cvt_pk_bf16_f32 v83, v86, v87\n"
    "v_mfma_f32_16x16x32_bf16 v[96:99], a[72:75], a[8:11], v[96:99]\n"
    "s_waitcnt lgkmcnt(5)\n"
    "v_exp_f32 v92, v92\n"
    "v_mfma_f32_16x16x32_bf16 v[96:99], a[76:79], a[12:15], v[96:99]\n"
    "ds_read_b64_tr_b16 a[98:99], v24 offset:61440\n"
    "v_mfma_f32_16x16x32_bf16 v[104:107], a[64:67], a[16:19], v[52:55]\n"
    "v_exp_f32 v93, v93\n"
    "v_mfma_f32_16x16x32_bf16 v[104:107], a[68:71], a[20:23], v[104:107]\n"
    "ds_read_b64_tr_b16 a[100:101], v25 offset:57344\n"
    "v_cvt_pk_bf16_f32 v90, v92, v93\n"
    "v_mfma_f32_16x16x32_bf16 v[104:107], a[72:75], a[24:27], v[104:107]\n"
    "v_exp_f32 v94, v94\n"
    "v_mfma_f32_16x16x32_bf16 v[104:107], a[76:79], a[28:31], v[104:107]\n"
    "ds_read_b64_tr_b16 a[102:103], v25 offset:61440\n"
    "v_mfma_f32_16x16x32_bf16 v[112:115], a[64:67], a[32:35], v[56:59]\n"
    "v_exp_f32 v95, v95\n"
    "v_mfma_f32_16x16x32_bf16 v[112:115], a[68:71], a[36:39], v[112:115]\n"
    "ds_read_b64_tr_b16 a[104:105], v26 offset:57344\n"
    "v_cvt_pk_bf16_f32 v91, v94, v95\n"
    "v_mfma_f32_16x16x32_bf16 v[112:115], a[72:75], a[40:43], v[112:115]\n"
    "v_exp_f32 v96, v96\n"
    "v_mfma_f32_16x16x32_bf16 v[112:115], a[76:79], a[44:47], v[112:115]\n"
    "ds_read_b64_tr_b16 a[106:107], v26 offset:61440\n"
    "v_mfma_f32_16x16x32_bf16 v[120:123], a[64:67], a[48:51], v[60:63]\n"
    "v_exp_f32 v97, v97\n"
    "v_mfma_f32_16x16x32_bf16 v[120:123], a[68:71], a[52:55], v[120:123]\n"
    "ds_read_b64_tr_b16 a[108:109], v27 offset:57344\n"
    "v_cvt_pk_bf16_f32 v96, v96, v97\n"
    "v_mfma_f32_16x16x32_bf16 v[120:123], a[72:75], a[56:59], v[120:123]\n"
    "v_exp_f32 v98, v98\n"
    "v_mfma_f32_16x16x32_bf16 v[120:123], a[76:79], a[60:63], v[120:123]\n"
    "s_waitcnt lgkmcnt(10)\n"
    "ds_read_b64_tr_b16 a[110:111], v27 offset:61440\n"
    "v_mfma_f32_16x16x32_bf16 v[100:103], a[80:83], a[0:3], v[48:51]\n"
    "s_waitcnt lgkmcnt(10)\n"
    "v_exp_f32 v99, v99\n"
    "v_mfma_f32_16x16x32_bf16 v[100:103], a[84:87], a[4:7], v[100:103]\n"
    "s_waitcnt lgkmcnt(9)\n"
    "ds_read_b64_tr_b16 a[112:113], v28 offset:57344\n"
    "v_cvt_pk_bf16_f32 v97, v98, v99\n"
    "v_mfma_f32_16x16x32_bf16 v[100:103], a[88:91], a[8:11], v[100:103]\n"
    "s_waitcnt lgkmcnt(9)\n"
    "v_exp_f32 v104, v104\n"
    "v_mfma_f32_16x16x32_bf16 v[100:103], a[92:95], a[12:15], v[100:103]\n"
    "ds_read_b64_tr_b16 a[114:115], v28 offset:61440\n"
    "v_mfma_f32_16x16x32_bf16 v[108:111], a[80:83], a[16:19], v[52:55]\n"
    "v_exp_f32 v105, v105\n"
    "v_mfma_f32_16x16x32_bf16 v[108:111], a[84:87], a[20:23], v[108:111]\n"
    "ds_read_b64_tr_b16 a[116:117], v29 offset:57344\n"
    "v_cvt_pk_bf16_f32 v104, v104, v105\n"
    "v_mfma_f32_16x16x32_bf16 v[108:111], a[88:91], a[24:27], v[108:111]\n"
    "v_exp_f32 v106, v106\n"
    "v_mfma_f32_16x16x32_bf16 v[108:111], a[92:95], a[28:31], v[108:111]\n"
    "ds_read_b64_tr_b16 a[118:119], v29 offset:61440\n"
    "v_mfma_f32_16x16x32_bf16 v[116:119], a[80:83], a[32:35], v[56:59]\n"
    "v_exp_f32 v107, v107\n"
    "v_mfma_f32_16x16x32_bf16 v[116:119], a[84:87], a[36:39], v[116:119]\n"
    "ds_read_b64_tr_b16 a[120:121], v30 offset:57344\n"
    "v_cvt_pk_bf16_f32 v105, v106, v107\n"
    "v_mfma_f32_16x16x32_bf16 v[116:119], a[88:91], a[40:43], v[116:119]\n"
    "v_exp_f32 v112, v112\n"
    "v_mfma_f32_16x16x32_bf16 v[116:119], a[92:95], a[44:47], v[116:119]\n"
    "ds_read_b64_tr_b16 a[122:123], v30 offset:61440\n"
    "v_mfma_f32_16x16x32_bf16 v[124:127], a[80:83], a[48:51], v[60:63]\n"
    "v_exp_f32 v113, v113\n"
    "v_mfma_f32_16x16x32_bf16 v[124:127], a[84:87], a[52:55], v[124:127]\n"
    "ds_read_b64_tr_b16 a[124:125], v31 offset:57344\n"
    "v_cvt_pk_bf16_f32 v112, v112, v113\n"
    "v_mfma_f32_16x16x32_bf16 v[124:127], a[88:91], a[56:59], v[124:127]\n"
    "v_exp_f32 v114, v114\n"
    "v_mfma_f32_16x16x32_bf16 v[124:127], a[92:95], a[60:63], v[124:127]\n"
    "s_mov_b32 s91, 50462976\n"
    "s_cmp_eq_u32 s37, 0\n"
    "s_cselect_b32 s91, 50462976, s91\n"
    "ds_read_b64_tr_b16 a[126:127], v31 offset:61440\n"
    "v_mfma_f32_16x16x32_bf16 a[128:131], a[144:147], v[64:67], a[128:131]\n"
    "s_lshr_b32 s91, s91, s36\n"
    "s_xor_b32 s91, s91, s16\n"
    "s_and_b32 s91, s91, 31\n"
    "v_exp_f32 v115, v115\n"
    "v_mfma_f32_16x16x32_bf16 a[132:135], a[144:147], v[72:75], a[132:135]\n"
    "s_mul_i32 s91, s91, 36864\n"
    "buffer_load_dwordx4 a[180:183], v0, s[20:23], s92 offen offset:1024 sc1\n"
    "v_cvt_pk_bf16_f32 v113, v114, v115\n"
    "v_mfma_f32_16x16x32_bf16 a[136:139], a[144:147], v[80:83], a[136:139]\n"
    "s_add_u32 m0, s17, 90112\n"
    "v_exp_f32 v120, v120\n"
    "v_mfma_f32_16x16x32_bf16 a[140:143], a[144:147], v[88:91], a[140:143]\n"
    "s_waitcnt lgkmcnt(14)\n"
    "buffer_load_dwordx4 v2, s[40:43], s91 offen lds\n"
    "v_mfma_f32_16x16x32_bf16 v[128:131], a[96:99], v[64:67], v[128:131]\n"
    "v_exp_f32 v121, v121\n"
    "v_mfma_f32_16x16x32_bf16 v[160:163], a[96:99], v[72:75], v[160:163]\n"
    "buffer_load_dwordx4 v3, s[40:43], s91 offen offset:1024 lds\n"
    "v_cvt_pk_bf16_f32 v120, v120, v121\n"
    "v_mfma_f32_16x16x32_bf16 v[192:195], a[96:99], v[80:83], v[192:195]\n"
    "v_exp_f32 v122, v122\n"
    "v_mfma_f32_16x16x32_bf16 v[224:227], a[96:99], v[88:91], v[224:227]\n"
    "s_waitcnt lgkmcnt(12)\n"
    "buffer_load_dwordx4 v4, s[40:43], s91 offen offset:2048 lds\n"
    "v_mfma_f32_16x16x32_bf16 v[132:135], a[100:103], v[64:67], v[132:135]\n"
    "v_exp_f32 v123, v123\n"
    "v_mfma_f32_16x16x32_bf16 v[164:167], a[100:103], v[72:75], v[164:167]\n"
    "ds_read_b128 a[64:67], v20 offset:40960\n"
    "v_cvt_pk_bf16_f32 v121, v122, v123\n"
    "v_mfma_f32_16x16x32_bf16 v[196:199], a[100:103], v[80:83], v[196:199]\n"
    "v_exp_f32 v100, v100\n"
    "v_mfma_f32_16x16x32_bf16 v[228:231], a[100:103], v[88:91], v[228:231]\n"
    "s_waitcnt lgkmcnt(11)\n"
    "ds_read_b128 a[68:71], v21 offset:40960\n"
    "v_mfma_f32_16x16x32_bf16 v[136:139], a[104:107], v[64:67], v[136:139]\n"
    "v_exp_f32 v101, v101\n"
    "v_mfma_f32_16x16x32_bf16 v[168:171], a[104:107], v[72:75], v[168:171]\n"
    "ds_read_b128 a[72:75], v22 offset:40960\n"
    "v_cvt_pk_bf16_f32 v98, v100, v101\n"
    "v_mfma_f32_16x16x32_bf16 v[200:203], a[104:107], v[80:83], v[200:203]\n"
    "v_exp_f32 v102, v102\n"
    "v_mfma_f32_16x16x32_bf16 v[232:235], a[104:107], v[88:91], v[232:235]\n"
    "s_waitcnt lgkmcnt(11)\n"
    "ds_read_b128 a[76:79], v23 offset:40960\n"
    "v_mfma_f32_16x16x32_bf16 v[140:143], a[108:111], v[64:67], v[140:143]\n"
    "v_exp_f32 v103, v103\n"
    "v_mfma_f32_16x16x32_bf16 v[172:175], a[108:111], v[72:75], v[172:175]\n"
    "ds_read_b128 a[80:83], v20 offset:45056\n"
    "v_cvt_pk_bf16_f32 v99, v102, v103\n"
    "v_mfma_f32_16x16x32_bf16 v[204:207], a[108:111], v[80:83], v[204:207]\n"
    "v_exp_f32 v108, v108\n"
    "v_mfma_f32_16x16x32_bf16 v[236:239], a[108:111], v[88:91], v[236:239]\n"
    "s_waitcnt lgkmcnt(11)\n"
    "ds_read_b128 a[84:87], v21 offset:45056\n"
    "v_mfma_f32_16x16x32_bf16 v[144:147], a[112:115], v[64:67], v[144:147]\n"
    "v_exp_f32 v109, v109\n"
    "v_mfma_f32_16x16x32_bf16 v[176:179], a[112:115], v[72:75], v[176:179]\n"
    "ds_read_b128 a[88:91], v22 offset:45056\n"
    "v_cvt_pk_bf16_f32 v106, v108, v109\n"
    "v_mfma_f32_16x16x32_bf16 v[208:211], a[112:115], v[80:83], v[208:211]\n"
    "v_exp_f32 v110, v110\n"
    "v_mfma_f32_16x16x32_bf16 v[240:243], a[112:115], v[88:91], v[240:243]\n"
    "s_waitcnt lgkmcnt(11)\n"
    "ds_read_b128 a[92:95], v23 offset:45056\n"
    "v_mfma_f32_16x16x32_bf16 v[148:151], a[116:119], v[64:67], v[148:151]\n"
    "v_exp_f32 v111, v111\n"
    "v_mfma_f32_16x16x32_bf16 v[180:183], a[116:119], v[72:75], v[180:183]\n"
    "buffer_load_dwordx4 v5, s[40:43], s91 offen offset:3072 lds\n"
    "v_cvt_pk_bf16_f32 v107, v110, v111\n"
    "v_mfma_f32_16x16x32_bf16 v[212:215], a[116:119], v[80:83], v[212:215]\n"
    "s_add_u32 m0, s17, 94208\n"
    "v_exp_f32 v116, v116\n"
    "v_mfma_f32_16x16x32_bf16 v[244:247], a[116:119], v[88:91], v[244:247]\n"
    "s_waitcnt lgkmcnt(10)\n"
    "buffer_load_dwordx4 v6, s[40:43], s91 offen lds\n"
    "v_mfma_f32_16x16x32_bf16 v[152:155], a[120:123], v[64:67], v[152:155]\n"
    "v_exp_f32 v117, v117\n"
    "v_mfma_f32_16x16x32_bf16 v[184:187], a[120:123], v[72:75], v[184:187]\n"
    "buffer_load_dwordx4 v7, s[40:43], s91 offen offset:1024 lds\n"
    "v_cvt_pk_bf16_f32 v114, v116, v117\n"
    "v_mfma_f32_16x16x32_bf16 v[216:219], a[120:123], v[80:83], v[216:219]\n"
    "v_exp_f32 v118, v118\n"
    "v_mfma_f32_16x16x32_bf16 v[248:251], a[120:123], v[88:91], v[248:251]\n"
    "s_waitcnt lgkmcnt(8)\n"
    "buffer_load_dwordx4 v8, s[40:43], s91 offen offset:2048 lds\n"
    "v_mfma_f32_16x16x32_bf16 v[156:159], a[124:127], v[64:67], v[156:159]\n"
    "buffer_load_dwordx4 v9, s[40:43], s91 offen offset:3072 lds\n"
    "v_mfma_f32_16x16x32_bf16 v[188:191], a[124:127], v[72:75], v[188:191]\n"
    "s_add_u32 m0, s17, 98304\n"
    "buffer_load_dwordx4 a[176:179], v0, s[20:23], s92 offen offset:0 sc1\n"
    "v_mfma_f32_16x16x32_bf16 v[220:223], a[124:127], v[80:83], v[220:223]\n"
    "s_add_u32 s92, s18, 8192\n"
    "buffer_load_dwordx4 v10, s[40:43], s91 offen lds\n"
    "v_mfma_f32_16x16x32_bf16 v[252:255], a[124:127], v[88:91], v[252:255]\n"
    "s_waitcnt lgkmcnt(7)\n"
    "buffer_load_dwordx4 a[204:207], v0, s[20:23], s92 offen offset:3072 sc1\n"
    "v_mfma_f32_16x16x32_bf16 v[64:67], a[64:67], a[0:3], v[48:51]\n"
    "s_waitcnt lgkmcnt(6)\n"
    "v_exp_f32 v119, v119\n"
    "v_mfma_f32_16x16x32_bf16 v[64:67], a[68:71], a[4:7], v[64:67]\n"
    "s_waitcnt lgkmcnt(5)\n"
    "ds_read_b64_tr_b16 a[96:97], v32 offset:49152\n"
    "v_cvt_pk_bf16_f32 v115, v118, v119\n"
    "v_mfma_f32_16x16x32_bf16 v[64:67], a[72:75], a[8:11], v[64:67]\n"
    "s_waitcnt lgkmcnt(5)\n"
    "v_exp_f32 v124, v124\n"
    "v_mfma_f32_16x16x32_bf16 v[64:67], a[76:79], a[12:15], v[64:67]\n"
    "ds_read_b64_tr_b16 a[98:99], v32 offset:53248\n"
    "v_mfma_f32_16x16x32_bf16 v[72:75], a[64:67], a[16:19], v[52:55]\n"
    "v_exp_f32 v125, v125\n"
    "v_mfma_f32_16x16x32_bf16 v[72:75], a[68:71], a[20:23], v[72:75]\n"
    "ds_read_b64_tr_b16 a[100:101], v33 offset:49152\n"
    "v_cvt_pk_bf16_f32 v122, v124, v125\n"
    "v_mfma_f32_16x16x32_bf16 v[72:75], a[72:75], a[24:27], v[72:75]\n"
    "v_exp_f32 v126, v126\n"
    "v_mfma_f32_16x16x32_bf16 v[72:75], a[76:79], a[28:31], v[72:75]\n"
    "ds_read_b64_tr_b16 a[102:103], v33 offset:53248\n"
    "v_mfma_f32_16x16x32_bf16 v[80:83], a[64:67], a[32:35], v[56:59]\n"
    "v_exp_f32 v127, v127\n"
    "v_mfma_f32_16x16x32_bf16 v[80:83], a[68:71], a[36:39], v[80:83]\n"
    "ds_read_b64_tr_b16 a[104:105], v34 offset:49152\n"
    "v_cvt_pk_bf16_f32 v123, v126, v127\n"
    "v_mfma_f32_16x16x32_bf16 v[80:83], a[72:75], a[40:43], v[80:83]\n"
    "v_exp_f32 v64, v64\n"
    "v_mfma_f32_16x16x32_bf16 v[80:83], a[76:79], a[44:47], v[80:83]\n"
    "ds_read_b64_tr_b16 a[106:107], v34 offset:53248\n"
    "v_mfma_f32_16x16x32_bf16 v[88:91], a[64:67], a[48:51], v[60:63]\n"
    "v_exp_f32 v65, v65\n"
    "v_mfma_f32_16x16x32_bf16 v[88:91], a[68:71], a[52:55], v[88:91]\n"
    "ds_read_b64_tr_b16 a[108:109], v35 offset:49152\n"
    "v_cvt_pk_bf16_f32 v64, v64, v65\n"
    "v_mfma_f32_16x16x32_bf16 v[88:91], a[72:75], a[56:59], v[88:91]\n"
    "v_exp_f32 v66, v66\n"
    "v_mfma_f32_16x16x32_bf16 v[88:91], a[76:79], a[60:63], v[88:91]\n"
    "s_waitcnt lgkmcnt(10)\n"
    "ds_read_b64_tr_b16 a[110:111], v35 offset:53248\n"
    "v_mfma_f32_16x16x32_bf16 v[68:71], a[80:83], a[0:3], v[48:51]\n"
    "s_waitcnt lgkmcnt(10)\n"
    "v_exp_f32 v67, v67\n"
    "v_mfma_f32_16x16x32_bf16 v[68:71], a[84:87], a[4:7], v[68:71]\n"
    "s_waitcnt lgkmcnt(9)\n"
    "ds_read_b64_tr_b16 a[112:113], v36 offset:49152\n"
    "v_cvt_pk_bf16_f32 v65, v66, v67\n"
    "v_mfma_f32_16x16x32_bf16 v[68:71], a[88:91], a[8:11], v[68:71]\n"
    "s_waitcnt lgkmcnt(9)\n"
    "v_exp_f32 v72, v72\n"
    "v_mfma_f32_16x16x32_bf16 v[68:71], a[92:95], a[12:15], v[68:71]\n"
    "ds_read_b64_tr_b16 a[114:115], v36 offset:53248\n"
    "v_mfma_f32_16x16x32_bf16 v[76:79], a[80:83], a[16:19], v[52:55]\n"
    "v_exp_f32 v73, v73\n"
    "v_mfma_f32_16x16x32_bf16 v[76:79], a[84:87], a[20:23], v[76:79]\n"
    "ds_read_b64_tr_b16 a[116:117], v37 offset:49152\n"
    "v_cvt_pk_bf16_f32 v72, v72, v73\n"
    "v_mfma_f32_16x16x32_bf16 v[76:79], a[88:91], a[24:27], v[76:79]\n"
    "v_exp_f32 v74, v74\n"
    "v_mfma_f32_16x16x32_bf16 v[76:79], a[92:95], a[28:31], v[76:79]\n"
    "ds_read_b64_tr_b16 a[118:119], v37 offset:53248\n"
    "v_mfma_f32_16x16x32_bf16 v[84:87], a[80:83], a[32:35], v[56:59]\n"
    "v_exp_f32 v75, v75\n"
    "v_mfma_f32_16x16x32_bf16 v[84:87], a[84:87], a[36:39], v[84:87]\n"
    "ds_read_b64_tr_b16 a[120:121], v38 offset:49152\n"
    "v_cvt_pk_bf16_f32 v73, v74, v75\n"
    "v_mfma_f32_16x16x32_bf16 v[84:87], a[88:91], a[40:43], v[84:87]\n"
    "v_exp_f32 v80, v80\n"
    "v_mfma_f32_16x16x32_bf16 v[84:87], a[92:95], a[44:47], v[84:87]\n"
    "ds_read_b64_tr_b16 a[122:123], v38 offset:53248\n"
    "v_mfma_f32_16x16x32_bf16 v[92:95], a[80:83], a[48:51], v[60:63]\n"
    "v_exp_f32 v81, v81\n"
    "v_mfma_f32_16x16x32_bf16 v[92:95], a[84:87], a[52:55], v[92:95]\n"
    "ds_read_b64_tr_b16 a[124:125], v39 offset:49152\n"
    "v_cvt_pk_bf16_f32 v80, v80, v81\n"
    "v_mfma_f32_16x16x32_bf16 v[92:95], a[88:91], a[56:59], v[92:95]\n"
    "v_exp_f32 v82, v82\n"
    "v_mfma_f32_16x16x32_bf16 v[92:95], a[92:95], a[60:63], v[92:95]\n"
    "ds_read_b64_tr_b16 a[126:127], v39 offset:53248\n"
    "v_mfma_f32_16x16x32_bf16 a[128:131], a[144:147], v[96:99], a[128:131]\n"
    "v_exp_f32 v83, v83\n"
    "v_mfma_f32_16x16x32_bf16 a[132:135], a[144:147], v[104:107], a[132:135]\n"
    "buffer_load_dwordx4 a[200:203], v0, s[20:23], s92 offen offset:2048 sc1\n"
    "v_cvt_pk_bf16_f32 v81, v82, v83\n"
    "v_mfma_f32_16x16x32_bf16 a[136:139], a[144:147], v[112:115], a[136:139]\n"
    "v_exp_f32 v88, v88\n"
    "v_mfma_f32_16x16x32_bf16 a[140:143], a[144:147], v[120:123], a[140:143]\n"
    "s_waitcnt lgkmcnt(14)\n"
    "buffer_load_dwordx4 a[196:199], v0, s[20:23], s92 offen offset:1024 sc1\n"
    "v_mfma_f32_16x16x32_bf16 v[128:131], a[96:99], v[96:99], v[128:131]\n"
    "v_exp_f32 v89, v89\n"
    "v_mfma_f32_16x16x32_bf16 v[160:163], a[96:99], v[104:107], v[160:163]\n"
    "s_waitcnt vmcnt(21)\n"
    "s_barrier\n"
    "buffer_load_dwordx4 a[192:195], v0, s[20:23], s92 offen offset:0 sc1\n"
    "v_cvt_pk_bf16_f32 v88, v88, v89\n"
    "v_mfma_f32_16x16x32_bf16 v[192:195], a[96:99], v[112:115], v[192:195]\n"
    "s_add_u32 s92, s18, 12288\n"
    "v_exp_f32 v90, v90\n"
    "v_mfma_f32_16x16x32_bf16 v[224:227], a[96:99], v[120:123], v[224:227]\n"
    "s_waitcnt lgkmcnt(12)\n"
    "buffer_load_dwordx4 a[220:223], v0, s[20:23], s92 offen offset:3072 sc1\n"
    "v_mfma_f32_16x16x32_bf16 v[132:135], a[100:103], v[96:99], v[132:135]\n"
    "v_exp_f32 v91, v91\n"
    "v_mfma_f32_16x16x32_bf16 v[164:167], a[100:103], v[104:107], v[164:167]\n"
    "ds_read_b128 a[64:67], v20 offset:0\n"
    "v_cvt_pk_bf16_f32 v89, v90, v91\n"
    "v_mfma_f32_16x16x32_bf16 v[196:199], a[100:103], v[112:115], v[196:199]\n"
    "v_exp_f32 v68, v68\n"
    "v_mfma_f32_16x16x32_bf16 v[228:231], a[100:103], v[120:123], v[228:231]\n"
    "s_waitcnt lgkmcnt(11)\n"
    "ds_read_b128 a[68:71], v21 offset:0\n"
    "v_mfma_f32_16x16x32_bf16 v[136:139], a[104:107], v[96:99], v[136:139]\n"
    "v_exp_f32 v69, v69\n"
    "v_mfma_f32_16x16x32_bf16 v[168:171], a[104:107], v[104:107], v[168:171]\n"
    "ds_read_b128 a[72:75], v22 offset:0\n"
    "v_cvt_pk_bf16_f32 v66, v68, v69\n"
    "v_mfma_f32_16x16x32_bf16 v[200:203], a[104:107], v[112:115], v[200:203]\n"
    "v_exp_f32 v70, v70\n"
    "v_mfma_f32_16x16x32_bf16 v[232:235], a[104:107], v[120:123], v[232:235]\n"
    "s_waitcnt lgkmcnt(11)\n"
    "ds_read_b128 a[76:79], v23 offset:0\n"
    "v_mfma_f32_16x16x32_bf16 v[140:143], a[108:111], v[96:99], v[140:143]\n"
    "v_exp_f32 v71, v71\n"
    "v_mfma_f32_16x16x32_bf16 v[172:175], a[108:111], v[104:107], v[172:175]\n"
    "ds_read_b128 a[80:83], v20 offset:4096\n"
    "v_cvt_pk_bf16_f32 v67, v70, v71\n"
    "v_mfma_f32_16x16x32_bf16 v[204:207], a[108:111], v[112:115], v[204:207]\n"
    "v_exp_f32 v76, v76\n"
    "v_mfma_f32_16x16x32_bf16 v[236:239], a[108:111], v[120:123], v[236:239]\n"
    "s_waitcnt lgkmcnt(11)\n"
    "ds_read_b128 a[84:87], v21 offset:4096\n"
    "v_mfma_f32_16x16x32_bf16 v[144:147], a[112:115], v[96:99], v[144:147]\n"
    "v_exp_f32 v77, v77\n"
    "v_mfma_f32_16x16x32_bf16 v[176:179], a[112:115], v[104:107], v[176:179]\n"
    "ds_read_b128 a[88:91], v22 offset:4096\n"
    "v_cvt_pk_bf16_f32 v74, v76, v77\n"
    "v_mfma_f32_16x16x32_bf16 v[208:211], a[112:115], v[112:115], v[208:211]\n"
    "v_exp_f32 v78, v78\n"
    "v_mfma_f32_16x16x32_bf16 v[240:243], a[112:115], v[120:123], v[240:243]\n"
    "s_waitcnt lgkmcnt(11)\n"
    "ds_read_b128 a[92:95], v23 offset:4096\n"
    "v_mfma_f32_16x16x32_bf16 v[148:151], a[116:119], v[96:99], v[148:151]\n"
    "v_exp_f32 v79, v79\n"
    "v_mfma_f32_16x16x32_bf16 v[180:183], a[116:119], v[104:107], v[180:183]\n"
    "buffer_load_dwordx4 a[216:219], v0, s[20:23], s92 offen offset:2048 sc1\n"
    "v_cvt_pk_bf16_f32 v75, v78, v79\n"
    "v_mfma_f32_16x16x32_bf16 v[212:215], a[116:119], v[112:115], v[212:215]\n"
    "v_exp_f32 v84, v84\n"
    "v_mfma_f32_16x16x32_bf16 v[244:247], a[116:119], v[120:123], v[244:247]\n"
    "s_waitcnt lgkmcnt(10)\n"
    "buffer_load_dwordx4 a[212:215], v0, s[20:23], s92 offen offset:1024 sc1\n"
    "v_mfma_f32_16x16x32_bf16 v[152:155], a[120:123], v[96:99], v[152:155]\n"
    "v_exp_f32 v85, v85\n"
    "v_mfma_f32_16x16x32_bf16 v[184:187], a[120:123], v[104:107], v[184:187]\n"
    "buffer_load_dwordx4 a[208:211], v0, s[20:23], s92 offen offset:0 sc1\n"
    "v_cvt_pk_bf16_f32 v82, v84, v85\n"
    "v_mfma_f32_16x16x32_bf16 v[216:219], a[120:123], v[112:115], v[216:219]\n"
    "s_add_u32 s92, s18, 16384\n"
    "v_exp_f32 v86, v86\n"
    "v_mfma_f32_16x16x32_bf16 v[248:251], a[120:123], v[120:123], v[248:251]\n"
    "s_waitcnt lgkmcnt(8)\n"
    "buffer_load_dwordx2 a[236:237], v13, s[20:23], s92 offen offset:3072 sc1\n"
    "v_mfma_f32_16x16x32_bf16 v[156:159], a[124:127], v[96:99], v[156:159]\n"
    "buffer_load_dwordx2 a[238:239], v13, s[20:23], s92 offen offset:3584 sc1\n"
    "v_mfma_f32_16x16x32_bf16 v[188:191], a[124:127], v[104:107], v[188:191]\n"
    "buffer_load_dwordx2 a[232:233], v13, s[20:23], s92 offen offset:2048 sc1\n"
    "v_mfma_f32_16x16x32_bf16 v[220:223], a[124:127], v[112:115], v[220:223]\n"
    "buffer_load_dwordx2 a[234:235], v13, s[20:23], s92 offen offset:2560 sc1\n"
    "v_mfma_f32_16x16x32_bf16 v[252:255], a[124:127], v[120:123], v[252:255]\n"
    "s_waitcnt lgkmcnt(7)\n"
    "buffer_load_dwordx2 a[228:229], v13, s[20:23], s92 offen offset:1024 sc1\n"
    "v_mfma_f32_16x16x32_bf16 v[96:99], a[64:67], a[0:3], v[48:51]\n"
    "s_waitcnt lgkmcnt(6)\n"
    "v_exp_f32 v87, v87\n"
    "v_mfma_f32_16x16x32_bf16 v[96:99], a[68:71], a[4:7], v[96:99]\n"
    "s_waitcnt lgkmcnt(5)\n"
    "ds_read_b64_tr_b16 a[96:97], v32 offset:57344\n"
    "v_cvt_pk_bf16_f32 v83, v86, v87\n"
    "v_mfma_f32_16x16x32_bf16 v[96:99], a[72:75], a[8:11], v[96:99]\n"
    "s_waitcnt lgkmcnt(5)\n"
    "v_exp_f32 v92, v92\n"
    "v_mfma_f32_16x16x32_bf16 v[96:99], a[76:79], a[12:15], v[96:99]\n"
    "ds_read_b64_tr_b16 a[98:99], v32 offset:61440\n"
    "v_mfma_f32_16x16x32_bf16 v[104:107], a[64:67], a[16:19], v[52:55]\n"
    "v_exp_f32 v93, v93\n"
    "v_mfma_f32_16x16x32_bf16 v[104:107], a[68:71], a[20:23], v[104:107]\n"
    "ds_read_b64_tr_b16 a[100:101], v33 offset:57344\n"
    "v_cvt_pk_bf16_f32 v90, v92, v93\n"
    "v_mfma_f32_16x16x32_bf16 v[104:107], a[72:75], a[24:27], v[104:107]\n"
    "v_exp_f32 v94, v94\n"
    "v_mfma_f32_16x16x32_bf16 v[104:107], a[76:79], a[28:31], v[104:107]\n"
    "ds_read_b64_tr_b16 a[102:103], v33 offset:61440\n"
    "v_mfma_f32_16x16x32_bf16 v[112:115], a[64:67], a[32:35], v[56:59]\n"
    "v_exp_f32 v95, v95\n"
    "v_mfma_f32_16x16x32_bf16 v[112:115], a[68:71], a[36:39], v[112:115]\n"
    "ds_read_b64_tr_b16 a[104:105], v34 offset:57344\n"
    "v_cvt_pk_bf16_f32 v91, v94, v95\n"
    "v_mfma_f32_16x16x32_bf16 v[112:115], a[72:75], a[40:43], v[112:115]\n"
    "v_exp_f32 v96, v96\n"
    "v_mfma_f32_16x16x32_bf16 v[112:115], a[76:79], a[44:47], v[112:115]\n"
    "ds_read_b64_tr_b16 a[106:107], v34 offset:61440\n"
    "v_mfma_f32_16x16x32_bf16 v[120:123], a[64:67], a[48:51], v[60:63]\n"
    "v_exp_f32 v97, v97\n"
    "v_mfma_f32_16x16x32_bf16 v[120:123], a[68:71], a[52:55], v[120:123]\n"
    "ds_read_b64_tr_b16 a[108:109], v35 offset:57344\n"
    "v_cvt_pk_bf16_f32 v96, v96, v97\n"
    "v_mfma_f32_16x16x32_bf16 v[120:123], a[72:75], a[56:59], v[120:123]\n"
    "v_exp_f32 v98, v98\n"
    "v_mfma_f32_16x16x32_bf16 v[120:123], a[76:79], a[60:63], v[120:123]\n"
    "s_waitcnt lgkmcnt(10)\n"
    "ds_read_b64_tr_b16 a[110:111], v35 offset:61440\n"
    "v_mfma_f32_16x16x32_bf16 v[100:103], a[80:83], a[0:3], v[48:51]\n"
    "s_waitcnt lgkmcnt(10)\n"
    "v_exp_f32 v99, v99\n"
    "v_mfma_f32_16x16x32_bf16 v[100:103], a[84:87], a[4:7], v[100:103]\n"
    "s_waitcnt lgkmcnt(9)\n"
    "ds_read_b64_tr_b16 a[112:113], v36 offset:57344\n"
    "v_cvt_pk_bf16_f32 v97, v98, v99\n"
    "v_mfma_f32_16x16x32_bf16 v[100:103], a[88:91], a[8:11], v[100:103]\n"
    "s_waitcnt lgkmcnt(9)\n"
    "v_exp_f32 v104, v104\n"
    "v_mfma_f32_16x16x32_bf16 v[100:103], a[92:95], a[12:15], v[100:103]\n"
    "ds_read_b64_tr_b16 a[114:115], v36 offset:61440\n"
    "v_mfma_f32_16x16x32_bf16 v[108:111], a[80:83], a[16:19], v[52:55]\n"
    "v_exp_f32 v105, v105\n"
    "v_mfma_f32_16x16x32_bf16 v[108:111], a[84:87], a[20:23], v[108:111]\n"
    "ds_read_b64_tr_b16 a[116:117], v37 offset:57344\n"
    "v_cvt_pk_bf16_f32 v104, v104, v105\n"
    "v_mfma_f32_16x16x32_bf16 v[108:111], a[88:91], a[24:27], v[108:111]\n"
    "v_exp_f32 v106, v106\n"
    "v_mfma_f32_16x16x32_bf16 v[108:111], a[92:95], a[28:31], v[108:111]\n"
    "ds_read_b64_tr_b16 a[118:119], v37 offset:61440\n"
    "v_mfma_f32_16x16x32_bf16 v[116:119], a[80:83], a[32:35], v[56:59]\n"
    "v_exp_f32 v107, v107\n"
    "v_mfma_f32_16x16x32_bf16 v[116:119], a[84:87], a[36:39], v[116:119]\n"
    "ds_read_b64_tr_b16 a[120:121], v38 offset:57344\n"
    "v_cvt_pk_bf16_f32 v105, v106, v107\n"
    "v_mfma_f32_16x16x32_bf16 v[116:119], a[88:91], a[40:43], v[116:119]\n"
    "v_exp_f32 v112, v112\n"
    "v_mfma_f32_16x16x32_bf16 v[116:119], a[92:95], a[44:47], v[116:119]\n"
    "ds_read_b64_tr_b16 a[122:123], v38 offset:61440\n"
    "v_mfma_f32_16x16x32_bf16 v[124:127], a[80:83], a[48:51], v[60:63]\n"
    "v_exp_f32 v113, v113\n"
    "v_mfma_f32_16x16x32_bf16 v[124:127], a[84:87], a[52:55], v[124:127]\n"
    "ds_read_b64_tr_b16 a[124:125], v39 offset:57344\n"
    "v_cvt_pk_bf16_f32 v112, v112, v113\n"
    "v_mfma_f32_16x16x32_bf16 v[124:127], a[88:91], a[56:59], v[124:127]\n"
    "v_exp_f32 v114, v114\n"
    "v_mfma_f32_16x16x32_bf16 v[124:127], a[92:95], a[60:63], v[124:127]\n"
    "s_mov_b32 s91, 471340048\n"
    "s_cmp_eq_u32 s37, 0\n"
    "s_cselect_b32 s91, 117835012, s91\n"
    "ds_read_b64_tr_b16 a[126:127], v39 offset:61440\n"
    "v_mfma_f32_16x16x32_bf16 a[128:131], a[144:147], v[64:67], a[128:131]\n"
    "s_lshr_b32 s91, s91, s36\n"
    "s_xor_b32 s91, s91, s16\n"
    "s_and_b32 s91, s91, 31\n"
    "v_exp_f32 v115, v115\n"
    "v_mfma_f32_16x16x32_bf16 a[132:135], a[144:147], v[72:75], a[132:135]\n"
    "s_mul_i32 s91, s91, 36864\n"
    "buffer_load_dwordx2 a[230:231], v13, s[20:23], s92 offen offset:1536 sc1\n"
    "v_cvt_pk_bf16_f32 v113, v114, v115\n"
    "v_mfma_f32_16x16x32_bf16 a[136:139], a[144:147], v[80:83], a[136:139]\n"
    "s_add_u32 m0, s17, 126976\n"
    "v_exp_f32 v120, v120\n"
    "v_mfma_f32_16x16x32_bf16 a[140:143], a[144:147], v[88:91], a[140:143]\n"
    "s_waitcnt lgkmcnt(14)\n"
    "buffer_load_dwordx4 v2, s[40:43], s91 offen lds\n"
    "v_mfma_f32_16x16x32_bf16 v[128:131], a[96:99], v[64:67], v[128:131]\n"
    "v_exp_f32 v121, v121\n"
    "v_mfma_f32_16x16x32_bf16 v[160:163], a[96:99], v[72:75], v[160:163]\n"
    "buffer_load_dwordx4 v3, s[40:43], s91 offen offset:1024 lds\n"
    "v_cvt_pk_bf16_f32 v120, v120, v121\n"
    "v_mfma_f32_16x16x32_bf16 v[192:195], a[96:99], v[80:83], v[192:195]\n"
    "v_exp_f32 v122, v122\n"
    "v_mfma_f32_16x16x32_bf16 v[224:227], a[96:99], v[88:91], v[224:227]\n"
    "s_waitcnt lgkmcnt(12)\n"
    "buffer_load_dwordx4 v4, s[40:43], s91 offen offset:2048 lds\n"
    "v_mfma_f32_16x16x32_bf16 v[132:135], a[100:103], v[64:67], v[132:135]\n"
    "v_exp_f32 v123, v123\n"
    "v_mfma_f32_16x16x32_bf16 v[164:167], a[100:103], v[72:75], v[164:167]\n"
    "ds_read_b128 a[64:67], v20 offset:8192\n"
    "v_cvt_pk_bf16_f32 v121, v122, v123\n"
    "v_mfma_f32_16x16x32_bf16 v[196:199], a[100:103], v[80:83], v[196:199]\n"
    "v_exp_f32 v100, v100\n"
    "v_mfma_f32_16x16x32_bf16 v[228:231], a[100:103], v[88:91], v[228:231]\n"
    "s_waitcnt lgkmcnt(11)\n"
    "ds_read_b128 a[68:71], v21 offset:8192\n"
    "v_mfma_f32_16x16x32_bf16 v[136:139], a[104:107], v[64:67], v[136:139]\n"
    "v_exp_f32 v101, v101\n"
    "v_mfma_f32_16x16x32_bf16 v[168:171], a[104:107], v[72:75], v[168:171]\n"
    "ds_read_b128 a[72:75], v22 offset:8192\n"
    "v_cvt_pk_bf16_f32 v98, v100, v101\n"
    "v_mfma_f32_16x16x32_bf16 v[200:203], a[104:107], v[80:83], v[200:203]\n"
    "v_exp_f32 v102, v102\n"
    "v_mfma_f32_16x16x32_bf16 v[232:235], a[104:107], v[88:91], v[232:235]\n"
    "s_waitcnt lgkmcnt(11)\n"
    "ds_read_b128 a[76:79], v23 offset:8192\n"
    "v_mfma_f32_16x16x32_bf16 v[140:143], a[108:111], v[64:67], v[140:143]\n"
    "v_exp_f32 v103, v103\n"
    "v_mfma_f32_16x16x32_bf16 v[172:175], a[108:111], v[72:75], v[172:175]\n"
    "ds_read_b128 a[80:83], v20 offset:12288\n"
    "v_cvt_pk_bf16_f32 v99, v102, v103\n"
    "v_mfma_f32_16x16x32_bf16 v[204:207], a[108:111], v[80:83], v[204:207]\n"
    "v_exp_f32 v108, v108\n"
    "v_mfma_f32_16x16x32_bf16 v[236:239], a[108:111], v[88:91], v[236:239]\n"
    "s_waitcnt lgkmcnt(11)\n"
    "ds_read_b128 a[84:87], v21 offset:12288\n"
    "v_mfma_f32_16x16x32_bf16 v[144:147], a[112:115], v[64:67], v[144:147]\n"
    "v_exp_f32 v109, v109\n"
    "v_mfma_f32_16x16x32_bf16 v[176:179], a[112:115], v[72:75], v[176:179]\n"
    "ds_read_b128 a[88:91], v22 offset:12288\n"
    "v_cvt_pk_bf16_f32 v106, v108, v109\n"
    "v_mfma_f32_16x16x32_bf16 v[208:211], a[112:115], v[80:83], v[208:211]\n"
    "v_exp_f32 v110, v110\n"
    "v_mfma_f32_16x16x32_bf16 v[240:243], a[112:115], v[88:91], v[240:243]\n"
    "s_waitcnt lgkmcnt(11)\n"
    "ds_read_b128 a[92:95], v23 offset:12288\n"
    "v_mfma_f32_16x16x32_bf16 v[148:151], a[116:119], v[64:67], v[148:151]\n"
    "v_exp_f32 v111, v111\n"
    "v_mfma_f32_16x16x32_bf16 v[180:183], a[116:119], v[72:75], v[180:183]\n"
    "buffer_load_dwordx4 v5, s[40:43], s91 offen offset:3072 lds\n"
    "v_cvt_pk_bf16_f32 v107, v110, v111\n"
    "v_mfma_f32_16x16x32_bf16 v[212:215], a[116:119], v[80:83], v[212:215]\n"
    "s_add_u32 m0, s17, 131072\n"
    "v_exp_f32 v116, v116\n"
    "v_mfma_f32_16x16x32_bf16 v[244:247], a[116:119], v[88:91], v[244:247]\n"
    "s_waitcnt lgkmcnt(10)\n"
    "buffer_load_dwordx4 v6, s[40:43], s91 offen lds\n"
    "v_mfma_f32_16x16x32_bf16 v[152:155], a[120:123], v[64:67], v[152:155]\n"
    "v_exp_f32 v117, v117\n"
    "v_mfma_f32_16x16x32_bf16 v[184:187], a[120:123], v[72:75], v[184:187]\n"
    "buffer_load_dwordx4 v7, s[40:43], s91 offen offset:1024 lds\n"
    "v_cvt_pk_bf16_f32 v114, v116, v117\n"
    "v_mfma_f32_16x16x32_bf16 v[216:219], a[120:123], v[80:83], v[216:219]\n"
    "v_exp_f32 v118, v118\n"
    "v_mfma_f32_16x16x32_bf16 v[248:251], a[120:123], v[88:91], v[248:251]\n"
    "s_waitcnt lgkmcnt(8)\n"
    "buffer_load_dwordx4 v8, s[40:43], s91 offen offset:2048 lds\n"
    "v_mfma_f32_16x16x32_bf16 v[156:159], a[124:127], v[64:67], v[156:159]\n"
    "buffer_load_dwordx4 v9, s[40:43], s91 offen offset:3072 lds\n"
    "v_mfma_f32_16x16x32_bf16 v[188:191], a[124:127], v[72:75], v[188:191]\n"
    "s_add_u32 m0, s17, 135168\n"
    "buffer_load_dwordx2 a[224:225], v13, s[20:23], s92 offen offset:0 sc1\n"
    "v_mfma_f32_16x16x32_bf16 v[220:223], a[124:127], v[80:83], v[220:223]\n"
    "buffer_load_dwordx4 v10, s[40:43], s91 offen lds\n"
    "v_mfma_f32_16x16x32_bf16 v[252:255], a[124:127], v[88:91], v[252:255]\n"
    "s_waitcnt lgkmcnt(7)\n"
    "buffer_load_dwordx2 a[226:227], v13, s[20:23], s92 offen offset:512 sc1\n"
    "v_mfma_f32_16x16x32_bf16 v[64:67], a[64:67], a[0:3], v[48:51]\n"
    "s_waitcnt lgkmcnt(6)\n"
    "v_exp_f32 v119, v119\n"
    "v_mfma_f32_16x16x32_bf16 v[64:67], a[68:71], a[4:7], v[64:67]\n"
    "s_waitcnt lgkmcnt(5)\n"
    "ds_read_b64_tr_b16 a[96:97], v32 offset:16384\n"
    "v_cvt_pk_bf16_f32 v115, v118, v119\n"
    "v_mfma_f32_16x16x32_bf16 v[64:67], a[72:75], a[8:11], v[64:67]\n"
    "s_waitcnt lgkmcnt(5)\n"
    "v_exp_f32 v124, v124\n"
    "v_mfma_f32_16x16x32_bf16 v[64:67], a[76:79], a[12:15], v[64:67]\n"
    "ds_read_b64_tr_b16 a[98:99], v32 offset:20480\n"
    "v_mfma_f32_16x16x32_bf16 v[72:75], a[64:67], a[16:19], v[52:55]\n"
    "v_exp_f32 v125, v125\n"
    "v_mfma_f32_16x16x32_bf16 v[72:75], a[68:71], a[20:23], v[72:75]\n"
    "ds_read_b64_tr_b16 a[100:101], v33 offset:16384\n"
    "v_cvt_pk_bf16_f32 v122, v124, v125\n"
    "v_mfma_f32_16x16x32_bf16 v[72:75], a[72:75], a[24:27], v[72:75]\n"
    "v_exp_f32 v126, v126\n"
    "v_mfma_f32_16x16x32_bf16 v[72:75], a[76:79], a[28:31], v[72:75]\n"
    "ds_read_b64_tr_b16 a[102:103], v33 offset:20480\n"
    "v_mfma_f32_16x16x32_bf16 v[80:83], a[64:67], a[32:35], v[56:59]\n"
    "s_waitcnt lgkmcnt(4)\n"
    "s_barrier\n"
    "v_exp_f32 v127, v127\n"
    "v_mfma_f32_16x16x32_bf16 v[80:83], a[68:71], a[36:39], v[80:83]\n"
    "ds_read_b64_tr_b16 a[104:105], v34 offset:16384\n"
    "v_cvt_pk_bf16_f32 v123, v126, v127\n"
    "v_mfma_f32_16x16x32_bf16 v[80:83], a[72:75], a[40:43], v[80:83]\n"
    "v_exp_f32 v64, v64\n"
    "v_mfma_f32_16x16x32_bf16 v[80:83], a[76:79], a[44:47], v[80:83]\n"
    "ds_read_b64_tr_b16 a[106:107], v34 offset:20480\n"
    "v_mfma_f32_16x16x32_bf16 v[88:91], a[64:67], a[48:51], v[60:63]\n"
    "v_exp_f32 v65, v65\n"
    "v_mfma_f32_16x16x32_bf16 v[88:91], a[68:71], a[52:55], v[88:91]\n"
    "ds_read_b64_tr_b16 a[108:109], v35 offset:16384\n"
    "v_cvt_pk_bf16_f32 v64, v64, v65\n"
    "v_mfma_f32_16x16x32_bf16 v[88:91], a[72:75], a[56:59], v[88:91]\n"
    "v_exp_f32 v66, v66\n"
    "v_mfma_f32_16x16x32_bf16 v[88:91], a[76:79], a[60:63], v[88:91]\n"
    "s_waitcnt lgkmcnt(10)\n"
    "ds_read_b64_tr_b16 a[110:111], v35 offset:20480\n"
    "v_mfma_f32_16x16x32_bf16 v[68:71], a[80:83], a[0:3], v[48:51]\n"
    "s_waitcnt lgkmcnt(10)\n"
    "v_exp_f32 v67, v67\n"
    "v_mfma_f32_16x16x32_bf16 v[68:71], a[84:87], a[4:7], v[68:71]\n"
    "s_waitcnt lgkmcnt(9)\n"
    "ds_read_b64_tr_b16 a[112:113], v36 offset:16384\n"
    "v_cvt_pk_bf16_f32 v65, v66, v67\n"
    "v_mfma_f32_16x16x32_bf16 v[68:71], a[88:91], a[8:11], v[68:71]\n"
    "s_waitcnt lgkmcnt(9)\n"
    "v_exp_f32 v72, v72\n"
    "v_mfma_f32_16x16x32_bf16 v[68:71], a[92:95], a[12:15], v[68:71]\n"
    "ds_read_b64_tr_b16 a[114:115], v36 offset:20480\n"
    "v_mfma_f32_16x16x32_bf16 v[76:79], a[80:83], a[16:19], v[52:55]\n"
    "v_exp_f32 v73, v73\n"
    "v_mfma_f32_16x16x32_bf16 v[76:79], a[84:87], a[20:23], v[76:79]\n"
    "ds_read_b64_tr_b16 a[116:117], v37 offset:16384\n"
    "v_cvt_pk_bf16_f32 v72, v72, v73\n"
    "v_mfma_f32_16x16x32_bf16 v[76:79], a[88:91], a[24:27], v[76:79]\n"
    "v_exp_f32 v74, v74\n"
    "v_mfma_f32_16x16x32_bf16 v[76:79], a[92:95], a[28:31], v[76:79]\n"
    "ds_read_b64_tr_b16 a[118:119], v37 offset:20480\n"
    "v_mfma_f32_16x16x32_bf16 v[84:87], a[80:83], a[32:35], v[56:59]\n"
    "v_exp_f32 v75, v75\n"
    "v_mfma_f32_16x16x32_bf16 v[84:87], a[84:87], a[36:39], v[84:87]\n"
    "ds_read_b64_tr_b16 a[120:121], v38 offset:16384\n"
    "v_cvt_pk_bf16_f32 v73, v74, v75\n"
    "v_mfma_f32_16x16x32_bf16 v[84:87], a[88:91], a[40:43], v[84:87]\n"
    "v_exp_f32 v80, v80\n"
    "v_mfma_f32_16x16x32_bf16 v[84:87], a[92:95], a[44:47], v[84:87]\n"
    "ds_read_b64_tr_b16 a[122:123], v38 offset:20480\n"
    "v_mfma_f32_16x16x32_bf16 v[92:95], a[80:83], a[48:51], v[60:63]\n"
    "v_exp_f32 v81, v81\n"
    "v_mfma_f32_16x16x32_bf16 v[92:95], a[84:87], a[52:55], v[92:95]\n"
    "ds_read_b64_tr_b16 a[124:125], v39 offset:16384\n"
    "v_cvt_pk_bf16_f32 v80, v80, v81\n"
    "v_mfma_f32_16x16x32_bf16 v[92:95], a[88:91], a[56:59], v[92:95]\n"
    "v_exp_f32 v82, v82\n"
    "v_mfma_f32_16x16x32_bf16 v[92:95], a[92:95], a[60:63], v[92:95]\n"
    "s_mov_b32 s91, 286001156\n"
    "s_cmp_eq_u32 s37, 0\n"
    "s_cselect_b32 s91, 185207048, s91\n"
    "ds_read_b64_tr_b16 a[126:127], v39 offset:20480\n"
    "v_mfma_f32_16x16x32_bf16 a[128:131], a[144:147], v[96:99], a[128:131]\n"
    "s_lshr_b32 s91, s91, s36\n"
    "s_xor_b32 s91, s91, s16\n"
    "s_and_b32 s91, s91, 31\n"
    "s_add_u32 s92, s18, 20480\n"
    "v_exp_f32 v83, v83\n"
    "v_mfma_f32_16x16x32_bf16 a[132:135], a[144:147], v[104:107], a[132:135]\n"
    "s_mul_i32 s91, s91, 36864\n"
    "buffer_load_dwordx4 a[252:255], v0, s[20:23], s92 offen offset:3072 sc1\n"
    "v_cvt_pk_bf16_f32 v81, v82, v83\n"
    "v_mfma_f32_16x16x32_bf16 a[136:139], a[144:147], v[112:115], a[136:139]\n"
    "s_add_u32 m0, s17, 53248\n"
    "v_exp_f32 v88, v88\n"
    "v_mfma_f32_16x16x32_bf16 a[140:143], a[144:147], v[120:123], a[140:143]\n"
    "s_waitcnt lgkmcnt(14)\n"
    "buffer_load_dwordx4 v2, s[40:43], s91 offen lds\n"
    "v_mfma_f32_16x16x32_bf16 v[128:131], a[96:99], v[96:99], v[128:131]\n"
    "v_exp_f32 v89, v89\n"
    "v_mfma_f32_16x16x32_bf16 v[160:163], a[96:99], v[104:107], v[160:163]\n"
    "buffer_load_dwordx4 v3, s[40:43], s91 offen offset:1024 lds\n"
    "v_cvt_pk_bf16_f32 v88, v88, v89\n"
    "v_mfma_f32_16x16x32_bf16 v[192:195], a[96:99], v[112:115], v[192:195]\n"
    "v_exp_f32 v90, v90\n"
    "v_mfma_f32_16x16x32_bf16 v[224:227], a[96:99], v[120:123], v[224:227]\n"
    "s_waitcnt lgkmcnt(12)\n"
    "buffer_load_dwordx4 v4, s[40:43], s91 offen offset:2048 lds\n"
    "v_mfma_f32_16x16x32_bf16 v[132:135], a[100:103], v[96:99], v[132:135]\n"
    "v_exp_f32 v91, v91\n"
    "v_mfma_f32_16x16x32_bf16 v[164:167], a[100:103], v[104:107], v[164:167]\n"
    "buffer_load_dwordx4 v5, s[40:43], s91 offen offset:3072 lds\n"
    "v_cvt_pk_bf16_f32 v89, v90, v91\n"
    "v_mfma_f32_16x16x32_bf16 v[196:199], a[100:103], v[112:115], v[196:199]\n"
    "s_add_u32 m0, s17, 57344\n"
    "v_exp_f32 v68, v68\n"
    "v_mfma_f32_16x16x32_bf16 v[228:231], a[100:103], v[120:123], v[228:231]\n"
    "s_waitcnt lgkmcnt(10)\n"
    "buffer_load_dwordx4 v6, s[40:43], s91 offen lds\n"
    "v_mfma_f32_16x16x32_bf16 v[136:139], a[104:107], v[96:99], v[136:139]\n"
    "v_exp_f32 v69, v69\n"
    "v_mfma_f32_16x16x32_bf16 v[168:171], a[104:107], v[104:107], v[168:171]\n"
    "buffer_load_dwordx4 v7, s[40:43], s91 offen offset:1024 lds\n"
    "v_cvt_pk_bf16_f32 v66, v68, v69\n"
    "v_mfma_f32_16x16x32_bf16 v[200:203], a[104:107], v[112:115], v[200:203]\n"
    "v_exp_f32 v70, v70\n"
    "v_mfma_f32_16x16x32_bf16 v[232:235], a[104:107], v[120:123], v[232:235]\n"
    "s_waitcnt lgkmcnt(8)\n"
    "buffer_load_dwordx4 v8, s[40:43], s91 offen offset:2048 lds\n"
    "v_mfma_f32_16x16x32_bf16 v[140:143], a[108:111], v[96:99], v[140:143]\n"
    "v_exp_f32 v71, v71\n"
    "v_mfma_f32_16x16x32_bf16 v[172:175], a[108:111], v[104:107], v[172:175]\n"
    "buffer_load_dwordx4 v9, s[40:43], s91 offen offset:3072 lds\n"
    "v_cvt_pk_bf16_f32 v67, v70, v71\n"
    "v_mfma_f32_16x16x32_bf16 v[204:207], a[108:111], v[112:115], v[204:207]\n"
    "s_add_u32 m0, s17, 61440\n"
    "v_exp_f32 v76, v76\n"
    "v_mfma_f32_16x16x32_bf16 v[236:239], a[108:111], v[120:123], v[236:239]\n"
    "s_waitcnt lgkmcnt(6)\n"
    "buffer_load_dwordx4 v10, s[40:43], s91 offen lds\n"
    "v_mfma_f32_16x16x32_bf16 v[144:147], a[112:115], v[96:99], v[144:147]\n"
    "v_exp_f32 v77, v77\n"
    "v_mfma_f32_16x16x32_bf16 v[176:179], a[112:115], v[104:107], v[176:179]\n"
    "buffer_load_dwordx4 a[248:251], v0, s[20:23], s92 offen offset:2048 sc1\n"
    "v_cvt_pk_bf16_f32 v74, v76, v77\n"
    "v_mfma_f32_16x16x32_bf16 v[208:211], a[112:115], v[112:115], v[208:211]\n"
    "v_exp_f32 v78, v78\n"
    "v_mfma_f32_16x16x32_bf16 v[240:243], a[112:115], v[120:123], v[240:243]\n"
    "s_waitcnt lgkmcnt(4)\n"
    "buffer_load_dwordx4 a[244:247], v0, s[20:23], s92 offen offset:1024 sc1\n"
    "v_mfma_f32_16x16x32_bf16 v[148:151], a[116:119], v[96:99], v[148:151]\n"
    "v_exp_f32 v79, v79\n"
    "v_mfma_f32_16x16x32_bf16 v[180:183], a[116:119], v[104:107], v[180:183]\n"
    "buffer_load_dwordx4 a[240:243], v0, s[20:23], s92 offen offset:0 sc1\n"
    "v_cvt_pk_bf16_f32 v75, v78, v79\n"
    "v_mfma_f32_16x16x32_bf16 v[212:215], a[116:119], v[112:115], v[212:215]\n"
    "v_exp_f32 v84, v84\n"
    "v_mfma_f32_16x16x32_bf16 v[244:247], a[116:119], v[120:123], v[244:247]\n"
    "s_waitcnt lgkmcnt(2)\n"
    "v_mfma_f32_16x16x32_bf16 v[152:155], a[120:123], v[96:99], v[152:155]\n"
    "v_exp_f32 v85, v85\n"
    "v_mfma_f32_16x16x32_bf16 v[184:187], a[120:123], v[104:107], v[184:187]\n"
    "v_cvt_pk_bf16_f32 v82, v84, v85\n"
    "v_mfma_f32_16x16x32_bf16 v[216:219], a[120:123], v[112:115], v[216:219]\n"
    "v_exp_f32 v86, v86\n"
    "v_mfma_f32_16x16x32_bf16 v[248:251], a[120:123], v[120:123], v[248:251]\n"
    "s_waitcnt lgkmcnt(0)\n"
    "v_mfma_f32_16x16x32_bf16 v[156:159], a[124:127], v[96:99], v[156:159]\n"
    "v_mfma_f32_16x16x32_bf16 v[188:191], a[124:127], v[104:107], v[188:191]\n"
    "v_mfma_f32_16x16x32_bf16 v[220:223], a[124:127], v[112:115], v[220:223]\n"
    "v_mfma_f32_16x16x32_bf16 v[252:255], a[124:127], v[120:123], v[252:255]\n"
    "ds_read_b64_tr_b16 a[96:97], v32 offset:24576\n"
    "ds_read_b64_tr_b16 a[98:99], v32 offset:28672\n"
    "ds_read_b64_tr_b16 a[100:101], v33 offset:24576\n"
    "ds_read_b64_tr_b16 a[102:103], v33 offset:28672\n"
    "ds_read_b64_tr_b16 a[104:105], v34 offset:24576\n"
    "ds_read_b64_tr_b16 a[106:107], v34 offset:28672\n"
    "ds_read_b64_tr_b16 a[108:109], v35 offset:24576\n"
    "ds_read_b64_tr_b16 a[110:111], v35 offset:28672\n"
    "ds_read_b64_tr_b16 a[112:113], v36 offset:24576\n"
    "ds_read_b64_tr_b16 a[114:115], v36 offset:28672\n"
    "ds_read_b64_tr_b16 a[116:117], v37 offset:24576\n"
    "ds_read_b64_tr_b16 a[118:119], v37 offset:28672\n"
    "ds_read_b64_tr_b16 a[120:121], v38 offset:24576\n"
    "ds_read_b64_tr_b16 a[122:123], v38 offset:28672\n"
    "ds_read_b64_tr_b16 a[124:125], v39 offset:24576\n"
    "ds_read_b64_tr_b16 a[126:127], v39 offset:28672\n"
    "v_exp_f32 v87, v87\n"
    "s_nop 0\n"
    "v_cvt_pk_bf16_f32 v83, v86, v87\n"
    "v_exp_f32 v92, v92\n"
    "v_exp_f32 v93, v93\n"
    "s_nop 0\n"
    "v_cvt_pk_bf16_f32 v90, v92, v93\n"
    "v_exp_f32 v94, v94\n"
    "v_exp_f32 v95, v95\n"
    "s_nop 0\n"
    "v_cvt_pk_bf16_f32 v91, v94, v95\n"
    "v_mfma_f32_16x16x32_bf16 a[128:131], a[144:147], v[64:67], a[128:131]\n"
    "v_lshrrev_b32 v60, 6, v0\n"
    "v_mul_u32_u24 v60, 288, v60\n"
    "v_max3_f32 v100, |v144|, |v145|, |v146|\n"
    "v_mfma_f32_16x16x32_bf16 a[132:135], a[144:147], v[72:75], a[132:135]\n"
    "v_bfe_u32 v61, v0, 4, 2\n"
    "v_lshl_add_u32 v60, v61, 3, v60\n"
    "v_max3_f32 v101, |v176|, |v177|, |v178|\n"
    "v_mfma_f32_16x16x32_bf16 a[136:139], a[144:147], v[80:83], a[136:139]\n"
    "v_add_u32 v60, 16384, v60\n"
    "v_add_u32 v61, 64512, v60\n"
    "v_max3_f32 v102, |v208|, |v209|, |v210|\n"
    "v_mfma_f32_16x16x32_bf16 a[140:143], a[144:147], v[88:91], a[140:143]\n"
    "s_waitcnt lgkmcnt(14)\n"
    "s_lshl_b32 s84, s80, 2\n"
    "s_add_u32 s84, s84, 12288\n"
    "v_add_u32 v62, 129024, v60\n"
    "v_max3_f32 v103, |v240|, |v241|, |v242|\n"
    "v_mfma_f32_16x16x32_bf16 v[128:131], a[96:99], v[64:67], v[128:131]\n"
    "v_mov_b32 v96, s84\n"
    "v_mov_b32 v98, 12288\n"
    "v_min3_f32 v100, v100, v101, v102\n"
    "v_min_f32 v100, v100, v103\n"
    "v_mfma_f32_16x16x32_bf16 v[160:163], a[96:99], v[72:75], v[160:163]\n"
    "v_accvgpr_read_b32 v108, a128\n"
    "v_accvgpr_read_b32 v109, a132\n"
    "v_cmp_gt_f32 vcc, 0x0d800000, v100\n"
    "v_mfma_f32_16x16x32_bf16 v[192:195], a[96:99], v[80:83], v[192:195]\n"
    "v_accvgpr_read_b32 v110, a136\n"
    "v_accvgpr_read_b32 v111, a140\n"
    "v_mfma_f32_16x16x32_bf16 v[224:227], a[96:99], v[88:91], v[224:227]\n"
    "s_waitcnt lgkmcnt(12)\n"
    "s_cmp_lg_u64 vcc, 0\n"
    "s_cselect_b32 s83, 1, 0\n"
    "v_add_f32 v97, v108, v109\n"
    "v_add_f32 v99, v110, v111\n"
    "v_mfma_f32_16x16x32_bf16 v[132:135], a[100:103], v[64:67], v[132:135]\n"
    "v_add_f32 v97, v97, v99\n"
    "v_cmp_nge_f32 vcc, 0x45800000, v97\n"
    "v_mfma_f32_16x16x32_bf16 v[164:167], a[100:103], v[72:75], v[164:167]\n"
    "v_rcp_f32 v108, v108\n"
    "v_mfma_f32_16x16x32_bf16 v[196:199], a[100:103], v[80:83], v[196:199]\n"
    "s_cmp_lg_u64 vcc, 0\n"
    "s_cselect_b32 s83, 1, s83\n"
    "v_rcp_f32 v109, v109\n"
    "v_mfma_f32_16x16x32_bf16 v[228:231], a[100:103], v[88:91], v[228:231]\n"
    "s_waitcnt lgkmcnt(10)\n"
    "v_rcp_f32 v110, v110\n"
    "v_mfma_f32_16x16x32_bf16 v[136:139], a[104:107], v[64:67], v[136:139]\n"
    "v_rcp_f32 v111, v111\n"
    "v_mfma_f32_16x16x32_bf16 v[168:171], a[104:107], v[72:75], v[168:171]\n"
    "s_mov_b32 s87, 0\n"
    "v_mov_b32 v97, s83\n"
    "v_mul_f32 v128, v128, v108\n"
    "v_mfma_f32_16x16x32_bf16 v[200:203], a[104:107], v[80:83], v[200:203]\n"
    "ds_write_b32 v96, v97\n"
    "v_mul_f32 v129, v129, v108\n"
    "v_mfma_f32_16x16x32_bf16 v[232:235], a[104:107], v[88:91], v[232:235]\n"
    "s_waitcnt lgkmcnt(9)\n"
    "v_mul_f32 v130, v130, v108\n"
    "v_mul_f32 v131, v131, v108\n"
    "v_mfma_f32_16x16x32_bf16 v[140:143], a[108:111], v[64:67], v[140:143]\n"
    "v_mul_f32 v132, v132, v108\n"
    "v_mul_f32 v133, v133, v108\n"
    "v_mfma_f32_16x16x32_bf16 v[172:175], a[108:111], v[72:75], v[172:175]\n"
    "v_mul_f32 v134, v134, v108\n"
    "v_mul_f32 v135, v135, v108\n"
    "v_mfma_f32_16x16x32_bf16 v[204:207], a[108:111], v[80:83], v[204:207]\n"
    "s_waitcnt vmcnt(38) lgkmcnt(0)\n"
    "s_barrier\n"
    "v_cvt_pk_bf16_f32 v128, v128, v129\n"
    "v_cvt_pk_bf16_f32 v129, v130, v131\n"
    "v_mfma_f32_16x16x32_bf16 v[236:239], a[108:111], v[88:91], v[236:239]\n"
    "ds_read_b128 v[100:103], v98\n"
    "v_cvt_pk_bf16_f32 v130, v132, v133\n"
    "v_mfma_f32_16x16x32_bf16 v[144:147], a[112:115], v[64:67], v[144:147]\n"
    "v_cvt_pk_bf16_f32 v131, v134, v135\n"
    "v_mul_f32 v160, v160, v109\n"
    "v_mfma_f32_16x16x32_bf16 v[176:179], a[112:115], v[72:75], v[176:179]\n"
    "ds_read_b64_tr_b16 v[16:17], v61 offset:9216\n"
    "buffer_store_dwordx4 v[128:131], v0, s[64:67], s87 offen offset:0 sc1\n"
    "v_mfma_f32_16x16x32_bf16 v[208:211], a[112:115], v[80:83], v[208:211]\n"
    "v_mul_f32 v161, v161, v109\n"
    "v_mul_f32 v162, v162, v109\n"
    "v_mfma_f32_16x16x32_bf16 v[240:243], a[112:115], v[88:91], v[240:243]\n"
    "ds_read_b64_tr_b16 v[18:19], v61 offset:13824\n"
    "v_mul_f32 v163, v163, v109\n"
    "v_mfma_f32_16x16x32_bf16 v[148:151], a[116:119], v[64:67], v[148:151]\n"
    "v_mul_f32 v164, v164, v109\n"
    "v_mul_f32 v165, v165, v109\n"
    "v_mfma_f32_16x16x32_bf16 v[180:183], a[116:119], v[72:75], v[180:183]\n"
    "ds_read_b64_tr_b16 v[20:21], v61 offset:9248\n"
    "v_mul_f32 v166, v166, v109\n"
    "v_mfma_f32_16x16x32_bf16 v[212:215], a[116:119], v[80:83], v[212:215]\n"
    "v_mul_f32 v167, v167, v109\n"
    "v_cvt_pk_bf16_f32 v160, v160, v161\n"
    "v_mfma_f32_16x16x32_bf16 v[244:247], a[116:119], v[88:91], v[244:247]\n"
    "ds_read_b64_tr_b16 v[22:23], v61 offset:13856\n"
    "v_cvt_pk_bf16_f32 v161, v162, v163\n"
    "v_mfma_f32_16x16x32_bf16 v[152:155], a[120:123], v[64:67], v[152:155]\n"
    "v_cvt_pk_bf16_f32 v162, v164, v165\n"
    "v_cvt_pk_bf16_f32 v163, v166, v167\n"
    "v_mfma_f32_16x16x32_bf16 v[184:187], a[120:123], v[72:75], v[184:187]\n"
    "ds_read_b64_tr_b16 v[24:25], v61 offset:9280\n"
    "buffer_store_dwordx4 v[160:163], v0, s[64:67], s87 offen offset:1024 sc1\n"
    "v_mfma_f32_16x16x32_bf16 v[216:219], a[120:123], v[80:83], v[216:219]\n"
    "s_waitcnt lgkmcnt(5)\n"
    "v_or3_b32 v100, v100, v101, v102\n"
    "v_or_b32 v100, v100, v103\n"
    "v_mfma_f32_16x16x32_bf16 v[248:251], a[120:123], v[88:91], v[248:251]\n"
    "ds_read_b64_tr_b16 v[26:27], v61 offset:13888\n"
    "v_mul_f32 v192, v192, v110\n"
    "v_mfma_f32_16x16x32_bf16 v[156:159], a[124:127], v[64:67], v[156:159]\n"
    "v_readfirstlane_b32 s83, v100\n"
    "v_mul_f32 v193, v193, v110\n"
    "v_mfma_f32_16x16x32_bf16 v[188:191], a[124:127], v[72:75], v[188:191]\n"
    "v_mul_f32 v194, v194, v110\n"
    "v_mul_f32 v195, v195, v110\n"
    "v_mfma_f32_16x16x32_bf16 v[220:223], a[124:127], v[80:83], v[220:223]\n"
    "v_mul_f32 v196, v196, v110\n"
    "v_mul_f32 v197, v197, v110\n"
    "v_mfma_f32_16x16x32_bf16 v[252:255], a[124:127], v[88:91], v[252:255]\n"
    "v_mul_f32 v198, v198, v110\n"
    "v_mul_f32 v199, v199, v110\n"
    "v_cvt_pk_bf16_f32 v192, v192, v193\n"
    "v_cvt_pk_bf16_f32 v193, v194, v195\n"
    "v_cvt_pk_bf16_f32 v194, v196, v197\n"
    "v_cvt_pk_bf16_f32 v195, v198, v199\n"
    "buffer_store_dwordx4 v[192:195], v0, s[64:67], s87 offen offset:2048 sc1\n"
    "v_mul_f32 v224, v224, v111\n"
    "v_mul_f32 v225, v225, v111\n"
    "v_mul_f32 v226, v226, v111\n"
    "v_mul_f32 v227, v227, v111\n"
    "v_mul_f32 v228, v228, v111\n"
    "v_mul_f32 v229, v229, v111\n"
    "v_mul_f32 v230, v230, v111\n"
    "v_mul_f32 v231, v231, v111\n"
    "v_cvt_pk_bf16_f32 v224, v224, v225\n"
    "v_cvt_pk_bf16_f32 v225, v226, v227\n"
    "v_cvt_pk_bf16_f32 v226, v228, v229\n"
    "v_cvt_pk_bf16_f32 v227, v230, v231\n"
    "buffer_store_dwordx4 v[224:227], v0, s[64:67], s87 offen offset:3072 sc1\n"
    "v_mul_f32 v136, v136, v108\n"
    "v_mul_f32 v137, v137, v108\n"
    "v_mul_f32 v138, v138, v108\n"
    "v_mul_f32 v139, v139, v108\n"
    "v_mul_f32 v140, v140, v108\n"
    "v_mul_f32 v141, v141, v108\n"
    "v_mul_f32 v142, v142, v108\n"
    "v_mul_f32 v143, v143, v108\n"
    "s_andn2_b32 s83, s83, s93\n"
    "s_cbranch_scc1 L_FB%=\n"
    "s_cmp_lg_u32 s37, 0\n"
    "s_cbranch_scc1 L_CS%=\n"
    "s_waitcnt lgkmcnt(4)\n"
    "v_mfma_f32_16x16x32_bf16 a[0:3], v[16:19], a[160:163], 0\n"
    "ds_read_b64_tr_b16 v[28:29], v61 offset:9312\n"
    "v_mfma_f32_16x16x32_bf16 a[4:7], v[16:19], a[164:167], 0\n"
    "s_mov_b32 s92, 24576\n"
    "ds_read_b64_tr_b16 v[30:31], v61 offset:13920\n"
    "v_mfma_f32_16x16x32_bf16 a[8:11], v[16:19], a[168:171], 0\n"
    "buffer_load_dwordx4 a[156:159], v0, s[20:23], s92 offen offset:3072 sc1\n"
    "v_mfma_f32_16x16x32_bf16 a[12:15], v[16:19], a[172:175], 0\n"
    "s_waitcnt lgkmcnt(4)\n"
    "v_mfma_f32_16x16x32_bf16 a[16:19], v[20:23], a[160:163], 0\n"
    "ds_read_b64_tr_b16 v[32:33], v61 offset:9344\n"
    "v_mfma_f32_16x16x32_bf16 a[20:23], v[20:23], a[164:167], 0\n"
    "ds_read_b64_tr_b16 v[34:35], v61 offset:13952\n"
    "v_mfma_f32_16x16x32_bf16 a[24:27], v[20:23], a[168:171], 0\n"
    "buffer_load_dwordx4 a[152:155], v0, s[20:23], s92 offen offset:2048 sc1\n"
    "v_mfma_f32_16x16x32_bf16 a[28:31], v[20:23], a[172:175], 0\n"
    "s_waitcnt lgkmcnt(4)\n"
    "v_cvt_pk_bf16_f32 v136, v136, v137\n"
    "v_cvt_pk_bf16_f32 v137, v138, v139\n"
    "v_mfma_f32_16x16x32_bf16 a[32:35], v[24:27], a[160:163], 0\n"
    "ds_read_b64_tr_b16 v[36:37], v61 offset:9376\n"
    "v_cvt_pk_bf16_f32 v138, v140, v141\n"
    "v_mfma_f32_16x16x32_bf16 a[36:39], v[24:27], a[164:167], 0\n"
    "ds_read_b64_tr_b16 v[38:39], v61 offset:13984\n"
    "v_cvt_pk_bf16_f32 v139, v142, v143\n"
    "v_mfma_f32_16x16x32_bf16 a[40:43], v[24:27], a[168:171], 0\n"
    "buffer_load_dwordx4 a[148:151], v0, s[20:23], s92 offen offset:1024 sc1\n"
    "v_mfma_f32_16x16x32_bf16 a[44:47], v[24:27], a[172:175], 0\n"
    "s_waitcnt lgkmcnt(4)\n"
    "v_mfma_f32_16x16x32_bf16 a[48:51], v[28:31], a[160:163], 0\n"
    "ds_read_b64_tr_b16 v[48:49], v61 offset:9408\n"
    "v_mfma_f32_16x16x32_bf16 a[52:55], v[28:31], a[164:167], 0\n"
    "ds_read_b64_tr_b16 v[50:51], v61 offset:14016\n"
    "v_mfma_f32_16x16x32_bf16 a[56:59], v[28:31], a[168:171], 0\n"
    "v_mfma_f32_16x16x32_bf16 a[60:63], v[28:31], a[172:175], 0\n"
    "s_waitcnt lgkmcnt(4)\n"
    "buffer_load_dwordx4 a[144:147], v0, s[20:23], s92 offen offset:0 sc1\n"
    "v_mfma_f32_16x16x32_bf16 a[64:67], v[32:35], a[160:163], 0\n"
    "ds_read_b64_tr_b16 v[52:53], v61 offset:9440\n"
    "v_mfma_f32_16x16x32_bf16 a[68:71], v[32:35], a[164:167], 0\n"
    "ds_read_b64_tr_b16 v[54:55], v61 offset:14048\n"
    "v_mfma_f32_16x16x32_bf16 a[72:75], v[32:35], a[168:171], 0\n"
    "v_mfma_f32_16x16x32_bf16 a[76:79], v[32:35], a[172:175], 0\n"
    "s_waitcnt lgkmcnt(4)\n"
    "v_mfma_f32_16x16x32_bf16 a[80:83], v[36:39], a[160:163], 0\n"
    "ds_read_b64_tr_b16 v[56:57], v61 offset:9472\n"
    "v_mfma_f32_16x16x32_bf16 a[84:87], v[36:39], a[164:167], 0\n"
    "s_mov_b32 s87, 4096\n"
    "ds_read_b64_tr_b16 v[58:59], v61 offset:14080\n"
    "v_mfma_f32_16x16x32_bf16 a[88:91], v[36:39], a[168:171], 0\n"
    "buffer_store_dwordx4 v[136:139], v0, s[64:67], s87 offen offset:0 sc1\n"
    "v_mfma_f32_16x16x32_bf16 a[92:95], v[36:39], a[172:175], 0\n"
    "s_waitcnt lgkmcnt(4)\n"
    "v_mfma_f32_16x16x32_bf16 a[96:99], v[48:51], a[160:163], 0\n"
    "ds_read_b64_tr_b16 v[16:17], v61 offset:18432\n"
    "v_mfma_f32_16x16x32_bf16 a[100:103], v[48:51], a[164:167], 0\n"
    "s_mov_b32 s92, 28672\n"
    "ds_read_b64_tr_b16 v[18:19], v61 offset:23040\n"
    "v_mfma_f32_16x16x32_bf16 a[104:107], v[48:51], a[168:171], 0\n"
    "buffer_load_dwordx4 v[76:79], v0, s[20:23], s92 offen offset:3072 sc1\n"
    "v_mfma_f32_16x16x32_bf16 a[108:111], v[48:51], a[172:175], 0\n"
    "s_waitcnt lgkmcnt(4)\n"
    "v_mul_f32 v168, v168, v109\n"
    "v_mul_f32 v169, v169, v109\n"
    "v_mfma_f32_16x16x32_bf16 a[112:115], v[52:55], a[160:163], 0\n"
    "ds_read_b64_tr_b16 v[20:21], v61 offset:18464\n"
    "v_mul_f32 v170, v170, v109\n"
    "v_mfma_f32_16x16x32_bf16 a[116:119], v[52:55], a[164:167], 0\n"
    "ds_read_b64_tr_b16 v[22:23], v61 offset:23072\n"
    "v_mul_f32 v171, v171, v109\n"
    "v_mfma_f32_16x16x32_bf16 a[120:123], v[52:55], a[168:171], 0\n"
    "buffer_load_dwordx4 v[72:75], v0, s[20:23], s92 offen offset:2048 sc1\n"
    "v_mul_f32 v172, v172, v109\n"
    "v_mfma_f32_16x16x32_bf16 a[124:127], v[52:55], a[172:175], 0\n"
    "s_waitcnt lgkmcnt(4)\n"
    "v_mul_f32 v173, v173, v109\n"
    "v_mul_f32 v174, v174, v109\n"
    "v_mfma_f32_16x16x32_bf16 a[128:131], v[56:59], a[160:163], 0\n"
    "ds_read_b64_tr_b16 v[24:25], v61 offset:18496\n"
    "v_mul_f32 v175, v175, v109\n"
    "v_mfma_f32_16x16x32_bf16 a[132:135], v[56:59], a[164:167], 0\n"
    "ds_read_b64_tr_b16 v[26:27], v61 offset:23104\n"
    "v_cvt_pk_bf16_f32 v168, v168, v169\n"
    "v_mfma_f32_16x16x32_bf16 a[136:139], v[56:59], a[168:171], 0\n"
    "v_cvt_pk_bf16_f32 v169, v170, v171\n"
    "v_cvt_pk_bf16_f32 v170, v172, v173\n"
    "v_mfma_f32_16x16x32_bf16 a[140:143], v[56:59], a[172:175], 0\n"
    "s_waitcnt lgkmcnt(4)\n"
    "buffer_load_dwordx4 v[68:71], v0, s[20:23], s92 offen offset:1024 sc1\n"
    "v_cvt_pk_bf16_f32 v171, v174, v175\n"
    "v_mfma_f32_16x16x32_bf16 a[0:3], v[16:19], a[176:179], a[0:3]\n"
    "ds_read_b64_tr_b16 v[28:29], v61 offset:18528\n"
    "v_mfma_f32_16x16x32_bf16 a[4:7], v[16:19], a[180:183], a[4:7]\n"
    "ds_read_b64_tr_b16 v[30:31], v61 offset:23136\n"
    "v_mfma_f32_16x16x32_bf16 a[8:11], v[16:19], a[184:187], a[8:11]\n"
    "v_mfma_f32_16x16x32_bf16 a[12:15], v[16:19], a[188:191], a[12:15]\n"
    "s_waitcnt lgkmcnt(4)\n"
    "v_mfma_f32_16x16x32_bf16 a[16:19], v[20:23], a[176:179], a[16:19]\n"
    "ds_read_b64_tr_b16 v[32:33], v61 offset:18560\n"
    "v_mfma_f32_16x16x32_bf16 a[20:23], v[20:23], a[180:183], a[20:23]\n"
    "ds_read_b64_tr_b16 v[34:35], v61 offset:23168\n"
    "v_mfma_f32_16x16x32_bf16 a[24:27], v[20:23], a[184:187], a[24:27]\n"
    "buffer_store_dwordx4 v[168:171], v0, s[64:67], s87 offen offset:1024 sc1\n"
    "v_mfma_f32_16x16x32_bf16 a[28:31], v[20:23], a[188:191], a[28:31]\n"
    "s_waitcnt lgkmcnt(4)\n"
    "v_mfma_f32_16x16x32_bf16 a[32:35], v[24:27], a[176:179], a[32:35]\n"
    "ds_read_b64_tr_b16 v[36:37], v61 offset:18592\n"
    "v_mfma_f32_16x16x32_bf16 a[36:39], v[24:27], a[180:183], a[36:39]\n"
    "ds_read_b64_tr_b16 v[38:39], v61 offset:23200\n"
    "v_mfma_f32_16x16x32_bf16 a[40:43], v[24:27], a[184:187], a[40:43]\n"
    "buffer_load_dwordx4 v[64:67], v0, s[20:23], s92 offen offset:0 sc1\n"
    "v_mfma_f32_16x16x32_bf16 a[44:47], v[24:27], a[188:191], a[44:47]\n"
    "s_waitcnt lgkmcnt(4)\n"
    "v_mfma_f32_16x16x32_bf16 a[48:51], v[28:31], a[176:179], a[48:51]\n"
    "ds_read_b64_tr_b16 v[48:49], v61 offset:18624\n"
    "v_mfma_f32_16x16x32_bf16 a[52:55], v[28:31], a[180:183], a[52:55]\n"
    "s_mov_b32 s92, 32768\n"
    "ds_read_b64_tr_b16 v[50:51], v61 offset:23232\n"
    "v_mul_f32 v200, v200, v110\n"
    "v_mfma_f32_16x16x32_bf16 a[56:59], v[28:31], a[184:187], a[56:59]\n"
    "buffer_load_dwordx4 v[92:95], v0, s[20:23], s92 offen offset:3072 sc1\n"
    "v_mul_f32 v201, v201, v110\n"
    "v_mfma_f32_16x16x32_bf16 a[60:63], v[28:31], a[188:191], a[60:63]\n"
    "s_waitcnt lgkmcnt(4)\n"
    "v_mul_f32 v202, v202, v110\n"
    "v_mul_f32 v203, v203, v110\n"
    "v_mfma_f32_16x16x32_bf16 a[64:67], v[32:35], a[176:179], a[64:67]\n"
    "ds_read_b64_tr_b16 v[52:53], v61 offset:18656\n"
    "v_mul_f32 v204, v204, v110\n"
    "v_mfma_f32_16x16x32_bf16 a[68:71], v[32:35], a[180:183], a[68:71]\n"
    "ds_read_b64_tr_b16 v[54:55], v61 offset:23264\n"
    "v_mul_f32 v205, v205, v110\n"
    "v_mfma_f32_16x16x32_bf16 a[72:75], v[32:35], a[184:187], a[72:75]\n"
    "v_mul_f32 v206, v206, v110\n"
    "v_mul_f32 v207, v207, v110\n"
    "v_mfma_f32_16x16x32_bf16 a[76:79], v[32:35], a[188:191], a[76:79]\n"
    "s_waitcnt lgkmcnt(4)\n"
    "buffer_load_dwordx4 v[88:91], v0, s[20:23], s92 offen offset:2048 sc1\n"
    "v_cvt_pk_bf16_f32 v200, v200, v201\n"
    "v_mfma_f32_16x16x32_bf16 a[80:83], v[36:39], a[176:179], a[80:83]\n"
    "ds_read_b64_tr_b16 v[56:57], v61 offset:18688\n"
    "v_cvt_pk_bf16_f32 v201, v202, v203\n"
    "v_mfma_f32_16x16x32_bf16 a[84:87], v[36:39], a[180:183], a[84:87]\n"
    "ds_read_b64_tr_b16 v[58:59], v61 offset:23296\n"
    "v_cvt_pk_bf16_f32 v202, v204, v205\n"
    "v_mfma_f32_16x16x32_bf16 a[88:91], v[36:39], a[184:187], a[88:91]\n"
    "v_add_u32 v46, 1, v46\n"
    "v_cvt_pk_bf16_f32 v203, v206, v207\n"
    "v_mfma_f32_16x16x32_bf16 a[92:95], v[36:39], a[188:191], a[92:95]\n"
    "s_waitcnt vmcnt(12) lgkmcnt(4)\n"
    "buffer_store_dwordx2 v[46:47], v11, s[76:79], 0 offen sc1\n"
    "v_mfma_f32_16x16x32_bf16 a[96:99], v[48:51], a[176:179], a[96:99]\n"
    "ds_read_b64_tr_b16 v[16:17], v61 offset:27648\n"
    "v_mfma_f32_16x16x32_bf16 a[100:103], v[48:51], a[180:183], a[100:103]\n"
    "ds_read_b64_tr_b16 v[18:19], v61 offset:32256\n"
    "v_mfma_f32_16x16x32_bf16 a[104:107], v[48:51], a[184:187], a[104:107]\n"
    "ds_read_b64_tr_b16 v[20:21], v61 offset:36864\n"
    "v_mfma_f32_16x16x32_bf16 a[108:111], v[48:51], a[188:191], a[108:111]\n"
    "s_waitcnt lgkmcnt(5)\n"
    "ds_read_b64_tr_b16 v[22:23], v61 offset:41472\n"
    "v_mfma_f32_16x16x32_bf16 a[112:115], v[52:55], a[176:179], a[112:115]\n"
    "buffer_store_dwordx4 v[200:203], v0, s[64:67], s87 offen offset:2048 sc1\n"
    "v_mfma_f32_16x16x32_bf16 a[116:119], v[52:55], a[180:183], a[116:119]\n"
    "v_mul_f32 v232, v232, v111\n"
    "v_mul_f32 v233, v233, v111\n"
    "v_mfma_f32_16x16x32_bf16 a[120:123], v[52:55], a[184:187], a[120:123]\n"
    "v_mul_f32 v234, v234, v111\n"
    "v_mul_f32 v235, v235, v111\n"
    "v_mfma_f32_16x16x32_bf16 a[124:127], v[52:55], a[188:191], a[124:127]\n"
    "s_waitcnt lgkmcnt(4)\n"
    "v_mul_f32 v236, v236, v111\n"
    "v_mul_f32 v237, v237, v111\n"
    "v_mfma_f32_16x16x32_bf16 a[128:131], v[56:59], a[176:179], a[128:131]\n"
    "ds_read_b64_tr_b16 v[24:25], v61 offset:27680\n"
    "v_mul_f32 v238, v238, v111\n"
    "v_mfma_f32_16x16x32_bf16 a[132:135], v[56:59], a[180:183], a[132:135]\n"
    "ds_read_b64_tr_b16 v[26:27], v61 offset:32288\n"
    "v_mul_f32 v239, v239, v111\n"
    "v_mfma_f32_16x16x32_bf16 a[136:139], v[56:59], a[184:187], a[136:139]\n"
    "ds_read_b64_tr_b16 v[28:29], v61 offset:36896\n"
    "v_cvt_pk_bf16_f32 v232, v232, v233\n"
    "v_mfma_f32_16x16x32_bf16 a[140:143], v[56:59], a[188:191], a[140:143]\n"
    "s_waitcnt lgkmcnt(5)\n"
    "ds_read_b64_tr_b16 v[30:31], v61 offset:41504\n"
    "v_cvt_pk_bf16_f32 v233, v234, v235\n"
    "v_mfma_f32_16x16x32_bf16 a[0:3], v[16:19], a[192:195], a[0:3]\n"
    "s_waitcnt lgkmcnt(4)\n"
    "buffer_load_dwordx4 v[84:87], v0, s[20:23], s92 offen offset:1024 sc1\n"
    "v_cvt_pk_bf16_f32 v234, v236, v237\n"
    "v_mfma_f32_16x16x32_bf16 a[0:3], v[20:23], a[208:211], a[0:3]\n"
    "v_cvt_pk_bf16_f32 v235, v238, v239\n"
    "v_mfma_f32_16x16x32_bf16 a[4:7], v[16:19], a[196:199], a[4:7]\n"
    "v_mfma_f32_16x16x32_bf16 a[4:7], v[20:23], a[212:215], a[4:7]\n"
    "buffer_load_dwordx4 v[80:83], v0, s[20:23], s92 offen offset:0 sc1\n"
    "v_mfma_f32_16x16x32_bf16 a[8:11], v[16:19], a[200:203], a[8:11]\n"
    "ds_read_b64_tr_b16 v[32:33], v61 offset:27712\n"
    "v_mfma_f32_16x16x32_bf16 a[8:11], v[20:23], a[216:219], a[8:11]\n"
    "ds_read_b64_tr_b16 v[34:35], v61 offset:32320\n"
    "v_mfma_f32_16x16x32_bf16 a[12:15], v[16:19], a[204:207], a[12:15]\n"
    "ds_read_b64_tr_b16 v[36:37], v61 offset:36928\n"
    "v_mfma_f32_16x16x32_bf16 a[12:15], v[20:23], a[220:223], a[12:15]\n"
    "s_waitcnt lgkmcnt(5)\n"
    "ds_read_b64_tr_b16 v[38:39], v61 offset:41536\n"
    "v_mfma_f32_16x16x32_bf16 a[16:19], v[24:27], a[192:195], a[16:19]\n"
    "s_waitcnt lgkmcnt(4)\n"
    "buffer_store_dwordx4 v[232:235], v0, s[64:67], s87 offen offset:3072 sc1\n"
    "v_mfma_f32_16x16x32_bf16 a[16:19], v[28:31], a[208:211], a[16:19]\n"
    "v_mfma_f32_16x16x32_bf16 a[20:23], v[24:27], a[196:199], a[20:23]\n"
    "v_mfma_f32_16x16x32_bf16 a[20:23], v[28:31], a[212:215], a[20:23]\n"
    "v_mfma_f32_16x16x32_bf16 a[24:27], v[24:27], a[200:203], a[24:27]\n"
    "ds_read_b64_tr_b16 v[48:49], v61 offset:27744\n"
    "v_mfma_f32_16x16x32_bf16 a[24:27], v[28:31], a[216:219], a[24:27]\n"
    "ds_read_b64_tr_b16 v[50:51], v61 offset:32352\n"
    "v_mfma_f32_16x16x32_bf16 a[28:31], v[24:27], a[204:207], a[28:31]\n"
    "ds_read_b64_tr_b16 v[52:53], v61 offset:36960\n"
    "v_mfma_f32_16x16x32_bf16 a[28:31], v[28:31], a[220:223], a[28:31]\n"
    "s_waitcnt lgkmcnt(5)\n"
    "s_mov_b32 s92, 36864\n"
    "ds_read_b64_tr_b16 v[54:55], v61 offset:41568\n"
    "v_mfma_f32_16x16x32_bf16 a[32:35], v[32:35], a[192:195], a[32:35]\n"
    "s_waitcnt lgkmcnt(4)\n"
    "buffer_load_dwordx4 a[172:175], v0, s[20:23], s92 offen offset:3072 sc1\n"
    "v_mfma_f32_16x16x32_bf16 a[32:35], v[36:39], a[208:211], a[32:35]\n"
    "v_mul_f32 v144, v144, v108\n"
    "v_mul_f32 v145, v145, v108\n"
    "v_mfma_f32_16x16x32_bf16 a[36:39], v[32:35], a[196:199], a[36:39]\n"
    "buffer_load_dwordx4 a[168:171], v0, s[20:23], s92 offen offset:2048 sc1\n"
    "v_mul_f32 v146, v146, v108\n"
    "v_mfma_f32_16x16x32_bf16 a[36:39], v[36:39], a[212:215], a[36:39]\n"
    "v_mul_f32 v147, v147, v108\n"
    "v_mul_f32 v148, v148, v108\n"
    "v_mfma_f32_16x16x32_bf16 a[40:43], v[32:35], a[200:203], a[40:43]\n"
    "ds_read_b64_tr_b16 v[56:57], v61 offset:27776\n"
    "v_mul_f32 v149, v149, v108\n"
    "v_mfma_f32_16x16x32_bf16 a[40:43], v[36:39], a[216:219], a[40:43]\n"
    "ds_read_b64_tr_b16 v[58:59], v61 offset:32384\n"
    "v_mul_f32 v150, v150, v108\n"
    "v_mfma_f32_16x16x32_bf16 a[44:47], v[32:35], a[204:207], a[44:47]\n"
    "ds_read_b64_tr_b16 v[16:17], v61 offset:36992\n"
    "v_mul_f32 v151, v151, v108\n"
    "v_mfma_f32_16x16x32_bf16 a[44:47], v[36:39], a[220:223], a[44:47]\n"
    "s_waitcnt lgkmcnt(5)\n"
    "ds_read_b64_tr_b16 v[18:19], v61 offset:41600\n"
    "v_cvt_pk_bf16_f32 v144, v144, v145\n"
    "v_mfma_f32_16x16x32_bf16 a[48:51], v[48:51], a[192:195], a[48:51]\n"
    "s_waitcnt lgkmcnt(4)\n"
    "buffer_load_dwordx4 a[164:167], v0, s[20:23], s92 offen offset:1024 sc1\n"
    "v_cvt_pk_bf16_f32 v145, v146, v147\n"
    "v_mfma_f32_16x16x32_bf16 a[48:51], v[52:55], a[208:211], a[48:51]\n"
    "v_cvt_pk_bf16_f32 v146, v148, v149\n"
    "v_cvt_pk_bf16_f32 v147, v150, v151\n"
    "v_mfma_f32_16x16x32_bf16 a[52:55], v[48:51], a[196:199], a[52:55]\n"
    "v_mfma_f32_16x16x32_bf16 a[52:55], v[52:55], a[212:215], a[52:55]\n"
    "v_mfma_f32_16x16x32_bf16 a[56:59], v[48:51], a[200:203], a[56:59]\n"
    "ds_read_b64_tr_b16 v[20:21], v61 offset:27808\n"
    "v_mfma_f32_16x16x32_bf16 a[56:59], v[52:55], a[216:219], a[56:59]\n"
    "ds_read_b64_tr_b16 v[22:23], v61 offset:32416\n"
    "v_mfma_f32_16x16x32_bf16 a[60:63], v[48:51], a[204:207], a[60:63]\n"
    "ds_read_b64_tr_b16 v[24:25], v61 offset:37024\n"
    "v_mfma_f32_16x16x32_bf16 a[60:63], v[52:55], a[220:223], a[60:63]\n"
    "s_waitcnt lgkmcnt(5)\n"
    "s_mov_b32 s87, 8192\n"
    "ds_read_b64_tr_b16 v[26:27], v61 offset:41632\n"
    "v_mfma_f32_16x16x32_bf16 a[64:67], v[56:59], a[192:195], a[64:67]\n"
    "s_waitcnt lgkmcnt(4)\n"
    "buffer_store_dwordx4 v[144:147], v0, s[64:67], s87 offen offset:0 sc1\n"
    "v_mfma_f32_16x16x32_bf16 a[64:67], v[16:19], a[208:211], a[64:67]\n"
    "v_mul_f32 v176, v176, v109\n"
    "v_mul_f32 v177, v177, v109\n"
    "v_mfma_f32_16x16x32_bf16 a[68:71], v[56:59], a[196:199], a[68:71]\n"
    "buffer_load_dwordx4 a[160:163], v0, s[20:23], s92 offen offset:0 sc1\n"
    "v_mul_f32 v178, v178, v109\n"
    "v_mfma_f32_16x16x32_bf16 a[68:71], v[16:19], a[212:215], a[68:71]\n"
    "v_mul_f32 v179, v179, v109\n"
    "v_mul_f32 v180, v180, v109\n"
    "v_mfma_f32_16x16x32_bf16 a[72:75], v[56:59], a[200:203], a[72:75]\n"
    "ds_read_b64_tr_b16 v[28:29], v61 offset:27840\n"
    "v_mul_f32 v181, v181, v109\n"
    "v_mfma_f32_16x16x32_bf16 a[72:75], v[16:19], a[216:219], a[72:75]\n"
    "ds_read_b64_tr_b16 v[30:31], v61 offset:32448\n"
    "v_mul_f32 v182, v182, v109\n"
    "v_mfma_f32_16x16x32_bf16 a[76:79], v[56:59], a[204:207], a[76:79]\n"
    "ds_read_b64_tr_b16 v[32:33], v61 offset:37056\n"
    "v_mul_f32 v183, v183, v109\n"
    "v_mfma_f32_16x16x32_bf16 a[76:79], v[16:19], a[220:223], a[76:79]\n"
    "s_waitcnt lgkmcnt(5)\n"
    "s_mov_b32 s92, 40960\n"
    "ds_read_b64_tr_b16 v[34:35], v61 offset:41664\n"
    "v_cvt_pk_bf16_f32 v176, v176, v177\n"
    "v_mfma_f32_16x16x32_bf16 a[80:83], v[20:23], a[192:195], a[80:83]\n"
    "s_waitcnt lgkmcnt(4)\n"
    "buffer_load_dwordx4 a[188:191], v0, s[20:23], s92 offen offset:3072 sc1\n"
    "v_cvt_pk_bf16_f32 v177, v178, v179\n"
    "v_mfma_f32_16x16x32_bf16 a[80:83], v[24:27], a[208:211], a[80:83]\n"
    "v_cvt_pk_bf16_f32 v178, v180, v181\n"
    "v_cvt_pk_bf16_f32 v179, v182, v183\n"
    "v_mfma_f32_16x16x32_bf16 a[84:87], v[20:23], a[196:199], a[84:87]\n"
    "v_mfma_f32_16x16x32_bf16 a[84:87], v[24:27], a[212:215], a[84:87]\n"
    "v_mfma_f32_16x16x32_bf16 a[88:91], v[20:23], a[200:203], a[88:91]\n"
    "ds_read_b64_tr_b16 v[36:37], v61 offset:27872\n"
    "v_mfma_f32_16x16x32_bf16 a[88:91], v[24:27], a[216:219], a[88:91]\n"
    "ds_read_b64_tr_b16 v[38:39], v61 offset:32480\n"
    "v_mfma_f32_16x16x32_bf16 a[92:95], v[20:23], a[204:207], a[92:95]\n"
    "ds_read_b64_tr_b16 v[48:49], v61 offset:37088\n"
    "v_mfma_f32_16x16x32_bf16 a[92:95], v[24:27], a[220:223], a[92:95]\n"
    "s_waitcnt lgkmcnt(5)\n"
    "ds_read_b64_tr_b16 v[50:51], v61 offset:41696\n"
    "v_mfma_f32_16x16x32_bf16 a[96:99], v[28:31], a[192:195], a[96:99]\n"
    "s_waitcnt lgkmcnt(4)\n"
    "buffer_store_dwordx4 v[176:179], v0, s[64:67], s87 offen offset:1024 sc1\n"
    "v_mfma_f32_16x16x32_bf16 a[96:99], v[32:35], a[208:211], a[96:99]\n"
    "v_mfma_f32_16x16x32_bf16 a[100:103], v[28:31], a[196:199], a[100:103]\n"
    "buffer_load_dwordx4 a[184:187], v0, s[20:23], s92 offen offset:2048 sc1\n"
    "v_mfma_f32_16x16x32_bf16 a[100:103], v[32:35], a[212:215], a[100:103]\n"
    "v_mfma_f32_16x16x32_bf16 a[104:107], v[28:31], a[200:203], a[104:107]\n"
    "ds_read_b64_tr_b16 v[52:53], v61 offset:27904\n"
    "v_mfma_f32_16x16x32_bf16 a[104:107], v[32:35], a[216:219], a[104:107]\n"
    "ds_read_b64_tr_b16 v[54:55], v61 offset:32512\n"
    "v_mfma_f32_16x16x32_bf16 a[108:111], v[28:31], a[204:207], a[108:111]\n"
    "ds_read_b64_tr_b16 v[56:57], v61 offset:37120\n"
    "v_mfma_f32_16x16x32_bf16 a[108:111], v[32:35], a[220:223], a[108:111]\n"
    "s_waitcnt lgkmcnt(5)\n"
    "ds_read_b64_tr_b16 v[58:59], v61 offset:41728\n"
    "v_mfma_f32_16x16x32_bf16 a[112:115], v[36:39], a[192:195], a[112:115]\n"
    "s_waitcnt lgkmcnt(4)\n"
    "buffer_load_dwordx4 a[180:183], v0, s[20:23], s92 offen offset:1024 sc1\n"
    "v_mfma_f32_16x16x32_bf16 a[112:115], v[48:51], a[208:211], a[112:115]\n"
    "v_mul_f32 v208, v208, v110\n"
    "v_mul_f32 v209, v209, v110\n"
    "v_mfma_f32_16x16x32_bf16 a[116:119], v[36:39], a[196:199], a[116:119]\n"
    "v_mul_f32 v210, v210, v110\n"
    "v_mul_f32 v211, v211, v110\n"
    "v_mfma_f32_16x16x32_bf16 a[116:119], v[48:51], a[212:215], a[116:119]\n"
    "v_mul_f32 v212, v212, v110\n"
    "v_mul_f32 v213, v213, v110\n"
    "v_mfma_f32_16x16x32_bf16 a[120:123], v[36:39], a[200:203], a[120:123]\n"
    "buffer_load_dwordx4 a[176:179], v0, s[20:23], s92 offen offset:0 sc1\n"
    "v_mul_f32 v214, v214, v110\n"
    "v_mfma_f32_16x16x32_bf16 a[120:123], v[48:51], a[216:219], a[120:123]\n"
    "v_mul_f32 v215, v215, v110\n"
    "v_cvt_pk_bf16_f32 v208, v208, v209\n"
    "v_mfma_f32_16x16x32_bf16 a[124:127], v[36:39], a[204:207], a[124:127]\n"
    "s_barrier\n"
    "ds_read_b64_tr_b16 v[16:17], v61 offset:46080\n"
    "v_cvt_pk_bf16_f32 v209, v210, v211\n"
    "v_mfma_f32_16x16x32_bf16 a[124:127], v[48:51], a[220:223], a[124:127]\n"
    "s_waitcnt lgkmcnt(3)\n"
    "s_add_u32 s91, s80, 12\n"
    "ds_read_b64_tr_b16 v[18:19], v61 offset:50688\n"
    "v_cvt_pk_bf16_f32 v210, v212, v213\n"
    "v_mfma_f32_16x16x32_bf16 a[128:131], v[52:55], a[192:195], a[128:131]\n"
    "s_waitcnt lgkmcnt(2)\n"
    "s_mul_i32 s91, s91, 36864\n"
    "ds_read_b64_tr_b16 v[20:21], v61 offset:55296\n"
    "v_cvt_pk_bf16_f32 v211, v214, v215\n"
    "v_mfma_f32_16x16x32_bf16 a[128:131], v[56:59], a[208:211], a[128:131]\n"
    "s_add_u32 m0, s17, 16384\n"
    "ds_read_b64_tr_b16 v[22:23], v61 offset:59904\n"
    "v_mfma_f32_16x16x32_bf16 a[132:135], v[52:55], a[196:199], a[132:135]\n"
    "buffer_load_dwordx4 v2, s[40:43], s91 offen lds\n"
    "v_mfma_f32_16x16x32_bf16 a[132:135], v[56:59], a[212:215], a[132:135]\n"
    "v_mfma_f32_16x16x32_bf16 a[136:139], v[52:55], a[200:203], a[136:139]\n"
    "ds_read_b64_tr_b16 v[24:25], v61 offset:46112\n"
    "v_mfma_f32_16x16x32_bf16 a[136:139], v[56:59], a[216:219], a[136:139]\n"
    "ds_read_b64_tr_b16 v[26:27], v61 offset:50720\n"
    "v_mfma_f32_16x16x32_bf16 a[140:143], v[52:55], a[204:207], a[140:143]\n"
    "ds_read_b64_tr_b16 v[28:29], v61 offset:55328\n"
    "v_mfma_f32_16x16x32_bf16 a[140:143], v[56:59], a[220:223], a[140:143]\n"
    "s_waitcnt lgkmcnt(5)\n"
    "ds_read_b64_tr_b16 v[30:31], v61 offset:59936\n"
    "v_mfma_f32_16x16x32_bf16 a[0:3], v[16:19], a[224:227], a[0:3]\n"
    "s_waitcnt lgkmcnt(4)\n"
    "buffer_store_dwordx4 v[208:211], v0, s[64:67], s87 offen offset:2048 sc1\n"
    "v_mfma_f32_16x16x32_bf16 a[0:3], v[20:23], a[240:243], a[0:3]\n"
    "v_mul_f32 v240, v240, v111\n"
    "v_mul_f32 v241, v241, v111\n"
    "v_mfma_f32_16x16x32_bf16 a[4:7], v[16:19], a[228:231], a[4:7]\n"
    "s_mov_b32 s92, 45056\n"
    "v_mul_f32 v242, v242, v111\n"
    "v_mul_f32 v243, v243, v111\n"
    "v_mfma_f32_16x16x32_bf16 a[4:7], v[20:23], a[244:247], a[4:7]\n"
    "buffer_load_dwordx4 a[204:207], v0, s[20:23], s92 offen offset:3072 sc1\n"
    "v_mul_f32 v244, v244, v111\n"
    "v_mfma_f32_16x16x32_bf16 a[8:11], v[16:19], a[232:235], a[8:11]\n"
    "ds_read_b64_tr_b16 v[32:33], v61 offset:46144\n"
    "v_mul_f32 v245, v245, v111\n"
    "v_mfma_f32_16x16x32_bf16 a[8:11], v[20:23], a[248:251], a[8:11]\n"
    "ds_read_b64_tr_b16 v[34:35], v61 offset:50752\n"
    "v_mul_f32 v246, v246, v111\n"
    "v_mfma_f32_16x16x32_bf16 a[12:15], v[16:19], a[236:239], a[12:15]\n"
    "ds_read_b64_tr_b16 v[36:37], v61 offset:55360\n"
    "v_mul_f32 v247, v247, v111\n"
    "v_mfma_f32_16x16x32_bf16 a[12:15], v[20:23], a[252:255], a[12:15]\n"
    "s_waitcnt lgkmcnt(5)\n"
    "ds_read_b64_tr_b16 v[38:39], v61 offset:59968\n"
    "v_cvt_pk_bf16_f32 v240, v240, v241\n"
    "v_mfma_f32_16x16x32_bf16 a[16:19], v[24:27], a[224:227], a[16:19]\n"
    "s_waitcnt lgkmcnt(4)\n"
    "buffer_load_dwordx4 a[200:203], v0, s[20:23], s92 offen offset:2048 sc1\n"
    "v_cvt_pk_bf16_f32 v241, v242, v243\n"
    "v_mfma_f32_16x16x32_bf16 a[16:19], v[28:31], a[240:243], a[16:19]\n"
    "v_cvt_pk_bf16_f32 v242, v244, v245\n"
    "v_cvt_pk_bf16_f32 v243, v246, v247\n"
    "v_mfma_f32_16x16x32_bf16 a[20:23], v[24:27], a[228:231], a[20:23]\n"
    "v_mfma_f32_16x16x32_bf16 a[20:23], v[28:31], a[244:247], a[20:23]\n"
    "v_mfma_f32_16x16x32_bf16 a[24:27], v[24:27], a[232:235], a[24:27]\n"
    "ds_read_b64_tr_b16 v[48:49], v61 offset:46176\n"
    "v_mfma_f32_16x16x32_bf16 a[24:27], v[28:31], a[248:251], a[24:27]\n"
    "ds_read_b64_tr_b16 v[50:51], v61 offset:50784\n"
    "v_mfma_f32_16x16x32_bf16 a[28:31], v[24:27], a[236:239], a[28:31]\n"
    "ds_read_b64_tr_b16 v[52:53], v61 offset:55392\n"
    "v_mfma_f32_16x16x32_bf16 a[28:31], v[28:31], a[252:255], a[28:31]\n"
    "s_waitcnt lgkmcnt(5)\n"
    "ds_read_b64_tr_b16 v[54:55], v61 offset:60000\n"
    "v_mfma_f32_16x16x32_bf16 a[32:35], v[32:35], a[224:227], a[32:35]\n"
    "s_waitcnt lgkmcnt(4)\n"
    "buffer_store_dwordx4 v[240:243], v0, s[64:67], s87 offen offset:3072 sc1\n"
    "v_mfma_f32_16x16x32_bf16 a[32:35], v[36:39], a[240:243], a[32:35]\n"
    "v_mfma_f32_16x16x32_bf16 a[36:39], v[32:35], a[228:231], a[36:39]\n"
    "buffer_load_dwordx4 a[196:199], v0, s[20:23], s92 offen offset:1024 sc1\n"
    "v_mfma_f32_16x16x32_bf16 a[36:39], v[36:39], a[244:247], a[36:39]\n"
    "v_mfma_f32_16x16x32_bf16 a[40:43], v[32:35], a[232:235], a[40:43]\n"
    "ds_read_b64_tr_b16 v[56:57], v61 offset:46208\n"
    "v_mfma_f32_16x16x32_bf16 a[40:43], v[36:39], a[248:251], a[40:43]\n"
    "ds_read_b64_tr_b16 v[58:59], v61 offset:50816\n"
    "v_mfma_f32_16x16x32_bf16 a[44:47], v[32:35], a[236:239], a[44:47]\n"
    "ds_read_b64_tr_b16 v[16:17], v61 offset:55424\n"
    "v_mfma_f32_16x16x32_bf16 a[44:47], v[36:39], a[252:255], a[44:47]\n"
    "s_waitcnt lgkmcnt(5)\n"
    "ds_read_b64_tr_b16 v[18:19], v61 offset:60032\n"
    "v_mfma_f32_16x16x32_bf16 a[48:51], v[48:51], a[224:227], a[48:51]\n"
    "s_waitcnt lgkmcnt(4)\n"
    "buffer_load_dwordx4 a[192:195], v0, s[20:23], s92 offen offset:0 sc1\n"
    "v_mfma_f32_16x16x32_bf16 a[48:51], v[52:55], a[240:243], a[48:51]\n"
    "v_mul_f32 v152, v152, v108\n"
    "v_mul_f32 v153, v153, v108\n"
    "v_mfma_f32_16x16x32_bf16 a[52:55], v[48:51], a[228:231], a[52:55]\n"
    "v_mul_f32 v154, v154, v108\n"
    "v_mul_f32 v155, v155, v108\n"
    "v_mfma_f32_16x16x32_bf16 a[52:55], v[52:55], a[244:247], a[52:55]\n"
    "v_mul_f32 v156, v156, v108\n"
    "v_mul_f32 v157, v157, v108\n"
    "v_mfma_f32_16x16x32_bf16 a[56:59], v[48:51], a[232:235], a[56:59]\n"
    "ds_read_b64_tr_b16 v[20:21], v61 offset:46240\n"
    "v_mul_f32 v158, v158, v108\n"
    "v_mfma_f32_16x16x32_bf16 a[56:59], v[52:55], a[248:251], a[56:59]\n"
    "ds_read_b64_tr_b16 v[22:23], v61 offset:50848\n"
    "v_mul_f32 v159, v159, v108\n"
    "v_mfma_f32_16x16x32_bf16 a[60:63], v[48:51], a[236:239], a[60:63]\n"
    "ds_read_b64_tr_b16 v[24:25], v61 offset:55456\n"
    "v_cvt_pk_bf16_f32 v152, v152, v153\n"
    "v_mfma_f32_16x16x32_bf16 a[60:63], v[52:55], a[252:255], a[60:63]\n"
    "s_waitcnt lgkmcnt(5)\n"
    "ds_read_b64_tr_b16 v[26:27], v61 offset:60064\n"
    "v_cvt_pk_bf16_f32 v153, v154, v155\n"
    "v_mfma_f32_16x16x32_bf16 a[64:67], v[56:59], a[224:227], a[64:67]\n"
    "s_waitcnt lgkmcnt(4)\n"
    "buffer_load_dwordx4 v3, s[40:43], s91 offen offset:1024 lds\n"
    "v_cvt_pk_bf16_f32 v154, v156, v157\n"
    "v_mfma_f32_16x16x32_bf16 a[64:67], v[16:19], a[240:243], a[64:67]\n"
    "v_cvt_pk_bf16_f32 v155, v158, v159\n"
    "v_mfma_f32_16x16x32_bf16 a[68:71], v[56:59], a[228:231], a[68:71]\n"
    "buffer_load_dwordx4 v4, s[40:43], s91 offen offset:2048 lds\n"
    "v_mfma_f32_16x16x32_bf16 a[68:71], v[16:19], a[244:247], a[68:71]\n"
    "v_mfma_f32_16x16x32_bf16 a[72:75], v[56:59], a[232:235], a[72:75]\n"
    "ds_read_b64_tr_b16 v[28:29], v61 offset:46272\n"
    "v_mfma_f32_16x16x32_bf16 a[72:75], v[16:19], a[248:251], a[72:75]\n"
    "ds_read_b64_tr_b16 v[30:31], v61 offset:50880\n"
    "v_mfma_f32_16x16x32_bf16 a[76:79], v[56:59], a[236:239], a[76:79]\n"
    "ds_read_b64_tr_b16 v[32:33], v61 offset:55488\n"
    "v_mfma_f32_16x16x32_bf16 a[76:79], v[16:19], a[252:255], a[76:79]\n"
    "s_waitcnt lgkmcnt(5)\n"
    "s_mov_b32 s87, 12288\n"
    "ds_read_b64_tr_b16 v[34:35], v61 offset:60096\n"
    "v_mfma_f32_16x16x32_bf16 a[80:83], v[20:23], a[224:227], a[80:83]\n"
    "s_waitcnt lgkmcnt(4)\n"
    "buffer_store_dwordx4 v[152:155], v0, s[64:67], s87 offen offset:0 sc1\n"
    "v_mfma_f32_16x16x32_bf16 a[80:83], v[24:27], a[240:243], a[80:83]\n"
    "v_mul_f32 v184, v184, v109\n"
    "v_mul_f32 v185, v185, v109\n"
    "v_mfma_f32_16x16x32_bf16 a[84:87], v[20:23], a[228:231], a[84:87]\n"
    "v_mul_f32 v186, v186, v109\n"
    "v_mul_f32 v187, v187, v109\n"
    "v_mfma_f32_16x16x32_bf16 a[84:87], v[24:27], a[244:247], a[84:87]\n"
    "v_mul_f32 v188, v188, v109\n"
    "v_mul_f32 v189, v189, v109\n"
    "v_mfma_f32_16x16x32_bf16 a[88:91], v[20:23], a[232:235], a[88:91]\n"
    "ds_read_b64_tr_b16 v[36:37], v61 offset:46304\n"
    "v_mul_f32 v190, v190, v109\n"
    "v_mfma_f32_16x16x32_bf16 a[88:91], v[24:27], a[248:251], a[88:91]\n"
    "ds_read_b64_tr_b16 v[38:39], v61 offset:50912\n"
    "v_mul_f32 v191, v191, v109\n"
    "v_mfma_f32_16x16x32_bf16 a[92:95], v[20:23], a[236:239], a[92:95]\n"
    "ds_read_b64_tr_b16 v[48:49], v61 offset:55520\n"
    "v_cvt_pk_bf16_f32 v184, v184, v185\n"
    "v_mfma_f32_16x16x32_bf16 a[92:95], v[24:27], a[252:255], a[92:95]\n"
    "s_waitcnt lgkmcnt(5)\n"
    "ds_read_b64_tr_b16 v[50:51], v61 offset:60128\n"
    "v_cvt_pk_bf16_f32 v185, v186, v187\n"
    "v_mfma_f32_16x16x32_bf16 a[96:99], v[28:31], a[224:227], a[96:99]\n"
    "s_waitcnt lgkmcnt(4)\n"
    "buffer_load_dwordx4 v5, s[40:43], s91 offen offset:3072 lds\n"
    "v_cvt_pk_bf16_f32 v186, v188, v189\n"
    "v_mfma_f32_16x16x32_bf16 a[96:99], v[32:35], a[240:243], a[96:99]\n"
    "s_add_u32 m0, s17, 20480\n"
    "v_cvt_pk_bf16_f32 v187, v190, v191\n"
    "v_mfma_f32_16x16x32_bf16 a[100:103], v[28:31], a[228:231], a[100:103]\n"
    "buffer_load_dwordx4 v6, s[40:43], s91 offen lds\n"
    "v_mfma_f32_16x16x32_bf16 a[100:103], v[32:35], a[244:247], a[100:103]\n"
    "v_mfma_f32_16x16x32_bf16 a[104:107], v[28:31], a[232:235], a[104:107]\n"
    "ds_read_b64_tr_b16 v[52:53], v61 offset:46336\n"
    "v_mfma_f32_16x16x32_bf16 a[104:107], v[32:35], a[248:251], a[104:107]\n"
    "ds_read_b64_tr_b16 v[54:55], v61 offset:50944\n"
    "v_mfma_f32_16x16x32_bf16 a[108:111], v[28:31], a[236:239], a[108:111]\n"
    "ds_read_b64_tr_b16 v[56:57], v61 offset:55552\n"
    "v_mfma_f32_16x16x32_bf16 a[108:111], v[32:35], a[252:255], a[108:111]\n"
    "s_waitcnt lgkmcnt(5)\n"
    "ds_read_b64_tr_b16 v[58:59], v61 offset:60160\n"
    "v_add_u32 v46, 1, v46\n"
    "v_mfma_f32_16x16x32_bf16 a[112:115], v[36:39], a[224:227], a[112:115]\n"
    "s_waitcnt lgkmcnt(4)\n"
    "buffer_store_dwordx4 v[184:187], v0, s[64:67], s87 offen offset:1024 sc1\n"
    "v_mfma_f32_16x16x32_bf16 a[112:115], v[48:51], a[240:243], a[112:115]\n"
    "s_waitcnt vmcnt(23)\n"
    "buffer_store_dwordx2 v[46:47], v11, s[76:79], 0 offen sc1\n"
    "v_mfma_f32_16x16x32_bf16 a[116:119], v[36:39], a[228:231], a[116:119]\n"
    "v_mfma_f32_16x16x32_bf16 a[116:119], v[48:51], a[244:247], a[116:119]\n"
    "v_mfma_f32_16x16x32_bf16 a[120:123], v[36:39], a[232:235], a[120:123]\n"
    "ds_read_b64_tr_b16 v[16:17], v62 offset:0\n"
    "v_mfma_f32_16x16x32_bf16 a[120:123], v[48:51], a[248:251], a[120:123]\n"
    "ds_read_b64_tr_b16 v[18:19], v62 offset:4608\n"
    "v_mfma_f32_16x16x32_bf16 a[124:127], v[36:39], a[236:239], a[124:127]\n"
    "ds_read_b64_tr_b16 v[20:21], v62 offset:9216\n"
    "v_mfma_f32_16x16x32_bf16 a[124:127], v[48:51], a[252:255], a[124:127]\n"
    "s_waitcnt lgkmcnt(5)\n"
    "ds_read_b64_tr_b16 v[22:23], v62 offset:13824\n"
    "v_mfma_f32_16x16x32_bf16 a[128:131], v[52:55], a[224:227], a[128:131]\n"
    "s_waitcnt lgkmcnt(4)\n"
    "v_mfma_f32_16x16x32_bf16 a[128:131], v[56:59], a[240:243], a[128:131]\n"
    "buffer_load_dwordx4 v7, s[40:43], s91 offen offset:1024 lds\n"
    "v_mul_f32 v216, v216, v110\n"
    "v_mfma_f32_16x16x32_bf16 a[132:135], v[52:55], a[228:231], a[132:135]\n"
    "v_mul_f32 v217, v217, v110\n"
    "v_mul_f32 v218, v218, v110\n"
    "v_mfma_f32_16x16x32_bf16 a[132:135], v[56:59], a[244:247], a[132:135]\n"
    "v_mul_f32 v219, v219, v110\n"
    "v_mul_f32 v220, v220, v110\n"
    "v_mfma_f32_16x16x32_bf16 a[136:139], v[52:55], a[232:235], a[136:139]\n"
    "ds_read_b64_tr_b16 v[24:25], v62 offset:32\n"
    "v_mul_f32 v221, v221, v110\n"
    "v_mfma_f32_16x16x32_bf16 a[136:139], v[56:59], a[248:251], a[136:139]\n"
    "ds_read_b64_tr_b16 v[26:27], v62 offset:4640\n"
    "v_mul_f32 v222, v222, v110\n"
    "v_mfma_f32_16x16x32_bf16 a[140:143], v[52:55], a[236:239], a[140:143]\n"
    "ds_read_b64_tr_b16 v[28:29], v62 offset:9248\n"
    "v_mul_f32 v223, v223, v110\n"
    "v_mfma_f32_16x16x32_bf16 a[140:143], v[56:59], a[252:255], a[140:143]\n"
    "s_waitcnt lgkmcnt(5)\n"
    "ds_read_b64_tr_b16 v[30:31], v62 offset:13856\n"
    "v_cvt_pk_bf16_f32 v216, v216, v217\n"
    "v_mfma_f32_16x16x32_bf16 a[0:3], v[16:19], a[144:147], a[0:3]\n"
    "s_waitcnt lgkmcnt(4)\n"
    "buffer_load_dwordx4 v8, s[40:43], s91 offen offset:2048 lds\n"
    "v_cvt_pk_bf16_f32 v217, v218, v219\n"
    "v_mfma_f32_16x16x32_bf16 a[0:3], v[20:23], v[64:67], a[0:3]\n"
    "v_cvt_pk_bf16_f32 v218, v220, v221\n"
    "v_cvt_pk_bf16_f32 v219, v222, v223\n"
    "v_mfma_f32_16x16x32_bf16 a[4:7], v[16:19], a[148:151], a[4:7]\n"
    "v_mfma_f32_16x16x32_bf16 a[4:7], v[20:23], v[68:71], a[4:7]\n"
    "buffer_load_dwordx4 v9, s[40:43], s91 offen offset:3072 lds\n"
    "v_mfma_f32_16x16x32_bf16 a[8:11], v[16:19], a[152:155], a[8:11]\n"
    "ds_read_b64_tr_b16 v[32:33], v62 offset:64\n"
    "v_mfma_f32_16x16x32_bf16 a[8:11], v[20:23], v[72:75], a[8:11]\n"
    "ds_read_b64_tr_b16 v[34:35], v62 offset:4672\n"
    "v_mfma_f32_16x16x32_bf16 a[12:15], v[16:19], a[156:159], a[12:15]\n"
    "ds_read_b64_tr_b16 v[36:37], v62 offset:9280\n"
    "v_mfma_f32_16x16x32_bf16 a[12:15], v[20:23], v[76:79], a[12:15]\n"
    "s_waitcnt lgkmcnt(5)\n"
    "ds_read_b64_tr_b16 v[38:39], v62 offset:13888\n"
    "v_mfma_f32_16x16x32_bf16 a[16:19], v[24:27], a[144:147], a[16:19]\n"
    "s_waitcnt lgkmcnt(4)\n"
    "buffer_store_dwordx4 v[216:219], v0, s[64:67], s87 offen offset:2048 sc1\n"
    "v_mfma_f32_16x16x32_bf16 a[16:19], v[28:31], v[64:67], a[16:19]\n"
    "v_mul_f32 v248, v248, v111\n"
    "v_mul_f32 v249, v249, v111\n"
    "v_mfma_f32_16x16x32_bf16 a[20:23], v[24:27], a[148:151], a[20:23]\n"
    "v_mul_f32 v250, v250, v111\n"
    "v_mul_f32 v251, v251, v111\n"
    "v_mfma_f32_16x16x32_bf16 a[20:23], v[28:31], v[68:71], a[20:23]\n"
    "v_mul_f32 v252, v252, v111\n"
    "v_mul_f32 v253, v253, v111\n"
    "v_mfma_f32_16x16x32_bf16 a[24:27], v[24:27], a[152:155], a[24:27]\n"
    "ds_read_b64_tr_b16 v[48:49], v62 offset:96\n"
    "v_mul_f32 v254, v254, v111\n"
    "v_mfma_f32_16x16x32_bf16 a[24:27], v[28:31], v[72:75], a[24:27]\n"
    "ds_read_b64_tr_b16 v[50:51], v62 offset:4704\n"
    "v_mul_f32 v255, v255, v111\n"
    "v_mfma_f32_16x16x32_bf16 a[28:31], v[24:27], a[156:159], a[28:31]\n"
    "ds_read_b64_tr_b16 v[52:53], v62 offset:9312\n"
    "v_cvt_pk_bf16_f32 v248, v248, v249\n"
    "v_mfma_f32_16x16x32_bf16 a[28:31], v[28:31], v[76:79], a[28:31]\n"
    "s_waitcnt lgkmcnt(5)\n"
    "s_add_u32 m0, s17, 24576\n"
    "ds_read_b64_tr_b16 v[54:55], v62 offset:13920\n"
    "v_cvt_pk_bf16_f32 v249, v250, v251\n"
    "v_mfma_f32_16x16x32_bf16 a[32:35], v[32:35], a[144:147], a[32:35]\n"
    "s_waitcnt lgkmcnt(4)\n"
    "buffer_load_dwordx4 v10, s[40:43], s91 offen lds\n"
    "v_cvt_pk_bf16_f32 v250, v252, v253\n"
    "v_mfma_f32_16x16x32_bf16 a[32:35], v[36:39], v[64:67], a[32:35]\n"
    "s_mov_b32 s92, 49152\n"
    "v_cvt_pk_bf16_f32 v251, v254, v255\n"
    "v_mfma_f32_16x16x32_bf16 a[36:39], v[32:35], a[148:151], a[36:39]\n"
    "buffer_load_dwordx4 a[220:223], v0, s[20:23], s92 offen offset:3072 sc1\n"
    "v_mfma_f32_16x16x32_bf16 a[36:39], v[36:39], v[68:71], a[36:39]\n"
    "v_mfma_f32_16x16x32_bf16 a[40:43], v[32:35], a[152:155], a[40:43]\n"
    "ds_read_b64_tr_b16 v[56:57], v62 offset:128\n"
    "v_mfma_f32_16x16x32_bf16 a[40:43], v[36:39], v[72:75], a[40:43]\n"
    "ds_read_b64_tr_b16 v[58:59], v62 offset:4736\n"
    "v_mfma_f32_16x16x32_bf16 a[44:47], v[32:35], a[156:159], a[44:47]\n"
    "ds_read_b64_tr_b16 v[16:17], v62 offset:9344\n"
    "v_mfma_f32_16x16x32_bf16 a[44:47], v[36:39], v[76:79], a[44:47]\n"
    "s_waitcnt lgkmcnt(5)\n"
    "ds_read_b64_tr_b16 v[18:19], v62 offset:13952\n"
    "v_mfma_f32_16x16x32_bf16 a[48:51], v[48:51], a[144:147], a[48:51]\n"
    "s_waitcnt lgkmcnt(4)\n"
    "buffer_store_dwordx4 v[248:251], v0, s[64:67], s87 offen offset:3072 sc1\n"
    "v_mfma_f32_16x16x32_bf16 a[48:51], v[52:55], v[64:67], a[48:51]\n"
    "v_mfma_f32_16x16x32_bf16 a[52:55], v[48:51], a[148:151], a[52:55]\n"
    "v_mfma_f32_16x16x32_bf16 a[52:55], v[52:55], v[68:71], a[52:55]\n"
    "v_mfma_f32_16x16x32_bf16 a[56:59], v[48:51], a[152:155], a[56:59]\n"
    "ds_read_b64_tr_b16 v[20:21], v62 offset:160\n"
    "v_mfma_f32_16x16x32_bf16 a[56:59], v[52:55], v[72:75], a[56:59]\n"
    "ds_read_b64_tr_b16 v[22:23], v62 offset:4768\n"
    "v_mfma_f32_16x16x32_bf16 a[60:63], v[48:51], a[156:159], a[60:63]\n"
    "ds_read_b64_tr_b16 v[24:25], v62 offset:9376\n"
    "v_mfma_f32_16x16x32_bf16 a[60:63], v[52:55], v[76:79], a[60:63]\n"
    "s_waitcnt lgkmcnt(5)\n"
    "ds_read_b64_tr_b16 v[26:27], v62 offset:13984\n"
    "v_mfma_f32_16x16x32_bf16 a[64:67], v[56:59], a[144:147], a[64:67]\n"
    "s_waitcnt lgkmcnt(4)\n"
    "buffer_load_dwordx4 a[216:219], v0, s[20:23], s92 offen offset:2048 sc1\n"
    "v_mfma_f32_16x16x32_bf16 a[64:67], v[16:19], v[64:67], a[64:67]\n"
    "v_mfma_f32_16x16x32_bf16 a[68:71], v[56:59], a[148:151], a[68:71]\n"
    "buffer_load_dwordx4 a[212:215], v0, s[20:23], s92 offen offset:1024 sc1\n"
    "v_mfma_f32_16x16x32_bf16 a[68:71], v[16:19], v[68:71], a[68:71]\n"
    "v_mfma_f32_16x16x32_bf16 a[72:75], v[56:59], a[152:155], a[72:75]\n"
    "ds_read_b64_tr_b16 v[28:29], v62 offset:192\n"
    "v_mfma_f32_16x16x32_bf16 a[72:75], v[16:19], v[72:75], a[72:75]\n"
    "ds_read_b64_tr_b16 v[30:31], v62 offset:4800\n"
    "v_mfma_f32_16x16x32_bf16 a[76:79], v[56:59], a[156:159], a[76:79]\n"
    "ds_read_b64_tr_b16 v[32:33], v62 offset:9408\n"
    "v_mfma_f32_16x16x32_bf16 a[76:79], v[16:19], v[76:79], a[76:79]\n"
    "s_waitcnt lgkmcnt(5)\n"
    "ds_read_b64_tr_b16 v[34:35], v62 offset:14016\n"
    "v_mfma_f32_16x16x32_bf16 a[80:83], v[20:23], a[144:147], a[80:83]\n"
    "s_waitcnt lgkmcnt(4)\n"
    "buffer_load_dwordx4 a[208:211], v0, s[20:23], s92 offen offset:0 sc1\n"
    "v_mfma_f32_16x16x32_bf16 a[80:83], v[24:27], v[64:67], a[80:83]\n"
    "v_mfma_f32_16x16x32_bf16 a[84:87], v[20:23], a[148:151], a[84:87]\n"
    "v_mfma_f32_16x16x32_bf16 a[84:87], v[24:27], v[68:71], a[84:87]\n"
    "v_mfma_f32_16x16x32_bf16 a[88:91], v[20:23], a[152:155], a[88:91]\n"
    "ds_read_b64_tr_b16 v[36:37], v62 offset:224\n"
    "v_mfma_f32_16x16x32_bf16 a[88:91], v[24:27], v[72:75], a[88:91]\n"
    "ds_read_b64_tr_b16 v[38:39], v62 offset:4832\n"
    "v_mfma_f32_16x16x32_bf16 a[92:95], v[20:23], a[156:159], a[92:95]\n"
    "ds_read_b64_tr_b16 v[48:49], v62 offset:9440\n"
    "v_mfma_f32_16x16x32_bf16 a[92:95], v[24:27], v[76:79], a[92:95]\n"
    "s_waitcnt lgkmcnt(5)\n"
    "s_mov_b32 s92, 53248\n"
    "ds_read_b64_tr_b16 v[50:51], v62 offset:14048\n"
    "v_mfma_f32_16x16x32_bf16 a[96:99], v[28:31], a[144:147], a[96:99]\n"
    "s_waitcnt lgkmcnt(4)\n"
    "buffer_load_dwordx2 a[236:237], v13, s[20:23], s92 offen offset:3072 sc1\n"
    "v_mfma_f32_16x16x32_bf16 a[96:99], v[32:35], v[64:67], a[96:99]\n"
    "buffer_load_dwordx2 a[238:239], v13, s[20:23], s92 offen offset:3584 sc1\n"
    "v_mfma_f32_16x16x32_bf16 a[100:103], v[28:31], a[148:151], a[100:103]\n"
    "buffer_load_dwordx2 a[232:233], v13, s[20:23], s92 offen offset:2048 sc1\n"
    "v_mfma_f32_16x16x32_bf16 a[100:103], v[32:35], v[68:71], a[100:103]\n"
    "v_mfma_f32_16x16x32_bf16 a[104:107], v[28:31], a[152:155], a[104:107]\n"
    "ds_read_b64_tr_b16 v[52:53], v62 offset:256\n"
    "v_mfma_f32_16x16x32_bf16 a[104:107], v[32:35], v[72:75], a[104:107]\n"
    "ds_read_b64_tr_b16 v[54:55], v62 offset:4864\n"
    "v_mfma_f32_16x16x32_bf16 a[108:111], v[28:31], a[156:159], a[108:111]\n"
    "ds_read_b64_tr_b16 v[56:57], v62 offset:9472\n"
    "v_mfma_f32_16x16x32_bf16 a[108:111], v[32:35], v[76:79], a[108:111]\n"
    "s_waitcnt lgkmcnt(5)\n"
    "ds_read_b64_tr_b16 v[58:59], v62 offset:14080\n"
    "v_mfma_f32_16x16x32_bf16 a[112:115], v[36:39], a[144:147], a[112:115]\n"
    "s_waitcnt lgkmcnt(4)\n"
    "buffer_load_dwordx2 a[234:235], v13, s[20:23], s92 offen offset:2560 sc1\n"
    "v_mfma_f32_16x16x32_bf16 a[112:115], v[48:51], v[64:67], a[112:115]\n"
    "buffer_load_dwordx2 a[228:229], v13, s[20:23], s92 offen offset:1024 sc1\n"
    "v_mfma_f32_16x16x32_bf16 a[116:119], v[36:39], a[148:151], a[116:119]\n"
    "buffer_load_dwordx2 a[230:231], v13, s[20:23], s92 offen offset:1536 sc1\n"
    "v_mfma_f32_16x16x32_bf16 a[116:119], v[48:51], v[68:71], a[116:119]\n"
    "v_mfma_f32_16x16x32_bf16 a[120:123], v[36:39], a[152:155], a[120:123]\n"
    "buffer_load_dwordx2 a[224:225], v13, s[20:23], s92 offen offset:0 sc1\n"
    "v_mfma_f32_16x16x32_bf16 a[120:123], v[48:51], v[72:75], a[120:123]\n"
    "v_mfma_f32_16x16x32_bf16 a[124:127], v[36:39], a[156:159], a[124:127]\n"
    "s_barrier\n"
    "ds_read_b64_tr_b16 v[16:17], v60 offset:36864\n"
    "v_mfma_f32_16x16x32_bf16 a[124:127], v[48:51], v[76:79], a[124:127]\n"
    "s_waitcnt lgkmcnt(3)\n"
    "ds_read_b64_tr_b16 v[18:19], v60 offset:41472\n"
    "v_mfma_f32_16x16x32_bf16 a[128:131], v[52:55], a[144:147], a[128:131]\n"
    "s_waitcnt lgkmcnt(2)\n"
    "s_add_u32 s91, s80, 16\n"
    "ds_read_b64_tr_b16 v[20:21], v60 offset:46080\n"
    "v_mfma_f32_16x16x32_bf16 a[128:131], v[56:59], v[64:67], a[128:131]\n"
    "s_mul_i32 s91, s91, 36864\n"
    "ds_read_b64_tr_b16 v[22:23], v60 offset:50688\n"
    "v_mfma_f32_16x16x32_bf16 a[132:135], v[52:55], a[148:151], a[132:135]\n"
    "s_add_u32 m0, s17, 90112\n"
    "buffer_load_dwordx2 a[226:227], v13, s[20:23], s92 offen offset:512 sc1\n"
    "v_mfma_f32_16x16x32_bf16 a[132:135], v[56:59], v[68:71], a[132:135]\n"
    "buffer_load_dwordx4 v2, s[40:43], s91 offen lds\n"
    "v_mfma_f32_16x16x32_bf16 a[136:139], v[52:55], a[152:155], a[136:139]\n"
    "ds_read_b64_tr_b16 v[24:25], v60 offset:36896\n"
    "v_mfma_f32_16x16x32_bf16 a[136:139], v[56:59], v[72:75], a[136:139]\n"
    "ds_read_b64_tr_b16 v[26:27], v60 offset:41504\n"
    "v_mfma_f32_16x16x32_bf16 a[140:143], v[52:55], a[156:159], a[140:143]\n"
    "ds_read_b64_tr_b16 v[28:29], v60 offset:46112\n"
    "v_mfma_f32_16x16x32_bf16 a[140:143], v[56:59], v[76:79], a[140:143]\n"
    "s_waitcnt lgkmcnt(5)\n"
    "s_mov_b32 s92, 57344\n"
    "ds_read_b64_tr_b16 v[30:31], v60 offset:50720\n"
    "v_mfma_f32_16x16x32_bf16 a[0:3], v[16:19], v[80:83], a[0:3]\n"
    "s_waitcnt vmcnt(38) lgkmcnt(4)\n"
    "buffer_load_dwordx4 a[252:255], v0, s[20:23], s92 offen offset:3072 sc1\n"
    "v_mfma_f32_16x16x32_bf16 a[0:3], v[20:23], a[160:163], a[0:3]\n"
    "v_mfma_f32_16x16x32_bf16 a[4:7], v[16:19], v[84:87], a[4:7]\n"
    "v_mfma_f32_16x16x32_bf16 a[4:7], v[20:23], a[164:167], a[4:7]\n"
    "buffer_load_dwordx4 a[248:251], v0, s[20:23], s92 offen offset:2048 sc1\n"
    "v_mfma_f32_16x16x32_bf16 a[8:11], v[16:19], v[88:91], a[8:11]\n"
    "ds_read_b64_tr_b16 v[32:33], v60 offset:36928\n"
    "v_mfma_f32_16x16x32_bf16 a[8:11], v[20:23], a[168:171], a[8:11]\n"
    "ds_read_b64_tr_b16 v[34:35], v60 offset:41536\n"
    "v_mfma_f32_16x16x32_bf16 a[12:15], v[16:19], v[92:95], a[12:15]\n"
    "ds_read_b64_tr_b16 v[36:37], v60 offset:46144\n"
    "v_mfma_f32_16x16x32_bf16 a[12:15], v[20:23], a[172:175], a[12:15]\n"
    "s_waitcnt lgkmcnt(5)\n"
    "ds_read_b64_tr_b16 v[38:39], v60 offset:50752\n"
    "v_mfma_f32_16x16x32_bf16 a[16:19], v[24:27], v[80:83], a[16:19]\n"
    "s_waitcnt lgkmcnt(4)\n"
    "buffer_load_dwordx4 a[244:247], v0, s[20:23], s92 offen offset:1024 sc1\n"
    "v_mfma_f32_16x16x32_bf16 a[16:19], v[28:31], a[160:163], a[16:19]\n"
    "v_mfma_f32_16x16x32_bf16 a[20:23], v[24:27], v[84:87], a[20:23]\n"
    "v_mfma_f32_16x16x32_bf16 a[20:23], v[28:31], a[164:167], a[20:23]\n"
    "v_mfma_f32_16x16x32_bf16 a[24:27], v[24:27], v[88:91], a[24:27]\n"
    "ds_read_b64_tr_b16 v[48:49], v60 offset:36960\n"
    "v_mfma_f32_16x16x32_bf16 a[24:27], v[28:31], a[168:171], a[24:27]\n"
    "ds_read_b64_tr_b16 v[50:51], v60 offset:41568\n"
    "v_mfma_f32_16x16x32_bf16 a[28:31], v[24:27], v[92:95], a[28:31]\n"
    "ds_read_b64_tr_b16 v[52:53], v60 offset:46176\n"
    "v_mfma_f32_16x16x32_bf16 a[28:31], v[28:31], a[172:175], a[28:31]\n"
    "s_waitcnt lgkmcnt(5)\n"
    "ds_read_b64_tr_b16 v[54:55], v60 offset:50784\n"
    "v_mfma_f32_16x16x32_bf16 a[32:35], v[32:35], v[80:83], a[32:35]\n"
    "s_waitcnt lgkmcnt(4)\n"
    "buffer_load_dwordx4 a[240:243], v0, s[20:23], s92 offen offset:0 sc1\n"
    "v_mfma_f32_16x16x32_bf16 a[32:35], v[36:39], a[160:163], a[32:35]\n"
    "s_mov_b32 s92, 61440\n"
    "v_mfma_f32_16x16x32_bf16 a[36:39], v[32:35], v[84:87], a[36:39]\n"
    "buffer_load_dwordx4 v[140:143], v0, s[20:23], s92 offen offset:3072 sc1\n"
    "v_mfma_f32_16x16x32_bf16 a[36:39], v[36:39], a[164:167], a[36:39]\n"
    "v_mfma_f32_16x16x32_bf16 a[40:43], v[32:35], v[88:91], a[40:43]\n"
    "ds_read_b64_tr_b16 v[56:57], v60 offset:36992\n"
    "v_mfma_f32_16x16x32_bf16 a[40:43], v[36:39], a[168:171], a[40:43]\n"
    "ds_read_b64_tr_b16 v[58:59], v60 offset:41600\n"
    "v_mfma_f32_16x16x32_bf16 a[44:47], v[32:35], v[92:95], a[44:47]\n"
    "ds_read_b64_tr_b16 v[16:17], v60 offset:46208\n"
    "v_mfma_f32_16x16x32_bf16 a[44:47], v[36:39], a[172:175], a[44:47]\n"
    "s_waitcnt lgkmcnt(5)\n"
    "ds_read_b64_tr_b16 v[18:19], v60 offset:50816\n"
    "v_mfma_f32_16x16x32_bf16 a[48:51], v[48:51], v[80:83], a[48:51]\n"
    "s_waitcnt lgkmcnt(4)\n"
    "buffer_load_dwordx4 v[136:139], v0, s[20:23], s92 offen offset:2048 sc1\n"
    "v_mfma_f32_16x16x32_bf16 a[48:51], v[52:55], a[160:163], a[48:51]\n"
    "v_mfma_f32_16x16x32_bf16 a[52:55], v[48:51], v[84:87], a[52:55]\n"
    "v_mfma_f32_16x16x32_bf16 a[52:55], v[52:55], a[164:167], a[52:55]\n"
    "v_mfma_f32_16x16x32_bf16 a[56:59], v[48:51], v[88:91], a[56:59]\n"
    "ds_read_b64_tr_b16 v[20:21], v60 offset:37024\n"
    "v_mfma_f32_16x16x32_bf16 a[56:59], v[52:55], a[168:171], a[56:59]\n"
    "ds_read_b64_tr_b16 v[22:23], v60 offset:41632\n"
    "v_mfma_f32_16x16x32_bf16 a[60:63], v[48:51], v[92:95], a[60:63]\n"
    "ds_read_b64_tr_b16 v[24:25], v60 offset:46240\n"
    "v_mfma_f32_16x16x32_bf16 a[60:63], v[52:55], a[172:175], a[60:63]\n"
    "s_waitcnt lgkmcnt(5)\n"
    "ds_read_b64_tr_b16 v[26:27], v60 offset:50848\n"
    "v_mfma_f32_16x16x32_bf16 a[64:67], v[56:59], v[80:83], a[64:67]\n"
    "s_waitcnt lgkmcnt(4)\n"
    "buffer_load_dwordx4 v[132:135], v0, s[20:23], s92 offen offset:1024 sc1\n"
    "v_mfma_f32_16x16x32_bf16 a[64:67], v[16:19], a[160:163], a[64:67]\n"
    "v_mfma_f32_16x16x32_bf16 a[68:71], v[56:59], v[84:87], a[68:71]\n"
    "buffer_load_dwordx4 v[128:131], v0, s[20:23], s92 offen offset:0 sc1\n"
    "v_mfma_f32_16x16x32_bf16 a[68:71], v[16:19], a[164:167], a[68:71]\n"
    "v_mfma_f32_16x16x32_bf16 a[72:75], v[56:59], v[88:91], a[72:75]\n"
    "ds_read_b64_tr_b16 v[28:29], v60 offset:37056\n"
    "v_mfma_f32_16x16x32_bf16 a[72:75], v[16:19], a[168:171], a[72:75]\n"
    "ds_read_b64_tr_b16 v[30:31], v60 offset:41664\n"
    "v_mfma_f32_16x16x32_bf16 a[76:79], v[56:59], v[92:95], a[76:79]\n"
    "ds_read_b64_tr_b16 v[32:33], v60 offset:46272\n"
    "v_mfma_f32_16x16x32_bf16 a[76:79], v[16:19], a[172:175], a[76:79]\n"
    "s_waitcnt lgkmcnt(5)\n"
    "ds_read_b64_tr_b16 v[34:35], v60 offset:50880\n"
    "v_mfma_f32_16x16x32_bf16 a[80:83], v[20:23], v[80:83], a[80:83]\n"
    "s_waitcnt lgkmcnt(4)\n"
    "buffer_load_dwordx4 v3, s[40:43], s91 offen offset:1024 lds\n"
    "v_mfma_f32_16x16x32_bf16 a[80:83], v[24:27], a[160:163], a[80:83]\n"
    "v_mfma_f32_16x16x32_bf16 a[84:87], v[20:23], v[84:87], a[84:87]\n"
    "v_mfma_f32_16x16x32_bf16 a[84:87], v[24:27], a[164:167], a[84:87]\n"
    "v_mfma_f32_16x16x32_bf16 a[88:91], v[20:23], v[88:91], a[88:91]\n"
    "ds_read_b64_tr_b16 v[36:37], v60 offset:37088\n"
    "v_mfma_f32_16x16x32_bf16 a[88:91], v[24:27], a[168:171], a[88:91]\n"
    "ds_read_b64_tr_b16 v[38:39], v60 offset:41696\n"
    "v_mfma_f32_16x16x32_bf16 a[92:95], v[20:23], v[92:95], a[92:95]\n"
    "ds_read_b64_tr_b16 v[48:49], v60 offset:46304\n"
    "v_mfma_f32_16x16x32_bf16 a[92:95], v[24:27], a[172:175], a[92:95]\n"
    "s_waitcnt lgkmcnt(5)\n"
    "ds_read_b64_tr_b16 v[50:51], v60 offset:50912\n"
    "v_mfma_f32_16x16x32_bf16 a[96:99], v[28:31], v[80:83], a[96:99]\n"
    "s_waitcnt lgkmcnt(4)\n"
    "buffer_load_dwordx4 v4, s[40:43], s91 offen offset:2048 lds\n"
    "v_mfma_f32_16x16x32_bf16 a[96:99], v[32:35], a[160:163], a[96:99]\n"
    "v_mfma_f32_16x16x32_bf16 a[100:103], v[28:31], v[84:87], a[100:103]\n"
    "buffer_load_dwordx4 v5, s[40:43], s91 offen offset:3072 lds\n"
    "v_mfma_f32_16x16x32_bf16 a[100:103], v[32:35], a[164:167], a[100:103]\n"
    "v_mfma_f32_16x16x32_bf16 a[104:107], v[28:31], v[88:91], a[104:107]\n"
    "ds_read_b64_tr_b16 v[52:53], v60 offset:37120\n"
    "v_mfma_f32_16x16x32_bf16 a[104:107], v[32:35], a[168:171], a[104:107]\n"
    "ds_read_b64_tr_b16 v[54:55], v60 offset:41728\n"
    "v_mfma_f32_16x16x32_bf16 a[108:111], v[28:31], v[92:95], a[108:111]\n"
    "ds_read_b64_tr_b16 v[56:57], v60 offset:46336\n"
    "v_mfma_f32_16x16x32_bf16 a[108:111], v[32:35], a[172:175], a[108:111]\n"
    "s_waitcnt lgkmcnt(5)\n"
    "s_add_u32 m0, s17, 94208\n"
    "ds_read_b64_tr_b16 v[58:59], v60 offset:50944\n"
    "v_mfma_f32_16x16x32_bf16 a[112:115], v[36:39], v[80:83], a[112:115]\n"
    "s_waitcnt lgkmcnt(4)\n"
    "buffer_load_dwordx4 v6, s[40:43], s91 offen lds\n"
    "v_mfma_f32_16x16x32_bf16 a[112:115], v[48:51], a[160:163], a[112:115]\n"
    "v_mfma_f32_16x16x32_bf16 a[116:119], v[36:39], v[84:87], a[116:119]\n"
    "v_mfma_f32_16x16x32_bf16 a[116:119], v[48:51], a[164:167], a[116:119]\n"
    "v_mfma_f32_16x16x32_bf16 a[120:123], v[36:39], v[88:91], a[120:123]\n"
    "ds_read_b64_tr_b16 v[16:17], v60 offset:55296\n"
    "v_mfma_f32_16x16x32_bf16 a[120:123], v[48:51], a[168:171], a[120:123]\n"
    "ds_read_b64_tr_b16 v[18:19], v60 offset:59904\n"
    "v_mfma_f32_16x16x32_bf16 a[124:127], v[36:39], v[92:95], a[124:127]\n"
    "ds_read_b64_tr_b16 v[20:21], v61 offset:0\n"
    "v_mfma_f32_16x16x32_bf16 a[124:127], v[48:51], a[172:175], a[124:127]\n"
    "s_waitcnt lgkmcnt(5)\n"
    "ds_read_b64_tr_b16 v[22:23], v61 offset:4608\n"
    "v_mfma_f32_16x16x32_bf16 a[128:131], v[52:55], v[80:83], a[128:131]\n"
    "s_waitcnt lgkmcnt(4)\n"
    "buffer_load_dwordx4 v7, s[40:43], s91 offen offset:1024 lds\n"
    "v_mfma_f32_16x16x32_bf16 a[128:131], v[56:59], a[160:163], a[128:131]\n"
    "v_mfma_f32_16x16x32_bf16 a[132:135], v[52:55], v[84:87], a[132:135]\n"
    "buffer_load_dwordx4 v8, s[40:43], s91 offen offset:2048 lds\n"
    "v_mfma_f32_16x16x32_bf16 a[132:135], v[56:59], a[164:167], a[132:135]\n"
    "v_mfma_f32_16x16x32_bf16 a[136:139], v[52:55], v[88:91], a[136:139]\n"
    "ds_read_b64_tr_b16 v[24:25], v60 offset:55328\n"
    "v_mfma_f32_16x16x32_bf16 a[136:139], v[56:59], a[168:171], a[136:139]\n"
    "ds_read_b64_tr_b16 v[26:27], v60 offset:59936\n"
    "v_mfma_f32_16x16x32_bf16 a[140:143], v[52:55], v[92:95], a[140:143]\n"
    "ds_read_b64_tr_b16 v[28:29], v61 offset:32\n"
    "v_mfma_f32_16x16x32_bf16 a[140:143], v[56:59], a[172:175], a[140:143]\n"
    "s_waitcnt vmcnt(47) lgkmcnt(5)\n"
    "ds_read_b64_tr_b16 v[30:31], v61 offset:4640\n"
    "v_add_u32 v46, 1, v46\n"
    "v_mfma_f32_16x16x32_bf16 a[0:3], v[16:19], a[176:179], a[0:3]\n"
    "s_waitcnt vmcnt(26) lgkmcnt(4)\n"
    "buffer_store_dwordx2 v[46:47], v11, s[76:79], 0 offen sc1\n"
    "v_mfma_f32_16x16x32_bf16 a[0:3], v[20:23], a[192:195], a[0:3]\n"
    "v_mfma_f32_16x16x32_bf16 a[4:7], v[16:19], a[180:183], a[4:7]\n"
    "v_mfma_f32_16x16x32_bf16 a[4:7], v[20:23], a[196:199], a[4:7]\n"
    "v_mfma_f32_16x16x32_bf16 a[8:11], v[16:19], a[184:187], a[8:11]\n"
    "ds_read_b64_tr_b16 v[32:33], v60 offset:55360\n"
    "v_mfma_f32_16x16x32_bf16 a[8:11], v[20:23], a[200:203], a[8:11]\n"
    "ds_read_b64_tr_b16 v[34:35], v60 offset:59968\n"
    "v_mfma_f32_16x16x32_bf16 a[12:15], v[16:19], a[188:191], a[12:15]\n"
    "ds_read_b64_tr_b16 v[36:37], v61 offset:64\n"
    "v_mfma_f32_16x16x32_bf16 a[12:15], v[20:23], a[204:207], a[12:15]\n"
    "s_waitcnt lgkmcnt(5)\n"
    "ds_read_b64_tr_b16 v[38:39], v61 offset:4672\n"
    "v_mfma_f32_16x16x32_bf16 a[16:19], v[24:27], a[176:179], a[16:19]\n"
    "s_waitcnt lgkmcnt(4)\n"
    "buffer_load_dwordx4 v9, s[40:43], s91 offen offset:3072 lds\n"
    "v_mfma_f32_16x16x32_bf16 a[16:19], v[28:31], a[192:195], a[16:19]\n"
    "s_add_u32 m0, s17, 98304\n"
    "v_mfma_f32_16x16x32_bf16 a[20:23], v[24:27], a[180:183], a[20:23]\n"
    "buffer_load_dwordx4 v10, s[40:43], s91 offen lds\n"
    "v_mfma_f32_16x16x32_bf16 a[20:23], v[28:31], a[196:199], a[20:23]\n"
    "v_mfma_f32_16x16x32_bf16 a[24:27], v[24:27], a[184:187], a[24:27]\n"
    "ds_read_b64_tr_b16 v[48:49], v60 offset:55392\n"
    "v_mfma_f32_16x16x32_bf16 a[24:27], v[28:31], a[200:203], a[24:27]\n"
    "ds_read_b64_tr_b16 v[50:51], v60 offset:60000\n"
    "v_mfma_f32_16x16x32_bf16 a[28:31], v[24:27], a[188:191], a[28:31]\n"
    "ds_read_b64_tr_b16 v[52:53], v61 offset:96\n"
    "v_mfma_f32_16x16x32_bf16 a[28:31], v[28:31], a[204:207], a[28:31]\n"
    "s_waitcnt lgkmcnt(5)\n"
    "s_mov_b32 s92, 65536\n"
    "ds_read_b64_tr_b16 v[54:55], v61 offset:4704\n"
    "v_mfma_f32_16x16x32_bf16 a[32:35], v[32:35], a[176:179], a[32:35]\n"
    "s_waitcnt lgkmcnt(4)\n"
    "buffer_load_dwordx4 v[156:159], v0, s[20:23], s92 offen offset:3072 sc1\n"
    "v_mfma_f32_16x16x32_bf16 a[32:35], v[36:39], a[192:195], a[32:35]\n"
    "v_mfma_f32_16x16x32_bf16 a[36:39], v[32:35], a[180:183], a[36:39]\n"
    "v_mfma_f32_16x16x32_bf16 a[36:39], v[36:39], a[196:199], a[36:39]\n"
    "v_mfma_f32_16x16x32_bf16 a[40:43], v[32:35], a[184:187], a[40:43]\n"
    "ds_read_b64_tr_b16 v[56:57], v60 offset:55424\n"
    "v_mfma_f32_16x16x32_bf16 a[40:43], v[36:39], a[200:203], a[40:43]\n"
    "ds_read_b64_tr_b16 v[58:59], v60 offset:60032\n"
    "v_mfma_f32_16x16x32_bf16 a[44:47], v[32:35], a[188:191], a[44:47]\n"
    "ds_read_b64_tr_b16 v[16:17], v61 offset:128\n"
    "v_mfma_f32_16x16x32_bf16 a[44:47], v[36:39], a[204:207], a[44:47]\n"
    "s_waitcnt lgkmcnt(5)\n"
    "ds_read_b64_tr_b16 v[18:19], v61 offset:4736\n"
    "v_mfma_f32_16x16x32_bf16 a[48:51], v[48:51], a[176:179], a[48:51]\n"
    "s_waitcnt lgkmcnt(4)\n"
    "buffer_load_dwordx4 v[152:155], v0, s[20:23], s92 offen offset:2048 sc1\n"
    "v_mfma_f32_16x16x32_bf16 a[48:51], v[52:55], a[192:195], a[48:51]\n"
    "v_mfma_f32_16x16x32_bf16 a[52:55], v[48:51], a[180:183], a[52:55]\n"
    "buffer_load_dwordx4 v[148:151], v0, s[20:23], s92 offen offset:1024 sc1\n"
    "v_mfma_f32_16x16x32_bf16 a[52:55], v[52:55], a[196:199], a[52:55]\n"
    "v_mfma_f32_16x16x32_bf16 a[56:59], v[48:51], a[184:187], a[56:59]\n"
    "ds_read_b64_tr_b16 v[20:21], v60 offset:55456\n"
    "v_mfma_f32_16x16x32_bf16 a[56:59], v[52:55], a[200:203], a[56:59]\n"
    "ds_read_b64_tr_b16 v[22:23], v60 offset:60064\n"
    "v_mfma_f32_16x16x32_bf16 a[60:63], v[48:51], a[188:191], a[60:63]\n"
    "ds_read_b64_tr_b16 v[24:25], v61 offset:160\n"
    "v_mfma_f32_16x16x32_bf16 a[60:63], v[52:55], a[204:207], a[60:63]\n"
    "s_waitcnt lgkmcnt(5)\n"
    "ds_read_b64_tr_b16 v[26:27], v61 offset:4768\n"
    "v_mfma_f32_16x16x32_bf16 a[64:67], v[56:59], a[176:179], a[64:67]\n"
    "s_waitcnt lgkmcnt(4)\n"
    "buffer_load_dwordx4 v[144:147], v0, s[20:23], s92 offen offset:0 sc1\n"
    "v_mfma_f32_16x16x32_bf16 a[64:67], v[16:19], a[192:195], a[64:67]\n"
    "v_mfma_f32_16x16x32_bf16 a[68:71], v[56:59], a[180:183], a[68:71]\n"
    "v_mfma_f32_16x16x32_bf16 a[68:71], v[16:19], a[196:199], a[68:71]\n"
    "v_mfma_f32_16x16x32_bf16 a[72:75], v[56:59], a[184:187], a[72:75]\n"
    "ds_read_b64_tr_b16 v[28:29], v60 offset:55488\n"
    "v_mfma_f32_16x16x32_bf16 a[72:75], v[16:19], a[200:203], a[72:75]\n"
    "ds_read_b64_tr_b16 v[30:31], v60 offset:60096\n"
    "v_mfma_f32_16x16x32_bf16 a[76:79], v[56:59], a[188:191], a[76:79]\n"
    "ds_read_b64_tr_b16 v[32:33], v61 offset:192\n"
    "v_mfma_f32_16x16x32_bf16 a[76:79], v[16:19], a[204:207], a[76:79]\n"
    "s_waitcnt lgkmcnt(5)\n"
    "s_mov_b32 s92, 69632\n"
    "ds_read_b64_tr_b16 v[34:35], v61 offset:4800\n"
    "v_mfma_f32_16x16x32_bf16 a[80:83], v[20:23], a[176:179], a[80:83]\n"
    "s_waitcnt lgkmcnt(4)\n"
    "buffer_load_dwordx4 v[172:175], v0, s[20:23], s92 offen offset:3072 sc1\n"
    "v_mfma_f32_16x16x32_bf16 a[80:83], v[24:27], a[192:195], a[80:83]\n"
    "v_mfma_f32_16x16x32_bf16 a[84:87], v[20:23], a[180:183], a[84:87]\n"
    "buffer_load_dwordx4 v[168:171], v0, s[20:23], s92 offen offset:2048 sc1\n"
    "v_mfma_f32_16x16x32_bf16 a[84:87], v[24:27], a[196:199], a[84:87]\n"
    "v_mfma_f32_16x16x32_bf16 a[88:91], v[20:23], a[184:187], a[88:91]\n"
    "ds_read_b64_tr_b16 v[36:37], v60 offset:55520\n"
    "v_mfma_f32_16x16x32_bf16 a[88:91], v[24:27], a[200:203], a[88:91]\n"
    "ds_read_b64_tr_b16 v[38:39], v60 offset:60128\n"
    "v_mfma_f32_16x16x32_bf16 a[92:95], v[20:23], a[188:191], a[92:95]\n"
    "ds_read_b64_tr_b16 v[48:49], v61 offset:224\n"
    "v_mfma_f32_16x16x32_bf16 a[92:95], v[24:27], a[204:207], a[92:95]\n"
    "s_waitcnt lgkmcnt(5)\n"
    "ds_read_b64_tr_b16 v[50:51], v61 offset:4832\n"
    "v_mfma_f32_16x16x32_bf16 a[96:99], v[28:31], a[176:179], a[96:99]\n"
    "s_waitcnt lgkmcnt(4)\n"
    "buffer_load_dwordx4 v[164:167], v0, s[20:23], s92 offen offset:1024 sc1\n"
    "v_mfma_f32_16x16x32_bf16 a[96:99], v[32:35], a[192:195], a[96:99]\n"
    "v_mfma_f32_16x16x32_bf16 a[100:103], v[28:31], a[180:183], a[100:103]\n"
    "v_mfma_f32_16x16x32_bf16 a[100:103], v[32:35], a[196:199], a[100:103]\n"
    "v_mfma_f32_16x16x32_bf16 a[104:107], v[28:31], a[184:187], a[104:107]\n"
    "ds_read_b64_tr_b16 v[52:53], v60 offset:55552\n"
    "v_mfma_f32_16x16x32_bf16 a[104:107], v[32:35], a[200:203], a[104:107]\n"
    "ds_read_b64_tr_b16 v[54:55], v60 offset:60160\n"
    "v_mfma_f32_16x16x32_bf16 a[108:111], v[28:31], a[188:191], a[108:111]\n"
    "ds_read_b64_tr_b16 v[56:57], v61 offset:256\n"
    "v_mfma_f32_16x16x32_bf16 a[108:111], v[32:35], a[204:207], a[108:111]\n"
    "s_waitcnt lgkmcnt(5)\n"
    "ds_read_b64_tr_b16 v[58:59], v61 offset:4864\n"
    "v_mfma_f32_16x16x32_bf16 a[112:115], v[36:39], a[176:179], a[112:115]\n"
    "s_waitcnt lgkmcnt(4)\n"
    "buffer_load_dwordx4 v[160:163], v0, s[20:23], s92 offen offset:0 sc1\n"
    "v_mfma_f32_16x16x32_bf16 a[112:115], v[48:51], a[192:195], a[112:115]\n"
    "s_mov_b32 s92, 73728\n"
    "v_mfma_f32_16x16x32_bf16 a[116:119], v[36:39], a[180:183], a[116:119]\n"
    "buffer_load_dwordx4 v[188:191], v0, s[20:23], s92 offen offset:3072 sc1\n"
    "v_mfma_f32_16x16x32_bf16 a[116:119], v[48:51], a[196:199], a[116:119]\n"
    "v_mfma_f32_16x16x32_bf16 a[120:123], v[36:39], a[184:187], a[120:123]\n"
    "v_mfma_f32_16x16x32_bf16 a[120:123], v[48:51], a[200:203], a[120:123]\n"
    "v_mfma_f32_16x16x32_bf16 a[124:127], v[36:39], a[188:191], a[124:127]\n"
    "s_barrier\n"
    "ds_read_b64_tr_b16 v[16:17], v60 offset:0\n"
    "v_mfma_f32_16x16x32_bf16 a[124:127], v[48:51], a[204:207], a[124:127]\n"
    "s_waitcnt lgkmcnt(3)\n"
    "ds_read_b64_tr_b16 v[18:19], v60 offset:4608\n"
    "v_mfma_f32_16x16x32_bf16 a[128:131], v[52:55], a[176:179], a[128:131]\n"
    "s_waitcnt lgkmcnt(2)\n"
    "ds_read_b64_tr_b16 v[20:21], v60 offset:9216\n"
    "v_mfma_f32_16x16x32_bf16 a[128:131], v[56:59], a[192:195], a[128:131]\n"
    "ds_read_b64_tr_b16 v[22:23], v60 offset:13824\n"
    "v_mfma_f32_16x16x32_bf16 a[132:135], v[52:55], a[180:183], a[132:135]\n"
    "buffer_load_dwordx4 v[184:187], v0, s[20:23], s92 offen offset:2048 sc1\n"
    "v_mfma_f32_16x16x32_bf16 a[132:135], v[56:59], a[196:199], a[132:135]\n"
    "v_mfma_f32_16x16x32_bf16 a[136:139], v[52:55], a[184:187], a[136:139]\n"
    "ds_read_b64_tr_b16 v[24:25], v60 offset:32\n"
    "v_mfma_f32_16x16x32_bf16 a[136:139], v[56:59], a[200:203], a[136:139]\n"
    "ds_read_b64_tr_b16 v[26:27], v60 offset:4640\n"
    "v_mfma_f32_16x16x32_bf16 a[140:143], v[52:55], a[188:191], a[140:143]\n"
    "ds_read_b64_tr_b16 v[28:29], v60 offset:9248\n"
    "v_mfma_f32_16x16x32_bf16 a[140:143], v[56:59], a[204:207], a[140:143]\n"
    "s_waitcnt vmcnt(36) lgkmcnt(5)\n"
    "ds_read_b64_tr_b16 v[30:31], v60 offset:13856\n"
    "v_mfma_f32_16x16x32_bf16 a[0:3], v[16:19], a[208:211], a[0:3]\n"
    "s_waitcnt vmcnt(28) lgkmcnt(4)\n"
    "buffer_load_dwordx4 v[180:183], v0, s[20:23], s92 offen offset:1024 sc1\n"
    "v_mfma_f32_16x16x32_bf16 a[0:3], v[20:23], a[224:227], a[0:3]\n"
    "v_mfma_f32_16x16x32_bf16 a[4:7], v[16:19], a[212:215], a[4:7]\n"
    "buffer_load_dwordx4 v[176:179], v0, s[20:23], s92 offen offset:0 sc1\n"
    "v_mfma_f32_16x16x32_bf16 a[4:7], v[20:23], a[228:231], a[4:7]\n"
    "v_mfma_f32_16x16x32_bf16 a[8:11], v[16:19], a[216:219], a[8:11]\n"
    "ds_read_b64_tr_b16 v[32:33], v60 offset:64\n"
    "v_mfma_f32_16x16x32_bf16 a[8:11], v[20:23], a[232:235], a[8:11]\n"
    "ds_read_b64_tr_b16 v[34:35], v60 offset:4672\n"
    "v_mfma_f32_16x16x32_bf16 a[12:15], v[16:19], a[220:223], a[12:15]\n"
    "ds_read_b64_tr_b16 v[36:37], v60 offset:9280\n"
    "v_mfma_f32_16x16x32_bf16 a[12:15], v[20:23], a[236:239], a[12:15]\n"
    "s_waitcnt lgkmcnt(5)\n"
    "s_mov_b32 s92, 77824\n"
    "ds_read_b64_tr_b16 v[38:39], v60 offset:13888\n"
    "v_mfma_f32_16x16x32_bf16 a[16:19], v[24:27], a[208:211], a[16:19]\n"
    "s_waitcnt lgkmcnt(4)\n"
    "buffer_load_dwordx4 v[204:207], v0, s[20:23], s92 offen offset:3072 sc1\n"
    "v_mfma_f32_16x16x32_bf16 a[16:19], v[28:31], a[224:227], a[16:19]\n"
    "v_mfma_f32_16x16x32_bf16 a[20:23], v[24:27], a[212:215], a[20:23]\n"
    "v_mfma_f32_16x16x32_bf16 a[20:23], v[28:31], a[228:231], a[20:23]\n"
    "v_mfma_f32_16x16x32_bf16 a[24:27], v[24:27], a[216:219], a[24:27]\n"
    "ds_read_b64_tr_b16 v[48:49], v60 offset:96\n"
    "v_mfma_f32_16x16x32_bf16 a[24:27], v[28:31], a[232:235], a[24:27]\n"
    "ds_read_b64_tr_b16 v[50:51], v60 offset:4704\n"
    "v_mfma_f32_16x16x32_bf16 a[28:31], v[24:27], a[220:223], a[28:31]\n"
    "ds_read_b64_tr_b16 v[52:53], v60 offset:9312\n"
    "v_mfma_f32_16x16x32_bf16 a[28:31], v[28:31], a[236:239], a[28:31]\n"
    "s_waitcnt lgkmcnt(5)\n"
    "ds_read_b64_tr_b16 v[54:55], v60 offset:13920\n"
    "v_mfma_f32_16x16x32_bf16 a[32:35], v[32:35], a[208:211], a[32:35]\n"
    "s_waitcnt lgkmcnt(4)\n"
    "buffer_load_dwordx4 v[200:203], v0, s[20:23], s92 offen offset:2048 sc1\n"
    "v_mfma_f32_16x16x32_bf16 a[32:35], v[36:39], a[224:227], a[32:35]\n"
    "v_mfma_f32_16x16x32_bf16 a[36:39], v[32:35], a[212:215], a[36:39]\n"
    "buffer_load_dwordx4 v[196:199], v0, s[20:23], s92 offen offset:1024 sc1\n"
    "v_mfma_f32_16x16x32_bf16 a[36:39], v[36:39], a[228:231], a[36:39]\n"
    "v_mfma_f32_16x16x32_bf16 a[40:43], v[32:35], a[216:219], a[40:43]\n"
    "ds_read_b64_tr_b16 v[56:57], v60 offset:128\n"
    "v_mfma_f32_16x16x32_bf16 a[40:43], v[36:39], a[232:235], a[40:43]\n"
    "ds_read_b64_tr_b16 v[58:59], v60 offset:4736\n"
    "v_mfma_f32_16x16x32_bf16 a[44:47], v[32:35], a[220:223], a[44:47]\n"
    "ds_read_b64_tr_b16 v[16:17], v60 offset:9344\n"
    "v_mfma_f32_16x16x32_bf16 a[44:47], v[36:39], a[236:239], a[44:47]\n"
    "s_waitcnt lgkmcnt(5)\n"
    "ds_read_b64_tr_b16 v[18:19], v60 offset:13952\n"
    "v_mfma_f32_16x16x32_bf16 a[48:51], v[48:51], a[208:211], a[48:51]\n"
    "s_waitcnt lgkmcnt(4)\n"
    "buffer_load_dwordx4 v[192:195], v0, s[20:23], s92 offen offset:0 sc1\n"
    "v_mfma_f32_16x16x32_bf16 a[48:51], v[52:55], a[224:227], a[48:51]\n"
    "v_mfma_f32_16x16x32_bf16 a[52:55], v[48:51], a[212:215], a[52:55]\n"
    "v_mfma_f32_16x16x32_bf16 a[52:55], v[52:55], a[228:231], a[52:55]\n"
    "v_mfma_f32_16x16x32_bf16 a[56:59], v[48:51], a[216:219], a[56:59]\n"
    "ds_read_b64_tr_b16 v[20:21], v60 offset:160\n"
    "v_mfma_f32_16x16x32_bf16 a[56:59], v[52:55], a[232:235], a[56:59]\n"
    "s_add_u32 s91, s80, 20\n"
    "ds_read_b64_tr_b16 v[22:23], v60 offset:4768\n"
    "v_mfma_f32_16x16x32_bf16 a[60:63], v[48:51], a[220:223], a[60:63]\n"
    "s_mul_i32 s91, s91, 36864\n"
    "ds_read_b64_tr_b16 v[24:25], v60 offset:9376\n"
    "v_mfma_f32_16x16x32_bf16 a[60:63], v[52:55], a[236:239], a[60:63]\n"
    "s_waitcnt lgkmcnt(5)\n"
    "s_add_u32 m0, s17, 126976\n"
    "ds_read_b64_tr_b16 v[26:27], v60 offset:13984\n"
    "v_mfma_f32_16x16x32_bf16 a[64:67], v[56:59], a[208:211], a[64:67]\n"
    "s_waitcnt lgkmcnt(4)\n"
    "buffer_load_dwordx4 v2, s[40:43], s91 offen lds\n"
    "v_mfma_f32_16x16x32_bf16 a[64:67], v[16:19], a[224:227], a[64:67]\n"
    "v_mfma_f32_16x16x32_bf16 a[68:71], v[56:59], a[212:215], a[68:71]\n"
    "buffer_load_dwordx4 v3, s[40:43], s91 offen offset:1024 lds\n"
    "v_mfma_f32_16x16x32_bf16 a[68:71], v[16:19], a[228:231], a[68:71]\n"
    "v_mfma_f32_16x16x32_bf16 a[72:75], v[56:59], a[216:219], a[72:75]\n"
    "ds_read_b64_tr_b16 v[28:29], v60 offset:192\n"
    "v_mfma_f32_16x16x32_bf16 a[72:75], v[16:19], a[232:235], a[72:75]\n"
    "ds_read_b64_tr_b16 v[30:31], v60 offset:4800\n"
    "v_mfma_f32_16x16x32_bf16 a[76:79], v[56:59], a[220:223], a[76:79]\n"
    "ds_read_b64_tr_b16 v[32:33], v60 offset:9408\n"
    "v_mfma_f32_16x16x32_bf16 a[76:79], v[16:19], a[236:239], a[76:79]\n"
    "s_waitcnt lgkmcnt(5)\n"
    "ds_read_b64_tr_b16 v[34:35], v60 offset:14016\n"
    "v_mfma_f32_16x16x32_bf16 a[80:83], v[20:23], a[208:211], a[80:83]\n"
    "s_waitcnt lgkmcnt(4)\n"
    "buffer_load_dwordx4 v4, s[40:43], s91 offen offset:2048 lds\n"
    "v_mfma_f32_16x16x32_bf16 a[80:83], v[24:27], a[224:227], a[80:83]\n"
    "v_mfma_f32_16x16x32_bf16 a[84:87], v[20:23], a[212:215], a[84:87]\n"
    "v_mfma_f32_16x16x32_bf16 a[84:87], v[24:27], a[228:231], a[84:87]\n"
    "v_mfma_f32_16x16x32_bf16 a[88:91], v[20:23], a[216:219], a[88:91]\n"
    "ds_read_b64_tr_b16 v[36:37], v60 offset:224\n"
    "v_mfma_f32_16x16x32_bf16 a[88:91], v[24:27], a[232:235], a[88:91]\n"
    "ds_read_b64_tr_b16 v[38:39], v60 offset:4832\n"
    "v_mfma_f32_16x16x32_bf16 a[92:95], v[20:23], a[220:223], a[92:95]\n"
    "ds_read_b64_tr_b16 v[48:49], v60 offset:9440\n"
    "v_mfma_f32_16x16x32_bf16 a[92:95], v[24:27], a[236:239], a[92:95]\n"
    "s_waitcnt lgkmcnt(5)\n"
    "ds_read_b64_tr_b16 v[50:51], v60 offset:14048\n"
    "v_mfma_f32_16x16x32_bf16 a[96:99], v[28:31], a[208:211], a[96:99]\n"
    "s_waitcnt lgkmcnt(4)\n"
    "buffer_load_dwordx4 v5, s[40:43], s91 offen offset:3072 lds\n"
    "v_mfma_f32_16x16x32_bf16 a[96:99], v[32:35], a[224:227], a[96:99]\n"
    "s_add_u32 m0, s17, 131072\n"
    "v_mfma_f32_16x16x32_bf16 a[100:103], v[28:31], a[212:215], a[100:103]\n"
    "buffer_load_dwordx4 v6, s[40:43], s91 offen lds\n"
    "v_mfma_f32_16x16x32_bf16 a[100:103], v[32:35], a[228:231], a[100:103]\n"
    "v_mfma_f32_16x16x32_bf16 a[104:107], v[28:31], a[216:219], a[104:107]\n"
    "ds_read_b64_tr_b16 v[52:53], v60 offset:256\n"
    "v_mfma_f32_16x16x32_bf16 a[104:107], v[32:35], a[232:235], a[104:107]\n"
    "ds_read_b64_tr_b16 v[54:55], v60 offset:4864\n"
    "v_mfma_f32_16x16x32_bf16 a[108:111], v[28:31], a[220:223], a[108:111]\n"
    "ds_read_b64_tr_b16 v[56:57], v60 offset:9472\n"
    "v_mfma_f32_16x16x32_bf16 a[108:111], v[32:35], a[236:239], a[108:111]\n"
    "s_waitcnt lgkmcnt(5)\n"
    "ds_read_b64_tr_b16 v[58:59], v60 offset:14080\n"
    "v_mfma_f32_16x16x32_bf16 a[112:115], v[36:39], a[208:211], a[112:115]\n"
    "s_waitcnt lgkmcnt(4)\n"
    "buffer_load_dwordx4 v7, s[40:43], s91 offen offset:1024 lds\n"
    "v_mfma_f32_16x16x32_bf16 a[112:115], v[48:51], a[224:227], a[112:115]\n"
    "v_mfma_f32_16x16x32_bf16 a[116:119], v[36:39], a[212:215], a[116:119]\n"
    "v_mfma_f32_16x16x32_bf16 a[116:119], v[48:51], a[228:231], a[116:119]\n"
    "v_mfma_f32_16x16x32_bf16 a[120:123], v[36:39], a[216:219], a[120:123]\n"
    "ds_read_b64_tr_b16 v[16:17], v60 offset:18432\n"
    "v_mfma_f32_16x16x32_bf16 a[120:123], v[48:51], a[232:235], a[120:123]\n"
    "ds_read_b64_tr_b16 v[18:19], v60 offset:23040\n"
    "v_mfma_f32_16x16x32_bf16 a[124:127], v[36:39], a[220:223], a[124:127]\n"
    "ds_read_b64_tr_b16 v[20:21], v60 offset:27648\n"
    "v_mfma_f32_16x16x32_bf16 a[124:127], v[48:51], a[236:239], a[124:127]\n"
    "s_waitcnt lgkmcnt(5)\n"
    "ds_read_b64_tr_b16 v[22:23], v60 offset:32256\n"
    "v_mfma_f32_16x16x32_bf16 a[128:131], v[52:55], a[208:211], a[128:131]\n"
    "s_waitcnt lgkmcnt(4)\n"
    "buffer_load_dwordx4 v8, s[40:43], s91 offen offset:2048 lds\n"
    "v_mfma_f32_16x16x32_bf16 a[128:131], v[56:59], a[224:227], a[128:131]\n"
    "v_mfma_f32_16x16x32_bf16 a[132:135], v[52:55], a[212:215], a[132:135]\n"
    "buffer_load_dwordx4 v9, s[40:43], s91 offen offset:3072 lds\n"
    "v_mfma_f32_16x16x32_bf16 a[132:135], v[56:59], a[228:231], a[132:135]\n"
    "v_mfma_f32_16x16x32_bf16 a[136:139], v[52:55], a[216:219], a[136:139]\n"
    "ds_read_b64_tr_b16 v[24:25], v60 offset:18464\n"
    "v_mfma_f32_16x16x32_bf16 a[136:139], v[56:59], a[232:235], a[136:139]\n"
    "ds_read_b64_tr_b16 v[26:27], v60 offset:23072\n"
    "v_mfma_f32_16x16x32_bf16 a[140:143], v[52:55], a[220:223], a[140:143]\n"
    "ds_read_b64_tr_b16 v[28:29], v60 offset:27680\n"
    "v_mfma_f32_16x16x32_bf16 a[140:143], v[56:59], a[236:239], a[140:143]\n"
    "s_waitcnt vmcnt(37) lgkmcnt(5)\n"
    "s_add_u32 m0, s17, 135168\n"
    "ds_read_b64_tr_b16 v[30:31], v60 offset:32288\n"
    "v_mfma_f32_16x16x32_bf16 a[0:3], v[16:19], a[240:243], a[0:3]\n"
    "s_waitcnt vmcnt(33) lgkmcnt(4)\n"
    "buffer_load_dwordx4 v10, s[40:43], s91 offen lds\n"
    "v_mfma_f32_16x16x32_bf16 a[0:3], v[20:23], v[128:131], a[0:3]\n"
    "v_mfma_f32_16x16x32_bf16 a[4:7], v[16:19], a[244:247], a[4:7]\n"
    "v_mfma_f32_16x16x32_bf16 a[4:7], v[20:23], v[132:135], a[4:7]\n"
    "v_mfma_f32_16x16x32_bf16 a[8:11], v[16:19], a[248:251], a[8:11]\n"
    "ds_read_b64_tr_b16 v[32:33], v60 offset:18496\n"
    "v_mfma_f32_16x16x32_bf16 a[8:11], v[20:23], v[136:139], a[8:11]\n"
    "ds_read_b64_tr_b16 v[34:35], v60 offset:23104\n"
    "v_mfma_f32_16x16x32_bf16 a[12:15], v[16:19], a[252:255], a[12:15]\n"
    "ds_read_b64_tr_b16 v[36:37], v60 offset:27712\n"
    "v_mfma_f32_16x16x32_bf16 a[12:15], v[20:23], v[140:143], a[12:15]\n"
    "s_waitcnt lgkmcnt(5)\n"
    "s_mov_b32 s92, 81920\n"
    "ds_read_b64_tr_b16 v[38:39], v60 offset:32320\n"
    "v_mfma_f32_16x16x32_bf16 a[16:19], v[24:27], a[240:243], a[16:19]\n"
    "s_waitcnt lgkmcnt(4)\n"
    "buffer_load_dwordx4 v[220:223], v0, s[20:23], s92 offen offset:3072 sc1\n"
    "v_mfma_f32_16x16x32_bf16 a[16:19], v[28:31], v[128:131], a[16:19]\n"
    "v_mfma_f32_16x16x32_bf16 a[20:23], v[24:27], a[244:247], a[20:23]\n"
    "buffer_load_dwordx4 v[216:219], v0, s[20:23], s92 offen offset:2048 sc1\n"
    "v_mfma_f32_16x16x32_bf16 a[20:23], v[28:31], v[132:135], a[20:23]\n"
    "v_mfma_f32_16x16x32_bf16 a[24:27], v[24:27], a[248:251], a[24:27]\n"
    "ds_read_b64_tr_b16 v[48:49], v60 offset:18528\n"
    "v_mfma_f32_16x16x32_bf16 a[24:27], v[28:31], v[136:139], a[24:27]\n"
    "ds_read_b64_tr_b16 v[50:51], v60 offset:23136\n"
    "v_mfma_f32_16x16x32_bf16 a[28:31], v[24:27], a[252:255], a[28:31]\n"
    "ds_read_b64_tr_b16 v[52:53], v60 offset:27744\n"
    "v_mfma_f32_16x16x32_bf16 a[28:31], v[28:31], v[140:143], a[28:31]\n"
    "s_waitcnt lgkmcnt(5)\n"
    "ds_read_b64_tr_b16 v[54:55], v60 offset:32352\n"
    "v_mfma_f32_16x16x32_bf16 a[32:35], v[32:35], a[240:243], a[32:35]\n"
    "s_waitcnt lgkmcnt(4)\n"
    "buffer_load_dwordx4 v[212:215], v0, s[20:23], s92 offen offset:1024 sc1\n"
    "v_mfma_f32_16x16x32_bf16 a[32:35], v[36:39], v[128:131], a[32:35]\n"
    "v_mfma_f32_16x16x32_bf16 a[36:39], v[32:35], a[244:247], a[36:39]\n"
    "v_mfma_f32_16x16x32_bf16 a[36:39], v[36:39], v[132:135], a[36:39]\n"
    "v_mfma_f32_16x16x32_bf16 a[40:43], v[32:35], a[248:251], a[40:43]\n"
    "ds_read_b64_tr_b16 v[56:57], v60 offset:18560\n"
    "v_mfma_f32_16x16x32_bf16 a[40:43], v[36:39], v[136:139], a[40:43]\n"
    "ds_read_b64_tr_b16 v[58:59], v60 offset:23168\n"
    "v_mfma_f32_16x16x32_bf16 a[44:47], v[32:35], a[252:255], a[44:47]\n"
    "ds_read_b64_tr_b16 v[16:17], v60 offset:27776\n"
    "v_mfma_f32_16x16x32_bf16 a[44:47], v[36:39], v[140:143], a[44:47]\n"
    "s_waitcnt lgkmcnt(5)\n"
    "ds_read_b64_tr_b16 v[18:19], v60 offset:32384\n"
    "v_mfma_f32_16x16x32_bf16 a[48:51], v[48:51], a[240:243], a[48:51]\n"
    "s_waitcnt lgkmcnt(4)\n"
    "buffer_load_dwordx4 v[208:211], v0, s[20:23], s92 offen offset:0 sc1\n"
    "v_mfma_f32_16x16x32_bf16 a[48:51], v[52:55], v[128:131], a[48:51]\n"
    "s_mov_b32 s92, 86016\n"
    "v_mfma_f32_16x16x32_bf16 a[52:55], v[48:51], a[244:247], a[52:55]\n"
    "buffer_load_dwordx4 v[236:239], v0, s[20:23], s92 offen offset:3072 sc1\n"
    "v_mfma_f32_16x16x32_bf16 a[52:55], v[52:55], v[132:135], a[52:55]\n"
    "v_mfma_f32_16x16x32_bf16 a[56:59], v[48:51], a[248:251], a[56:59]\n"
    "ds_read_b64_tr_b16 v[20:21], v60 offset:18592\n"
    "v_mfma_f32_16x16x32_bf16 a[56:59], v[52:55], v[136:139], a[56:59]\n"
    "ds_read_b64_tr_b16 v[22:23], v60 offset:23200\n"
    "v_mfma_f32_16x16x32_bf16 a[60:63], v[48:51], a[252:255], a[60:63]\n"
    "ds_read_b64_tr_b16 v[24:25], v60 offset:27808\n"
    "v_mfma_f32_16x16x32_bf16 a[60:63], v[52:55], v[140:143], a[60:63]\n"
    "s_waitcnt lgkmcnt(5)\n"
    "ds_read_b64_tr_b16 v[26:27], v60 offset:32416\n"
    "v_mfma_f32_16x16x32_bf16 a[64:67], v[56:59], a[240:243], a[64:67]\n"
    "s_waitcnt lgkmcnt(4)\n"
    "buffer_load_dwordx4 v[232:235], v0, s[20:23], s92 offen offset:2048 sc1\n"
    "v_mfma_f32_16x16x32_bf16 a[64:67], v[16:19], v[128:131], a[64:67]\n"
    "v_mfma_f32_16x16x32_bf16 a[68:71], v[56:59], a[244:247], a[68:71]\n"
    "v_mfma_f32_16x16x32_bf16 a[68:71], v[16:19], v[132:135], a[68:71]\n"
    "v_mfma_f32_16x16x32_bf16 a[72:75], v[56:59], a[248:251], a[72:75]\n"
    "ds_read_b64_tr_b16 v[28:29], v60 offset:18624\n"
    "v_mfma_f32_16x16x32_bf16 a[72:75], v[16:19], v[136:139], a[72:75]\n"
    "ds_read_b64_tr_b16 v[30:31], v60 offset:23232\n"
    "v_mfma_f32_16x16x32_bf16 a[76:79], v[56:59], a[252:255], a[76:79]\n"
    "ds_read_b64_tr_b16 v[32:33], v60 offset:27840\n"
    "v_mfma_f32_16x16x32_bf16 a[76:79], v[16:19], v[140:143], a[76:79]\n"
    "s_waitcnt lgkmcnt(5)\n"
    "ds_read_b64_tr_b16 v[34:35], v60 offset:32448\n"
    "v_mfma_f32_16x16x32_bf16 a[80:83], v[20:23], a[240:243], a[80:83]\n"
    "s_waitcnt lgkmcnt(4)\n"
    "buffer_load_dwordx4 v[228:231], v0, s[20:23], s92 offen offset:1024 sc1\n"
    "v_mfma_f32_16x16x32_bf16 a[80:83], v[24:27], v[128:131], a[80:83]\n"
    "v_mfma_f32_16x16x32_bf16 a[84:87], v[20:23], a[244:247], a[84:87]\n"
    "buffer_load_dwordx4 v[224:227], v0, s[20:23], s92 offen offset:0 sc1\n"
    "v_mfma_f32_16x16x32_bf16 a[84:87], v[24:27], v[132:135], a[84:87]\n"
    "v_mfma_f32_16x16x32_bf16 a[88:91], v[20:23], a[248:251], a[88:91]\n"
    "ds_read_b64_tr_b16 v[36:37], v60 offset:18656\n"
    "v_mfma_f32_16x16x32_bf16 a[88:91], v[24:27], v[136:139], a[88:91]\n"
    "ds_read_b64_tr_b16 v[38:39], v60 offset:23264\n"
    "v_mfma_f32_16x16x32_bf16 a[92:95], v[20:23], a[252:255], a[92:95]\n"
    "ds_read_b64_tr_b16 v[48:49], v60 offset:27872\n"
    "v_mfma_f32_16x16x32_bf16 a[92:95], v[24:27], v[140:143], a[92:95]\n"
    "s_waitcnt lgkmcnt(5)\n"
    "s_mov_b32 s92, 90112\n"
    "ds_read_b64_tr_b16 v[50:51], v60 offset:32480\n"
    "v_mfma_f32_16x16x32_bf16 a[96:99], v[28:31], a[240:243], a[96:99]\n"
    "s_waitcnt lgkmcnt(4)\n"
    "buffer_load_dwordx2 v[252:253], v13, s[20:23], s92 offen offset:3072 sc1\n"
    "v_mfma_f32_16x16x32_bf16 a[96:99], v[32:35], v[128:131], a[96:99]\n"
    "buffer_load_dwordx2 v[254:255], v13, s[20:23], s92 offen offset:3584 sc1\n"
    "v_mfma_f32_16x16x32_bf16 a[100:103], v[28:31], a[244:247], a[100:103]\n"
    "v_mfma_f32_16x16x32_bf16 a[100:103], v[32:35], v[132:135], a[100:103]\n"
    "v_mfma_f32_16x16x32_bf16 a[104:107], v[28:31], a[248:251], a[104:107]\n"
    "ds_read_b64_tr_b16 v[52:53], v60 offset:18688\n"
    "v_mfma_f32_16x16x32_bf16 a[104:107], v[32:35], v[136:139], a[104:107]\n"
    "ds_read_b64_tr_b16 v[54:55], v60 offset:23296\n"
    "v_mfma_f32_16x16x32_bf16 a[108:111], v[28:31], a[252:255], a[108:111]\n"
    "ds_read_b64_tr_b16 v[56:57], v60 offset:27904\n"
    "v_mfma_f32_16x16x32_bf16 a[108:111], v[32:35], v[140:143], a[108:111]\n"
    "s_waitcnt lgkmcnt(5)\n"
    "ds_read_b64_tr_b16 v[58:59], v60 offset:32512\n"
    "v_mfma_f32_16x16x32_bf16 a[112:115], v[36:39], a[240:243], a[112:115]\n"
    "s_waitcnt lgkmcnt(4)\n"
    "buffer_load_dwordx2 v[248:249], v13, s[20:23], s92 offen offset:2048 sc1\n"
    "v_mfma_f32_16x16x32_bf16 a[112:115], v[48:51], v[128:131], a[112:115]\n"
    "buffer_load_dwordx2 v[250:251], v13, s[20:23], s92 offen offset:2560 sc1\n"
    "v_mfma_f32_16x16x32_bf16 a[116:119], v[36:39], a[244:247], a[116:119]\n"
    "buffer_load_dwordx2 v[244:245], v13, s[20:23], s92 offen offset:1024 sc1\n"
    "v_mfma_f32_16x16x32_bf16 a[116:119], v[48:51], v[132:135], a[116:119]\n"
    "v_mfma_f32_16x16x32_bf16 a[120:123], v[36:39], a[248:251], a[120:123]\n"
    "buffer_load_dwordx2 v[246:247], v13, s[20:23], s92 offen offset:1536 sc1\n"
    "v_mfma_f32_16x16x32_bf16 a[120:123], v[48:51], v[136:139], a[120:123]\n"
    "v_mfma_f32_16x16x32_bf16 a[124:127], v[36:39], a[252:255], a[124:127]\n"
    "s_waitcnt vmcnt(39)\n"
    "s_barrier\n"
    "ds_read_b64_tr_b16 v[16:17], v61 offset:9216\n"
    "v_mfma_f32_16x16x32_bf16 a[124:127], v[48:51], v[140:143], a[124:127]\n"
    "s_waitcnt lgkmcnt(3)\n"
    "ds_read_b64_tr_b16 v[18:19], v61 offset:13824\n"
    "v_mfma_f32_16x16x32_bf16 a[128:131], v[52:55], a[240:243], a[128:131]\n"
    "s_waitcnt lgkmcnt(2)\n"
    "ds_read_b64_tr_b16 v[20:21], v61 offset:18432\n"
    "v_mfma_f32_16x16x32_bf16 a[128:131], v[56:59], v[128:131], a[128:131]\n"
    "ds_read_b64_tr_b16 v[22:23], v61 offset:23040\n"
    "v_mfma_f32_16x16x32_bf16 a[132:135], v[52:55], a[244:247], a[132:135]\n"
    "buffer_load_dwordx2 v[240:241], v13, s[20:23], s92 offen offset:0 sc1\n"
    "v_mfma_f32_16x16x32_bf16 a[132:135], v[56:59], v[132:135], a[132:135]\n"
    "buffer_load_dwordx2 v[242:243], v13, s[20:23], s92 offen offset:512 sc1\n"
    "v_mfma_f32_16x16x32_bf16 a[136:139], v[52:55], a[248:251], a[136:139]\n"
    "ds_read_b64_tr_b16 v[24:25], v61 offset:9248\n"
    "v_mfma_f32_16x16x32_bf16 a[136:139], v[56:59], v[136:139], a[136:139]\n"
    "ds_read_b64_tr_b16 v[26:27], v61 offset:13856\n"
    "v_mfma_f32_16x16x32_bf16 a[140:143], v[52:55], a[252:255], a[140:143]\n"
    "ds_read_b64_tr_b16 v[28:29], v61 offset:18464\n"
    "v_mfma_f32_16x16x32_bf16 a[140:143], v[56:59], v[140:143], a[140:143]\n"
    "s_waitcnt vmcnt(37) lgkmcnt(5)\n"
    "s_mov_b32 s92, 94208\n"
    "ds_read_b64_tr_b16 v[30:31], v61 offset:23072\n"
    "v_mfma_f32_16x16x32_bf16 a[0:3], v[16:19], v[144:147], a[0:3]\n"
    "s_waitcnt vmcnt(33) lgkmcnt(4)\n"
    "buffer_load_dwordx4 a[156:159], v0, s[20:23], s92 offen offset:3072 sc1\n"
    "v_mfma_f32_16x16x32_bf16 a[0:3], v[20:23], v[160:163], a[0:3]\n"
    "v_mfma_f32_16x16x32_bf16 a[4:7], v[16:19], v[148:151], a[4:7]\n"
    "buffer_load_dwordx4 a[152:155], v0, s[20:23], s92 offen offset:2048 sc1\n"
    "v_mfma_f32_16x16x32_bf16 a[4:7], v[20:23], v[164:167], a[4:7]\n"
    "v_mfma_f32_16x16x32_bf16 a[8:11], v[16:19], v[152:155], a[8:11]\n"
    "ds_read_b64_tr_b16 v[32:33], v61 offset:9280\n"
    "v_mfma_f32_16x16x32_bf16 a[8:11], v[20:23], v[168:171], a[8:11]\n"
    "ds_read_b64_tr_b16 v[34:35], v61 offset:13888\n"
    "v_mfma_f32_16x16x32_bf16 a[12:15], v[16:19], v[156:159], a[12:15]\n"
    "ds_read_b64_tr_b16 v[36:37], v61 offset:18496\n"
    "v_mfma_f32_16x16x32_bf16 a[12:15], v[20:23], v[172:175], a[12:15]\n"
    "s_waitcnt lgkmcnt(5)\n"
    "ds_read_b64_tr_b16 v[38:39], v61 offset:23104\n"
    "v_mfma_f32_16x16x32_bf16 a[16:19], v[24:27], v[144:147], a[16:19]\n"
    "s_waitcnt lgkmcnt(4)\n"
    "buffer_load_dwordx4 a[148:151], v0, s[20:23], s92 offen offset:1024 sc1\n"
    "v_mfma_f32_16x16x32_bf16 a[16:19], v[28:31], v[160:163], a[16:19]\n"
    "v_mfma_f32_16x16x32_bf16 a[20:23], v[24:27], v[148:151], a[20:23]\n"
    "v_mfma_f32_16x16x32_bf16 a[20:23], v[28:31], v[164:167], a[20:23]\n"
    "v_mfma_f32_16x16x32_bf16 a[24:27], v[24:27], v[152:155], a[24:27]\n"
    "ds_read_b64_tr_b16 v[48:49], v61 offset:9312\n"
    "v_mfma_f32_16x16x32_bf16 a[24:27], v[28:31], v[168:171], a[24:27]\n"
    "ds_read_b64_tr_b16 v[50:51], v61 offset:13920\n"
    "v_mfma_f32_16x16x32_bf16 a[28:31], v[24:27], v[156:159], a[28:31]\n"
    "ds_read_b64_tr_b16 v[52:53], v61 offset:18528\n"
    "v_mfma_f32_16x16x32_bf16 a[28:31], v[28:31], v[172:175], a[28:31]\n"
    "s_waitcnt lgkmcnt(5)\n"
    "s_add_u32 s91, s80, 24\n"
    "ds_read_b64_tr_b16 v[54:55], v61 offset:23136\n"
    "v_mfma_f32_16x16x32_bf16 a[32:35], v[32:35], v[144:147], a[32:35]\n"
    "s_waitcnt lgkmcnt(4)\n"
    "s_mul_i32 s91, s91, 36864\n"
    "buffer_load_dwordx4 a[144:147], v0, s[20:23], s92 offen offset:0 sc1\n"
    "v_mfma_f32_16x16x32_bf16 a[32:35], v[36:39], v[160:163], a[32:35]\n"
    "s_add_u32 m0, s17, 53248\n"
    "v_mfma_f32_16x16x32_bf16 a[36:39], v[32:35], v[148:151], a[36:39]\n"
    "buffer_load_dwordx4 v2, s[40:43], s91 offen lds\n"
    "v_mfma_f32_16x16x32_bf16 a[36:39], v[36:39], v[164:167], a[36:39]\n"
    "v_mfma_f32_16x16x32_bf16 a[40:43], v[32:35], v[152:155], a[40:43]\n"
    "ds_read_b64_tr_b16 v[56:57], v61 offset:9344\n"
    "v_mfma_f32_16x16x32_bf16 a[40:43], v[36:39], v[168:171], a[40:43]\n"
    "ds_read_b64_tr_b16 v[58:59], v61 offset:13952\n"
    "v_mfma_f32_16x16x32_bf16 a[44:47], v[32:35], v[156:159], a[44:47]\n"
    "ds_read_b64_tr_b16 v[16:17], v61 offset:18560\n"
    "v_mfma_f32_16x16x32_bf16 a[44:47], v[36:39], v[172:175], a[44:47]\n"
    "s_waitcnt lgkmcnt(5)\n"
    "ds_read_b64_tr_b16 v[18:19], v61 offset:23168\n"
    "v_mfma_f32_16x16x32_bf16 a[48:51], v[48:51], v[144:147], a[48:51]\n"
    "s_waitcnt lgkmcnt(4)\n"
    "buffer_load_dwordx4 v3, s[40:43], s91 offen offset:1024 lds\n"
    "v_mfma_f32_16x16x32_bf16 a[48:51], v[52:55], v[160:163], a[48:51]\n"
    "v_mfma_f32_16x16x32_bf16 a[52:55], v[48:51], v[148:151], a[52:55]\n"
    "v_mfma_f32_16x16x32_bf16 a[52:55], v[52:55], v[164:167], a[52:55]\n"
    "v_mfma_f32_16x16x32_bf16 a[56:59], v[48:51], v[152:155], a[56:59]\n"
    "ds_read_b64_tr_b16 v[20:21], v61 offset:9376\n"
    "v_mfma_f32_16x16x32_bf16 a[56:59], v[52:55], v[168:171], a[56:59]\n"
    "ds_read_b64_tr_b16 v[22:23], v61 offset:13984\n"
    "v_mfma_f32_16x16x32_bf16 a[60:63], v[48:51], v[156:159], a[60:63]\n"
    "ds_read_b64_tr_b16 v[24:25], v61 offset:18592\n"
    "v_mfma_f32_16x16x32_bf16 a[60:63], v[52:55], v[172:175], a[60:63]\n"
    "s_waitcnt lgkmcnt(5)\n"
    "ds_read_b64_tr_b16 v[26:27], v61 offset:23200\n"
    "v_mfma_f32_16x16x32_bf16 a[64:67], v[56:59], v[144:147], a[64:67]\n"
    "s_waitcnt lgkmcnt(4)\n"
    "buffer_load_dwordx4 v4, s[40:43], s91 offen offset:2048 lds\n"
    "v_mfma_f32_16x16x32_bf16 a[64:67], v[16:19], v[160:163], a[64:67]\n"
    "v_mfma_f32_16x16x32_bf16 a[68:71], v[56:59], v[148:151], a[68:71]\n"
    "buffer_load_dwordx4 v5, s[40:43], s91 offen offset:3072 lds\n"
    "v_mfma_f32_16x16x32_bf16 a[68:71], v[16:19], v[164:167], a[68:71]\n"
    "v_mfma_f32_16x16x32_bf16 a[72:75], v[56:59], v[152:155], a[72:75]\n"
    "ds_read_b64_tr_b16 v[28:29], v61 offset:9408\n"
    "v_mfma_f32_16x16x32_bf16 a[72:75], v[16:19], v[168:171], a[72:75]\n"
    "ds_read_b64_tr_b16 v[30:31], v61 offset:14016\n"
    "v_mfma_f32_16x16x32_bf16 a[76:79], v[56:59], v[156:159], a[76:79]\n"
    "ds_read_b64_tr_b16 v[32:33], v61 offset:18624\n"
    "v_mfma_f32_16x16x32_bf16 a[76:79], v[16:19], v[172:175], a[76:79]\n"
    "s_waitcnt lgkmcnt(5)\n"
    "s_add_u32 m0, s17, 57344\n"
    "ds_read_b64_tr_b16 v[34:35], v61 offset:23232\n"
    "v_mfma_f32_16x16x32_bf16 a[80:83], v[20:23], v[144:147], a[80:83]\n"
    "s_waitcnt lgkmcnt(4)\n"
    "buffer_load_dwordx4 v6, s[40:43], s91 offen lds\n"
    "v_mfma_f32_16x16x32_bf16 a[80:83], v[24:27], v[160:163], a[80:83]\n"
    "v_mfma_f32_16x16x32_bf16 a[84:87], v[20:23], v[148:151], a[84:87]\n"
    "v_mfma_f32_16x16x32_bf16 a[84:87], v[24:27], v[164:167], a[84:87]\n"
    "v_mfma_f32_16x16x32_bf16 a[88:91], v[20:23], v[152:155], a[88:91]\n"
    "ds_read_b64_tr_b16 v[36:37], v61 offset:9440\n"
    "v_mfma_f32_16x16x32_bf16 a[88:91], v[24:27], v[168:171], a[88:91]\n"
    "ds_read_b64_tr_b16 v[38:39], v61 offset:14048\n"
    "v_mfma_f32_16x16x32_bf16 a[92:95], v[20:23], v[156:159], a[92:95]\n"
    "ds_read_b64_tr_b16 v[48:49], v61 offset:18656\n"
    "v_mfma_f32_16x16x32_bf16 a[92:95], v[24:27], v[172:175], a[92:95]\n"
    "s_waitcnt lgkmcnt(5)\n"
    "ds_read_b64_tr_b16 v[50:51], v61 offset:23264\n"
    "v_mfma_f32_16x16x32_bf16 a[96:99], v[28:31], v[144:147], a[96:99]\n"
    "s_waitcnt lgkmcnt(4)\n"
    "buffer_load_dwordx4 v7, s[40:43], s91 offen offset:1024 lds\n"
    "v_mfma_f32_16x16x32_bf16 a[96:99], v[32:35], v[160:163], a[96:99]\n"
    "v_mfma_f32_16x16x32_bf16 a[100:103], v[28:31], v[148:151], a[100:103]\n"
    "buffer_load_dwordx4 v8, s[40:43], s91 offen offset:2048 lds\n"
    "v_mfma_f32_16x16x32_bf16 a[100:103], v[32:35], v[164:167], a[100:103]\n"
    "v_mfma_f32_16x16x32_bf16 a[104:107], v[28:31], v[152:155], a[104:107]\n"
    "ds_read_b64_tr_b16 v[52:53], v61 offset:9472\n"
    "v_mfma_f32_16x16x32_bf16 a[104:107], v[32:35], v[168:171], a[104:107]\n"
    "ds_read_b64_tr_b16 v[54:55], v61 offset:14080\n"
    "v_mfma_f32_16x16x32_bf16 a[108:111], v[28:31], v[156:159], a[108:111]\n"
    "ds_read_b64_tr_b16 v[56:57], v61 offset:18688\n"
    "v_mfma_f32_16x16x32_bf16 a[108:111], v[32:35], v[172:175], a[108:111]\n"
    "s_waitcnt lgkmcnt(5)\n"
    "ds_read_b64_tr_b16 v[58:59], v61 offset:23296\n"
    "v_mfma_f32_16x16x32_bf16 a[112:115], v[36:39], v[144:147], a[112:115]\n"
    "s_waitcnt lgkmcnt(4)\n"
    "buffer_load_dwordx4 v9, s[40:43], s91 offen offset:3072 lds\n"
    "v_mfma_f32_16x16x32_bf16 a[112:115], v[48:51], v[160:163], a[112:115]\n"
    "v_mfma_f32_16x16x32_bf16 a[116:119], v[36:39], v[148:151], a[116:119]\n"
    "v_mfma_f32_16x16x32_bf16 a[116:119], v[48:51], v[164:167], a[116:119]\n"
    "v_mfma_f32_16x16x32_bf16 a[120:123], v[36:39], v[152:155], a[120:123]\n"
    "ds_read_b64_tr_b16 v[16:17], v61 offset:27648\n"
    "v_mfma_f32_16x16x32_bf16 a[120:123], v[48:51], v[168:171], a[120:123]\n"
    "ds_read_b64_tr_b16 v[18:19], v61 offset:32256\n"
    "v_mfma_f32_16x16x32_bf16 a[124:127], v[36:39], v[156:159], a[124:127]\n"
    "ds_read_b64_tr_b16 v[20:21], v61 offset:36864\n"
    "v_mfma_f32_16x16x32_bf16 a[124:127], v[48:51], v[172:175], a[124:127]\n"
    "s_waitcnt lgkmcnt(5)\n"
    "s_add_u32 m0, s17, 61440\n"
    "ds_read_b64_tr_b16 v[22:23], v61 offset:41472\n"
    "v_mfma_f32_16x16x32_bf16 a[128:131], v[52:55], v[144:147], a[128:131]\n"
    "s_waitcnt lgkmcnt(4)\n"
    "buffer_load_dwordx4 v10, s[40:43], s91 offen lds\n"
    "v_mfma_f32_16x16x32_bf16 a[128:131], v[56:59], v[160:163], a[128:131]\n"
    "s_mov_b32 s92, 98304\n"
    "v_mfma_f32_16x16x32_bf16 a[132:135], v[52:55], v[148:151], a[132:135]\n"
    "buffer_load_dwordx4 v[76:79], v0, s[20:23], s92 offen offset:3072 sc1\n"
    "v_mfma_f32_16x16x32_bf16 a[132:135], v[56:59], v[164:167], a[132:135]\n"
    "v_mfma_f32_16x16x32_bf16 a[136:139], v[52:55], v[152:155], a[136:139]\n"
    "ds_read_b64_tr_b16 v[24:25], v61 offset:27680\n"
    "v_mfma_f32_16x16x32_bf16 a[136:139], v[56:59], v[168:171], a[136:139]\n"
    "ds_read_b64_tr_b16 v[26:27], v61 offset:32288\n"
    "v_mfma_f32_16x16x32_bf16 a[140:143], v[52:55], v[156:159], a[140:143]\n"
    "ds_read_b64_tr_b16 v[28:29], v61 offset:36896\n"
    "v_mfma_f32_16x16x32_bf16 a[140:143], v[56:59], v[172:175], a[140:143]\n"
    "s_waitcnt vmcnt(43) lgkmcnt(5)\n"
    "ds_read_b64_tr_b16 v[30:31], v61 offset:41504\n"
    "v_mfma_f32_16x16x32_bf16 a[0:3], v[16:19], v[176:179], a[0:3]\n"
    "s_waitcnt vmcnt(39) lgkmcnt(4)\n"
    "buffer_load_dwordx4 v[72:75], v0, s[20:23], s92 offen offset:2048 sc1\n"
    "v_mfma_f32_16x16x32_bf16 a[0:3], v[20:23], v[192:195], a[0:3]\n"
    "v_mfma_f32_16x16x32_bf16 a[4:7], v[16:19], v[180:183], a[4:7]\n"
    "v_mfma_f32_16x16x32_bf16 a[4:7], v[20:23], v[196:199], a[4:7]\n"
    "v_mfma_f32_16x16x32_bf16 a[8:11], v[16:19], v[184:187], a[8:11]\n"
    "ds_read_b64_tr_b16 v[32:33], v61 offset:27712\n"
    "v_mfma_f32_16x16x32_bf16 a[8:11], v[20:23], v[200:203], a[8:11]\n"
    "ds_read_b64_tr_b16 v[34:35], v61 offset:32320\n"
    "v_mfma_f32_16x16x32_bf16 a[12:15], v[16:19], v[188:191], a[12:15]\n"
    "ds_read_b64_tr_b16 v[36:37], v61 offset:36928\n"
    "v_mfma_f32_16x16x32_bf16 a[12:15], v[20:23], v[204:207], a[12:15]\n"
    "s_waitcnt lgkmcnt(5)\n"
    "ds_read_b64_tr_b16 v[38:39], v61 offset:41536\n"
    "v_mfma_f32_16x16x32_bf16 a[16:19], v[24:27], v[176:179], a[16:19]\n"
    "s_waitcnt lgkmcnt(4)\n"
    "buffer_load_dwordx4 v[68:71], v0, s[20:23], s92 offen offset:1024 sc1\n"
    "v_mfma_f32_16x16x32_bf16 a[16:19], v[28:31], v[192:195], a[16:19]\n"
    "v_mfma_f32_16x16x32_bf16 a[20:23], v[24:27], v[180:183], a[20:23]\n"
    "buffer_load_dwordx4 v[64:67], v0, s[20:23], s92 offen offset:0 sc1\n"
    "v_mfma_f32_16x16x32_bf16 a[20:23], v[28:31], v[196:199], a[20:23]\n"
    "v_mfma_f32_16x16x32_bf16 a[24:27], v[24:27], v[184:187], a[24:27]\n"
    "ds_read_b64_tr_b16 v[48:49], v61 offset:27744\n"
    "v_mfma_f32_16x16x32_bf16 a[24:27], v[28:31], v[200:203], a[24:27]\n"
    "ds_read_b64_tr_b16 v[50:51], v61 offset:32352\n"
    "v_mfma_f32_16x16x32_bf16 a[28:31], v[24:27], v[188:191], a[28:31]\n"
    "ds_read_b64_tr_b16 v[52:53], v61 offset:36960\n"
    "v_mfma_f32_16x16x32_bf16 a[28:31], v[28:31], v[204:207], a[28:31]\n"
    "s_waitcnt lgkmcnt(5)\n"
    "s_mov_b32 s92, 102400\n"
    "ds_read_b64_tr_b16 v[54:55], v61 offset:41568\n"
    "v_mfma_f32_16x16x32_bf16 a[32:35], v[32:35], v[176:179], a[32:35]\n"
    "s_waitcnt lgkmcnt(4)\n"
    "buffer_load_dwordx4 v[92:95], v0, s[20:23], s92 offen offset:3072 sc1\n"
    "v_mfma_f32_16x16x32_bf16 a[32:35], v[36:39], v[192:195], a[32:35]\n"
    "v_mfma_f32_16x16x32_bf16 a[36:39], v[32:35], v[180:183], a[36:39]\n"
    "v_mfma_f32_16x16x32_bf16 a[36:39], v[36:39], v[196:199], a[36:39]\n"
    "v_mfma_f32_16x16x32_bf16 a[40:43], v[32:35], v[184:187], a[40:43]\n"
    "ds_read_b64_tr_b16 v[56:57], v61 offset:27776\n"
    "v_mfma_f32_16x16x32_bf16 a[40:43], v[36:39], v[200:203], a[40:43]\n"
    "ds_read_b64_tr_b16 v[58:59], v61 offset:32384\n"
    "v_mfma_f32_16x16x32_bf16 a[44:47], v[32:35], v[188:191], a[44:47]\n"
    "ds_read_b64_tr_b16 v[16:17], v61 offset:36992\n"
    "v_mfma_f32_16x16x32_bf16 a[44:47], v[36:39], v[204:207], a[44:47]\n"
    "s_waitcnt lgkmcnt(5)\n"
    "ds_read_b64_tr_b16 v[18:19], v61 offset:41600\n"
    "v_mfma_f32_16x16x32_bf16 a[48:51], v[48:51], v[176:179], a[48:51]\n"
    "s_waitcnt lgkmcnt(4)\n"
    "buffer_load_dwordx4 v[88:91], v0, s[20:23], s92 offen offset:2048 sc1\n"
    "v_mfma_f32_16x16x32_bf16 a[48:51], v[52:55], v[192:195], a[48:51]\n"
    "v_mfma_f32_16x16x32_bf16 a[52:55], v[48:51], v[180:183], a[52:55]\n"
    "buffer_load_dwordx4 v[84:87], v0, s[20:23], s92 offen offset:1024 sc1\n"
    "v_mfma_f32_16x16x32_bf16 a[52:55], v[52:55], v[196:199], a[52:55]\n"
    "v_mfma_f32_16x16x32_bf16 a[56:59], v[48:51], v[184:187], a[56:59]\n"
    "ds_read_b64_tr_b16 v[20:21], v61 offset:27808\n"
    "v_mfma_f32_16x16x32_bf16 a[56:59], v[52:55], v[200:203], a[56:59]\n"
    "ds_read_b64_tr_b16 v[22:23], v61 offset:32416\n"
    "v_mfma_f32_16x16x32_bf16 a[60:63], v[48:51], v[188:191], a[60:63]\n"
    "ds_read_b64_tr_b16 v[24:25], v61 offset:37024\n"
    "v_mfma_f32_16x16x32_bf16 a[60:63], v[52:55], v[204:207], a[60:63]\n"
    "s_waitcnt lgkmcnt(5)\n"
    "ds_read_b64_tr_b16 v[26:27], v61 offset:41632\n"
    "v_mfma_f32_16x16x32_bf16 a[64:67], v[56:59], v[176:179], a[64:67]\n"
    "s_waitcnt lgkmcnt(4)\n"
    "buffer_load_dwordx4 v[80:83], v0, s[20:23], s92 offen offset:0 sc1\n"
    "v_mfma_f32_16x16x32_bf16 a[64:67], v[16:19], v[192:195], a[64:67]\n"
    "v_mfma_f32_16x16x32_bf16 a[68:71], v[56:59], v[180:183], a[68:71]\n"
    "v_mfma_f32_16x16x32_bf16 a[68:71], v[16:19], v[196:199], a[68:71]\n"
    "v_mfma_f32_16x16x32_bf16 a[72:75], v[56:59], v[184:187], a[72:75]\n"
    "ds_read_b64_tr_b16 v[28:29], v61 offset:27840\n"
    "v_mfma_f32_16x16x32_bf16 a[72:75], v[16:19], v[200:203], a[72:75]\n"
    "ds_read_b64_tr_b16 v[30:31], v61 offset:32448\n"
    "v_mfma_f32_16x16x32_bf16 a[76:79], v[56:59], v[188:191], a[76:79]\n"
    "ds_read_b64_tr_b16 v[32:33], v61 offset:37056\n"
    "v_mfma_f32_16x16x32_bf16 a[76:79], v[16:19], v[204:207], a[76:79]\n"
    "s_waitcnt lgkmcnt(5)\n"
    "s_mov_b32 s92, 106496\n"
    "ds_read_b64_tr_b16 v[34:35], v61 offset:41664\n"
    "v_mfma_f32_16x16x32_bf16 a[80:83], v[20:23], v[176:179], a[80:83]\n"
    "s_waitcnt lgkmcnt(4)\n"
    "buffer_load_dwordx4 a[172:175], v0, s[20:23], s92 offen offset:3072 sc1\n"
    "v_mfma_f32_16x16x32_bf16 a[80:83], v[24:27], v[192:195], a[80:83]\n"
    "v_mfma_f32_16x16x32_bf16 a[84:87], v[20:23], v[180:183], a[84:87]\n"
    "buffer_load_dwordx4 a[168:171], v0, s[20:23], s92 offen offset:2048 sc1\n"
    "v_mfma_f32_16x16x32_bf16 a[84:87], v[24:27], v[196:199], a[84:87]\n"
    "v_mfma_f32_16x16x32_bf16 a[88:91], v[20:23], v[184:187], a[88:91]\n"
    "ds_read_b64_tr_b16 v[36:37], v61 offset:27872\n"
    "v_mfma_f32_16x16x32_bf16 a[88:91], v[24:27], v[200:203], a[88:91]\n"
    "ds_read_b64_tr_b16 v[38:39], v61 offset:32480\n"
    "v_mfma_f32_16x16x32_bf16 a[92:95], v[20:23], v[188:191], a[92:95]\n"
    "ds_read_b64_tr_b16 v[48:49], v61 offset:37088\n"
    "v_mfma_f32_16x16x32_bf16 a[92:95], v[24:27], v[204:207], a[92:95]\n"
    "s_waitcnt lgkmcnt(5)\n"
    "ds_read_b64_tr_b16 v[50:51], v61 offset:41696\n"
    "v_mfma_f32_16x16x32_bf16 a[96:99], v[28:31], v[176:179], a[96:99]\n"
    "s_waitcnt lgkmcnt(4)\n"
    "buffer_load_dwordx4 a[164:167], v0, s[20:23], s92 offen offset:1024 sc1\n"
    "v_mfma_f32_16x16x32_bf16 a[96:99], v[32:35], v[192:195], a[96:99]\n"
    "v_mfma_f32_16x16x32_bf16 a[100:103], v[28:31], v[180:183], a[100:103]\n"
    "v_mfma_f32_16x16x32_bf16 a[100:103], v[32:35], v[196:199], a[100:103]\n"
    "v_mfma_f32_16x16x32_bf16 a[104:107], v[28:31], v[184:187], a[104:107]\n"
    "ds_read_b64_tr_b16 v[52:53], v61 offset:27904\n"
    "v_mfma_f32_16x16x32_bf16 a[104:107], v[32:35], v[200:203], a[104:107]\n"
    "ds_read_b64_tr_b16 v[54:55], v61 offset:32512\n"
    "v_mfma_f32_16x16x32_bf16 a[108:111], v[28:31], v[188:191], a[108:111]\n"
    "ds_read_b64_tr_b16 v[56:57], v61 offset:37120\n"
    "v_mfma_f32_16x16x32_bf16 a[108:111], v[32:35], v[204:207], a[108:111]\n"
    "s_waitcnt lgkmcnt(5)\n"
    "ds_read_b64_tr_b16 v[58:59], v61 offset:41728\n"
    "v_mfma_f32_16x16x32_bf16 a[112:115], v[36:39], v[176:179], a[112:115]\n"
    "s_waitcnt lgkmcnt(4)\n"
    "buffer_load_dwordx4 a[160:163], v0, s[20:23], s92 offen offset:0 sc1\n"
    "v_mfma_f32_16x16x32_bf16 a[112:115], v[48:51], v[192:195], a[112:115]\n"
    "s_mov_b32 s92, 110592\n"
    "v_mfma_f32_16x16x32_bf16 a[116:119], v[36:39], v[180:183], a[116:119]\n"
    "buffer_load_dwordx4 a[188:191], v0, s[20:23], s92 offen offset:3072 sc1\n"
    "v_mfma_f32_16x16x32_bf16 a[116:119], v[48:51], v[196:199], a[116:119]\n"
    "v_mfma_f32_16x16x32_bf16 a[120:123], v[36:39], v[184:187], a[120:123]\n"
    "v_mfma_f32_16x16x32_bf16 a[120:123], v[48:51], v[200:203], a[120:123]\n"
    "v_mfma_f32_16x16x32_bf16 a[124:127], v[36:39], v[188:191], a[124:127]\n"
    "s_waitcnt vmcnt(42) lgkmcnt(0)\n"
    "s_barrier\n"
    "ds_read_b64_tr_b16 v[16:17], v61 offset:46080\n"
    "v_mfma_f32_16x16x32_bf16 a[124:127], v[48:51], v[204:207], a[124:127]\n"
    "s_waitcnt lgkmcnt(3)\n"
    "ds_read_b64_tr_b16 v[18:19], v61 offset:50688\n"
    "v_mfma_f32_16x16x32_bf16 a[128:131], v[52:55], v[176:179], a[128:131]\n"
    "s_waitcnt lgkmcnt(2)\n"
    "ds_read_b64_tr_b16 v[20:21], v61 offset:55296\n"
    "v_mfma_f32_16x16x32_bf16 a[128:131], v[56:59], v[192:195], a[128:131]\n"
    "ds_read_b64_tr_b16 v[22:23], v61 offset:59904\n"
    "v_mfma_f32_16x16x32_bf16 a[132:135], v[52:55], v[180:183], a[132:135]\n"
    "buffer_load_dwordx4 a[184:187], v0, s[20:23], s92 offen offset:2048 sc1\n"
    "v_mfma_f32_16x16x32_bf16 a[132:135], v[56:59], v[196:199], a[132:135]\n"
    "v_mfma_f32_16x16x32_bf16 a[136:139], v[52:55], v[184:187], a[136:139]\n"
    "ds_read_b64_tr_b16 v[24:25], v61 offset:46112\n"
    "v_mfma_f32_16x16x32_bf16 a[136:139], v[56:59], v[200:203], a[136:139]\n"
    "ds_read_b64_tr_b16 v[26:27], v61 offset:50720\n"
    "v_mfma_f32_16x16x32_bf16 a[140:143], v[52:55], v[188:191], a[140:143]\n"
    "ds_read_b64_tr_b16 v[28:29], v61 offset:55328\n"
    "v_mfma_f32_16x16x32_bf16 a[140:143], v[56:59], v[204:207], a[140:143]\n"
    "s_waitcnt vmcnt(39) lgkmcnt(5)\n"
    "ds_read_b64_tr_b16 v[30:31], v61 offset:59936\n"
    "v_mfma_f32_16x16x32_bf16 a[0:3], v[16:19], v[208:211], a[0:3]\n"
    "s_waitcnt vmcnt(35) lgkmcnt(4)\n"
    "buffer_load_dwordx4 a[180:183], v0, s[20:23], s92 offen offset:1024 sc1\n"
    "v_mfma_f32_16x16x32_bf16 a[0:3], v[20:23], v[224:227], a[0:3]\n"
    "v_mfma_f32_16x16x32_bf16 a[4:7], v[16:19], v[212:215], a[4:7]\n"
    "buffer_load_dwordx4 a[176:179], v0, s[20:23], s92 offen offset:0 sc1\n"
    "v_mfma_f32_16x16x32_bf16 a[4:7], v[20:23], v[228:231], a[4:7]\n"
    "v_mfma_f32_16x16x32_bf16 a[8:11], v[16:19], v[216:219], a[8:11]\n"
    "ds_read_b64_tr_b16 v[32:33], v61 offset:46144\n"
    "v_mfma_f32_16x16x32_bf16 a[8:11], v[20:23], v[232:235], a[8:11]\n"
    "s_add_u32 s91, s80, 28\n"
    "ds_read_b64_tr_b16 v[34:35], v61 offset:50752\n"
    "v_mfma_f32_16x16x32_bf16 a[12:15], v[16:19], v[220:223], a[12:15]\n"
    "s_mul_i32 s91, s91, 36864\n"
    "ds_read_b64_tr_b16 v[36:37], v61 offset:55360\n"
    "v_mfma_f32_16x16x32_bf16 a[12:15], v[20:23], v[236:239], a[12:15]\n"
    "s_waitcnt lgkmcnt(5)\n"
    "s_add_u32 m0, s17, 16384\n"
    "ds_read_b64_tr_b16 v[38:39], v61 offset:59968\n"
    "v_mfma_f32_16x16x32_bf16 a[16:19], v[24:27], v[208:211], a[16:19]\n"
    "s_waitcnt lgkmcnt(4)\n"
    "buffer_load_dwordx4 v2, s[40:43], s91 offen lds\n"
    "v_mfma_f32_16x16x32_bf16 a[16:19], v[28:31], v[224:227], a[16:19]\n"
    "v_mfma_f32_16x16x32_bf16 a[20:23], v[24:27], v[212:215], a[20:23]\n"
    "v_mfma_f32_16x16x32_bf16 a[20:23], v[28:31], v[228:231], a[20:23]\n"
    "v_mfma_f32_16x16x32_bf16 a[24:27], v[24:27], v[216:219], a[24:27]\n"
    "ds_read_b64_tr_b16 v[48:49], v61 offset:46176\n"
    "v_mfma_f32_16x16x32_bf16 a[24:27], v[28:31], v[232:235], a[24:27]\n"
    "ds_read_b64_tr_b16 v[50:51], v61 offset:50784\n"
    "v_mfma_f32_16x16x32_bf16 a[28:31], v[24:27], v[220:223], a[28:31]\n"
    "ds_read_b64_tr_b16 v[52:53], v61 offset:55392\n"
    "v_mfma_f32_16x16x32_bf16 a[28:31], v[28:31], v[236:239], a[28:31]\n"
    "s_waitcnt lgkmcnt(5)\n"
    "ds_read_b64_tr_b16 v[54:55], v61 offset:60000\n"
    "v_mfma_f32_16x16x32_bf16 a[32:35], v[32:35], v[208:211], a[32:35]\n"
    "s_waitcnt lgkmcnt(4)\n"
    "buffer_load_dwordx4 v3, s[40:43], s91 offen offset:1024 lds\n"
    "v_mfma_f32_16x16x32_bf16 a[32:35], v[36:39], v[224:227], a[32:35]\n"
    "v_mfma_f32_16x16x32_bf16 a[36:39], v[32:35], v[212:215], a[36:39]\n"
    "buffer_load_dwordx4 v4, s[40:43], s91 offen offset:2048 lds\n"
    "v_mfma_f32_16x16x32_bf16 a[36:39], v[36:39], v[228:231], a[36:39]\n"
    "v_mfma_f32_16x16x32_bf16 a[40:43], v[32:35], v[216:219], a[40:43]\n"
    "ds_read_b64_tr_b16 v[56:57], v61 offset:46208\n"
    "v_mfma_f32_16x16x32_bf16 a[40:43], v[36:39], v[232:235], a[40:43]\n"
    "ds_read_b64_tr_b16 v[58:59], v61 offset:50816\n"
    "v_mfma_f32_16x16x32_bf16 a[44:47], v[32:35], v[220:223], a[44:47]\n"
    "ds_read_b64_tr_b16 v[16:17], v61 offset:55424\n"
    "v_mfma_f32_16x16x32_bf16 a[44:47], v[36:39], v[236:239], a[44:47]\n"
    "s_waitcnt lgkmcnt(5)\n"
    "ds_read_b64_tr_b16 v[18:19], v61 offset:60032\n"
    "v_mfma_f32_16x16x32_bf16 a[48:51], v[48:51], v[208:211], a[48:51]\n"
    "s_waitcnt lgkmcnt(4)\n"
    "buffer_load_dwordx4 v5, s[40:43], s91 offen offset:3072 lds\n"
    "v_mfma_f32_16x16x32_bf16 a[48:51], v[52:55], v[224:227], a[48:51]\n"
    "v_mfma_f32_16x16x32_bf16 a[52:55], v[48:51], v[212:215], a[52:55]\n"
    "v_mfma_f32_16x16x32_bf16 a[52:55], v[52:55], v[228:231], a[52:55]\n"
    "v_mfma_f32_16x16x32_bf16 a[56:59], v[48:51], v[216:219], a[56:59]\n"
    "ds_read_b64_tr_b16 v[20:21], v61 offset:46240\n"
    "v_mfma_f32_16x16x32_bf16 a[56:59], v[52:55], v[232:235], a[56:59]\n"
    "ds_read_b64_tr_b16 v[22:23], v61 offset:50848\n"
    "v_mfma_f32_16x16x32_bf16 a[60:63], v[48:51], v[220:223], a[60:63]\n"
    "ds_read_b64_tr_b16 v[24:25], v61 offset:55456\n"
    "v_mfma_f32_16x16x32_bf16 a[60:63], v[52:55], v[236:239], a[60:63]\n"
    "s_waitcnt lgkmcnt(5)\n"
    "s_add_u32 m0, s17, 20480\n"
    "ds_read_b64_tr_b16 v[26:27], v61 offset:60064\n"
    "v_mfma_f32_16x16x32_bf16 a[64:67], v[56:59], v[208:211], a[64:67]\n"
    "s_waitcnt lgkmcnt(4)\n"
    "buffer_load_dwordx4 v6, s[40:43], s91 offen lds\n"
    "v_mfma_f32_16x16x32_bf16 a[64:67], v[16:19], v[224:227], a[64:67]\n"
    "v_mfma_f32_16x16x32_bf16 a[68:71], v[56:59], v[212:215], a[68:71]\n"
    "buffer_load_dwordx4 v7, s[40:43], s91 offen offset:1024 lds\n"
    "v_mfma_f32_16x16x32_bf16 a[68:71], v[16:19], v[228:231], a[68:71]\n"
    "v_mfma_f32_16x16x32_bf16 a[72:75], v[56:59], v[216:219], a[72:75]\n"
    "ds_read_b64_tr_b16 v[28:29], v61 offset:46272\n"
    "v_mfma_f32_16x16x32_bf16 a[72:75], v[16:19], v[232:235], a[72:75]\n"
    "ds_read_b64_tr_b16 v[30:31], v61 offset:50880\n"
    "v_mfma_f32_16x16x32_bf16 a[76:79], v[56:59], v[220:223], a[76:79]\n"
    "ds_read_b64_tr_b16 v[32:33], v61 offset:55488\n"
    "v_mfma_f32_16x16x32_bf16 a[76:79], v[16:19], v[236:239], a[76:79]\n"
    "s_waitcnt lgkmcnt(5)\n"
    "ds_read_b64_tr_b16 v[34:35], v61 offset:60096\n"
    "v_mfma_f32_16x16x32_bf16 a[80:83], v[20:23], v[208:211], a[80:83]\n"
    "s_waitcnt lgkmcnt(4)\n"
    "buffer_load_dwordx4 v8, s[40:43], s91 offen offset:2048 lds\n"
    "v_mfma_f32_16x16x32_bf16 a[80:83], v[24:27], v[224:227], a[80:83]\n"
    "v_mfma_f32_16x16x32_bf16 a[84:87], v[20:23], v[212:215], a[84:87]\n"
    "v_mfma_f32_16x16x32_bf16 a[84:87], v[24:27], v[228:231], a[84:87]\n"
    "v_mfma_f32_16x16x32_bf16 a[88:91], v[20:23], v[216:219], a[88:91]\n"
    "ds_read_b64_tr_b16 v[36:37], v61 offset:46304\n"
    "v_mfma_f32_16x16x32_bf16 a[88:91], v[24:27], v[232:235], a[88:91]\n"
    "ds_read_b64_tr_b16 v[38:39], v61 offset:50912\n"
    "v_mfma_f32_16x16x32_bf16 a[92:95], v[20:23], v[220:223], a[92:95]\n"
    "ds_read_b64_tr_b16 v[48:49], v61 offset:55520\n"
    "v_mfma_f32_16x16x32_bf16 a[92:95], v[24:27], v[236:239], a[92:95]\n"
    "s_waitcnt lgkmcnt(5)\n"
    "ds_read_b64_tr_b16 v[50:51], v61 offset:60128\n"
    "v_mfma_f32_16x16x32_bf16 a[96:99], v[28:31], v[208:211], a[96:99]\n"
    "s_waitcnt lgkmcnt(4)\n"
    "buffer_load_dwordx4 v9, s[40:43], s91 offen offset:3072 lds\n"
    "v_mfma_f32_16x16x32_bf16 a[96:99], v[32:35], v[224:227], a[96:99]\n"
    "s_add_u32 m0, s17, 24576\n"
    "v_mfma_f32_16x16x32_bf16 a[100:103], v[28:31], v[212:215], a[100:103]\n"
    "buffer_load_dwordx4 v10, s[40:43], s91 offen lds\n"
    "v_mfma_f32_16x16x32_bf16 a[100:103], v[32:35], v[228:231], a[100:103]\n"
    "v_mfma_f32_16x16x32_bf16 a[104:107], v[28:31], v[216:219], a[104:107]\n"
    "ds_read_b64_tr_b16 v[52:53], v61 offset:46336\n"
    "v_mfma_f32_16x16x32_bf16 a[104:107], v[32:35], v[232:235], a[104:107]\n"
    "s_add_u32 s91, s80, 32\n"
    "ds_read_b64_tr_b16 v[54:55], v61 offset:50944\n"
    "v_mfma_f32_16x16x32_bf16 a[108:111], v[28:31], v[220:223], a[108:111]\n"
    "s_mul_i32 s91, s91, 36864\n"
    "ds_read_b64_tr_b16 v[56:57], v61 offset:55552\n"
    "v_mfma_f32_16x16x32_bf16 a[108:111], v[32:35], v[236:239], a[108:111]\n"
    "s_waitcnt lgkmcnt(5)\n"
    "s_add_u32 m0, s17, 90112\n"
    "ds_read_b64_tr_b16 v[58:59], v61 offset:60160\n"
    "v_mfma_f32_16x16x32_bf16 a[112:115], v[36:39], v[208:211], a[112:115]\n"
    "s_waitcnt lgkmcnt(4)\n"
    "buffer_load_dwordx4 v2, s[40:43], s91 offen lds\n"
    "v_mfma_f32_16x16x32_bf16 a[112:115], v[48:51], v[224:227], a[112:115]\n"
    "v_mfma_f32_16x16x32_bf16 a[116:119], v[36:39], v[212:215], a[116:119]\n"
    "v_mfma_f32_16x16x32_bf16 a[116:119], v[48:51], v[228:231], a[116:119]\n"
    "v_mfma_f32_16x16x32_bf16 a[120:123], v[36:39], v[216:219], a[120:123]\n"
    "ds_read_b64_tr_b16 v[16:17], v62 offset:0\n"
    "v_mfma_f32_16x16x32_bf16 a[120:123], v[48:51], v[232:235], a[120:123]\n"
    "ds_read_b64_tr_b16 v[18:19], v62 offset:4608\n"
    "v_mfma_f32_16x16x32_bf16 a[124:127], v[36:39], v[220:223], a[124:127]\n"
    "ds_read_b64_tr_b16 v[20:21], v62 offset:9216\n"
    "v_mfma_f32_16x16x32_bf16 a[124:127], v[48:51], v[236:239], a[124:127]\n"
    "s_waitcnt lgkmcnt(5)\n"
    "ds_read_b64_tr_b16 v[22:23], v62 offset:13824\n"
    "v_mfma_f32_16x16x32_bf16 a[128:131], v[52:55], v[208:211], a[128:131]\n"
    "s_waitcnt lgkmcnt(4)\n"
    "buffer_load_dwordx4 v3, s[40:43], s91 offen offset:1024 lds\n"
    "v_mfma_f32_16x16x32_bf16 a[128:131], v[56:59], v[224:227], a[128:131]\n"
    "v_mfma_f32_16x16x32_bf16 a[132:135], v[52:55], v[212:215], a[132:135]\n"
    "buffer_load_dwordx4 v4, s[40:43], s91 offen offset:2048 lds\n"
    "v_mfma_f32_16x16x32_bf16 a[132:135], v[56:59], v[228:231], a[132:135]\n"
    "v_mfma_f32_16x16x32_bf16 a[136:139], v[52:55], v[216:219], a[136:139]\n"
    "ds_read_b64_tr_b16 v[24:25], v62 offset:32\n"
    "v_mfma_f32_16x16x32_bf16 a[136:139], v[56:59], v[232:235], a[136:139]\n"
    "ds_read_b64_tr_b16 v[26:27], v62 offset:4640\n"
    "v_mfma_f32_16x16x32_bf16 a[140:143], v[52:55], v[220:223], a[140:143]\n"
    "ds_read_b64_tr_b16 v[28:29], v62 offset:9248\n"
    "v_mfma_f32_16x16x32_bf16 a[140:143], v[56:59], v[236:239], a[140:143]\n"
    "s_waitcnt vmcnt(41) lgkmcnt(5)\n"
    "ds_read_b64_tr_b16 v[30:31], v62 offset:13856\n"
    "v_mfma_f32_16x16x32_bf16 a[0:3], v[16:19], v[240:243], a[0:3]\n"
    "s_waitcnt vmcnt(37) lgkmcnt(4)\n"
    "buffer_load_dwordx4 v5, s[40:43], s91 offen offset:3072 lds\n"
    "v_mfma_f32_16x16x32_bf16 a[0:3], v[20:23], a[144:147], a[0:3]\n"
    "v_mfma_f32_16x16x32_bf16 a[4:7], v[16:19], v[244:247], a[4:7]\n"
    "v_mfma_f32_16x16x32_bf16 a[4:7], v[20:23], a[148:151], a[4:7]\n"
    "v_mfma_f32_16x16x32_bf16 a[8:11], v[16:19], v[248:251], a[8:11]\n"
    "ds_read_b64_tr_b16 v[32:33], v62 offset:64\n"
    "v_mfma_f32_16x16x32_bf16 a[8:11], v[20:23], a[152:155], a[8:11]\n"
    "ds_read_b64_tr_b16 v[34:35], v62 offset:4672\n"
    "v_mfma_f32_16x16x32_bf16 a[12:15], v[16:19], v[252:255], a[12:15]\n"
    "ds_read_b64_tr_b16 v[36:37], v62 offset:9280\n"
    "v_mfma_f32_16x16x32_bf16 a[12:15], v[20:23], a[156:159], a[12:15]\n"
    "s_waitcnt lgkmcnt(5)\n"
    "s_add_u32 m0, s17, 94208\n"
    "ds_read_b64_tr_b16 v[38:39], v62 offset:13888\n"
    "v_mfma_f32_16x16x32_bf16 a[16:19], v[24:27], v[240:243], a[16:19]\n"
    "s_waitcnt lgkmcnt(4)\n"
    "buffer_load_dwordx4 v6, s[40:43], s91 offen lds\n"
    "v_mfma_f32_16x16x32_bf16 a[16:19], v[28:31], a[144:147], a[16:19]\n"
    "v_mfma_f32_16x16x32_bf16 a[20:23], v[24:27], v[244:247], a[20:23]\n"
    "buffer_load_dwordx4 v7, s[40:43], s91 offen offset:1024 lds\n"
    "v_mfma_f32_16x16x32_bf16 a[20:23], v[28:31], a[148:151], a[20:23]\n"
    "v_mfma_f32_16x16x32_bf16 a[24:27], v[24:27], v[248:251], a[24:27]\n"
    "ds_read_b64_tr_b16 v[48:49], v62 offset:96\n"
    "v_mfma_f32_16x16x32_bf16 a[24:27], v[28:31], a[152:155], a[24:27]\n"
    "ds_read_b64_tr_b16 v[50:51], v62 offset:4704\n"
    "v_mfma_f32_16x16x32_bf16 a[28:31], v[24:27], v[252:255], a[28:31]\n"
    "ds_read_b64_tr_b16 v[52:53], v62 offset:9312\n"
    "v_mfma_f32_16x16x32_bf16 a[28:31], v[28:31], a[156:159], a[28:31]\n"
    "s_waitcnt lgkmcnt(5)\n"
    "ds_read_b64_tr_b16 v[54:55], v62 offset:13920\n"
    "v_mfma_f32_16x16x32_bf16 a[32:35], v[32:35], v[240:243], a[32:35]\n"
    "s_waitcnt lgkmcnt(4)\n"
    "buffer_load_dwordx4 v8, s[40:43], s91 offen offset:2048 lds\n"
    "v_mfma_f32_16x16x32_bf16 a[32:35], v[36:39], a[144:147], a[32:35]\n"
    "v_mfma_f32_16x16x32_bf16 a[36:39], v[32:35], v[244:247], a[36:39]\n"
    "v_mfma_f32_16x16x32_bf16 a[36:39], v[36:39], a[148:151], a[36:39]\n"
    "v_mfma_f32_16x16x32_bf16 a[40:43], v[32:35], v[248:251], a[40:43]\n"
    "ds_read_b64_tr_b16 v[56:57], v62 offset:128\n"
    "v_mfma_f32_16x16x32_bf16 a[40:43], v[36:39], a[152:155], a[40:43]\n"
    "ds_read_b64_tr_b16 v[58:59], v62 offset:4736\n"
    "v_mfma_f32_16x16x32_bf16 a[44:47], v[32:35], v[252:255], a[44:47]\n"
    "ds_read_b64_tr_b16 v[16:17], v62 offset:9344\n"
    "v_mfma_f32_16x16x32_bf16 a[44:47], v[36:39], a[156:159], a[44:47]\n"
    "s_waitcnt lgkmcnt(5)\n"
    "ds_read_b64_tr_b16 v[18:19], v62 offset:13952\n"
    "v_mfma_f32_16x16x32_bf16 a[48:51], v[48:51], v[240:243], a[48:51]\n"
    "s_waitcnt lgkmcnt(4)\n"
    "buffer_load_dwordx4 v9, s[40:43], s91 offen offset:3072 lds\n"
    "v_mfma_f32_16x16x32_bf16 a[48:51], v[52:55], a[144:147], a[48:51]\n"
    "s_add_u32 m0, s17, 98304\n"
    "v_mfma_f32_16x16x32_bf16 a[52:55], v[48:51], v[244:247], a[52:55]\n"
    "buffer_load_dwordx4 v10, s[40:43], s91 offen lds\n"
    "v_mfma_f32_16x16x32_bf16 a[52:55], v[52:55], a[148:151], a[52:55]\n"
    "v_mfma_f32_16x16x32_bf16 a[56:59], v[48:51], v[248:251], a[56:59]\n"
    "ds_read_b64_tr_b16 v[20:21], v62 offset:160\n"
    "v_mfma_f32_16x16x32_bf16 a[56:59], v[52:55], a[152:155], a[56:59]\n"
    "ds_read_b64_tr_b16 v[22:23], v62 offset:4768\n"
    "v_mfma_f32_16x16x32_bf16 a[60:63], v[48:51], v[252:255], a[60:63]\n"
    "ds_read_b64_tr_b16 v[24:25], v62 offset:9376\n"
    "v_mfma_f32_16x16x32_bf16 a[60:63], v[52:55], a[156:159], a[60:63]\n"
    "s_waitcnt lgkmcnt(5)\n"
    "s_mov_b32 s92, 114688\n"
    "ds_read_b64_tr_b16 v[26:27], v62 offset:13984\n"
    "v_mfma_f32_16x16x32_bf16 a[64:67], v[56:59], v[240:243], a[64:67]\n"
    "s_waitcnt lgkmcnt(4)\n"
    "buffer_load_dwordx4 a[204:207], v0, s[20:23], s92 offen offset:3072 sc1\n"
    "v_mfma_f32_16x16x32_bf16 a[64:67], v[16:19], a[144:147], a[64:67]\n"
    "v_mfma_f32_16x16x32_bf16 a[68:71], v[56:59], v[244:247], a[68:71]\n"
    "v_mfma_f32_16x16x32_bf16 a[68:71], v[16:19], a[148:151], a[68:71]\n"
    "v_mfma_f32_16x16x32_bf16 a[72:75], v[56:59], v[248:251], a[72:75]\n"
    "ds_read_b64_tr_b16 v[28:29], v62 offset:192\n"
    "v_mfma_f32_16x16x32_bf16 a[72:75], v[16:19], a[152:155], a[72:75]\n"
    "ds_read_b64_tr_b16 v[30:31], v62 offset:4800\n"
    "v_mfma_f32_16x16x32_bf16 a[76:79], v[56:59], v[252:255], a[76:79]\n"
    "ds_read_b64_tr_b16 v[32:33], v62 offset:9408\n"
    "v_mfma_f32_16x16x32_bf16 a[76:79], v[16:19], a[156:159], a[76:79]\n"
    "s_waitcnt lgkmcnt(5)\n"
    "ds_read_b64_tr_b16 v[34:35], v62 offset:14016\n"
    "v_mfma_f32_16x16x32_bf16 a[80:83], v[20:23], v[240:243], a[80:83]\n"
    "s_waitcnt lgkmcnt(4)\n"
    "buffer_load_dwordx4 a[200:203], v0, s[20:23], s92 offen offset:2048 sc1\n"
    "v_mfma_f32_16x16x32_bf16 a[80:83], v[24:27], a[144:147], a[80:83]\n"
    "v_mfma_f32_16x16x32_bf16 a[84:87], v[20:23], v[244:247], a[84:87]\n"
    "buffer_load_dwordx4 a[196:199], v0, s[20:23], s92 offen offset:1024 sc1\n"
    "v_mfma_f32_16x16x32_bf16 a[84:87], v[24:27], a[148:151], a[84:87]\n"
    "v_mfma_f32_16x16x32_bf16 a[88:91], v[20:23], v[248:251], a[88:91]\n"
    "ds_read_b64_tr_b16 v[36:37], v62 offset:224\n"
    "v_mfma_f32_16x16x32_bf16 a[88:91], v[24:27], a[152:155], a[88:91]\n"
    "ds_read_b64_tr_b16 v[38:39], v62 offset:4832\n"
    "v_mfma_f32_16x16x32_bf16 a[92:95], v[20:23], v[252:255], a[92:95]\n"
    "ds_read_b64_tr_b16 v[48:49], v62 offset:9440\n"
    "v_mfma_f32_16x16x32_bf16 a[92:95], v[24:27], a[156:159], a[92:95]\n"
    "s_waitcnt lgkmcnt(5)\n"
    "ds_read_b64_tr_b16 v[50:51], v62 offset:14048\n"
    "v_mfma_f32_16x16x32_bf16 a[96:99], v[28:31], v[240:243], a[96:99]\n"
    "s_waitcnt lgkmcnt(4)\n"
    "buffer_load_dwordx4 a[192:195], v0, s[20:23], s92 offen offset:0 sc1\n"
    "v_mfma_f32_16x16x32_bf16 a[96:99], v[32:35], a[144:147], a[96:99]\n"
    "v_mfma_f32_16x16x32_bf16 a[100:103], v[28:31], v[244:247], a[100:103]\n"
    "v_mfma_f32_16x16x32_bf16 a[100:103], v[32:35], a[148:151], a[100:103]\n"
    "v_mfma_f32_16x16x32_bf16 a[104:107], v[28:31], v[248:251], a[104:107]\n"
    "ds_read_b64_tr_b16 v[52:53], v62 offset:256\n"
    "v_mfma_f32_16x16x32_bf16 a[104:107], v[32:35], a[152:155], a[104:107]\n"
    "ds_read_b64_tr_b16 v[54:55], v62 offset:4864\n"
    "v_mfma_f32_16x16x32_bf16 a[108:111], v[28:31], v[252:255], a[108:111]\n"
    "ds_read_b64_tr_b16 v[56:57], v62 offset:9472\n"
    "v_mfma_f32_16x16x32_bf16 a[108:111], v[32:35], a[156:159], a[108:111]\n"
    "s_waitcnt lgkmcnt(5)\n"
    "s_mov_b32 s92, 118784\n"
    "ds_read_b64_tr_b16 v[58:59], v62 offset:14080\n"
    "v_mfma_f32_16x16x32_bf16 a[112:115], v[36:39], v[240:243], a[112:115]\n"
    "s_waitcnt lgkmcnt(4)\n"
    "buffer_load_dwordx4 a[220:223], v0, s[20:23], s92 offen offset:3072 sc1\n"
    "v_mfma_f32_16x16x32_bf16 a[112:115], v[48:51], a[144:147], a[112:115]\n"
    "v_mfma_f32_16x16x32_bf16 a[116:119], v[36:39], v[244:247], a[116:119]\n"
    "buffer_load_dwordx4 a[216:219], v0, s[20:23], s92 offen offset:2048 sc1\n"
    "v_mfma_f32_16x16x32_bf16 a[116:119], v[48:51], a[148:151], a[116:119]\n"
    "v_mfma_f32_16x16x32_bf16 a[120:123], v[36:39], v[248:251], a[120:123]\n"
    "v_mfma_f32_16x16x32_bf16 a[120:123], v[48:51], a[152:155], a[120:123]\n"
    "v_mfma_f32_16x16x32_bf16 a[124:127], v[36:39], v[252:255], a[124:127]\n"
    "s_waitcnt vmcnt(40)\n"
    "s_barrier\n"
    "ds_read_b64_tr_b16 v[16:17], v60 offset:36864\n"
    "v_mfma_f32_16x16x32_bf16 a[124:127], v[48:51], a[156:159], a[124:127]\n"
    "s_waitcnt lgkmcnt(3)\n"
    "ds_read_b64_tr_b16 v[18:19], v60 offset:41472\n"
    "v_mfma_f32_16x16x32_bf16 a[128:131], v[52:55], v[240:243], a[128:131]\n"
    "s_waitcnt lgkmcnt(2)\n"
    "ds_read_b64_tr_b16 v[20:21], v60 offset:46080\n"
    "v_mfma_f32_16x16x32_bf16 a[128:131], v[56:59], a[144:147], a[128:131]\n"
    "ds_read_b64_tr_b16 v[22:23], v60 offset:50688\n"
    "v_mfma_f32_16x16x32_bf16 a[132:135], v[52:55], v[244:247], a[132:135]\n"
    "buffer_load_dwordx4 a[212:215], v0, s[20:23], s92 offen offset:1024 sc1\n"
    "v_mfma_f32_16x16x32_bf16 a[132:135], v[56:59], a[148:151], a[132:135]\n"
    "v_mfma_f32_16x16x32_bf16 a[136:139], v[52:55], v[248:251], a[136:139]\n"
    "ds_read_b64_tr_b16 v[24:25], v60 offset:36896\n"
    "v_mfma_f32_16x16x32_bf16 a[136:139], v[56:59], a[152:155], a[136:139]\n"
    "ds_read_b64_tr_b16 v[26:27], v60 offset:41504\n"
    "v_mfma_f32_16x16x32_bf16 a[140:143], v[52:55], v[252:255], a[140:143]\n"
    "ds_read_b64_tr_b16 v[28:29], v60 offset:46112\n"
    "v_mfma_f32_16x16x32_bf16 a[140:143], v[56:59], a[156:159], a[140:143]\n"
    "s_waitcnt vmcnt(37) lgkmcnt(5)\n"
    "ds_read_b64_tr_b16 v[30:31], v60 offset:50720\n"
    "v_mfma_f32_16x16x32_bf16 a[0:3], v[16:19], v[64:67], a[0:3]\n"
    "s_waitcnt vmcnt(33) lgkmcnt(4)\n"
    "buffer_load_dwordx4 a[208:211], v0, s[20:23], s92 offen offset:0 sc1\n"
    "v_mfma_f32_16x16x32_bf16 a[0:3], v[20:23], v[80:83], a[0:3]\n"
    "s_mov_b32 s92, 122880\n"
    "v_mfma_f32_16x16x32_bf16 a[4:7], v[16:19], v[68:71], a[4:7]\n"
    "buffer_load_dwordx4 a[236:239], v0, s[20:23], s92 offen offset:3072 sc1\n"
    "v_mfma_f32_16x16x32_bf16 a[4:7], v[20:23], v[84:87], a[4:7]\n"
    "v_mfma_f32_16x16x32_bf16 a[8:11], v[16:19], v[72:75], a[8:11]\n"
    "ds_read_b64_tr_b16 v[32:33], v60 offset:36928\n"
    "v_mfma_f32_16x16x32_bf16 a[8:11], v[20:23], v[88:91], a[8:11]\n"
    "ds_read_b64_tr_b16 v[34:35], v60 offset:41536\n"
    "v_mfma_f32_16x16x32_bf16 a[12:15], v[16:19], v[76:79], a[12:15]\n"
    "ds_read_b64_tr_b16 v[36:37], v60 offset:46144\n"
    "v_mfma_f32_16x16x32_bf16 a[12:15], v[20:23], v[92:95], a[12:15]\n"
    "s_waitcnt lgkmcnt(5)\n"
    "ds_read_b64_tr_b16 v[38:39], v60 offset:50752\n"
    "v_mfma_f32_16x16x32_bf16 a[16:19], v[24:27], v[64:67], a[16:19]\n"
    "s_waitcnt lgkmcnt(4)\n"
    "buffer_load_dwordx4 a[232:235], v0, s[20:23], s92 offen offset:2048 sc1\n"
    "v_mfma_f32_16x16x32_bf16 a[16:19], v[28:31], v[80:83], a[16:19]\n"
    "v_mfma_f32_16x16x32_bf16 a[20:23], v[24:27], v[68:71], a[20:23]\n"
    "v_mfma_f32_16x16x32_bf16 a[20:23], v[28:31], v[84:87], a[20:23]\n"
    "v_mfma_f32_16x16x32_bf16 a[24:27], v[24:27], v[72:75], a[24:27]\n"
    "ds_read_b64_tr_b16 v[48:49], v60 offset:36960\n"
    "v_mfma_f32_16x16x32_bf16 a[24:27], v[28:31], v[88:91], a[24:27]\n"
    "ds_read_b64_tr_b16 v[50:51], v60 offset:41568\n"
    "v_mfma_f32_16x16x32_bf16 a[28:31], v[24:27], v[76:79], a[28:31]\n"
    "ds_read_b64_tr_b16 v[52:53], v60 offset:46176\n"
    "v_mfma_f32_16x16x32_bf16 a[28:31], v[28:31], v[92:95], a[28:31]\n"
    "s_waitcnt lgkmcnt(5)\n"
    "ds_read_b64_tr_b16 v[54:55], v60 offset:50784\n"
    "v_mfma_f32_16x16x32_bf16 a[32:35], v[32:35], v[64:67], a[32:35]\n"
    "s_waitcnt lgkmcnt(4)\n"
    "buffer_load_dwordx4 a[228:231], v0, s[20:23], s92 offen offset:1024 sc1\n"
    "v_mfma_f32_16x16x32_bf16 a[32:35], v[36:39], v[80:83], a[32:35]\n"
    "v_mfma_f32_16x16x32_bf16 a[36:39], v[32:35], v[68:71], a[36:39]\n"
    "buffer_load_dwordx4 a[224:227], v0, s[20:23], s92 offen offset:0 sc1\n"
    "v_mfma_f32_16x16x32_bf16 a[36:39], v[36:39], v[84:87], a[36:39]\n"
    "v_mfma_f32_16x16x32_bf16 a[40:43], v[32:35], v[72:75], a[40:43]\n"
    "ds_read_b64_tr_b16 v[56:57], v60 offset:36992\n"
    "v_mfma_f32_16x16x32_bf16 a[40:43], v[36:39], v[88:91], a[40:43]\n"
    "ds_read_b64_tr_b16 v[58:59], v60 offset:41600\n"
    "v_mfma_f32_16x16x32_bf16 a[44:47], v[32:35], v[76:79], a[44:47]\n"
    "ds_read_b64_tr_b16 v[16:17], v60 offset:46208\n"
    "v_mfma_f32_16x16x32_bf16 a[44:47], v[36:39], v[92:95], a[44:47]\n"
    "s_waitcnt lgkmcnt(5)\n"
    "s_mov_b32 s92, 126976\n"
    "ds_read_b64_tr_b16 v[18:19], v60 offset:50816\n"
    "v_mfma_f32_16x16x32_bf16 a[48:51], v[48:51], v[64:67], a[48:51]\n"
    "s_waitcnt lgkmcnt(4)\n"
    "buffer_load_dwordx2 a[252:253], v13, s[20:23], s92 offen offset:3072 sc1\n"
    "v_mfma_f32_16x16x32_bf16 a[48:51], v[52:55], v[80:83], a[48:51]\n"
    "buffer_load_dwordx2 a[254:255], v13, s[20:23], s92 offen offset:3584 sc1\n"
    "v_mfma_f32_16x16x32_bf16 a[52:55], v[48:51], v[68:71], a[52:55]\n"
    "v_mfma_f32_16x16x32_bf16 a[52:55], v[52:55], v[84:87], a[52:55]\n"
    "v_mfma_f32_16x16x32_bf16 a[56:59], v[48:51], v[72:75], a[56:59]\n"
    "ds_read_b64_tr_b16 v[20:21], v60 offset:37024\n"
    "v_mfma_f32_16x16x32_bf16 a[56:59], v[52:55], v[88:91], a[56:59]\n"
    "ds_read_b64_tr_b16 v[22:23], v60 offset:41632\n"
    "v_mfma_f32_16x16x32_bf16 a[60:63], v[48:51], v[76:79], a[60:63]\n"
    "ds_read_b64_tr_b16 v[24:25], v60 offset:46240\n"
    "v_mfma_f32_16x16x32_bf16 a[60:63], v[52:55], v[92:95], a[60:63]\n"
    "s_waitcnt lgkmcnt(5)\n"
    "ds_read_b64_tr_b16 v[26:27], v60 offset:50848\n"
    "v_mfma_f32_16x16x32_bf16 a[64:67], v[56:59], v[64:67], a[64:67]\n"
    "s_waitcnt lgkmcnt(4)\n"
    "buffer_load_dwordx2 a[248:249], v13, s[20:23], s92 offen offset:2048 sc1\n"
    "v_mfma_f32_16x16x32_bf16 a[64:67], v[16:19], v[80:83], a[64:67]\n"
    "buffer_load_dwordx2 a[250:251], v13, s[20:23], s92 offen offset:2560 sc1\n"
    "v_mfma_f32_16x16x32_bf16 a[68:71], v[56:59], v[68:71], a[68:71]\n"
    "buffer_load_dwordx2 a[244:245], v13, s[20:23], s92 offen offset:1024 sc1\n"
    "v_mfma_f32_16x16x32_bf16 a[68:71], v[16:19], v[84:87], a[68:71]\n"
    "v_mfma_f32_16x16x32_bf16 a[72:75], v[56:59], v[72:75], a[72:75]\n"
    "ds_read_b64_tr_b16 v[28:29], v60 offset:37056\n"
    "v_mfma_f32_16x16x32_bf16 a[72:75], v[16:19], v[88:91], a[72:75]\n"
    "ds_read_b64_tr_b16 v[30:31], v60 offset:41664\n"
    "v_mfma_f32_16x16x32_bf16 a[76:79], v[56:59], v[76:79], a[76:79]\n"
    "ds_read_b64_tr_b16 v[32:33], v60 offset:46272\n"
    "v_mfma_f32_16x16x32_bf16 a[76:79], v[16:19], v[92:95], a[76:79]\n"
    "s_waitcnt lgkmcnt(5)\n"
    "ds_read_b64_tr_b16 v[34:35], v60 offset:50880\n"
    "v_mfma_f32_16x16x32_bf16 a[80:83], v[20:23], v[64:67], a[80:83]\n"
    "s_waitcnt lgkmcnt(4)\n"
    "buffer_load_dwordx2 a[246:247], v13, s[20:23], s92 offen offset:1536 sc1\n"
    "v_mfma_f32_16x16x32_bf16 a[80:83], v[24:27], v[80:83], a[80:83]\n"
    "buffer_load_dwordx2 a[240:241], v13, s[20:23], s92 offen offset:0 sc1\n"
    "v_mfma_f32_16x16x32_bf16 a[84:87], v[20:23], v[68:71], a[84:87]\n"
    "buffer_load_dwordx2 a[242:243], v13, s[20:23], s92 offen offset:512 sc1\n"
    "v_mfma_f32_16x16x32_bf16 a[84:87], v[24:27], v[84:87], a[84:87]\n"
    "v_mfma_f32_16x16x32_bf16 a[88:91], v[20:23], v[72:75], a[88:91]\n"
    "ds_read_b64_tr_b16 v[36:37], v60 offset:37088\n"
    "v_mfma_f32_16x16x32_bf16 a[88:91], v[24:27], v[88:91], a[88:91]\n"
    "ds_read_b64_tr_b16 v[38:39], v60 offset:41696\n"
    "v_mfma_f32_16x16x32_bf16 a[92:95], v[20:23], v[76:79], a[92:95]\n"
    "ds_read_b64_tr_b16 v[48:49], v60 offset:46304\n"
    "v_mfma_f32_16x16x32_bf16 a[92:95], v[24:27], v[92:95], a[92:95]\n"
    "s_waitcnt lgkmcnt(5)\n"
    "s_mov_b32 s92, 131072\n"
    "ds_read_b64_tr_b16 v[50:51], v60 offset:50912\n"
    "v_mfma_f32_16x16x32_bf16 a[96:99], v[28:31], v[64:67], a[96:99]\n"
    "s_waitcnt lgkmcnt(4)\n"
    "buffer_load_dwordx4 v[140:143], v0, s[20:23], s92 offen offset:3072 sc1\n"
    "v_mfma_f32_16x16x32_bf16 a[96:99], v[32:35], v[80:83], a[96:99]\n"
    "v_mfma_f32_16x16x32_bf16 a[100:103], v[28:31], v[68:71], a[100:103]\n"
    "buffer_load_dwordx4 v[136:139], v0, s[20:23], s92 offen offset:2048 sc1\n"
    "v_mfma_f32_16x16x32_bf16 a[100:103], v[32:35], v[84:87], a[100:103]\n"
    "v_mfma_f32_16x16x32_bf16 a[104:107], v[28:31], v[72:75], a[104:107]\n"
    "ds_read_b64_tr_b16 v[52:53], v60 offset:37120\n"
    "v_mfma_f32_16x16x32_bf16 a[104:107], v[32:35], v[88:91], a[104:107]\n"
    "ds_read_b64_tr_b16 v[54:55], v60 offset:41728\n"
    "v_mfma_f32_16x16x32_bf16 a[108:111], v[28:31], v[76:79], a[108:111]\n"
    "ds_read_b64_tr_b16 v[56:57], v60 offset:46336\n"
    "v_mfma_f32_16x16x32_bf16 a[108:111], v[32:35], v[92:95], a[108:111]\n"
    "s_waitcnt lgkmcnt(5)\n"
    "ds_read_b64_tr_b16 v[58:59], v60 offset:50944\n"
    "v_mfma_f32_16x16x32_bf16 a[112:115], v[36:39], v[64:67], a[112:115]\n"
    "s_waitcnt lgkmcnt(4)\n"
    "buffer_load_dwordx4 v[132:135], v0, s[20:23], s92 offen offset:1024 sc1\n"
    "v_mfma_f32_16x16x32_bf16 a[112:115], v[48:51], v[80:83], a[112:115]\n"
    "v_mfma_f32_16x16x32_bf16 a[116:119], v[36:39], v[68:71], a[116:119]\n"
    "v_mfma_f32_16x16x32_bf16 a[116:119], v[48:51], v[84:87], a[116:119]\n"
    "v_mfma_f32_16x16x32_bf16 a[120:123], v[36:39], v[72:75], a[120:123]\n"
    "ds_read_b64_tr_b16 v[16:17], v60 offset:55296\n"
    "v_mfma_f32_16x16x32_bf16 a[120:123], v[48:51], v[88:91], a[120:123]\n"
    "ds_read_b64_tr_b16 v[18:19], v60 offset:59904\n"
    "v_mfma_f32_16x16x32_bf16 a[124:127], v[36:39], v[76:79], a[124:127]\n"
    "ds_read_b64_tr_b16 v[20:21], v61 offset:0\n"
    "v_mfma_f32_16x16x32_bf16 a[124:127], v[48:51], v[92:95], a[124:127]\n"
    "s_waitcnt lgkmcnt(5)\n"
    "ds_read_b64_tr_b16 v[22:23], v61 offset:4608\n"
    "v_mfma_f32_16x16x32_bf16 a[128:131], v[52:55], v[64:67], a[128:131]\n"
    "s_waitcnt lgkmcnt(4)\n"
    "buffer_load_dwordx4 v[128:131], v0, s[20:23], s92 offen offset:0 sc1\n"
    "v_mfma_f32_16x16x32_bf16 a[128:131], v[56:59], v[80:83], a[128:131]\n"
    "s_mov_b32 s92, 135168\n"
    "v_mfma_f32_16x16x32_bf16 a[132:135], v[52:55], v[68:71], a[132:135]\n"
    "buffer_load_dwordx4 v[156:159], v0, s[20:23], s92 offen offset:3072 sc1\n"
    "v_mfma_f32_16x16x32_bf16 a[132:135], v[56:59], v[84:87], a[132:135]\n"
    "v_mfma_f32_16x16x32_bf16 a[136:139], v[52:55], v[72:75], a[136:139]\n"
    "ds_read_b64_tr_b16 v[24:25], v60 offset:55328\n"
    "v_mfma_f32_16x16x32_bf16 a[136:139], v[56:59], v[88:91], a[136:139]\n"
    "ds_read_b64_tr_b16 v[26:27], v60 offset:59936\n"
    "v_mfma_f32_16x16x32_bf16 a[140:143], v[52:55], v[76:79], a[140:143]\n"
    "ds_read_b64_tr_b16 v[28:29], v61 offset:32\n"
    "v_mfma_f32_16x16x32_bf16 a[140:143], v[56:59], v[92:95], a[140:143]\n"
    "s_waitcnt vmcnt(47) lgkmcnt(5)\n"
    "ds_read_b64_tr_b16 v[30:31], v61 offset:4640\n"
    "v_mfma_f32_16x16x32_bf16 a[0:3], v[16:19], a[160:163], a[0:3]\n"
    "s_waitcnt vmcnt(43) lgkmcnt(4)\n"
    "buffer_load_dwordx4 v[152:155], v0, s[20:23], s92 offen offset:2048 sc1\n"
    "v_mfma_f32_16x16x32_bf16 a[0:3], v[20:23], a[176:179], a[0:3]\n"
    "v_mfma_f32_16x16x32_bf16 a[4:7], v[16:19], a[164:167], a[4:7]\n"
    "v_mfma_f32_16x16x32_bf16 a[4:7], v[20:23], a[180:183], a[4:7]\n"
    "v_mfma_f32_16x16x32_bf16 a[8:11], v[16:19], a[168:171], a[8:11]\n"
    "ds_read_b64_tr_b16 v[32:33], v60 offset:55360\n"
    "v_mfma_f32_16x16x32_bf16 a[8:11], v[20:23], a[184:187], a[8:11]\n"
    "ds_read_b64_tr_b16 v[34:35], v60 offset:59968\n"
    "v_mfma_f32_16x16x32_bf16 a[12:15], v[16:19], a[172:175], a[12:15]\n"
    "ds_read_b64_tr_b16 v[36:37], v61 offset:64\n"
    "v_mfma_f32_16x16x32_bf16 a[12:15], v[20:23], a[188:191], a[12:15]\n"
    "s_waitcnt lgkmcnt(5)\n"
    "ds_read_b64_tr_b16 v[38:39], v61 offset:4672\n"
    "v_mfma_f32_16x16x32_bf16 a[16:19], v[24:27], a[160:163], a[16:19]\n"
    "s_waitcnt lgkmcnt(4)\n"
    "buffer_load_dwordx4 v[148:151], v0, s[20:23], s92 offen offset:1024 sc1\n"
    "v_mfma_f32_16x16x32_bf16 a[16:19], v[28:31], a[176:179], a[16:19]\n"
    "v_mfma_f32_16x16x32_bf16 a[20:23], v[24:27], a[164:167], a[20:23]\n"
    "buffer_load_dwordx4 v[144:147], v0, s[20:23], s92 offen offset:0 sc1\n"
    "v_mfma_f32_16x16x32_bf16 a[20:23], v[28:31], a[180:183], a[20:23]\n"
    "v_mfma_f32_16x16x32_bf16 a[24:27], v[24:27], a[168:171], a[24:27]\n"
    "s_lshr_b32 s61, s7, 1\n"
    "s_cmp_eq_u32 s61, s80\n"
    "s_cselect_b32 s60, 0x40000000, 0\n"
    "s_cselect_b32 s49, 0, 126976\n"
    "s_or_b32 s14, s14, s60\n"
    "s_lshl_b32 s56, 1, s7\n"
    "s_xor_b32 s56, s56, 0xff\n"
    "s_and_b32 s58, s56, 0x55\n"
    "s_mov_b32 s57, 0\n"
    "s_mov_b32 s59, 0\n"
    "s_mul_i32 s50, s80, 3072\n"
    "s_xor_b32 s52, s61, 0\n"
    "s_mul_i32 s52, s52, 18432\n"
    "s_xor_b32 s53, s61, 1\n"
    "s_mul_i32 s53, s53, 18432\n"
    "s_xor_b32 s54, s61, 2\n"
    "s_mul_i32 s54, s54, 18432\n"
    "s_xor_b32 s55, s61, 3\n"
    "s_mul_i32 s55, s55, 18432\n"
    "s_lshl_b32 s51, s37, 2\n"
    "s_add_u32 s51, s51, s80\n"
    "s_cmp_eq_u32 s51, 3\n"
    "s_addc_u32 s39, s7, 0\n"
    "s_lshl_b32 s51, s51, 2\n"
    "s_lshr_b32 s51, 0x33102210, s51\n"
    "s_and_b32 s51, s51, 3\n"
    "s_add_u32 s38, s95, s51\n"
    "s_add_u32 s38, s38, 6\n"
    "s_mul_i32 s39, s39, 24\n"
    "s_add_u32 s39, s39, s51\n"
    "s_mul_i32 s39, s39, 3072\n"
    "ds_read_b64_tr_b16 v[48:49], v60 offset:55392\n"
    "v_lshrrev_b32 v124, 8, v0\n"
    "v_mfma_f32_16x16x32_bf16 a[24:27], v[28:31], a[184:187], a[24:27]\n"
    "ds_read_b64_tr_b16 v[50:51], v60 offset:60000\n"
    "v_bfe_u32 v125, v0, 6, 2\n"
    "v_mfma_f32_16x16x32_bf16 a[28:31], v[24:27], a[172:175], a[28:31]\n"
    "ds_read_b64_tr_b16 v[52:53], v61 offset:96\n"
    "v_cmp_eq_u32 vcc, v124, v125\n"
    "v_mfma_f32_16x16x32_bf16 a[28:31], v[28:31], a[188:191], a[28:31]\n"
    "s_waitcnt lgkmcnt(5)\n"
    "s_mov_b32 s92, 139264\n"
    "ds_read_b64_tr_b16 v[54:55], v61 offset:4704\n"
    "v_and_b32 v124, 16, v0\n"
    "v_mfma_f32_16x16x32_bf16 a[32:35], v[32:35], a[160:163], a[32:35]\n"
    "s_waitcnt lgkmcnt(4)\n"
    "buffer_load_dwordx4 v[172:175], v0, s[20:23], s92 offen offset:3072 sc1\n"
    "v_mov_b32 v125, 0x3f80\n"
    "v_mfma_f32_16x16x32_bf16 a[32:35], v[36:39], a[176:179], a[32:35]\n"
    "v_lshlrev_b32 v124, v124, v125\n"
    "v_cndmask_b32 v124, 0, v124, vcc\n"
    "v_mfma_f32_16x16x32_bf16 a[36:39], v[32:35], a[164:167], a[36:39]\n"
    "v_and_b32 v125, 32, v0\n"
    "v_cmp_eq_u32 vcc, 0, v125\n"
    "v_mfma_f32_16x16x32_bf16 a[36:39], v[36:39], a[180:183], a[36:39]\n"
    "s_nop 0\n"
    "v_cndmask_b32 v40, 0, v124, vcc\n"
    "v_cndmask_b32 v41, v124, 0, vcc\n"
    "v_mfma_f32_16x16x32_bf16 a[40:43], v[32:35], a[168:171], a[40:43]\n"
    "ds_read_b64_tr_b16 v[56:57], v60 offset:55424\n"
    "v_mov_b32 v42, 0\n"
    "v_mfma_f32_16x16x32_bf16 a[40:43], v[36:39], a[184:187], a[40:43]\n"
    "ds_read_b64_tr_b16 v[58:59], v60 offset:60032\n"
    "v_mov_b32 v43, 0\n"
    "v_mfma_f32_16x16x32_bf16 a[44:47], v[32:35], a[172:175], a[44:47]\n"
    "ds_read_b64_tr_b16 v[16:17], v61 offset:128\n"
    "v_mov_b32 v44, v40\n"
    "v_mfma_f32_16x16x32_bf16 a[44:47], v[36:39], a[188:191], a[44:47]\n"
    "s_waitcnt lgkmcnt(5)\n"
    "ds_read_b64_tr_b16 v[18:19], v61 offset:4736\n"
    "v_mov_b32 v45, v41\n"
    "v_mfma_f32_16x16x32_bf16 a[48:51], v[48:51], a[160:163], a[48:51]\n"
    "s_waitcnt lgkmcnt(4)\n"
    "buffer_load_dwordx4 v[168:171], v0, s[20:23], s92 offen offset:2048 sc1\n"
    "v_lshlrev_b32 v1, 2, v1\n"
    "v_mfma_f32_16x16x32_bf16 a[48:51], v[52:55], a[176:179], a[48:51]\n"
    "v_add_u32 v9, s49, v0\n"
    "v_add_u32 v8, s50, v0\n"
    "v_mfma_f32_16x16x32_bf16 a[52:55], v[48:51], a[164:167], a[52:55]\n"
    "buffer_load_dwordx4 v[164:167], v0, s[20:23], s92 offen offset:1024 sc1\n"
    "v_mfma_f32_16x16x32_bf16 a[52:55], v[52:55], a[180:183], a[52:55]\n"
    "v_mfma_f32_16x16x32_bf16 a[56:59], v[48:51], a[168:171], a[56:59]\n"
    "ds_read_b64_tr_b16 v[20:21], v60 offset:55456\n"
    "v_mfma_f32_16x16x32_bf16 a[56:59], v[52:55], a[184:187], a[56:59]\n"
    "ds_read_b64_tr_b16 v[22:23], v60 offset:60064\n"
    "v_mfma_f32_16x16x32_bf16 a[60:63], v[48:51], a[172:175], a[60:63]\n"
    "ds_read_b64_tr_b16 v[24:25], v61 offset:160\n"
    "v_mfma_f32_16x16x32_bf16 a[60:63], v[52:55], a[188:191], a[60:63]\n"
    "s_waitcnt lgkmcnt(5)\n"
    "ds_read_b64_tr_b16 v[26:27], v61 offset:4768\n"
    "v_mfma_f32_16x16x32_bf16 a[64:67], v[56:59], a[160:163], a[64:67]\n"
    "s_waitcnt lgkmcnt(4)\n"
    "buffer_load_dwordx4 v[160:163], v0, s[20:23], s92 offen offset:0 sc1\n"
    "v_mfma_f32_16x16x32_bf16 a[64:67], v[16:19], a[176:179], a[64:67]\n"
    "v_mfma_f32_16x16x32_bf16 a[68:71], v[56:59], a[164:167], a[68:71]\n"
    "v_mfma_f32_16x16x32_bf16 a[68:71], v[16:19], a[180:183], a[68:71]\n"
    "v_mfma_f32_16x16x32_bf16 a[72:75], v[56:59], a[168:171], a[72:75]\n"
    "ds_read_b64_tr_b16 v[28:29], v60 offset:55488\n"
    "v_mfma_f32_16x16x32_bf16 a[72:75], v[16:19], a[184:187], a[72:75]\n"
    "ds_read_b64_tr_b16 v[30:31], v60 offset:60096\n"
    "v_mfma_f32_16x16x32_bf16 a[76:79], v[56:59], a[172:175], a[76:79]\n"
    "ds_read_b64_tr_b16 v[32:33], v61 offset:192\n"
    "v_mfma_f32_16x16x32_bf16 a[76:79], v[16:19], a[188:191], a[76:79]\n"
    "s_waitcnt lgkmcnt(5)\n"
    "s_mov_b32 s92, 143360\n"
    "ds_read_b64_tr_b16 v[34:35], v61 offset:4800\n"
    "v_mfma_f32_16x16x32_bf16 a[80:83], v[20:23], a[160:163], a[80:83]\n"
    "s_waitcnt lgkmcnt(4)\n"
    "buffer_load_dwordx4 v[188:191], v0, s[20:23], s92 offen offset:3072 sc1\n"
    "v_mfma_f32_16x16x32_bf16 a[80:83], v[24:27], a[176:179], a[80:83]\n"
    "v_mfma_f32_16x16x32_bf16 a[84:87], v[20:23], a[164:167], a[84:87]\n"
    "buffer_load_dwordx4 v[184:187], v0, s[20:23], s92 offen offset:2048 sc1\n"
    "v_mfma_f32_16x16x32_bf16 a[84:87], v[24:27], a[180:183], a[84:87]\n"
    "v_mfma_f32_16x16x32_bf16 a[88:91], v[20:23], a[168:171], a[88:91]\n"
    "ds_read_b64_tr_b16 v[36:37], v60 offset:55520\n"
    "v_mfma_f32_16x16x32_bf16 a[88:91], v[24:27], a[184:187], a[88:91]\n"
    "ds_read_b64_tr_b16 v[38:39], v60 offset:60128\n"
    "v_mfma_f32_16x16x32_bf16 a[92:95], v[20:23], a[172:175], a[92:95]\n"
    "ds_read_b64_tr_b16 v[48:49], v61 offset:224\n"
    "v_mfma_f32_16x16x32_bf16 a[92:95], v[24:27], a[188:191], a[92:95]\n"
    "s_waitcnt lgkmcnt(5)\n"
    "ds_read_b64_tr_b16 v[50:51], v61 offset:4832\n"
    "v_mfma_f32_16x16x32_bf16 a[96:99], v[28:31], a[160:163], a[96:99]\n"
    "s_waitcnt lgkmcnt(4)\n"
    "buffer_load_dwordx4 v[180:183], v0, s[20:23], s92 offen offset:1024 sc1\n"
    "v_mfma_f32_16x16x32_bf16 a[96:99], v[32:35], a[176:179], a[96:99]\n"
    "v_mfma_f32_16x16x32_bf16 a[100:103], v[28:31], a[164:167], a[100:103]\n"
    "v_mfma_f32_16x16x32_bf16 a[100:103], v[32:35], a[180:183], a[100:103]\n"
    "v_mfma_f32_16x16x32_bf16 a[104:107], v[28:31], a[168:171], a[104:107]\n"
    "ds_read_b64_tr_b16 v[52:53], v60 offset:55552\n"
    "v_mfma_f32_16x16x32_bf16 a[104:107], v[32:35], a[184:187], a[104:107]\n"
    "ds_read_b64_tr_b16 v[54:55], v60 offset:60160\n"
    "v_mfma_f32_16x16x32_bf16 a[108:111], v[28:31], a[172:175], a[108:111]\n"
    "ds_read_b64_tr_b16 v[56:57], v61 offset:256\n"
    "v_mfma_f32_16x16x32_bf16 a[108:111], v[32:35], a[188:191], a[108:111]\n"
    "s_waitcnt lgkmcnt(5)\n"
    "ds_read_b64_tr_b16 v[58:59], v61 offset:4864\n"
    "v_mfma_f32_16x16x32_bf16 a[112:115], v[36:39], a[160:163], a[112:115]\n"
    "s_waitcnt lgkmcnt(4)\n"
    "buffer_load_dwordx4 v[176:179], v0, s[20:23], s92 offen offset:0 sc1\n"
    "v_mfma_f32_16x16x32_bf16 a[112:115], v[48:51], a[176:179], a[112:115]\n"
    "v_mfma_f32_16x16x32_bf16 a[116:119], v[36:39], a[164:167], a[116:119]\n"
    "v_mfma_f32_16x16x32_bf16 a[116:119], v[48:51], a[180:183], a[116:119]\n"
    "v_mfma_f32_16x16x32_bf16 a[120:123], v[36:39], a[168:171], a[120:123]\n"
    "v_mfma_f32_16x16x32_bf16 a[120:123], v[48:51], a[184:187], a[120:123]\n"
    "v_mfma_f32_16x16x32_bf16 a[124:127], v[36:39], a[172:175], a[124:127]\n"
    "s_waitcnt vmcnt(36)\n"
    "s_barrier\n"
    "ds_read_b64_tr_b16 v[16:17], v60 offset:0\n"
    "v_mfma_f32_16x16x32_bf16 a[124:127], v[48:51], a[188:191], a[124:127]\n"
    "s_waitcnt lgkmcnt(3)\n"
    "ds_read_b64_tr_b16 v[18:19], v60 offset:4608\n"
    "v_mfma_f32_16x16x32_bf16 a[128:131], v[52:55], a[160:163], a[128:131]\n"
    "s_waitcnt lgkmcnt(2)\n"
    "ds_read_b64_tr_b16 v[20:21], v60 offset:9216\n"
    "v_mfma_f32_16x16x32_bf16 a[128:131], v[56:59], a[176:179], a[128:131]\n"
    "ds_read_b64_tr_b16 v[22:23], v60 offset:13824\n"
    "v_mfma_f32_16x16x32_bf16 a[132:135], v[52:55], a[164:167], a[132:135]\n"
    "v_mfma_f32_16x16x32_bf16 a[132:135], v[56:59], a[180:183], a[132:135]\n"
    "v_mfma_f32_16x16x32_bf16 a[136:139], v[52:55], a[168:171], a[136:139]\n"
    "ds_read_b64_tr_b16 v[24:25], v60 offset:18432\n"
    "v_mfma_f32_16x16x32_bf16 a[136:139], v[56:59], a[184:187], a[136:139]\n"
    "ds_read_b64_tr_b16 v[26:27], v60 offset:23040\n"
    "v_mfma_f32_16x16x32_bf16 a[140:143], v[52:55], a[172:175], a[140:143]\n"
    "ds_read_b64_tr_b16 v[28:29], v60 offset:27648\n"
    "v_mfma_f32_16x16x32_bf16 a[140:143], v[56:59], a[188:191], a[140:143]\n"
    "s_waitcnt vmcnt(32) lgkmcnt(5)\n"
    "ds_read_b64_tr_b16 v[30:31], v60 offset:32256\n"
    "v_mfma_f32_16x16x32_bf16 a[0:3], v[16:19], a[192:195], a[0:3]\n"
    "s_waitcnt vmcnt(28) lgkmcnt(4)\n"
    "v_mfma_f32_16x16x32_bf16 a[0:3], v[20:23], a[208:211], a[0:3]\n"
    "v_mfma_f32_16x16x32_bf16 a[4:7], v[16:19], a[196:199], a[4:7]\n"
    "v_bfe_u32 v12, v0, 4, 4\n"
    "v_mul_u32_u24 v12, 1152, v12\n"
    "v_mfma_f32_16x16x32_bf16 a[4:7], v[20:23], a[212:215], a[4:7]\n"
    "v_lshrrev_b32 v63, 8, v0\n"
    "v_lshl_add_u32 v12, v63, 3, v12\n"
    "v_mfma_f32_16x16x32_bf16 a[8:11], v[16:19], a[200:203], a[8:11]\n"
    "ds_read_b64_tr_b16 v[32:33], v61 offset:9216\n"
    "v_add_u32 v63, 18432, v12\n"
    "v_mfma_f32_16x16x32_bf16 a[8:11], v[20:23], a[216:219], a[8:11]\n"
    "ds_read_b64_tr_b16 v[34:35], v61 offset:13824\n"
    "v_mfma_f32_16x16x32_bf16 a[12:15], v[16:19], a[204:207], a[12:15]\n"
    "ds_read_b64_tr_b16 v[36:37], v61 offset:18432\n"
    "v_mfma_f32_16x16x32_bf16 a[12:15], v[20:23], a[220:223], a[12:15]\n"
    "s_waitcnt vmcnt(24) lgkmcnt(5)\n"
    "ds_read_b64_tr_b16 v[38:39], v61 offset:23040\n"
    "v_mfma_f32_16x16x32_bf16 a[0:3], v[24:27], a[224:227], a[0:3]\n"
    "s_waitcnt vmcnt(16) lgkmcnt(4)\n"
    "v_mfma_f32_16x16x32_bf16 a[0:3], v[28:31], a[240:243], a[0:3]\n"
    "v_mfma_f32_16x16x32_bf16 a[4:7], v[24:27], a[228:231], a[4:7]\n"
    "v_mfma_f32_16x16x32_bf16 a[4:7], v[28:31], a[244:247], a[4:7]\n"
    "v_mfma_f32_16x16x32_bf16 a[8:11], v[24:27], a[232:235], a[8:11]\n"
    "ds_read_b64_tr_b16 v[48:49], v61 offset:27648\n"
    "v_mfma_f32_16x16x32_bf16 a[8:11], v[28:31], a[248:251], a[8:11]\n"
    "ds_read_b64_tr_b16 v[50:51], v61 offset:32256\n"
    "v_mfma_f32_16x16x32_bf16 a[12:15], v[24:27], a[236:239], a[12:15]\n"
    "ds_read_b64_tr_b16 v[52:53], v61 offset:36864\n"
    "v_mfma_f32_16x16x32_bf16 a[12:15], v[28:31], a[252:255], a[12:15]\n"
    "s_waitcnt vmcnt(12) lgkmcnt(5)\n"
    "ds_read_b64_tr_b16 v[54:55], v61 offset:41472\n"
    "v_mfma_f32_16x16x32_bf16 a[0:3], v[32:35], v[128:131], a[0:3]\n"
    "s_waitcnt vmcnt(8) lgkmcnt(4)\n"
    "buffer_load_dwordx2 v[112:113], v12, s[24:27], 0 offen offset:0\n"
    "v_mfma_f32_16x16x32_bf16 a[0:3], v[36:39], v[144:147], a[0:3]\n"
    "v_mfma_f32_16x16x32_bf16 a[4:7], v[32:35], v[132:135], a[4:7]\n"
    "v_mfma_f32_16x16x32_bf16 a[4:7], v[36:39], v[148:151], a[4:7]\n"
    "v_mfma_f32_16x16x32_bf16 a[8:11], v[32:35], v[136:139], a[8:11]\n"
    "ds_read_b64_tr_b16 v[56:57], v60 offset:32\n"
    "v_mfma_f32_16x16x32_bf16 a[8:11], v[36:39], v[152:155], a[8:11]\n"
    "ds_read_b64_tr_b16 v[58:59], v60 offset:4640\n"
    "v_mfma_f32_16x16x32_bf16 a[12:15], v[32:35], v[140:143], a[12:15]\n"
    "ds_read_b64_tr_b16 v[16:17], v60 offset:9248\n"
    "v_mfma_f32_16x16x32_bf16 a[12:15], v[36:39], v[156:159], a[12:15]\n"
    "s_waitcnt vmcnt(5) lgkmcnt(5)\n"
    "ds_read_b64_tr_b16 v[18:19], v60 offset:13856\n"
    "v_mfma_f32_16x16x32_bf16 a[0:3], v[48:51], v[160:163], a[0:3]\n"
    "s_waitcnt vmcnt(1) lgkmcnt(4)\n"
    "buffer_load_dwordx2 v[114:115], v63, s[24:27], 0 offen offset:0\n"
    "v_mfma_f32_16x16x32_bf16 a[0:3], v[52:55], v[176:179], a[0:3]\n"
    "v_mfma_f32_16x16x32_bf16 a[4:7], v[48:51], v[164:167], a[4:7]\n"
    "v_mfma_f32_16x16x32_bf16 a[4:7], v[52:55], v[180:183], a[4:7]\n"
    "v_mfma_f32_16x16x32_bf16 a[8:11], v[48:51], v[168:171], a[8:11]\n"
    "ds_read_b64_tr_b16 v[20:21], v60 offset:18464\n"
    "v_mfma_f32_16x16x32_bf16 a[8:11], v[52:55], v[184:187], a[8:11]\n"
    "ds_read_b64_tr_b16 v[22:23], v60 offset:23072\n"
    "v_mfma_f32_16x16x32_bf16 a[12:15], v[48:51], v[172:175], a[12:15]\n"
    "ds_read_b64_tr_b16 v[24:25], v60 offset:27680\n"
    "v_mfma_f32_16x16x32_bf16 a[12:15], v[52:55], v[188:191], a[12:15]\n"
    "s_waitcnt lgkmcnt(5)\n"
    "ds_read_b64_tr_b16 v[26:27], v60 offset:32288\n"
    "v_mfma_f32_16x16x32_bf16 a[16:19], v[56:59], a[192:195], a[16:19]\n"
    "s_waitcnt lgkmcnt(4)\n"
    "buffer_load_dwordx2 v[116:117], v12, s[24:27], 0 offen offset:32\n"
    "v_mfma_f32_16x16x32_bf16 a[16:19], v[16:19], a[208:211], a[16:19]\n"
    "v_mfma_f32_16x16x32_bf16 a[20:23], v[56:59], a[196:199], a[20:23]\n"
    "v_accvgpr_read_b32 v96, a0\n"
    "v_accvgpr_read_b32 v97, a1\n"
    "v_mfma_f32_16x16x32_bf16 a[20:23], v[16:19], a[212:215], a[20:23]\n"
    "v_accvgpr_read_b32 v98, a2\n"
    "v_accvgpr_read_b32 v99, a3\n"
    "v_mfma_f32_16x16x32_bf16 a[24:27], v[56:59], a[200:203], a[24:27]\n"
    "ds_read_b64_tr_b16 v[28:29], v61 offset:9248\n"
    "v_accvgpr_read_b32 v100, a4\n"
    "v_mfma_f32_16x16x32_bf16 a[24:27], v[16:19], a[216:219], a[24:27]\n"
    "ds_read_b64_tr_b16 v[30:31], v61 offset:13856\n"
    "v_accvgpr_read_b32 v101, a5\n"
    "v_mfma_f32_16x16x32_bf16 a[28:31], v[56:59], a[204:207], a[28:31]\n"
    "ds_read_b64_tr_b16 v[32:33], v61 offset:18464\n"
    "v_accvgpr_read_b32 v102, a6\n"
    "v_mfma_f32_16x16x32_bf16 a[28:31], v[16:19], a[220:223], a[28:31]\n"
    "s_waitcnt lgkmcnt(5)\n"
    "ds_read_b64_tr_b16 v[34:35], v61 offset:23072\n"
    "v_accvgpr_read_b32 v103, a7\n"
    "v_mfma_f32_16x16x32_bf16 a[16:19], v[20:23], a[224:227], a[16:19]\n"
    "s_waitcnt lgkmcnt(4)\n"
    "buffer_load_dwordx2 v[118:119], v63, s[24:27], 0 offen offset:32\n"
    "v_cvt_pk_bf16_f32 v96, v96, v97\n"
    "v_mfma_f32_16x16x32_bf16 a[16:19], v[24:27], a[240:243], a[16:19]\n"
    "v_cvt_pk_bf16_f32 v97, v98, v99\n"
    "v_cvt_pk_bf16_f32 v98, v100, v101\n"
    "v_mfma_f32_16x16x32_bf16 a[20:23], v[20:23], a[228:231], a[20:23]\n"
    "v_cvt_pk_bf16_f32 v99, v102, v103\n"
    "v_accvgpr_read_b32 v104, a8\n"
    "v_mfma_f32_16x16x32_bf16 a[20:23], v[24:27], a[244:247], a[20:23]\n"
    "buffer_store_dwordx4 v[96:99], v0, s[72:75], s14 offen offset:0 sc1\n"
    "ds_write_b128 v9, v[96:99] offset:0\n"
    "v_mfma_f32_16x16x32_bf16 a[24:27], v[20:23], a[232:235], a[24:27]\n"
    "ds_read_b64_tr_b16 v[36:37], v61 offset:27680\n"
    "v_accvgpr_read_b32 v105, a9\n"
    "v_mfma_f32_16x16x32_bf16 a[24:27], v[24:27], a[248:251], a[24:27]\n"
    "ds_read_b64_tr_b16 v[38:39], v61 offset:32288\n"
    "v_accvgpr_read_b32 v106, a10\n"
    "v_mfma_f32_16x16x32_bf16 a[28:31], v[20:23], a[236:239], a[28:31]\n"
    "ds_read_b64_tr_b16 v[48:49], v61 offset:36896\n"
    "v_accvgpr_read_b32 v107, a11\n"
    "v_mfma_f32_16x16x32_bf16 a[28:31], v[24:27], a[252:255], a[28:31]\n"
    "s_waitcnt lgkmcnt(6)\n"
    "ds_read_b64_tr_b16 v[50:51], v61 offset:41504\n"
    "v_accvgpr_read_b32 v108, a12\n"
    "v_mfma_f32_16x16x32_bf16 a[16:19], v[28:31], v[128:131], a[16:19]\n"
    "s_waitcnt lgkmcnt(5)\n"
    "buffer_load_dwordx2 v[120:121], v12, s[24:27], 0 offen offset:64\n"
    "v_accvgpr_read_b32 v109, a13\n"
    "v_mfma_f32_16x16x32_bf16 a[16:19], v[32:35], v[144:147], a[16:19]\n"
    "v_accvgpr_read_b32 v110, a14\n"
    "v_accvgpr_read_b32 v111, a15\n"
    "v_mfma_f32_16x16x32_bf16 a[20:23], v[28:31], v[132:135], a[20:23]\n"
    "v_cvt_pk_bf16_f32 v104, v104, v105\n"
    "v_cvt_pk_bf16_f32 v105, v106, v107\n"
    "v_mfma_f32_16x16x32_bf16 a[20:23], v[32:35], v[148:151], a[20:23]\n"
    "v_cvt_pk_bf16_f32 v106, v108, v109\n"
    "v_cvt_pk_bf16_f32 v107, v110, v111\n"
    "v_mfma_f32_16x16x32_bf16 a[24:27], v[28:31], v[136:139], a[24:27]\n"
    "ds_read_b64_tr_b16 v[52:53], v60 offset:64\n"
    "buffer_store_dwordx4 v[104:107], v0, s[72:75], s15 offen offset:0 sc1\n"
    "v_mfma_f32_16x16x32_bf16 a[24:27], v[32:35], v[152:155], a[24:27]\n"
    "ds_read_b64_tr_b16 v[54:55], v60 offset:4672\n"
    "v_mfma_f32_16x16x32_bf16 a[28:31], v[28:31], v[140:143], a[28:31]\n"
    "ds_read_b64_tr_b16 v[56:57], v60 offset:9280\n"
    "v_mfma_f32_16x16x32_bf16 a[28:31], v[32:35], v[156:159], a[28:31]\n"
    "s_waitcnt lgkmcnt(5)\n"
    "ds_read_b64_tr_b16 v[58:59], v60 offset:13888\n"
    "v_mfma_f32_16x16x32_bf16 a[16:19], v[36:39], v[160:163], a[16:19]\n"
    "s_waitcnt lgkmcnt(4)\n"
    "buffer_load_dwordx2 v[122:123], v63, s[24:27], 0 offen offset:64\n"
    "v_mfma_f32_16x16x32_bf16 a[16:19], v[48:51], v[176:179], a[16:19]\n"
    "v_mfma_f32_16x16x32_bf16 a[20:23], v[36:39], v[164:167], a[20:23]\n"
    "v_mfma_f32_16x16x32_bf16 a[20:23], v[48:51], v[180:183], a[20:23]\n"
    "v_mfma_f32_16x16x32_bf16 a[24:27], v[36:39], v[168:171], a[24:27]\n"
    "ds_read_b64_tr_b16 v[16:17], v60 offset:18496\n"
    "v_mfma_f32_16x16x32_bf16 a[24:27], v[48:51], v[184:187], a[24:27]\n"
    "ds_read_b64_tr_b16 v[18:19], v60 offset:23104\n"
    "v_mfma_f32_16x16x32_bf16 a[28:31], v[36:39], v[172:175], a[28:31]\n"
    "ds_read_b64_tr_b16 v[20:21], v60 offset:27712\n"
    "v_mfma_f32_16x16x32_bf16 a[28:31], v[48:51], v[188:191], a[28:31]\n"
    "s_waitcnt lgkmcnt(5)\n"
    "ds_read_b64_tr_b16 v[22:23], v60 offset:32320\n"
    "v_mfma_f32_16x16x32_bf16 a[32:35], v[52:55], a[192:195], a[32:35]\n"
    "s_waitcnt lgkmcnt(4)\n"
    "v_mfma_f32_16x16x32_bf16 a[32:35], v[56:59], a[208:211], a[32:35]\n"
    "v_mfma_f32_16x16x32_bf16 a[36:39], v[52:55], a[196:199], a[36:39]\n"
    "v_accvgpr_read_b32 v96, a16\n"
    "v_accvgpr_read_b32 v97, a17\n"
    "v_mfma_f32_16x16x32_bf16 a[36:39], v[56:59], a[212:215], a[36:39]\n"
    "v_accvgpr_read_b32 v98, a18\n"
    "v_accvgpr_read_b32 v99, a19\n"
    "v_mfma_f32_16x16x32_bf16 a[40:43], v[52:55], a[200:203], a[40:43]\n"
    "ds_read_b64_tr_b16 v[24:25], v61 offset:9280\n"
    "v_accvgpr_read_b32 v100, a20\n"
    "v_mfma_f32_16x16x32_bf16 a[40:43], v[56:59], a[216:219], a[40:43]\n"
    "ds_read_b64_tr_b16 v[26:27], v61 offset:13888\n"
    "v_accvgpr_read_b32 v101, a21\n"
    "v_mfma_f32_16x16x32_bf16 a[44:47], v[52:55], a[204:207], a[44:47]\n"
    "ds_read_b64_tr_b16 v[28:29], v61 offset:18496\n"
    "v_accvgpr_read_b32 v102, a22\n"
    "v_mfma_f32_16x16x32_bf16 a[44:47], v[56:59], a[220:223], a[44:47]\n"
    "s_waitcnt lgkmcnt(5)\n"
    "ds_read_b64_tr_b16 v[30:31], v61 offset:23104\n"
    "v_accvgpr_read_b32 v103, a23\n"
    "v_mfma_f32_16x16x32_bf16 a[32:35], v[16:19], a[224:227], a[32:35]\n"
    "s_waitcnt lgkmcnt(4)\n"
    "v_cvt_pk_bf16_f32 v96, v96, v97\n"
    "v_cvt_pk_bf16_f32 v97, v98, v99\n"
    "v_mfma_f32_16x16x32_bf16 a[32:35], v[20:23], a[240:243], a[32:35]\n"
    "v_cvt_pk_bf16_f32 v98, v100, v101\n"
    "v_cvt_pk_bf16_f32 v99, v102, v103\n"
    "v_mfma_f32_16x16x32_bf16 a[36:39], v[16:19], a[228:231], a[36:39]\n"
    "buffer_store_dwordx4 v[96:99], v0, s[72:75], s14 offen offset:1024 sc1\n"
    "ds_write_b128 v9, v[96:99] offset:1024\n"
    "v_mfma_f32_16x16x32_bf16 a[36:39], v[20:23], a[244:247], a[36:39]\n"
    "v_accvgpr_read_b32 v104, a24\n"
    "v_accvgpr_read_b32 v105, a25\n"
    "v_mfma_f32_16x16x32_bf16 a[40:43], v[16:19], a[232:235], a[40:43]\n"
    "ds_read_b64_tr_b16 v[32:33], v61 offset:27712\n"
    "v_accvgpr_read_b32 v106, a26\n"
    "v_mfma_f32_16x16x32_bf16 a[40:43], v[20:23], a[248:251], a[40:43]\n"
    "ds_read_b64_tr_b16 v[34:35], v61 offset:32320\n"
    "v_accvgpr_read_b32 v107, a27\n"
    "v_mfma_f32_16x16x32_bf16 a[44:47], v[16:19], a[236:239], a[44:47]\n"
    "ds_read_b64_tr_b16 v[36:37], v61 offset:36928\n"
    "v_accvgpr_read_b32 v108, a28\n"
    "v_mfma_f32_16x16x32_bf16 a[44:47], v[20:23], a[252:255], a[44:47]\n"
    "s_waitcnt lgkmcnt(6)\n"
    "ds_read_b64_tr_b16 v[38:39], v61 offset:41536\n"
    "v_accvgpr_read_b32 v109, a29\n"
    "v_mfma_f32_16x16x32_bf16 a[32:35], v[24:27], v[128:131], a[32:35]\n"
    "s_waitcnt lgkmcnt(5)\n"
    "v_accvgpr_read_b32 v110, a30\n"
    "v_accvgpr_read_b32 v111, a31\n"
    "v_mfma_f32_16x16x32_bf16 a[32:35], v[28:31], v[144:147], a[32:35]\n"
    "v_cvt_pk_bf16_f32 v104, v104, v105\n"
    "v_cvt_pk_bf16_f32 v105, v106, v107\n"
    "v_mfma_f32_16x16x32_bf16 a[36:39], v[24:27], v[132:135], a[36:39]\n"
    "v_cvt_pk_bf16_f32 v106, v108, v109\n"
    "v_cvt_pk_bf16_f32 v107, v110, v111\n"
    "v_mfma_f32_16x16x32_bf16 a[36:39], v[28:31], v[148:151], a[36:39]\n"
    "buffer_store_dwordx4 v[104:107], v0, s[72:75], s15 offen offset:1024 sc1\n"
    "v_mfma_f32_16x16x32_bf16 a[40:43], v[24:27], v[136:139], a[40:43]\n"
    "ds_read_b64_tr_b16 v[48:49], v60 offset:96\n"
    "v_mfma_f32_16x16x32_bf16 a[40:43], v[28:31], v[152:155], a[40:43]\n"
    "ds_read_b64_tr_b16 v[50:51], v60 offset:4704\n"
    "v_mfma_f32_16x16x32_bf16 a[44:47], v[24:27], v[140:143], a[44:47]\n"
    "ds_read_b64_tr_b16 v[52:53], v60 offset:9312\n"
    "v_mfma_f32_16x16x32_bf16 a[44:47], v[28:31], v[156:159], a[44:47]\n"
    "s_waitcnt lgkmcnt(5)\n"
    "ds_read_b64_tr_b16 v[54:55], v60 offset:13920\n"
    "v_mfma_f32_16x16x32_bf16 a[32:35], v[32:35], v[160:163], a[32:35]\n"
    "s_waitcnt lgkmcnt(4)\n"
    "v_mfma_f32_16x16x32_bf16 a[32:35], v[36:39], v[176:179], a[32:35]\n"
    "v_mfma_f32_16x16x32_bf16 a[36:39], v[32:35], v[164:167], a[36:39]\n"
    "v_mfma_f32_16x16x32_bf16 a[36:39], v[36:39], v[180:183], a[36:39]\n"
    "v_mfma_f32_16x16x32_bf16 a[40:43], v[32:35], v[168:171], a[40:43]\n"
    "ds_read_b64_tr_b16 v[56:57], v60 offset:18528\n"
    "v_mfma_f32_16x16x32_bf16 a[40:43], v[36:39], v[184:187], a[40:43]\n"
    "ds_read_b64_tr_b16 v[58:59], v60 offset:23136\n"
    "v_mfma_f32_16x16x32_bf16 a[44:47], v[32:35], v[172:175], a[44:47]\n"
    "ds_read_b64_tr_b16 v[16:17], v60 offset:27744\n"
    "v_mfma_f32_16x16x32_bf16 a[44:47], v[36:39], v[188:191], a[44:47]\n"
    "s_waitcnt lgkmcnt(5)\n"
    "ds_read_b64_tr_b16 v[18:19], v60 offset:32352\n"
    "v_mfma_f32_16x16x32_bf16 a[48:51], v[48:51], a[192:195], a[48:51]\n"
    "s_waitcnt lgkmcnt(4)\n"
    "v_mfma_f32_16x16x32_bf16 a[48:51], v[52:55], a[208:211], a[48:51]\n"
    "v_mfma_f32_16x16x32_bf16 a[52:55], v[48:51], a[196:199], a[52:55]\n"
    "v_accvgpr_read_b32 v96, a32\n"
    "v_accvgpr_read_b32 v97, a33\n"
    "v_mfma_f32_16x16x32_bf16 a[52:55], v[52:55], a[212:215], a[52:55]\n"
    "v_accvgpr_read_b32 v98, a34\n"
    "v_accvgpr_read_b32 v99, a35\n"
    "v_mfma_f32_16x16x32_bf16 a[56:59], v[48:51], a[200:203], a[56:59]\n"
    "ds_read_b64_tr_b16 v[20:21], v61 offset:9312\n"
    "v_accvgpr_read_b32 v100, a36\n"
    "v_mfma_f32_16x16x32_bf16 a[56:59], v[52:55], a[216:219], a[56:59]\n"
    "ds_read_b64_tr_b16 v[22:23], v61 offset:13920\n"
    "v_accvgpr_read_b32 v101, a37\n"
    "v_mfma_f32_16x16x32_bf16 a[60:63], v[48:51], a[204:207], a[60:63]\n"
    "ds_read_b64_tr_b16 v[24:25], v61 offset:18528\n"
    "v_accvgpr_read_b32 v102, a38\n"
    "v_mfma_f32_16x16x32_bf16 a[60:63], v[52:55], a[220:223], a[60:63]\n"
    "s_waitcnt lgkmcnt(5)\n"
    "ds_read_b64_tr_b16 v[26:27], v61 offset:23136\n"
    "v_accvgpr_read_b32 v103, a39\n"
    "v_mfma_f32_16x16x32_bf16 a[48:51], v[56:59], a[224:227], a[48:51]\n"
    "s_waitcnt lgkmcnt(4)\n"
    "v_cvt_pk_bf16_f32 v96, v96, v97\n"
    "v_cvt_pk_bf16_f32 v97, v98, v99\n"
    "v_mfma_f32_16x16x32_bf16 a[48:51], v[16:19], a[240:243], a[48:51]\n"
    "v_cvt_pk_bf16_f32 v98, v100, v101\n"
    "v_cvt_pk_bf16_f32 v99, v102, v103\n"
    "v_mfma_f32_16x16x32_bf16 a[52:55], v[56:59], a[228:231], a[52:55]\n"
    "buffer_store_dwordx4 v[96:99], v0, s[72:75], s14 offen offset:2048 sc1\n"
    "ds_write_b128 v9, v[96:99] offset:2048\n"
    "v_mfma_f32_16x16x32_bf16 a[52:55], v[16:19], a[244:247], a[52:55]\n"
    "v_accvgpr_read_b32 v104, a40\n"
    "v_accvgpr_read_b32 v105, a41\n"
    "v_mfma_f32_16x16x32_bf16 a[56:59], v[56:59], a[232:235], a[56:59]\n"
    "ds_read_b64_tr_b16 v[28:29], v61 offset:27744\n"
    "v_accvgpr_read_b32 v106, a42\n"
    "v_mfma_f32_16x16x32_bf16 a[56:59], v[16:19], a[248:251], a[56:59]\n"
    "ds_read_b64_tr_b16 v[30:31], v61 offset:32352\n"
    "v_accvgpr_read_b32 v107, a43\n"
    "v_mfma_f32_16x16x32_bf16 a[60:63], v[56:59], a[236:239], a[60:63]\n"
    "ds_read_b64_tr_b16 v[32:33], v61 offset:36960\n"
    "v_accvgpr_read_b32 v108, a44\n"
    "v_mfma_f32_16x16x32_bf16 a[60:63], v[16:19], a[252:255], a[60:63]\n"
    "s_waitcnt lgkmcnt(6)\n"
    "ds_read_b64_tr_b16 v[34:35], v61 offset:41568\n"
    "v_accvgpr_read_b32 v109, a45\n"
    "v_mfma_f32_16x16x32_bf16 a[48:51], v[20:23], v[128:131], a[48:51]\n"
    "s_waitcnt lgkmcnt(5)\n"
    "v_accvgpr_read_b32 v110, a46\n"
    "v_accvgpr_read_b32 v111, a47\n"
    "v_mfma_f32_16x16x32_bf16 a[48:51], v[24:27], v[144:147], a[48:51]\n"
    "v_cvt_pk_bf16_f32 v104, v104, v105\n"
    "v_cvt_pk_bf16_f32 v105, v106, v107\n"
    "v_mfma_f32_16x16x32_bf16 a[52:55], v[20:23], v[132:135], a[52:55]\n"
    "v_cvt_pk_bf16_f32 v106, v108, v109\n"
    "v_cvt_pk_bf16_f32 v107, v110, v111\n"
    "v_mfma_f32_16x16x32_bf16 a[52:55], v[24:27], v[148:151], a[52:55]\n"
    "buffer_store_dwordx4 v[104:107], v0, s[72:75], s15 offen offset:2048 sc1\n"
    "v_mfma_f32_16x16x32_bf16 a[56:59], v[20:23], v[136:139], a[56:59]\n"
    "ds_read_b64_tr_b16 v[36:37], v60 offset:128\n"
    "v_mfma_f32_16x16x32_bf16 a[56:59], v[24:27], v[152:155], a[56:59]\n"
    "ds_read_b64_tr_b16 v[38:39], v60 offset:4736\n"
    "v_mfma_f32_16x16x32_bf16 a[60:63], v[20:23], v[140:143], a[60:63]\n"
    "ds_read_b64_tr_b16 v[48:49], v60 offset:9344\n"
    "v_mfma_f32_16x16x32_bf16 a[60:63], v[24:27], v[156:159], a[60:63]\n"
    "s_waitcnt lgkmcnt(5)\n"
    "ds_read_b64_tr_b16 v[50:51], v60 offset:13952\n"
    "v_mfma_f32_16x16x32_bf16 a[48:51], v[28:31], v[160:163], a[48:51]\n"
    "s_waitcnt lgkmcnt(4)\n"
    "v_mfma_f32_16x16x32_bf16 a[48:51], v[32:35], v[176:179], a[48:51]\n"
    "v_mfma_f32_16x16x32_bf16 a[52:55], v[28:31], v[164:167], a[52:55]\n"
    "v_mfma_f32_16x16x32_bf16 a[52:55], v[32:35], v[180:183], a[52:55]\n"
    "v_mfma_f32_16x16x32_bf16 a[56:59], v[28:31], v[168:171], a[56:59]\n"
    "ds_read_b64_tr_b16 v[52:53], v60 offset:18560\n"
    "v_mfma_f32_16x16x32_bf16 a[56:59], v[32:35], v[184:187], a[56:59]\n"
    "ds_read_b64_tr_b16 v[54:55], v60 offset:23168\n"
    "v_mfma_f32_16x16x32_bf16 a[60:63], v[28:31], v[172:175], a[60:63]\n"
    "ds_read_b64_tr_b16 v[56:57], v60 offset:27776\n"
    "v_mfma_f32_16x16x32_bf16 a[60:63], v[32:35], v[188:191], a[60:63]\n"
    "s_waitcnt lgkmcnt(5)\n"
    "ds_read_b64_tr_b16 v[58:59], v60 offset:32384\n"
    "v_mfma_f32_16x16x32_bf16 a[64:67], v[36:39], a[192:195], a[64:67]\n"
    "s_waitcnt lgkmcnt(4)\n"
    "v_mfma_f32_16x16x32_bf16 a[64:67], v[48:51], a[208:211], a[64:67]\n"
    "v_mfma_f32_16x16x32_bf16 a[68:71], v[36:39], a[196:199], a[68:71]\n"
    "v_accvgpr_read_b32 v96, a48\n"
    "v_accvgpr_read_b32 v97, a49\n"
    "v_mfma_f32_16x16x32_bf16 a[68:71], v[48:51], a[212:215], a[68:71]\n"
    "v_accvgpr_read_b32 v98, a50\n"
    "v_accvgpr_read_b32 v99, a51\n"
    "v_mfma_f32_16x16x32_bf16 a[72:75], v[36:39], a[200:203], a[72:75]\n"
    "ds_read_b64_tr_b16 v[16:17], v61 offset:9344\n"
    "v_accvgpr_read_b32 v100, a52\n"
    "v_mfma_f32_16x16x32_bf16 a[72:75], v[48:51], a[216:219], a[72:75]\n"
    "ds_read_b64_tr_b16 v[18:19], v61 offset:13952\n"
    "v_accvgpr_read_b32 v101, a53\n"
    "v_mfma_f32_16x16x32_bf16 a[76:79], v[36:39], a[204:207], a[76:79]\n"
    "ds_read_b64_tr_b16 v[20:21], v61 offset:18560\n"
    "v_accvgpr_read_b32 v102, a54\n"
    "v_mfma_f32_16x16x32_bf16 a[76:79], v[48:51], a[220:223], a[76:79]\n"
    "s_waitcnt lgkmcnt(5)\n"
    "ds_read_b64_tr_b16 v[22:23], v61 offset:23168\n"
    "v_accvgpr_read_b32 v103, a55\n"
    "v_mfma_f32_16x16x32_bf16 a[64:67], v[52:55], a[224:227], a[64:67]\n"
    "s_waitcnt lgkmcnt(4)\n"
    "v_cvt_pk_bf16_f32 v96, v96, v97\n"
    "v_cvt_pk_bf16_f32 v97, v98, v99\n"
    "v_mfma_f32_16x16x32_bf16 a[64:67], v[56:59], a[240:243], a[64:67]\n"
    "v_cvt_pk_bf16_f32 v98, v100, v101\n"
    "v_cvt_pk_bf16_f32 v99, v102, v103\n"
    "v_mfma_f32_16x16x32_bf16 a[68:71], v[52:55], a[228:231], a[68:71]\n"
    "buffer_store_dwordx4 v[96:99], v0, s[72:75], s14 offen offset:3072 sc1\n"
    "ds_write_b128 v9, v[96:99] offset:3072\n"
    "v_mfma_f32_16x16x32_bf16 a[68:71], v[56:59], a[244:247], a[68:71]\n"
    "v_accvgpr_read_b32 v104, a56\n"
    "v_accvgpr_read_b32 v105, a57\n"
    "v_mfma_f32_16x16x32_bf16 a[72:75], v[52:55], a[232:235], a[72:75]\n"
    "ds_read_b64_tr_b16 v[24:25], v61 offset:27776\n"
    "v_accvgpr_read_b32 v106, a58\n"
    "v_mfma_f32_16x16x32_bf16 a[72:75], v[56:59], a[248:251], a[72:75]\n"
    "ds_read_b64_tr_b16 v[26:27], v61 offset:32384\n"
    "v_accvgpr_read_b32 v107, a59\n"
    "v_mfma_f32_16x16x32_bf16 a[76:79], v[52:55], a[236:239], a[76:79]\n"
    "ds_read_b64_tr_b16 v[28:29], v61 offset:36992\n"
    "v_accvgpr_read_b32 v108, a60\n"
    "v_mfma_f32_16x16x32_bf16 a[76:79], v[56:59], a[252:255], a[76:79]\n"
    "s_waitcnt lgkmcnt(6)\n"
    "ds_read_b64_tr_b16 v[30:31], v61 offset:41600\n"
    "v_accvgpr_read_b32 v109, a61\n"
    "v_mfma_f32_16x16x32_bf16 a[64:67], v[16:19], v[128:131], a[64:67]\n"
    "s_waitcnt lgkmcnt(5)\n"
    "v_accvgpr_read_b32 v110, a62\n"
    "v_accvgpr_read_b32 v111, a63\n"
    "v_mfma_f32_16x16x32_bf16 a[64:67], v[20:23], v[144:147], a[64:67]\n"
    "v_cvt_pk_bf16_f32 v104, v104, v105\n"
    "v_cvt_pk_bf16_f32 v105, v106, v107\n"
    "v_mfma_f32_16x16x32_bf16 a[68:71], v[16:19], v[132:135], a[68:71]\n"
    "v_cvt_pk_bf16_f32 v106, v108, v109\n"
    "v_cvt_pk_bf16_f32 v107, v110, v111\n"
    "v_mfma_f32_16x16x32_bf16 a[68:71], v[20:23], v[148:151], a[68:71]\n"
    "buffer_store_dwordx4 v[104:107], v0, s[72:75], s15 offen offset:3072 sc1\n"
    "v_mfma_f32_16x16x32_bf16 a[72:75], v[16:19], v[136:139], a[72:75]\n"
    "ds_read_b64_tr_b16 v[32:33], v60 offset:160\n"
    "v_mfma_f32_16x16x32_bf16 a[72:75], v[20:23], v[152:155], a[72:75]\n"
    "ds_read_b64_tr_b16 v[34:35], v60 offset:4768\n"
    "v_mfma_f32_16x16x32_bf16 a[76:79], v[16:19], v[140:143], a[76:79]\n"
    "ds_read_b64_tr_b16 v[36:37], v60 offset:9376\n"
    "v_mfma_f32_16x16x32_bf16 a[76:79], v[20:23], v[156:159], a[76:79]\n"
    "s_waitcnt lgkmcnt(5)\n"
    "ds_read_b64_tr_b16 v[38:39], v60 offset:13984\n"
    "v_mfma_f32_16x16x32_bf16 a[64:67], v[24:27], v[160:163], a[64:67]\n"
    "s_waitcnt lgkmcnt(4)\n"
    "v_mfma_f32_16x16x32_bf16 a[64:67], v[28:31], v[176:179], a[64:67]\n"
    "v_mfma_f32_16x16x32_bf16 a[68:71], v[24:27], v[164:167], a[68:71]\n"
    "v_mfma_f32_16x16x32_bf16 a[68:71], v[28:31], v[180:183], a[68:71]\n"
    "v_mfma_f32_16x16x32_bf16 a[72:75], v[24:27], v[168:171], a[72:75]\n"
    "ds_read_b64_tr_b16 v[48:49], v60 offset:18592\n"
    "v_mfma_f32_16x16x32_bf16 a[72:75], v[28:31], v[184:187], a[72:75]\n"
    "ds_read_b64_tr_b16 v[50:51], v60 offset:23200\n"
    "v_mfma_f32_16x16x32_bf16 a[76:79], v[24:27], v[172:175], a[76:79]\n"
    "ds_read_b64_tr_b16 v[52:53], v60 offset:27808\n"
    "v_mfma_f32_16x16x32_bf16 a[76:79], v[28:31], v[188:191], a[76:79]\n"
    "s_waitcnt lgkmcnt(5)\n"
    "ds_read_b64_tr_b16 v[54:55], v60 offset:32416\n"
    "v_mfma_f32_16x16x32_bf16 a[80:83], v[32:35], a[192:195], a[80:83]\n"
    "s_waitcnt lgkmcnt(4)\n"
    "v_mfma_f32_16x16x32_bf16 a[80:83], v[36:39], a[208:211], a[80:83]\n"
    "v_mfma_f32_16x16x32_bf16 a[84:87], v[32:35], a[196:199], a[84:87]\n"
    "s_add_u32 s14, s14, 4096\n"
    "s_add_u32 s15, s15, 4096\n"
    "v_accvgpr_read_b32 v96, a64\n"
    "v_accvgpr_read_b32 v97, a65\n"
    "v_mfma_f32_16x16x32_bf16 a[84:87], v[36:39], a[212:215], a[84:87]\n"
    "v_accvgpr_read_b32 v98, a66\n"
    "v_accvgpr_read_b32 v99, a67\n"
    "v_mfma_f32_16x16x32_bf16 a[88:91], v[32:35], a[200:203], a[88:91]\n"
    "ds_read_b64_tr_b16 v[56:57], v61 offset:9376\n"
    "v_accvgpr_read_b32 v100, a68\n"
    "v_mfma_f32_16x16x32_bf16 a[88:91], v[36:39], a[216:219], a[88:91]\n"
    "ds_read_b64_tr_b16 v[58:59], v61 offset:13984\n"
    "v_accvgpr_read_b32 v101, a69\n"
    "v_mfma_f32_16x16x32_bf16 a[92:95], v[32:35], a[204:207], a[92:95]\n"
    "ds_read_b64_tr_b16 v[16:17], v61 offset:18592\n"
    "v_accvgpr_read_b32 v102, a70\n"
    "v_mfma_f32_16x16x32_bf16 a[92:95], v[36:39], a[220:223], a[92:95]\n"
    "s_waitcnt lgkmcnt(5)\n"
    "ds_read_b64_tr_b16 v[18:19], v61 offset:23200\n"
    "v_accvgpr_read_b32 v103, a71\n"
    "v_mfma_f32_16x16x32_bf16 a[80:83], v[48:51], a[224:227], a[80:83]\n"
    "s_waitcnt lgkmcnt(4)\n"
    "v_cvt_pk_bf16_f32 v96, v96, v97\n"
    "v_cvt_pk_bf16_f32 v97, v98, v99\n"
    "v_mfma_f32_16x16x32_bf16 a[80:83], v[52:55], a[240:243], a[80:83]\n"
    "v_add_u32 v46, 1, v46\n"
    "v_cvt_pk_bf16_f32 v98, v100, v101\n"
    "v_mfma_f32_16x16x32_bf16 a[84:87], v[48:51], a[228:231], a[84:87]\n"
    "s_waitcnt vmcnt(2)\n"
    "buffer_store_dwordx2 v[46:47], v11, s[76:79], 0 offen sc1\n"
    "v_cvt_pk_bf16_f32 v99, v102, v103\n"
    "v_mfma_f32_16x16x32_bf16 a[84:87], v[52:55], a[244:247], a[84:87]\n"
    "buffer_store_dwordx4 v[96:99], v0, s[72:75], s14 offen offset:0 sc1\n"
    "ds_write_b128 v9, v[96:99] offset:4096\n"
    "v_mfma_f32_16x16x32_bf16 a[88:91], v[48:51], a[232:235], a[88:91]\n"
    "ds_read_b64_tr_b16 v[20:21], v61 offset:27808\n"
    "v_accvgpr_read_b32 v104, a72\n"
    "v_mfma_f32_16x16x32_bf16 a[88:91], v[52:55], a[248:251], a[88:91]\n"
    "ds_read_b64_tr_b16 v[22:23], v61 offset:32416\n"
    "v_accvgpr_read_b32 v105, a73\n"
    "v_mfma_f32_16x16x32_bf16 a[92:95], v[48:51], a[236:239], a[92:95]\n"
    "ds_read_b64_tr_b16 v[24:25], v61 offset:37024\n"
    "v_accvgpr_read_b32 v106, a74\n"
    "v_mfma_f32_16x16x32_bf16 a[92:95], v[52:55], a[252:255], a[92:95]\n"
    "s_waitcnt lgkmcnt(6)\n"
    "ds_read_b64_tr_b16 v[26:27], v61 offset:41632\n"
    "v_accvgpr_read_b32 v107, a75\n"
    "v_mfma_f32_16x16x32_bf16 a[80:83], v[56:59], v[128:131], a[80:83]\n"
    "s_waitcnt lgkmcnt(5)\n"
    "v_accvgpr_read_b32 v108, a76\n"
    "v_accvgpr_read_b32 v109, a77\n"
    "v_mfma_f32_16x16x32_bf16 a[80:83], v[16:19], v[144:147], a[80:83]\n"
    "v_accvgpr_read_b32 v110, a78\n"
    "v_accvgpr_read_b32 v111, a79\n"
    "v_mfma_f32_16x16x32_bf16 a[84:87], v[56:59], v[132:135], a[84:87]\n"
    "v_cvt_pk_bf16_f32 v104, v104, v105\n"
    "v_cvt_pk_bf16_f32 v105, v106, v107\n"
    "v_mfma_f32_16x16x32_bf16 a[84:87], v[16:19], v[148:151], a[84:87]\n"
    "v_cvt_pk_bf16_f32 v106, v108, v109\n"
    "v_cvt_pk_bf16_f32 v107, v110, v111\n"
    "v_mfma_f32_16x16x32_bf16 a[88:91], v[56:59], v[136:139], a[88:91]\n"
    "ds_read_b64_tr_b16 v[28:29], v60 offset:192\n"
    "buffer_store_dwordx4 v[104:107], v0, s[72:75], s15 offen offset:0 sc1\n"
    "v_mfma_f32_16x16x32_bf16 a[88:91], v[16:19], v[152:155], a[88:91]\n"
    "ds_read_b64_tr_b16 v[30:31], v60 offset:4800\n"
    "v_mfma_f32_16x16x32_bf16 a[92:95], v[56:59], v[140:143], a[92:95]\n"
    "ds_read_b64_tr_b16 v[32:33], v60 offset:9408\n"
    "v_mfma_f32_16x16x32_bf16 a[92:95], v[16:19], v[156:159], a[92:95]\n"
    "s_waitcnt lgkmcnt(5)\n"
    "ds_read_b64_tr_b16 v[34:35], v60 offset:14016\n"
    "v_mfma_f32_16x16x32_bf16 a[80:83], v[20:23], v[160:163], a[80:83]\n"
    "s_waitcnt lgkmcnt(4)\n"
    "v_mfma_f32_16x16x32_bf16 a[80:83], v[24:27], v[176:179], a[80:83]\n"
    "v_mfma_f32_16x16x32_bf16 a[84:87], v[20:23], v[164:167], a[84:87]\n"
    "v_mfma_f32_16x16x32_bf16 a[84:87], v[24:27], v[180:183], a[84:87]\n"
    "v_mfma_f32_16x16x32_bf16 a[88:91], v[20:23], v[168:171], a[88:91]\n"
    "ds_read_b64_tr_b16 v[36:37], v60 offset:18624\n"
    "v_mfma_f32_16x16x32_bf16 a[88:91], v[24:27], v[184:187], a[88:91]\n"
    "ds_read_b64_tr_b16 v[38:39], v60 offset:23232\n"
    "v_mfma_f32_16x16x32_bf16 a[92:95], v[20:23], v[172:175], a[92:95]\n"
    "ds_read_b64_tr_b16 v[48:49], v60 offset:27840\n"
    "v_mfma_f32_16x16x32_bf16 a[92:95], v[24:27], v[188:191], a[92:95]\n"
    "s_waitcnt lgkmcnt(5)\n"
    "ds_read_b64_tr_b16 v[50:51], v60 offset:32448\n"
    "v_mfma_f32_16x16x32_bf16 a[96:99], v[28:31], a[192:195], a[96:99]\n"
    "s_waitcnt lgkmcnt(4)\n"
    "v_mfma_f32_16x16x32_bf16 a[96:99], v[32:35], a[208:211], a[96:99]\n"
    "v_mfma_f32_16x16x32_bf16 a[100:103], v[28:31], a[196:199], a[100:103]\n"
    "v_accvgpr_read_b32 v96, a80\n"
    "v_accvgpr_read_b32 v97, a81\n"
    "v_mfma_f32_16x16x32_bf16 a[100:103], v[32:35], a[212:215], a[100:103]\n"
    "v_accvgpr_read_b32 v98, a82\n"
    "v_accvgpr_read_b32 v99, a83\n"
    "v_mfma_f32_16x16x32_bf16 a[104:107], v[28:31], a[200:203], a[104:107]\n"
    "ds_read_b64_tr_b16 v[52:53], v61 offset:9408\n"
    "v_accvgpr_read_b32 v100, a84\n"
    "v_mfma_f32_16x16x32_bf16 a[104:107], v[32:35], a[216:219], a[104:107]\n"
    "ds_read_b64_tr_b16 v[54:55], v61 offset:14016\n"
    "v_accvgpr_read_b32 v101, a85\n"
    "v_mfma_f32_16x16x32_bf16 a[108:111], v[28:31], a[204:207], a[108:111]\n"
    "ds_read_b64_tr_b16 v[56:57], v61 offset:18624\n"
    "v_accvgpr_read_b32 v102, a86\n"
    "v_mfma_f32_16x16x32_bf16 a[108:111], v[32:35], a[220:223], a[108:111]\n"
    "s_waitcnt lgkmcnt(5)\n"
    "ds_read_b64_tr_b16 v[58:59], v61 offset:23232\n"
    "v_accvgpr_read_b32 v103, a87\n"
    "v_mfma_f32_16x16x32_bf16 a[96:99], v[36:39], a[224:227], a[96:99]\n"
    "s_waitcnt lgkmcnt(4)\n"
    "v_cvt_pk_bf16_f32 v96, v96, v97\n"
    "v_cvt_pk_bf16_f32 v97, v98, v99\n"
    "v_mfma_f32_16x16x32_bf16 a[96:99], v[48:51], a[240:243], a[96:99]\n"
    "v_cvt_pk_bf16_f32 v98, v100, v101\n"
    "v_cvt_pk_bf16_f32 v99, v102, v103\n"
    "v_mfma_f32_16x16x32_bf16 a[100:103], v[36:39], a[228:231], a[100:103]\n"
    "buffer_store_dwordx4 v[96:99], v0, s[72:75], s14 offen offset:1024 sc1\n"
    "ds_write_b128 v9, v[96:99] offset:5120\n"
    "v_mfma_f32_16x16x32_bf16 a[100:103], v[48:51], a[244:247], a[100:103]\n"
    "v_accvgpr_read_b32 v104, a88\n"
    "v_accvgpr_read_b32 v105, a89\n"
    "v_mfma_f32_16x16x32_bf16 a[104:107], v[36:39], a[232:235], a[104:107]\n"
    "ds_read_b64_tr_b16 v[16:17], v61 offset:27840\n"
    "v_accvgpr_read_b32 v106, a90\n"
    "v_mfma_f32_16x16x32_bf16 a[104:107], v[48:51], a[248:251], a[104:107]\n"
    "ds_read_b64_tr_b16 v[18:19], v61 offset:32448\n"
    "v_accvgpr_read_b32 v107, a91\n"
    "v_mfma_f32_16x16x32_bf16 a[108:111], v[36:39], a[236:239], a[108:111]\n"
    "ds_read_b64_tr_b16 v[20:21], v61 offset:37056\n"
    "v_accvgpr_read_b32 v108, a92\n"
    "v_mfma_f32_16x16x32_bf16 a[108:111], v[48:51], a[252:255], a[108:111]\n"
    "s_waitcnt lgkmcnt(6)\n"
    "ds_read_b64_tr_b16 v[22:23], v61 offset:41664\n"
    "v_accvgpr_read_b32 v109, a93\n"
    "v_mfma_f32_16x16x32_bf16 a[96:99], v[52:55], v[128:131], a[96:99]\n"
    "s_waitcnt lgkmcnt(5)\n"
    "v_accvgpr_read_b32 v110, a94\n"
    "v_accvgpr_read_b32 v111, a95\n"
    "v_mfma_f32_16x16x32_bf16 a[96:99], v[56:59], v[144:147], a[96:99]\n"
    "v_cvt_pk_bf16_f32 v104, v104, v105\n"
    "v_cvt_pk_bf16_f32 v105, v106, v107\n"
    "v_mfma_f32_16x16x32_bf16 a[100:103], v[52:55], v[132:135], a[100:103]\n"
    "v_cvt_pk_bf16_f32 v106, v108, v109\n"
    "v_cvt_pk_bf16_f32 v107, v110, v111\n"
    "v_mfma_f32_16x16x32_bf16 a[100:103], v[56:59], v[148:151], a[100:103]\n"
    "buffer_store_dwordx4 v[104:107], v0, s[72:75], s15 offen offset:1024 sc1\n"
    "v_mfma_f32_16x16x32_bf16 a[104:107], v[52:55], v[136:139], a[104:107]\n"
    "ds_read_b64_tr_b16 v[24:25], v60 offset:224\n"
    "v_mfma_f32_16x16x32_bf16 a[104:107], v[56:59], v[152:155], a[104:107]\n"
    "ds_read_b64_tr_b16 v[26:27], v60 offset:4832\n"
    "v_mfma_f32_16x16x32_bf16 a[108:111], v[52:55], v[140:143], a[108:111]\n"
    "ds_read_b64_tr_b16 v[28:29], v60 offset:9440\n"
    "v_mfma_f32_16x16x32_bf16 a[108:111], v[56:59], v[156:159], a[108:111]\n"
    "s_waitcnt lgkmcnt(5)\n"
    "ds_read_b64_tr_b16 v[30:31], v60 offset:14048\n"
    "v_mfma_f32_16x16x32_bf16 a[96:99], v[16:19], v[160:163], a[96:99]\n"
    "s_waitcnt lgkmcnt(4)\n"
    "v_mfma_f32_16x16x32_bf16 a[96:99], v[20:23], v[176:179], a[96:99]\n"
    "v_mfma_f32_16x16x32_bf16 a[100:103], v[16:19], v[164:167], a[100:103]\n"
    "v_mfma_f32_16x16x32_bf16 a[100:103], v[20:23], v[180:183], a[100:103]\n"
    "v_mfma_f32_16x16x32_bf16 a[104:107], v[16:19], v[168:171], a[104:107]\n"
    "ds_read_b64_tr_b16 v[32:33], v60 offset:18656\n"
    "v_mfma_f32_16x16x32_bf16 a[104:107], v[20:23], v[184:187], a[104:107]\n"
    "ds_read_b64_tr_b16 v[34:35], v60 offset:23264\n"
    "v_mfma_f32_16x16x32_bf16 a[108:111], v[16:19], v[172:175], a[108:111]\n"
    "ds_read_b64_tr_b16 v[36:37], v60 offset:27872\n"
    "v_mfma_f32_16x16x32_bf16 a[108:111], v[20:23], v[188:191], a[108:111]\n"
    "s_waitcnt lgkmcnt(5)\n"
    "ds_read_b64_tr_b16 v[38:39], v60 offset:32480\n"
    "v_mfma_f32_16x16x32_bf16 a[112:115], v[24:27], a[192:195], a[112:115]\n"
    "s_waitcnt lgkmcnt(4)\n"
    "v_mfma_f32_16x16x32_bf16 a[112:115], v[28:31], a[208:211], a[112:115]\n"
    "v_mfma_f32_16x16x32_bf16 a[116:119], v[24:27], a[196:199], a[116:119]\n"
    "s_xor_b32 s15, s15, s60\n"
    "v_accvgpr_read_b32 v96, a96\n"
    "v_accvgpr_read_b32 v97, a97\n"
    "v_mfma_f32_16x16x32_bf16 a[116:119], v[28:31], a[212:215], a[116:119]\n"
    "v_accvgpr_read_b32 v98, a98\n"
    "v_accvgpr_read_b32 v99, a99\n"
    "v_mfma_f32_16x16x32_bf16 a[120:123], v[24:27], a[200:203], a[120:123]\n"
    "ds_read_b64_tr_b16 v[48:49], v61 offset:9440\n"
    "v_accvgpr_read_b32 v100, a100\n"
    "v_mfma_f32_16x16x32_bf16 a[120:123], v[28:31], a[216:219], a[120:123]\n"
    "ds_read_b64_tr_b16 v[50:51], v61 offset:14048\n"
    "v_accvgpr_read_b32 v101, a101\n"
    "v_mfma_f32_16x16x32_bf16 a[124:127], v[24:27], a[204:207], a[124:127]\n"
    "ds_read_b64_tr_b16 v[52:53], v61 offset:18656\n"
    "v_accvgpr_read_b32 v102, a102\n"
    "v_mfma_f32_16x16x32_bf16 a[124:127], v[28:31], a[220:223], a[124:127]\n"
    "s_waitcnt lgkmcnt(5)\n"
    "ds_read_b64_tr_b16 v[54:55], v61 offset:23264\n"
    "v_accvgpr_read_b32 v103, a103\n"
    "v_mfma_f32_16x16x32_bf16 a[112:115], v[32:35], a[224:227], a[112:115]\n"
    "s_waitcnt lgkmcnt(4)\n"
    "v_cvt_pk_bf16_f32 v96, v96, v97\n"
    "v_cvt_pk_bf16_f32 v97, v98, v99\n"
    "v_mfma_f32_16x16x32_bf16 a[112:115], v[36:39], a[240:243], a[112:115]\n"
    "v_cvt_pk_bf16_f32 v98, v100, v101\n"
    "v_cvt_pk_bf16_f32 v99, v102, v103\n"
    "v_mfma_f32_16x16x32_bf16 a[116:119], v[32:35], a[228:231], a[116:119]\n"
    "buffer_store_dwordx4 v[96:99], v0, s[72:75], s14 offen offset:2048 sc1\n"
    "ds_write_b128 v9, v[96:99] offset:6144\n"
    "v_mfma_f32_16x16x32_bf16 a[116:119], v[36:39], a[244:247], a[116:119]\n"
    "v_accvgpr_read_b32 v104, a104\n"
    "v_accvgpr_read_b32 v105, a105\n"
    "v_mfma_f32_16x16x32_bf16 a[120:123], v[32:35], a[232:235], a[120:123]\n"
    "ds_read_b64_tr_b16 v[56:57], v61 offset:27872\n"
    "v_accvgpr_read_b32 v106, a106\n"
    "v_mfma_f32_16x16x32_bf16 a[120:123], v[36:39], a[248:251], a[120:123]\n"
    "ds_read_b64_tr_b16 v[58:59], v61 offset:32480\n"
    "v_accvgpr_read_b32 v107, a107\n"
    "v_mfma_f32_16x16x32_bf16 a[124:127], v[32:35], a[236:239], a[124:127]\n"
    "ds_read_b64_tr_b16 v[16:17], v61 offset:37088\n"
    "v_accvgpr_read_b32 v108, a108\n"
    "v_mfma_f32_16x16x32_bf16 a[124:127], v[36:39], a[252:255], a[124:127]\n"
    "s_waitcnt lgkmcnt(6)\n"
    "ds_read_b64_tr_b16 v[18:19], v61 offset:41696\n"
    "v_accvgpr_read_b32 v109, a109\n"
    "v_mfma_f32_16x16x32_bf16 a[112:115], v[48:51], v[128:131], a[112:115]\n"
    "s_waitcnt lgkmcnt(5)\n"
    "v_accvgpr_read_b32 v110, a110\n"
    "v_accvgpr_read_b32 v111, a111\n"
    "v_mfma_f32_16x16x32_bf16 a[112:115], v[52:55], v[144:147], a[112:115]\n"
    "v_cvt_pk_bf16_f32 v104, v104, v105\n"
    "v_cvt_pk_bf16_f32 v105, v106, v107\n"
    "v_mfma_f32_16x16x32_bf16 a[116:119], v[48:51], v[132:135], a[116:119]\n"
    "v_cvt_pk_bf16_f32 v106, v108, v109\n"
    "v_cvt_pk_bf16_f32 v107, v110, v111\n"
    "v_mfma_f32_16x16x32_bf16 a[116:119], v[52:55], v[148:151], a[116:119]\n"
    "buffer_store_dwordx4 v[104:107], v0, s[72:75], s15 offen offset:2048 sc1\n"
    "ds_write_b128 v9, v[104:107] offset:9216\n"
    "v_mfma_f32_16x16x32_bf16 a[120:123], v[48:51], v[136:139], a[120:123]\n"
    "ds_read_b64_tr_b16 v[20:21], v60 offset:256\n"
    "v_mfma_f32_16x16x32_bf16 a[120:123], v[52:55], v[152:155], a[120:123]\n"
    "ds_read_b64_tr_b16 v[22:23], v60 offset:4864\n"
    "v_mfma_f32_16x16x32_bf16 a[124:127], v[48:51], v[140:143], a[124:127]\n"
    "ds_read_b64_tr_b16 v[24:25], v60 offset:9472\n"
    "v_mfma_f32_16x16x32_bf16 a[124:127], v[52:55], v[156:159], a[124:127]\n"
    "s_waitcnt lgkmcnt(6)\n"
    "ds_read_b64_tr_b16 v[26:27], v60 offset:14080\n"
    "v_mfma_f32_16x16x32_bf16 a[112:115], v[56:59], v[160:163], a[112:115]\n"
    "s_waitcnt lgkmcnt(5)\n"
    "v_mfma_f32_16x16x32_bf16 a[112:115], v[16:19], v[176:179], a[112:115]\n"
    "v_mfma_f32_16x16x32_bf16 a[116:119], v[56:59], v[164:167], a[116:119]\n"
    "v_mfma_f32_16x16x32_bf16 a[116:119], v[16:19], v[180:183], a[116:119]\n"
    "v_mfma_f32_16x16x32_bf16 a[120:123], v[56:59], v[168:171], a[120:123]\n"
    "ds_read_b64_tr_b16 v[28:29], v60 offset:18688\n"
    "v_mfma_f32_16x16x32_bf16 a[120:123], v[16:19], v[184:187], a[120:123]\n"
    "ds_read_b64_tr_b16 v[30:31], v60 offset:23296\n"
    "v_mfma_f32_16x16x32_bf16 a[124:127], v[56:59], v[172:175], a[124:127]\n"
    "ds_read_b64_tr_b16 v[32:33], v60 offset:27904\n"
    "v_mfma_f32_16x16x32_bf16 a[124:127], v[16:19], v[188:191], a[124:127]\n"
    "s_waitcnt lgkmcnt(5)\n"
    "ds_read_b64_tr_b16 v[34:35], v60 offset:32512\n"
    "v_mfma_f32_16x16x32_bf16 a[128:131], v[20:23], a[192:195], a[128:131]\n"
    "s_waitcnt lgkmcnt(4)\n"
    "v_mfma_f32_16x16x32_bf16 a[128:131], v[24:27], a[208:211], a[128:131]\n"
    "v_mfma_f32_16x16x32_bf16 a[132:135], v[20:23], a[196:199], a[132:135]\n"
    "v_accvgpr_read_b32 v96, a112\n"
    "v_accvgpr_read_b32 v97, a113\n"
    "v_mfma_f32_16x16x32_bf16 a[132:135], v[24:27], a[212:215], a[132:135]\n"
    "v_accvgpr_read_b32 v98, a114\n"
    "v_accvgpr_read_b32 v99, a115\n"
    "v_mfma_f32_16x16x32_bf16 a[136:139], v[20:23], a[200:203], a[136:139]\n"
    "ds_read_b64_tr_b16 v[36:37], v61 offset:9472\n"
    "v_accvgpr_read_b32 v100, a116\n"
    "v_mfma_f32_16x16x32_bf16 a[136:139], v[24:27], a[216:219], a[136:139]\n"
    "ds_read_b64_tr_b16 v[38:39], v61 offset:14080\n"
    "v_accvgpr_read_b32 v101, a117\n"
    "v_mfma_f32_16x16x32_bf16 a[140:143], v[20:23], a[204:207], a[140:143]\n"
    "ds_read_b64_tr_b16 v[48:49], v61 offset:18688\n"
    "v_accvgpr_read_b32 v102, a118\n"
    "v_mfma_f32_16x16x32_bf16 a[140:143], v[24:27], a[220:223], a[140:143]\n"
    "s_waitcnt lgkmcnt(5)\n"
    "ds_read_b64_tr_b16 v[50:51], v61 offset:23296\n"
    "v_accvgpr_read_b32 v103, a119\n"
    "v_mfma_f32_16x16x32_bf16 a[128:131], v[28:31], a[224:227], a[128:131]\n"
    "s_waitcnt lgkmcnt(4)\n"
    "v_cvt_pk_bf16_f32 v96, v96, v97\n"
    "v_cvt_pk_bf16_f32 v97, v98, v99\n"
    "v_mfma_f32_16x16x32_bf16 a[128:131], v[32:35], a[240:243], a[128:131]\n"
    "v_add_u32 v46, 1, v46\n"
    "v_cvt_pk_bf16_f32 v98, v100, v101\n"
    "v_mfma_f32_16x16x32_bf16 a[132:135], v[28:31], a[228:231], a[132:135]\n"
    "s_waitcnt vmcnt(2)\n"
    "buffer_store_dwordx2 v[46:47], v11, s[76:79], 0 offen sc1\n"
    "v_cvt_pk_bf16_f32 v99, v102, v103\n"
    "v_mfma_f32_16x16x32_bf16 a[132:135], v[32:35], a[244:247], a[132:135]\n"
    "buffer_store_dwordx4 v[96:99], v0, s[72:75], s14 offen offset:3072 sc1\n"
    "ds_write_b128 v9, v[96:99] offset:7168\n"
    "v_mfma_f32_16x16x32_bf16 a[136:139], v[28:31], a[232:235], a[136:139]\n"
    "ds_read_b64_tr_b16 v[52:53], v61 offset:27904\n"
    "v_accvgpr_read_b32 v104, a120\n"
    "v_mfma_f32_16x16x32_bf16 a[136:139], v[32:35], a[248:251], a[136:139]\n"
    "ds_read_b64_tr_b16 v[54:55], v61 offset:32512\n"
    "v_accvgpr_read_b32 v105, a121\n"
    "v_mfma_f32_16x16x32_bf16 a[140:143], v[28:31], a[236:239], a[140:143]\n"
    "ds_read_b64_tr_b16 v[56:57], v61 offset:37120\n"
    "v_accvgpr_read_b32 v106, a122\n"
    "v_mfma_f32_16x16x32_bf16 a[140:143], v[32:35], a[252:255], a[140:143]\n"
    "s_waitcnt lgkmcnt(6)\n"
    "ds_read_b64_tr_b16 v[58:59], v61 offset:41728\n"
    "v_accvgpr_read_b32 v107, a123\n"
    "v_mfma_f32_16x16x32_bf16 a[128:131], v[36:39], v[128:131], a[128:131]\n"
    "s_waitcnt lgkmcnt(5)\n"
    "v_accvgpr_read_b32 v108, a124\n"
    "v_accvgpr_read_b32 v109, a125\n"
    "v_mfma_f32_16x16x32_bf16 a[128:131], v[48:51], v[144:147], a[128:131]\n"
    "v_accvgpr_read_b32 v110, a126\n"
    "v_accvgpr_read_b32 v111, a127\n"
    "v_mfma_f32_16x16x32_bf16 a[132:135], v[36:39], v[132:135], a[132:135]\n"
    "v_cvt_pk_bf16_f32 v104, v104, v105\n"
    "v_cvt_pk_bf16_f32 v105, v106, v107\n"
    "v_mfma_f32_16x16x32_bf16 a[132:135], v[48:51], v[148:151], a[132:135]\n"
    "v_cvt_pk_bf16_f32 v106, v108, v109\n"
    "v_cvt_pk_bf16_f32 v107, v110, v111\n"
    "v_mfma_f32_16x16x32_bf16 a[136:139], v[36:39], v[136:139], a[136:139]\n"
    "buffer_store_dwordx4 v[104:107], v0, s[72:75], s15 offen offset:3072 sc1\n"
    "ds_write_b128 v9, v[104:107] offset:10240\n"
    "v_mfma_f32_16x16x32_bf16 a[136:139], v[48:51], v[152:155], a[136:139]\n"
    "v_mfma_f32_16x16x32_bf16 a[140:143], v[36:39], v[140:143], a[140:143]\n"
    "v_mfma_f32_16x16x32_bf16 a[140:143], v[48:51], v[156:159], a[140:143]\n"
    "s_waitcnt lgkmcnt(3)\n"
    "v_mfma_f32_16x16x32_bf16 a[128:131], v[52:55], v[160:163], a[128:131]\n"
    "s_waitcnt lgkmcnt(1)\n"
    "v_mfma_f32_16x16x32_bf16 a[128:131], v[56:59], v[176:179], a[128:131]\n"
    "v_mfma_f32_16x16x32_bf16 a[132:135], v[52:55], v[164:167], a[132:135]\n"
    "v_mfma_f32_16x16x32_bf16 a[132:135], v[56:59], v[180:183], a[132:135]\n"
    "v_mfma_f32_16x16x32_bf16 a[136:139], v[52:55], v[168:171], a[136:139]\n"
    "v_mfma_f32_16x16x32_bf16 a[136:139], v[56:59], v[184:187], a[136:139]\n"
    "v_mfma_f32_16x16x32_bf16 a[140:143], v[52:55], v[172:175], a[140:143]\n"
    "v_mfma_f32_16x16x32_bf16 a[140:143], v[56:59], v[188:191], a[140:143]\n"
    "L_FIN0%=:\n"
    "s_add_u32 s14, s14, 4096\n"
    "s_nop 0\n"
    "v_accvgpr_read_b32 v96, a128\n"
    "v_accvgpr_read_b32 v97, a129\n"
    "v_accvgpr_read_b32 v98, a130\n"
    "v_accvgpr_read_b32 v99, a131\n"
    "v_accvgpr_read_b32 v100, a132\n"
    "v_accvgpr_read_b32 v101, a133\n"
    "v_accvgpr_read_b32 v102, a134\n"
    "v_accvgpr_read_b32 v103, a135\n"
    "v_cvt_pk_bf16_f32 v96, v96, v97\n"
    "v_cvt_pk_bf16_f32 v97, v98, v99\n"
    "v_cvt_pk_bf16_f32 v98, v100, v101\n"
    "v_cvt_pk_bf16_f32 v99, v102, v103\n"
    "buffer_store_dwordx4 v[96:99], v0, s[72:75], s14 offen offset:0 sc1\n"
    "ds_write_b128 v9, v[96:99] offset:8192\n"
    "s_add_u32 s15, s15, 4096\n"
    "v_accvgpr_read_b32 v104, a136\n"
    "v_accvgpr_read_b32 v105, a137\n"
    "v_accvgpr_read_b32 v106, a138\n"
    "v_accvgpr_read_b32 v107, a139\n"
    "v_accvgpr_read_b32 v108, a140\n"
    "v_accvgpr_read_b32 v109, a141\n"
    "v_accvgpr_read_b32 v110, a142\n"
    "v_accvgpr_read_b32 v111, a143\n"
    "v_cvt_pk_bf16_f32 v104, v104, v105\n"
    "v_cvt_pk_bf16_f32 v105, v106, v107\n"
    "v_cvt_pk_bf16_f32 v106, v108, v109\n"
    "v_cvt_pk_bf16_f32 v107, v110, v111\n"
    "buffer_store_dwordx4 v[104:107], v0, s[72:75], s15 offen offset:0 sc1\n"
    "ds_write_b128 v9, v[104:107] offset:11264\n"
    "v_add_u32 v46, 1, v46\n"
    "buffer_load_dwordx2 v[14:15], v1, s[76:79], s13 offen sc1\n"
    "s_waitcnt lgkmcnt(0)\n"
    "s_barrier\n"
    "ds_read_b128 a[0:3], v8 offset:0\n"
    "ds_read_b128 a[16:19], v8 offset:1024\n"
    "ds_read_b128 a[32:35], v8 offset:2048\n"
    "s_waitcnt vmcnt(1)\n"
    "buffer_store_dwordx2 v[46:47], v11, s[76:79], 0 offen sc1\n"
    "s_cmp_eq_u32 s51, 3\n"
    "s_cbranch_scc1 L_END%=\n"
    "s_waitcnt vmcnt(1)\n"
    "v_subrev_u32 v14, s38, v14\n"
    "v_xor_b32 v15, v47, v15\n"
    "v_cmp_lt_u64 vcc, 15, v[14:15]\n"
    "s_nop 0\n"
    "s_and_b64 s[98:99], vcc, s[58:59]\n"
    "s_cbranch_scc1 L_SP5_%=\n"
    "L_BK5_%=:\n"
    "s_add_u32 s92, s39, s53\n"
    "buffer_load_dwordx4 a[4:7], v0, s[72:75], s92 offen offset:0 sc1\n"
    "buffer_load_dwordx4 a[20:23], v0, s[72:75], s92 offen offset:1024 sc1\n"
    "buffer_load_dwordx4 a[36:39], v0, s[72:75], s92 offen offset:2048 sc1\n"
    "s_add_u32 s92, s39, s54\n"
    "buffer_load_dwordx4 a[8:11], v0, s[72:75], s92 offen offset:0 sc1\n"
    "buffer_load_dwordx4 a[24:27], v0, s[72:75], s92 offen offset:1024 sc1\n"
    "buffer_load_dwordx4 a[40:43], v0, s[72:75], s92 offen offset:2048 sc1\n"
    "s_add_u32 s92, s39, s55\n"
    "buffer_load_dwordx4 a[12:15], v0, s[72:75], s92 offen offset:0 sc1\n"
    "buffer_load_dwordx4 a[28:31], v0, s[72:75], s92 offen offset:1024 sc1\n"
    "buffer_load_dwordx4 a[44:47], v0, s[72:75], s92 offen offset:2048 sc1\n"
    "s_add_u32 s39, s39, 9216\n"
    "s_and_b64 s[98:99], vcc, s[56:57]\n"
    "s_cselect_b32 s48, 0x40000000, s39\n"
    "s_add_u32 s92, s48, s52\n"
    "buffer_load_dwordx4 a[48:51], v0, s[72:75], s92 offen offset:0 sc1\n"
    "buffer_load_dwordx4 a[64:67], v0, s[72:75], s92 offen offset:1024 sc1\n"
    "buffer_load_dwordx4 a[80:83], v0, s[72:75], s92 offen offset:2048 sc1\n"
    "s_add_u32 s92, s48, s53\n"
    "buffer_load_dwordx4 a[52:55], v0, s[72:75], s92 offen offset:0 sc1\n"
    "buffer_load_dwordx4 a[68:71], v0, s[72:75], s92 offen offset:1024 sc1\n"
    "buffer_load_dwordx4 a[84:87], v0, s[72:75], s92 offen offset:2048 sc1\n"
    "s_add_u32 s92, s48, s54\n"
    "buffer_load_dwordx4 a[56:59], v0, s[72:75], s92 offen offset:0 sc1\n"
    "buffer_load_dwordx4 a[72:75], v0, s[72:75], s92 offen offset:1024 sc1\n"
    "buffer_load_dwordx4 a[88:91], v0, s[72:75], s92 offen offset:2048 sc1\n"
    "s_add_u32 s92, s48, s55\n"
    "buffer_load_dwordx4 a[60:63], v0, s[72:75], s92 offen offset:0 sc1\n"
    "buffer_load_dwordx4 a[76:79], v0, s[72:75], s92 offen offset:1024 sc1\n"
    "buffer_load_dwordx4 a[92:95], v0, s[72:75], s92 offen offset:2048 sc1\n"
    "buffer_load_dwordx2 v[14:15], v1, s[76:79], s13 offen sc1\n"
    "s_waitcnt lgkmcnt(2)\n"
    "v_mfma_f32_16x16x32_bf16 v[64:67], v[40:43], a[0:3], 0\n"
    "v_lshlrev_b32 v16, 16, v112\n"
    "v_mfma_f32_16x16x32_bf16 v[68:71], v[42:45], a[0:3], 0\n"
    "s_waitcnt lgkmcnt(1)\n"
    "v_and_b32 v17, 0xffff0000, v112\n"
    "v_mfma_f32_16x16x32_bf16 v[72:75], v[40:43], a[16:19], 0\n"
    "v_lshlrev_b32 v18, 16, v113\n"
    "v_mfma_f32_16x16x32_bf16 v[76:79], v[42:45], a[16:19], 0\n"
    "s_waitcnt lgkmcnt(0)\n"
    "v_and_b32 v19, 0xffff0000, v113\n"
    "v_mfma_f32_16x16x32_bf16 v[80:83], v[40:43], a[32:35], 0\n"
    "v_lshlrev_b32 v20, 16, v114\n"
    "v_mfma_f32_16x16x32_bf16 v[84:87], v[42:45], a[32:35], 0\n"
    "s_waitcnt vmcnt(21)\n"
    "v_and_b32 v21, 0xffff0000, v114\n"
    "v_mfma_f32_16x16x32_bf16 v[64:67], v[40:43], a[4:7], v[64:67]\n"
    "v_lshlrev_b32 v22, 16, v115\n"
    "v_mfma_f32_16x16x32_bf16 v[68:71], v[42:45], a[4:7], v[68:71]\n"
    "s_waitcnt vmcnt(20)\n"
    "v_and_b32 v23, 0xffff0000, v115\n"
    "v_mfma_f32_16x16x32_bf16 v[72:75], v[40:43], a[20:23], v[72:75]\n"
    "v_lshlrev_b32 v24, 16, v116\n"
    "v_mfma_f32_16x16x32_bf16 v[76:79], v[42:45], a[20:23], v[76:79]\n"
    "s_waitcnt vmcnt(19)\n"
    "v_and_b32 v25, 0xffff0000, v116\n"
    "v_mfma_f32_16x16x32_bf16 v[80:83], v[40:43], a[36:39], v[80:83]\n"
    "v_lshlrev_b32 v26, 16, v117\n"
    "v_mfma_f32_16x16x32_bf16 v[84:87], v[42:45], a[36:39], v[84:87]\n"
    "s_waitcnt vmcnt(18)\n"
    "v_and_b32 v27, 0xffff0000, v117\n"
    "v_mfma_f32_16x16x32_bf16 v[64:67], v[40:43], a[8:11], v[64:67]\n"
    "v_lshlrev_b32 v28, 16, v118\n"
    "v_mfma_f32_16x16x32_bf16 v[68:71], v[42:45], a[8:11], v[68:71]\n"
    "s_waitcnt vmcnt(17)\n"
    "v_and_b32 v29, 0xffff0000, v118\n"
    "v_mfma_f32_16x16x32_bf16 v[72:75], v[40:43], a[24:27], v[72:75]\n"
    "v_lshlrev_b32 v30, 16, v119\n"
    "v_mfma_f32_16x16x32_bf16 v[76:79], v[42:45], a[24:27], v[76:79]\n"
    "s_waitcnt vmcnt(16)\n"
    "v_and_b32 v31, 0xffff0000, v119\n"
    "v_mfma_f32_16x16x32_bf16 v[80:83], v[40:43], a[40:43], v[80:83]\n"
    "v_lshlrev_b32 v32, 16, v120\n"
    "v_mfma_f32_16x16x32_bf16 v[84:87], v[42:45], a[40:43], v[84:87]\n"
    "s_waitcnt vmcnt(15)\n"
    "v_and_b32 v33, 0xffff0000, v120\n"
    "v_mfma_f32_16x16x32_bf16 v[64:67], v[40:43], a[12:15], v[64:67]\n"
    "v_lshlrev_b32 v34, 16, v121\n"
    "v_mfma_f32_16x16x32_bf16 v[68:71], v[42:45], a[12:15], v[68:71]\n"
    "s_waitcnt vmcnt(14)\n"
    "v_and_b32 v35, 0xffff0000, v121\n"
    "v_mfma_f32_16x16x32_bf16 v[72:75], v[40:43], a[28:31], v[72:75]\n"
    "v_lshlrev_b32 v36, 16, v122\n"
    "v_mfma_f32_16x16x32_bf16 v[76:79], v[42:45], a[28:31], v[76:79]\n"
    "s_waitcnt vmcnt(13)\n"
    "v_and_b32 v37, 0xffff0000, v122\n"
    "v_mfma_f32_16x16x32_bf16 v[80:83], v[40:43], a[44:47], v[80:83]\n"
    "v_lshlrev_b32 v38, 16, v123\n"
    "v_mfma_f32_16x16x32_bf16 v[84:87], v[42:45], a[44:47], v[84:87]\n"
    "v_and_b32 v39, 0xffff0000, v123\n"
    "v_mul_f32 v152, 0xbfb8aa3b, v64\n"
    "v_mul_f32 v153, 0xbfb8aa3b, v65\n"
    "v_mul_f32 v154, 0xbfb8aa3b, v66\n"
    "v_mul_f32 v155, 0xbfb8aa3b, v67\n"
    "v_mul_f32 v156, 0xbfb8aa3b, v68\n"
    "v_mul_f32 v157, 0xbfb8aa3b, v69\n"
    "v_mul_f32 v158, 0xbfb8aa3b, v70\n"
    "v_mul_f32 v159, 0xbfb8aa3b, v71\n"
    "v_mul_f32 v160, 0xbfb8aa3b, v72\n"
    "v_mul_f32 v161, 0xbfb8aa3b, v73\n"
    "v_mul_f32 v162, 0xbfb8aa3b, v74\n"
    "v_mul_f32 v163, 0xbfb8aa3b, v75\n"
    "v_mul_f32 v164, 0xbfb8aa3b, v76\n"
    "v_mul_f32 v165, 0xbfb8aa3b, v77\n"
    "v_mul_f32 v166, 0xbfb8aa3b, v78\n"
    "v_mul_f32 v167, 0xbfb8aa3b, v79\n"
    "v_mul_f32 v168, 0xbfb8aa3b, v80\n"
    "v_mul_f32 v169, 0xbfb8aa3b, v81\n"
    "v_mul_f32 v170, 0xbfb8aa3b, v82\n"
    "v_mul_f32 v171, 0xbfb8aa3b, v83\n"
    "v_mul_f32 v172, 0xbfb8aa3b, v84\n"
    "v_mul_f32 v173, 0xbfb8aa3b, v85\n"
    "v_mul_f32 v174, 0xbfb8aa3b, v86\n"
    "v_mul_f32 v175, 0xbfb8aa3b, v87\n"
    "v_exp_f32 v152, v152\n"
    "v_exp_f32 v153, v153\n"
    "v_exp_f32 v154, v154\n"
    "v_exp_f32 v155, v155\n"
    "v_exp_f32 v156, v156\n"
    "v_exp_f32 v157, v157\n"
    "v_exp_f32 v158, v158\n"
    "v_exp_f32 v159, v159\n"
    "v_exp_f32 v160, v160\n"
    "v_exp_f32 v161, v161\n"
    "v_exp_f32 v162, v162\n"
    "v_exp_f32 v163, v163\n"
    "v_exp_f32 v164, v164\n"
    "v_exp_f32 v165, v165\n"
    "v_exp_f32 v166, v166\n"
    "v_exp_f32 v167, v167\n"
    "v_exp_f32 v168, v168\n"
    "v_exp_f32 v169, v169\n"
    "v_exp_f32 v170, v170\n"
    "v_exp_f32 v171, v171\n"
    "v_exp_f32 v172, v172\n"
    "v_exp_f32 v173, v173\n"
    "v_exp_f32 v174, v174\n"
    "v_exp_f32 v175, v175\n"
    "v_add_f32 v152, 1.0, v152\n"
    "v_add_f32 v153, 1.0, v153\n"
    "v_add_f32 v154, 1.0, v154\n"
    "v_add_f32 v155, 1.0, v155\n"
    "v_add_f32 v156, 1.0, v156\n"
    "v_add_f32 v157, 1.0, v157\n"
    "v_add_f32 v158, 1.0, v158\n"
    "v_add_f32 v159, 1.0, v159\n"
    "v_add_f32 v160, 1.0, v160\n"
    "v_add_f32 v161, 1.0, v161\n"
    "v_add_f32 v162, 1.0, v162\n"
    "v_add_f32 v163, 1.0, v163\n"
    "v_add_f32 v164, 1.0, v164\n"
    "v_add_f32 v165, 1.0, v165\n"
    "v_add_f32 v166, 1.0, v166\n"
    "v_add_f32 v167, 1.0, v167\n"
    "v_add_f32 v168, 1.0, v168\n"
    "v_add_f32 v169, 1.0, v169\n"
    "v_add_f32 v170, 1.0, v170\n"
    "v_add_f32 v171, 1.0, v171\n"
    "v_add_f32 v172, 1.0, v172\n"
    "v_add_f32 v173, 1.0, v173\n"
    "v_add_f32 v174, 1.0, v174\n"
    "v_add_f32 v175, 1.0, v175\n"
    "v_rcp_f32 v152, v152\n"
    "v_rcp_f32 v153, v153\n"
    "v_rcp_f32 v154, v154\n"
    "v_rcp_f32 v155, v155\n"
    "v_rcp_f32 v156, v156\n"
    "v_rcp_f32 v157, v157\n"
    "v_rcp_f32 v158, v158\n"
    "v_rcp_f32 v159, v159\n"
    "v_rcp_f32 v160, v160\n"
    "v_rcp_f32 v161, v161\n"
    "v_rcp_f32 v162, v162\n"
    "v_rcp_f32 v163, v163\n"
    "v_rcp_f32 v164, v164\n"
    "v_rcp_f32 v165, v165\n"
    "v_rcp_f32 v166, v166\n"
    "v_rcp_f32 v167, v167\n"
    "v_rcp_f32 v168, v168\n"
    "v_rcp_f32 v169, v169\n"
    "v_rcp_f32 v170, v170\n"
    "v_rcp_f32 v171, v171\n"
    "v_rcp_f32 v172, v172\n"
    "v_rcp_f32 v173, v173\n"
    "v_rcp_f32 v174, v174\n"
    "v_rcp_f32 v175, v175\n"
    "v_mul_f32 v64, v64, v152\n"
    "v_mul_f32 v65, v65, v153\n"
    "v_mul_f32 v66, v66, v154\n"
    "v_mul_f32 v67, v67, v155\n"
    "v_mul_f32 v68, v68, v156\n"
    "v_mul_f32 v69, v69, v157\n"
    "v_mul_f32 v70, v70, v158\n"
    "v_mul_f32 v71, v71, v159\n"
    "v_mul_f32 v72, v72, v160\n"
    "v_mul_f32 v73, v73, v161\n"
    "v_mul_f32 v74, v74, v162\n"
    "v_mul_f32 v75, v75, v163\n"
    "v_mul_f32 v76, v76, v164\n"
    "v_mul_f32 v77, v77, v165\n"
    "v_mul_f32 v78, v78, v166\n"
    "v_mul_f32 v79, v79, v167\n"
    "v_mul_f32 v80, v80, v168\n"
    "v_mul_f32 v81, v81, v169\n"
    "v_mul_f32 v82, v82, v170\n"
    "v_mul_f32 v83, v83, v171\n"
    "v_mul_f32 v84, v84, v172\n"
    "v_mul_f32 v85, v85, v173\n"
    "v_mul_f32 v86, v86, v174\n"
    "v_mul_f32 v87, v87, v175\n"
    "s_cmp_eq_u32 s48, 0x40000000\n"
    "s_cbranch_scc0 L_SR0%=\n"
    "s_waitcnt vmcnt(0)\n"
    "v_subrev_u32 v14, s38, v14\n"
    "v_xor_b32 v15, v47, v15\n"
    "v_cmp_lt_u64 vcc, 15, v[14:15]\n"
    "s_nop 0\n"
    "s_and_b64 s[98:99], vcc, s[56:57]\n"
    "s_cbranch_scc1 L_SP6_%=\n"
    "L_BK6_%=:\n"
    "s_add_u32 s92, s39, s52\n"
    "buffer_load_dwordx4 a[48:51], v0, s[72:75], s92 offen offset:0 sc1\n"
    "buffer_load_dwordx4 a[64:67], v0, s[72:75], s92 offen offset:1024 sc1\n"
    "buffer_load_dwordx4 a[80:83], v0, s[72:75], s92 offen offset:2048 sc1\n"
    "s_add_u32 s92, s39, s53\n"
    "buffer_load_dwordx4 a[52:55], v0, s[72:75], s92 offen offset:0 sc1\n"
    "buffer_load_dwordx4 a[68:71], v0, s[72:75], s92 offen offset:1024 sc1\n"
    "buffer_load_dwordx4 a[84:87], v0, s[72:75], s92 offen offset:2048 sc1\n"
    "s_add_u32 s92, s39, s54\n"
    "buffer_load_dwordx4 a[56:59], v0, s[72:75], s92 offen offset:0 sc1\n"
    "buffer_load_dwordx4 a[72:75], v0, s[72:75], s92 offen offset:1024 sc1\n"
    "buffer_load_dwordx4 a[88:91], v0, s[72:75], s92 offen offset:2048 sc1\n"
    "s_add_u32 s92, s39, s55\n"
    "buffer_load_dwordx4 a[60:63], v0, s[72:75], s92 offen offset:0 sc1\n"
    "buffer_load_dwordx4 a[76:79], v0, s[72:75], s92 offen offset:1024 sc1\n"
    "buffer_load_dwordx4 a[92:95], v0, s[72:75], s92 offen offset:2048 sc1\n"
    "L_SR0%=:\n"
    "s_waitcnt vmcnt(11)\n"
    "v_mfma_f32_16x16x32_bf16 v[128:131], v[40:43], a[48:51], 0\n"
    "v_mfma_f32_16x16x32_bf16 v[132:135], v[42:45], a[48:51], 0\n"
    "s_waitcnt vmcnt(10)\n"
    "v_mfma_f32_16x16x32_bf16 v[136:139], v[40:43], a[64:67], 0\n"
    "v_mfma_f32_16x16x32_bf16 v[140:143], v[42:45], a[64:67], 0\n"
    "s_waitcnt vmcnt(9)\n"
    "v_mfma_f32_16x16x32_bf16 v[144:147], v[40:43], a[80:83], 0\n"
    "v_mfma_f32_16x16x32_bf16 v[148:151], v[42:45], a[80:83], 0\n"
    "s_waitcnt vmcnt(8)\n"
    "v_mfma_f32_16x16x32_bf16 v[128:131], v[40:43], a[52:55], v[128:131]\n"
    "v_mfma_f32_16x16x32_bf16 v[132:135], v[42:45], a[52:55], v[132:135]\n"
    "s_waitcnt vmcnt(7)\n"
    "v_mfma_f32_16x16x32_bf16 v[136:139], v[40:43], a[68:71], v[136:139]\n"
    "v_mfma_f32_16x16x32_bf16 v[140:143], v[42:45], a[68:71], v[140:143]\n"
    "s_waitcnt vmcnt(6)\n"
    "v_mfma_f32_16x16x32_bf16 v[144:147], v[40:43], a[84:87], v[144:147]\n"
    "v_mfma_f32_16x16x32_bf16 v[148:151], v[42:45], a[84:87], v[148:151]\n"
    "s_waitcnt vmcnt(5)\n"
    "v_mfma_f32_16x16x32_bf16 v[128:131], v[40:43], a[56:59], v[128:131]\n"
    "v_mfma_f32_16x16x32_bf16 v[132:135], v[42:45], a[56:59], v[132:135]\n"
    "s_waitcnt vmcnt(4)\n"
    "v_mfma_f32_16x16x32_bf16 v[136:139], v[40:43], a[72:75], v[136:139]\n"
    "v_mfma_f32_16x16x32_bf16 v[140:143], v[42:45], a[72:75], v[140:143]\n"
    "s_waitcnt vmcnt(3)\n"
    "v_mfma_f32_16x16x32_bf16 v[144:147], v[40:43], a[88:91], v[144:147]\n"
    "v_mfma_f32_16x16x32_bf16 v[148:151], v[42:45], a[88:91], v[148:151]\n"
    "s_waitcnt vmcnt(2)\n"
    "v_mfma_f32_16x16x32_bf16 v[128:131], v[40:43], a[60:63], v[128:131]\n"
    "v_mfma_f32_16x16x32_bf16 v[132:135], v[42:45], a[60:63], v[132:135]\n"
    "s_waitcnt vmcnt(1)\n"
    "v_mfma_f32_16x16x32_bf16 v[136:139], v[40:43], a[76:79], v[136:139]\n"
    "v_mfma_f32_16x16x32_bf16 v[140:143], v[42:45], a[76:79], v[140:143]\n"
    "s_waitcnt vmcnt(0)\n"
    "v_mfma_f32_16x16x32_bf16 v[144:147], v[40:43], a[92:95], v[144:147]\n"
    "v_mfma_f32_16x16x32_bf16 v[148:151], v[42:45], a[92:95], v[148:151]\n"
    "s_nop 2\n"
    "v_fma_f32 v128, v128, v64, v16\n"
    "v_fma_f32 v129, v129, v65, v17\n"
    "v_fma_f32 v130, v130, v66, v18\n"
    "v_fma_f32 v131, v131, v67, v19\n"
    "v_fma_f32 v132, v132, v68, v20\n"
    "v_fma_f32 v133, v133, v69, v21\n"
    "v_fma_f32 v134, v134, v70, v22\n"
    "v_fma_f32 v135, v135, v71, v23\n"
    "v_cvt_pk_bf16_f32 v128, v128, v129\n"
    "v_cvt_pk_bf16_f32 v129, v130, v131\n"
    "v_cvt_pk_bf16_f32 v130, v132, v133\n"
    "v_cvt_pk_bf16_f32 v131, v134, v135\n"
    "buffer_store_dwordx2 v[128:129], v12, s[28:31], 0 offen offset:0\n"
    "buffer_store_dwordx2 v[130:131], v63, s[28:31], 0 offen offset:0\n"
    "v_fma_f32 v136, v136, v72, v24\n"
    "v_fma_f32 v137, v137, v73, v25\n"
    "v_fma_f32 v138, v138, v74, v26\n"
    "v_fma_f32 v139, v139, v75, v27\n"
    "v_fma_f32 v140, v140, v76, v28\n"
    "v_fma_f32 v141, v141, v77, v29\n"
    "v_fma_f32 v142, v142, v78, v30\n"
    "v_fma_f32 v143, v143, v79, v31\n"
    "v_cvt_pk_bf16_f32 v136, v136, v137\n"
    "v_cvt_pk_bf16_f32 v137, v138, v139\n"
    "v_cvt_pk_bf16_f32 v138, v140, v141\n"
    "v_cvt_pk_bf16_f32 v139, v142, v143\n"
    "buffer_store_dwordx2 v[136:137], v12, s[28:31], 0 offen offset:32\n"
    "buffer_store_dwordx2 v[138:139], v63, s[28:31], 0 offen offset:32\n"
    "v_fma_f32 v144, v144, v80, v32\n"
    "v_fma_f32 v145, v145, v81, v33\n"
    "v_fma_f32 v146, v146, v82, v34\n"
    "v_fma_f32 v147, v147, v83, v35\n"
    "v_fma_f32 v148, v148, v84, v36\n"
    "v_fma_f32 v149, v149, v85, v37\n"
    "v_fma_f32 v150, v150, v86, v38\n"
    "v_fma_f32 v151, v151, v87, v39\n"
    "v_cvt_pk_bf16_f32 v144, v144, v145\n"
    "v_cvt_pk_bf16_f32 v145, v146, v147\n"
    "v_cvt_pk_bf16_f32 v146, v148, v149\n"
    "v_cvt_pk_bf16_f32 v147, v150, v151\n"
    "buffer_store_dwordx2 v[144:145], v12, s[28:31], 0 offen offset:64\n"
    "buffer_store_dwordx2 v[146:147], v63, s[28:31], 0 offen offset:64\n"
    "s_branch L_END%=\n"
    ".p2align 6\n"
    "L_CS%=:\n"
    "s_waitcnt lgkmcnt(4)\n"
    "v_mfma_f32_16x16x32_bf16 a[0:3], v[16:19], v[128:131], 0\n"
    "ds_read_b64_tr_b16 v[28:29], v61 offset:9312\n"
    "v_cvt_pk_bf16_f32 v136, v136, v137\n"
    "v_mfma_f32_16x16x32_bf16 a[4:7], v[16:19], v[160:163], 0\n"
    "s_add_u32 s38, s95, 3\n"
    "ds_read_b64_tr_b16 v[30:31], v61 offset:13920\n"
    "v_cvt_pk_bf16_f32 v137, v138, v139\n"
    "v_mfma_f32_16x16x32_bf16 a[8:11], v[16:19], v[192:195], 0\n"
    "s_mov_b32 s91, 85793045\n"
    "buffer_load_dwordx2 v[14:15], v1, s[76:79], s12 offen sc1\n"
    "v_cvt_pk_bf16_f32 v138, v140, v141\n"
    "v_mfma_f32_16x16x32_bf16 a[12:15], v[16:19], v[224:227], 0\n"
    "s_waitcnt lgkmcnt(4)\n"
    "s_lshr_b32 s91, s91, s36\n"
    "s_xor_b32 s91, s91, s16\n"
    "s_and_b32 s91, s91, 31\n"
    "v_cvt_pk_bf16_f32 v139, v142, v143\n"
    "v_mul_f32 v168, v168, v109\n"
    "v_mfma_f32_16x16x32_bf16 a[16:19], v[20:23], v[128:131], 0\n"
    "s_mul_i32 s91, s91, 36864\n"
    "ds_read_b64_tr_b16 v[32:33], v61 offset:9344\n"
    "v_mul_f32 v169, v169, v109\n"
    "v_mfma_f32_16x16x32_bf16 a[20:23], v[20:23], v[160:163], 0\n"
    "s_add_u32 m0, s17, 16384\n"
    "ds_read_b64_tr_b16 v[34:35], v61 offset:13952\n"
    "v_mul_f32 v170, v170, v109\n"
    "v_mfma_f32_16x16x32_bf16 a[24:27], v[20:23], v[192:195], 0\n"
    "buffer_load_dwordx4 v2, s[40:43], s91 offen lds\n"
    "v_mul_f32 v171, v171, v109\n"
    "v_mfma_f32_16x16x32_bf16 a[28:31], v[20:23], v[224:227], 0\n"
    "s_waitcnt lgkmcnt(4)\n"
    "v_mul_f32 v172, v172, v109\n"
    "v_mul_f32 v173, v173, v109\n"
    "v_mfma_f32_16x16x32_bf16 a[32:35], v[24:27], v[128:131], 0\n"
    "ds_read_b64_tr_b16 v[36:37], v61 offset:9376\n"
    "v_mul_f32 v174, v174, v109\n"
    "v_mfma_f32_16x16x32_bf16 a[36:39], v[24:27], v[160:163], 0\n"
    "ds_read_b64_tr_b16 v[38:39], v61 offset:13984\n"
    "v_mul_f32 v175, v175, v109\n"
    "v_mfma_f32_16x16x32_bf16 a[40:43], v[24:27], v[192:195], 0\n"
    "buffer_load_dwordx4 v3, s[40:43], s91 offen offset:1024 lds\n"
    "v_cvt_pk_bf16_f32 v168, v168, v169\n"
    "v_mfma_f32_16x16x32_bf16 a[44:47], v[24:27], v[224:227], 0\n"
    "s_waitcnt lgkmcnt(4)\n"
    "v_cvt_pk_bf16_f32 v169, v170, v171\n"
    "v_cvt_pk_bf16_f32 v170, v172, v173\n"
    "v_mfma_f32_16x16x32_bf16 a[48:51], v[28:31], v[128:131], 0\n"
    "ds_read_b64_tr_b16 v[48:49], v61 offset:9408\n"
    "v_cvt_pk_bf16_f32 v171, v174, v175\n"
    "v_mfma_f32_16x16x32_bf16 a[52:55], v[28:31], v[160:163], 0\n"
    "ds_read_b64_tr_b16 v[50:51], v61 offset:14016\n"
    "v_mul_f32 v200, v200, v110\n"
    "v_mfma_f32_16x16x32_bf16 a[56:59], v[28:31], v[192:195], 0\n"
    "v_mul_f32 v201, v201, v110\n"
    "v_mul_f32 v202, v202, v110\n"
    "v_mfma_f32_16x16x32_bf16 a[60:63], v[28:31], v[224:227], 0\n"
    "s_waitcnt lgkmcnt(4)\n"
    "buffer_load_dwordx4 v4, s[40:43], s91 offen offset:2048 lds\n"
    "v_mul_f32 v203, v203, v110\n"
    "v_mfma_f32_16x16x32_bf16 a[64:67], v[32:35], v[128:131], 0\n"
    "ds_read_b64_tr_b16 v[52:53], v61 offset:9440\n"
    "v_mul_f32 v204, v204, v110\n"
    "v_mfma_f32_16x16x32_bf16 a[68:71], v[32:35], v[160:163], 0\n"
    "ds_read_b64_tr_b16 v[54:55], v61 offset:14048\n"
    "v_mul_f32 v205, v205, v110\n"
    "v_mfma_f32_16x16x32_bf16 a[72:75], v[32:35], v[192:195], 0\n"
    "v_mul_f32 v206, v206, v110\n"
    "v_mul_f32 v207, v207, v110\n"
    "v_mfma_f32_16x16x32_bf16 a[76:79], v[32:35], v[224:227], 0\n"
    "s_waitcnt lgkmcnt(4)\n"
    "v_cvt_pk_bf16_f32 v200, v200, v201\n"
    "v_cvt_pk_bf16_f32 v201, v202, v203\n"
    "v_mfma_f32_16x16x32_bf16 a[80:83], v[36:39], v[128:131], 0\n"
    "ds_read_b64_tr_b16 v[56:57], v61 offset:9472\n"
    "v_cvt_pk_bf16_f32 v202, v204, v205\n"
    "v_mfma_f32_16x16x32_bf16 a[84:87], v[36:39], v[160:163], 0\n"
    "s_mov_b32 s87, 4096\n"
    "ds_read_b64_tr_b16 v[58:59], v61 offset:14080\n"
    "v_cvt_pk_bf16_f32 v203, v206, v207\n"
    "v_mfma_f32_16x16x32_bf16 a[88:91], v[36:39], v[192:195], 0\n"
    "buffer_store_dwordx4 v[136:139], v0, s[64:67], s87 offen offset:0 sc1\n"
    "v_mul_f32 v232, v232, v111\n"
    "v_mfma_f32_16x16x32_bf16 a[92:95], v[36:39], v[224:227], 0\n"
    "s_waitcnt lgkmcnt(4)\n"
    "v_mul_f32 v233, v233, v111\n"
    "v_mul_f32 v234, v234, v111\n"
    "v_mfma_f32_16x16x32_bf16 a[96:99], v[48:51], v[128:131], 0\n"
    "ds_read_b64_tr_b16 v[16:17], v61 offset:18432\n"
    "v_mul_f32 v235, v235, v111\n"
    "v_mfma_f32_16x16x32_bf16 a[100:103], v[48:51], v[160:163], 0\n"
    "ds_read_b64_tr_b16 v[18:19], v61 offset:23040\n"
    "v_mul_f32 v236, v236, v111\n"
    "v_mfma_f32_16x16x32_bf16 a[104:107], v[48:51], v[192:195], 0\n"
    "buffer_load_dwordx4 v5, s[40:43], s91 offen offset:3072 lds\n"
    "v_mul_f32 v237, v237, v111\n"
    "v_mfma_f32_16x16x32_bf16 a[108:111], v[48:51], v[224:227], 0\n"
    "s_waitcnt lgkmcnt(4)\n"
    "v_mul_f32 v238, v238, v111\n"
    "v_mul_f32 v239, v239, v111\n"
    "v_mfma_f32_16x16x32_bf16 a[112:115], v[52:55], v[128:131], 0\n"
    "ds_read_b64_tr_b16 v[20:21], v61 offset:18464\n"
    "v_cvt_pk_bf16_f32 v232, v232, v233\n"
    "v_mfma_f32_16x16x32_bf16 a[116:119], v[52:55], v[160:163], 0\n"
    "s_add_u32 m0, s17, 20480\n"
    "ds_read_b64_tr_b16 v[22:23], v61 offset:23072\n"
    "v_cvt_pk_bf16_f32 v233, v234, v235\n"
    "v_mfma_f32_16x16x32_bf16 a[120:123], v[52:55], v[192:195], 0\n"
    "buffer_load_dwordx4 v6, s[40:43], s91 offen lds\n"
    "v_cvt_pk_bf16_f32 v234, v236, v237\n"
    "v_mfma_f32_16x16x32_bf16 a[124:127], v[52:55], v[224:227], 0\n"
    "s_waitcnt lgkmcnt(4)\n"
    "v_cvt_pk_bf16_f32 v235, v238, v239\n"
    "v_mul_f32 v144, v144, v108\n"
    "v_mfma_f32_16x16x32_bf16 a[128:131], v[56:59], v[128:131], 0\n"
    "ds_read_b64_tr_b16 v[24:25], v61 offset:18496\n"
    "v_mul_f32 v145, v145, v108\n"
    "v_mfma_f32_16x16x32_bf16 a[132:135], v[56:59], v[160:163], 0\n"
    "ds_read_b64_tr_b16 v[26:27], v61 offset:23104\n"
    "v_mul_f32 v146, v146, v108\n"
    "v_mfma_f32_16x16x32_bf16 a[136:139], v[56:59], v[192:195], 0\n"
    "v_mul_f32 v147, v147, v108\n"
    "v_mul_f32 v148, v148, v108\n"
    "v_mfma_f32_16x16x32_bf16 a[140:143], v[56:59], v[224:227], 0\n"
    "s_waitcnt lgkmcnt(4)\n"
    "buffer_load_dwordx4 v7, s[40:43], s91 offen offset:1024 lds\n"
    "v_mul_f32 v149, v149, v108\n"
    "v_mfma_f32_16x16x32_bf16 a[0:3], v[16:19], v[136:139], a[0:3]\n"
    "ds_read_b64_tr_b16 v[28:29], v61 offset:18528\n"
    "v_mul_f32 v150, v150, v108\n"
    "v_mfma_f32_16x16x32_bf16 a[4:7], v[16:19], v[168:171], a[4:7]\n"
    "ds_read_b64_tr_b16 v[30:31], v61 offset:23136\n"
    "v_mul_f32 v151, v151, v108\n"
    "v_mfma_f32_16x16x32_bf16 a[8:11], v[16:19], v[200:203], a[8:11]\n"
    "v_cvt_pk_bf16_f32 v144, v144, v145\n"
    "v_cvt_pk_bf16_f32 v145, v146, v147\n"
    "v_mfma_f32_16x16x32_bf16 a[12:15], v[16:19], v[232:235], a[12:15]\n"
    "s_waitcnt lgkmcnt(4)\n"
    "v_cvt_pk_bf16_f32 v146, v148, v149\n"
    "v_cvt_pk_bf16_f32 v147, v150, v151\n"
    "v_mfma_f32_16x16x32_bf16 a[16:19], v[20:23], v[136:139], a[16:19]\n"
    "ds_read_b64_tr_b16 v[32:33], v61 offset:18560\n"
    "v_mul_f32 v176, v176, v109\n"
    "v_mfma_f32_16x16x32_bf16 a[20:23], v[20:23], v[168:171], a[20:23]\n"
    "ds_read_b64_tr_b16 v[34:35], v61 offset:23168\n"
    "v_mul_f32 v177, v177, v109\n"
    "v_mfma_f32_16x16x32_bf16 a[24:27], v[20:23], v[200:203], a[24:27]\n"
    "buffer_store_dwordx4 v[168:171], v0, s[64:67], s87 offen offset:1024 sc1\n"
    "v_mul_f32 v178, v178, v109\n"
    "v_mfma_f32_16x16x32_bf16 a[28:31], v[20:23], v[232:235], a[28:31]\n"
    "s_waitcnt vmcnt(8)\n"
    "v_subrev_u32 v14, s38, v14\n"
    "v_xor_b32 v15, v47, v15\n"
    "v_cmp_lt_u64 vcc, 15, v[14:15]\n"
    "s_nop 0\n"
    "s_and_b64 s[98:99], vcc, 15\n"
    "s_cbranch_scc1 L_SP1_%=\n"
    "L_BK1_%=:\n"
    "s_waitcnt lgkmcnt(4)\n"
    "v_mfma_f32_16x16x32_bf16 a[32:35], v[24:27], v[136:139], a[32:35]\n"
    "ds_read_b64_tr_b16 v[36:37], v61 offset:18592\n"
    "v_mul_f32 v179, v179, v109\n"
    "v_mfma_f32_16x16x32_bf16 a[36:39], v[24:27], v[168:171], a[36:39]\n"
    "s_xor_b32 s92, s19, 65536\n"
    "ds_read_b64_tr_b16 v[38:39], v61 offset:23200\n"
    "v_mul_f32 v180, v180, v109\n"
    "v_mfma_f32_16x16x32_bf16 a[40:43], v[24:27], v[200:203], a[40:43]\n"
    "buffer_load_dwordx4 a[172:175], v0, s[20:23], s92 offen offset:3072 sc1\n"
    "v_mul_f32 v181, v181, v109\n"
    "v_mfma_f32_16x16x32_bf16 a[44:47], v[24:27], v[232:235], a[44:47]\n"
    "s_waitcnt lgkmcnt(4)\n"
    "v_mul_f32 v182, v182, v109\n"
    "v_mul_f32 v183, v183, v109\n"
    "v_mfma_f32_16x16x32_bf16 a[48:51], v[28:31], v[136:139], a[48:51]\n"
    "ds_read_b64_tr_b16 v[48:49], v61 offset:18624\n"
    "v_cvt_pk_bf16_f32 v176, v176, v177\n"
    "v_mfma_f32_16x16x32_bf16 a[52:55], v[28:31], v[168:171], a[52:55]\n"
    "ds_read_b64_tr_b16 v[50:51], v61 offset:23232\n"
    "v_cvt_pk_bf16_f32 v177, v178, v179\n"
    "v_mfma_f32_16x16x32_bf16 a[56:59], v[28:31], v[200:203], a[56:59]\n"
    "buffer_load_dwordx4 a[168:171], v0, s[20:23], s92 offen offset:2048 sc1\n"
    "v_cvt_pk_bf16_f32 v178, v180, v181\n"
    "v_mfma_f32_16x16x32_bf16 a[60:63], v[28:31], v[232:235], a[60:63]\n"
    "s_waitcnt lgkmcnt(4)\n"
    "v_cvt_pk_bf16_f32 v179, v182, v183\n"
    "v_mul_f32 v208, v208, v110\n"
    "v_mfma_f32_16x16x32_bf16 a[64:67], v[32:35], v[136:139], a[64:67]\n"
    "ds_read_b64_tr_b16 v[52:53], v61 offset:18656\n"
    "v_mul_f32 v209, v209, v110\n"
    "v_mfma_f32_16x16x32_bf16 a[68:71], v[32:35], v[168:171], a[68:71]\n"
    "ds_read_b64_tr_b16 v[54:55], v61 offset:23264\n"
    "v_mul_f32 v210, v210, v110\n"
    "v_mfma_f32_16x16x32_bf16 a[72:75], v[32:35], v[200:203], a[72:75]\n"
    "v_mul_f32 v211, v211, v110\n"
    "v_mul_f32 v212, v212, v110\n"
    "v_mfma_f32_16x16x32_bf16 a[76:79], v[32:35], v[232:235], a[76:79]\n"
    "s_waitcnt lgkmcnt(4)\n"
    "buffer_load_dwordx4 a[164:167], v0, s[20:23], s92 offen offset:1024 sc1\n"
    "v_mul_f32 v213, v213, v110\n"
    "v_mfma_f32_16x16x32_bf16 a[80:83], v[36:39], v[136:139], a[80:83]\n"
    "ds_read_b64_tr_b16 v[56:57], v61 offset:18688\n"
    "v_mul_f32 v214, v214, v110\n"
    "v_mfma_f32_16x16x32_bf16 a[84:87], v[36:39], v[168:171], a[84:87]\n"
    "ds_read_b64_tr_b16 v[58:59], v61 offset:23296\n"
    "v_mul_f32 v215, v215, v110\n"
    "v_mfma_f32_16x16x32_bf16 a[88:91], v[36:39], v[200:203], a[88:91]\n"
    "v_add_u32 v46, 1, v46\n"
    "v_cvt_pk_bf16_f32 v208, v208, v209\n"
    "v_mfma_f32_16x16x32_bf16 a[92:95], v[36:39], v[232:235], a[92:95]\n"
    "s_waitcnt lgkmcnt(4)\n"
    "buffer_store_dwordx2 v[46:47], v11, s[76:79], 0 offen sc1\n"
    "v_cvt_pk_bf16_f32 v209, v210, v211\n"
    "v_mfma_f32_16x16x32_bf16 a[96:99], v[48:51], v[136:139], a[96:99]\n"
    "ds_read_b64_tr_b16 v[16:17], v61 offset:27648\n"
    "v_cvt_pk_bf16_f32 v210, v212, v213\n"
    "v_mfma_f32_16x16x32_bf16 a[100:103], v[48:51], v[168:171], a[100:103]\n"
    "ds_read_b64_tr_b16 v[18:19], v61 offset:32256\n"
    "v_cvt_pk_bf16_f32 v211, v214, v215\n"
    "v_mfma_f32_16x16x32_bf16 a[104:107], v[48:51], v[200:203], a[104:107]\n"
    "buffer_store_dwordx4 v[200:203], v0, s[64:67], s87 offen offset:2048 sc1\n"
    "v_mul_f32 v240, v240, v111\n"
    "v_mfma_f32_16x16x32_bf16 a[108:111], v[48:51], v[232:235], a[108:111]\n"
    "s_waitcnt lgkmcnt(4)\n"
    "v_mul_f32 v241, v241, v111\n"
    "v_mul_f32 v242, v242, v111\n"
    "v_mfma_f32_16x16x32_bf16 a[112:115], v[52:55], v[136:139], a[112:115]\n"
    "ds_read_b64_tr_b16 v[20:21], v61 offset:27680\n"
    "v_mul_f32 v243, v243, v111\n"
    "v_mfma_f32_16x16x32_bf16 a[116:119], v[52:55], v[168:171], a[116:119]\n"
    "ds_read_b64_tr_b16 v[22:23], v61 offset:32288\n"
    "v_mul_f32 v244, v244, v111\n"
    "v_mfma_f32_16x16x32_bf16 a[120:123], v[52:55], v[200:203], a[120:123]\n"
    "v_mul_f32 v245, v245, v111\n"
    "v_mul_f32 v246, v246, v111\n"
    "v_mfma_f32_16x16x32_bf16 a[124:127], v[52:55], v[232:235], a[124:127]\n"
    "s_waitcnt lgkmcnt(4)\n"
    "v_mul_f32 v247, v247, v111\n"
    "v_cvt_pk_bf16_f32 v240, v240, v241\n"
    "v_mfma_f32_16x16x32_bf16 a[128:131], v[56:59], v[136:139], a[128:131]\n"
    "ds_read_b64_tr_b16 v[24:25], v61 offset:27712\n"
    "v_cvt_pk_bf16_f32 v241, v242, v243\n"
    "v_mfma_f32_16x16x32_bf16 a[132:135], v[56:59], v[168:171], a[132:135]\n"
    "ds_read_b64_tr_b16 v[26:27], v61 offset:32320\n"
    "v_cvt_pk_bf16_f32 v242, v244, v245\n"
    "v_mfma_f32_16x16x32_bf16 a[136:139], v[56:59], v[200:203], a[136:139]\n"
    "buffer_load_dwordx4 a[160:163], v0, s[20:23], s92 offen offset:0 sc1\n"
    "v_cvt_pk_bf16_f32 v243, v246, v247\n"
    "v_mfma_f32_16x16x32_bf16 a[140:143], v[56:59], v[232:235], a[140:143]\n"
    "s_waitcnt lgkmcnt(4)\n"
    "v_mul_f32 v152, v152, v108\n"
    "v_mul_f32 v153, v153, v108\n"
    "v_mfma_f32_16x16x32_bf16 a[0:3], v[16:19], v[144:147], a[0:3]\n"
    "ds_read_b64_tr_b16 v[28:29], v61 offset:27744\n"
    "v_mul_f32 v154, v154, v108\n"
    "v_mfma_f32_16x16x32_bf16 a[4:7], v[16:19], v[176:179], a[4:7]\n"
    "ds_read_b64_tr_b16 v[30:31], v61 offset:32352\n"
    "v_mul_f32 v155, v155, v108\n"
    "v_mfma_f32_16x16x32_bf16 a[8:11], v[16:19], v[208:211], a[8:11]\n"
    "s_xor_b32 s92, s19, 81920\n"
    "v_mul_f32 v156, v156, v108\n"
    "v_mul_f32 v157, v157, v108\n"
    "v_mfma_f32_16x16x32_bf16 a[12:15], v[16:19], v[240:243], a[12:15]\n"
    "s_waitcnt lgkmcnt(4)\n"
    "buffer_load_dwordx4 a[188:191], v0, s[20:23], s92 offen offset:3072 sc1\n"
    "v_mul_f32 v158, v158, v108\n"
    "v_mfma_f32_16x16x32_bf16 a[16:19], v[20:23], v[144:147], a[16:19]\n"
    "ds_read_b64_tr_b16 v[32:33], v61 offset:27776\n"
    "v_mul_f32 v159, v159, v108\n"
    "v_mfma_f32_16x16x32_bf16 a[20:23], v[20:23], v[176:179], a[20:23]\n"
    "ds_read_b64_tr_b16 v[34:35], v61 offset:32384\n"
    "v_cvt_pk_bf16_f32 v152, v152, v153\n"
    "v_mfma_f32_16x16x32_bf16 a[24:27], v[20:23], v[208:211], a[24:27]\n"
    "v_cvt_pk_bf16_f32 v153, v154, v155\n"
    "v_cvt_pk_bf16_f32 v154, v156, v157\n"
    "v_mfma_f32_16x16x32_bf16 a[28:31], v[20:23], v[240:243], a[28:31]\n"
    "s_waitcnt lgkmcnt(4)\n"
    "v_cvt_pk_bf16_f32 v155, v158, v159\n"
    "v_mul_f32 v184, v184, v109\n"
    "v_mfma_f32_16x16x32_bf16 a[32:35], v[24:27], v[144:147], a[32:35]\n"
    "ds_read_b64_tr_b16 v[36:37], v61 offset:27808\n"
    "v_mul_f32 v185, v185, v109\n"
    "v_mfma_f32_16x16x32_bf16 a[36:39], v[24:27], v[176:179], a[36:39]\n"
    "ds_read_b64_tr_b16 v[38:39], v61 offset:32416\n"
    "v_mul_f32 v186, v186, v109\n"
    "v_mfma_f32_16x16x32_bf16 a[40:43], v[24:27], v[208:211], a[40:43]\n"
    "buffer_store_dwordx4 v[232:235], v0, s[64:67], s87 offen offset:3072 sc1\n"
    "v_mul_f32 v187, v187, v109\n"
    "v_mfma_f32_16x16x32_bf16 a[44:47], v[24:27], v[240:243], a[44:47]\n"
    "s_waitcnt lgkmcnt(4)\n"
    "v_mul_f32 v188, v188, v109\n"
    "v_mul_f32 v189, v189, v109\n"
    "v_mfma_f32_16x16x32_bf16 a[48:51], v[28:31], v[144:147], a[48:51]\n"
    "ds_read_b64_tr_b16 v[48:49], v61 offset:27840\n"
    "v_mul_f32 v190, v190, v109\n"
    "v_mfma_f32_16x16x32_bf16 a[52:55], v[28:31], v[176:179], a[52:55]\n"
    "ds_read_b64_tr_b16 v[50:51], v61 offset:32448\n"
    "v_mul_f32 v191, v191, v109\n"
    "v_mfma_f32_16x16x32_bf16 a[56:59], v[28:31], v[208:211], a[56:59]\n"
    "buffer_load_dwordx4 a[184:187], v0, s[20:23], s92 offen offset:2048 sc1\n"
    "v_cvt_pk_bf16_f32 v184, v184, v185\n"
    "v_mfma_f32_16x16x32_bf16 a[60:63], v[28:31], v[240:243], a[60:63]\n"
    "s_waitcnt lgkmcnt(4)\n"
    "v_cvt_pk_bf16_f32 v185, v186, v187\n"
    "v_cvt_pk_bf16_f32 v186, v188, v189\n"
    "v_mfma_f32_16x16x32_bf16 a[64:67], v[32:35], v[144:147], a[64:67]\n"
    "ds_read_b64_tr_b16 v[52:53], v61 offset:27872\n"
    "v_cvt_pk_bf16_f32 v187, v190, v191\n"
    "v_mfma_f32_16x16x32_bf16 a[68:71], v[32:35], v[176:179], a[68:71]\n"
    "ds_read_b64_tr_b16 v[54:55], v61 offset:32480\n"
    "v_mul_f32 v216, v216, v110\n"
    "v_mfma_f32_16x16x32_bf16 a[72:75], v[32:35], v[208:211], a[72:75]\n"
    "buffer_load_dwordx4 a[180:183], v0, s[20:23], s92 offen offset:1024 sc1\n"
    "v_mul_f32 v217, v217, v110\n"
    "v_mfma_f32_16x16x32_bf16 a[76:79], v[32:35], v[240:243], a[76:79]\n"
    "s_waitcnt lgkmcnt(4)\n"
    "v_mul_f32 v218, v218, v110\n"
    "v_mul_f32 v219, v219, v110\n"
    "v_mfma_f32_16x16x32_bf16 a[80:83], v[36:39], v[144:147], a[80:83]\n"
    "ds_read_b64_tr_b16 v[56:57], v61 offset:27904\n"
    "v_mul_f32 v220, v220, v110\n"
    "v_mfma_f32_16x16x32_bf16 a[84:87], v[36:39], v[176:179], a[84:87]\n"
    "ds_read_b64_tr_b16 v[58:59], v61 offset:32512\n"
    "v_mul_f32 v221, v221, v110\n"
    "v_mfma_f32_16x16x32_bf16 a[88:91], v[36:39], v[208:211], a[88:91]\n"
    "v_mul_f32 v222, v222, v110\n"
    "v_mul_f32 v223, v223, v110\n"
    "v_mfma_f32_16x16x32_bf16 a[92:95], v[36:39], v[240:243], a[92:95]\n"
    "s_waitcnt lgkmcnt(4)\n"
    "buffer_load_dwordx4 a[176:179], v0, s[20:23], s92 offen offset:0 sc1\n"
    "v_cvt_pk_bf16_f32 v216, v216, v217\n"
    "v_mfma_f32_16x16x32_bf16 a[96:99], v[48:51], v[144:147], a[96:99]\n"
    "ds_read_b64_tr_b16 v[16:17], v61 offset:36864\n"
    "v_cvt_pk_bf16_f32 v217, v218, v219\n"
    "v_mfma_f32_16x16x32_bf16 a[100:103], v[48:51], v[176:179], a[100:103]\n"
    "ds_read_b64_tr_b16 v[18:19], v61 offset:41472\n"
    "v_cvt_pk_bf16_f32 v218, v220, v221\n"
    "v_mfma_f32_16x16x32_bf16 a[104:107], v[48:51], v[208:211], a[104:107]\n"
    "v_cvt_pk_bf16_f32 v219, v222, v223\n"
    "v_mul_f32 v248, v248, v111\n"
    "v_mfma_f32_16x16x32_bf16 a[108:111], v[48:51], v[240:243], a[108:111]\n"
    "s_waitcnt lgkmcnt(4)\n"
    "v_mul_f32 v249, v249, v111\n"
    "v_mul_f32 v250, v250, v111\n"
    "v_mfma_f32_16x16x32_bf16 a[112:115], v[52:55], v[144:147], a[112:115]\n"
    "ds_read_b64_tr_b16 v[20:21], v61 offset:36896\n"
    "v_mul_f32 v251, v251, v111\n"
    "v_mfma_f32_16x16x32_bf16 a[116:119], v[52:55], v[176:179], a[116:119]\n"
    "s_mov_b32 s87, 8192\n"
    "ds_read_b64_tr_b16 v[22:23], v61 offset:41504\n"
    "v_mul_f32 v252, v252, v111\n"
    "v_mfma_f32_16x16x32_bf16 a[120:123], v[52:55], v[208:211], a[120:123]\n"
    "buffer_store_dwordx4 v[144:147], v0, s[64:67], s87 offen offset:0 sc1\n"
    "v_mul_f32 v253, v253, v111\n"
    "v_mfma_f32_16x16x32_bf16 a[124:127], v[52:55], v[240:243], a[124:127]\n"
    "s_waitcnt lgkmcnt(4)\n"
    "v_mul_f32 v254, v254, v111\n"
    "v_mul_f32 v255, v255, v111\n"
    "v_mfma_f32_16x16x32_bf16 a[128:131], v[56:59], v[144:147], a[128:131]\n"
    "ds_read_b64_tr_b16 v[24:25], v61 offset:36928\n"
    "v_cvt_pk_bf16_f32 v248, v248, v249\n"
    "v_mfma_f32_16x16x32_bf16 a[132:135], v[56:59], v[176:179], a[132:135]\n"
    "s_xor_b32 s92, s19, 98304\n"
    "ds_read_b64_tr_b16 v[26:27], v61 offset:41536\n"
    "v_cvt_pk_bf16_f32 v249, v250, v251\n"
    "v_mfma_f32_16x16x32_bf16 a[136:139], v[56:59], v[208:211], a[136:139]\n"
    "buffer_load_dwordx4 a[204:207], v0, s[20:23], s92 offen offset:3072 sc1\n"
    "v_cvt_pk_bf16_f32 v250, v252, v253\n"
    "v_mfma_f32_16x16x32_bf16 a[140:143], v[56:59], v[240:243], a[140:143]\n"
    "s_waitcnt lgkmcnt(4)\n"
    "v_cvt_pk_bf16_f32 v251, v254, v255\n"
    "v_mfma_f32_16x16x32_bf16 a[0:3], v[16:19], v[152:155], a[0:3]\n"
    "ds_read_b64_tr_b16 v[28:29], v61 offset:36960\n"
    "v_mfma_f32_16x16x32_bf16 a[4:7], v[16:19], v[184:187], a[4:7]\n"
    "s_add_u32 s38, s95, 3\n"
    "ds_read_b64_tr_b16 v[30:31], v61 offset:41568\n"
    "v_mfma_f32_16x16x32_bf16 a[8:11], v[16:19], v[216:219], a[8:11]\n"
    "buffer_load_dwordx2 v[14:15], v1, s[76:79], s12 offen sc1\n"
    "v_mfma_f32_16x16x32_bf16 a[12:15], v[16:19], v[248:251], a[12:15]\n"
    "s_waitcnt lgkmcnt(4)\n"
    "v_mfma_f32_16x16x32_bf16 a[16:19], v[20:23], v[152:155], a[16:19]\n"
    "ds_read_b64_tr_b16 v[32:33], v61 offset:36992\n"
    "v_mfma_f32_16x16x32_bf16 a[20:23], v[20:23], v[184:187], a[20:23]\n"
    "ds_read_b64_tr_b16 v[34:35], v61 offset:41600\n"
    "v_mfma_f32_16x16x32_bf16 a[24:27], v[20:23], v[216:219], a[24:27]\n"
    "v_mfma_f32_16x16x32_bf16 a[28:31], v[20:23], v[248:251], a[28:31]\n"
    "s_waitcnt lgkmcnt(4)\n"
    "buffer_load_dwordx4 a[200:203], v0, s[20:23], s92 offen offset:2048 sc1\n"
    "v_mfma_f32_16x16x32_bf16 a[32:35], v[24:27], v[152:155], a[32:35]\n"
    "ds_read_b64_tr_b16 v[36:37], v61 offset:37024\n"
    "v_mfma_f32_16x16x32_bf16 a[36:39], v[24:27], v[184:187], a[36:39]\n"
    "ds_read_b64_tr_b16 v[38:39], v61 offset:41632\n"
    "v_mfma_f32_16x16x32_bf16 a[40:43], v[24:27], v[216:219], a[40:43]\n"
    "v_mfma_f32_16x16x32_bf16 a[44:47], v[24:27], v[248:251], a[44:47]\n"
    "s_waitcnt lgkmcnt(4)\n"
    "v_mfma_f32_16x16x32_bf16 a[48:51], v[28:31], v[152:155], a[48:51]\n"
    "ds_read_b64_tr_b16 v[48:49], v61 offset:37056\n"
    "v_mfma_f32_16x16x32_bf16 a[52:55], v[28:31], v[184:187], a[52:55]\n"
    "ds_read_b64_tr_b16 v[50:51], v61 offset:41664\n"
    "v_mfma_f32_16x16x32_bf16 a[56:59], v[28:31], v[216:219], a[56:59]\n"
    "buffer_store_dwordx4 v[176:179], v0, s[64:67], s87 offen offset:1024 sc1\n"
    "v_mfma_f32_16x16x32_bf16 a[60:63], v[28:31], v[248:251], a[60:63]\n"
    "s_waitcnt lgkmcnt(4)\n"
    "v_mfma_f32_16x16x32_bf16 a[64:67], v[32:35], v[152:155], a[64:67]\n"
    "ds_read_b64_tr_b16 v[52:53], v61 offset:37088\n"
    "v_mfma_f32_16x16x32_bf16 a[68:71], v[32:35], v[184:187], a[68:71]\n"
    "ds_read_b64_tr_b16 v[54:55], v61 offset:41696\n"
    "v_mfma_f32_16x16x32_bf16 a[72:75], v[32:35], v[216:219], a[72:75]\n"
    "buffer_load_dwordx4 a[196:199], v0, s[20:23], s92 offen offset:1024 sc1\n"
    "v_mfma_f32_16x16x32_bf16 a[76:79], v[32:35], v[248:251], a[76:79]\n"
    "s_waitcnt lgkmcnt(4)\n"
    "v_mfma_f32_16x16x32_bf16 a[80:83], v[36:39], v[152:155], a[80:83]\n"
    "ds_read_b64_tr_b16 v[56:57], v61 offset:37120\n"
    "v_mfma_f32_16x16x32_bf16 a[84:87], v[36:39], v[184:187], a[84:87]\n"
    "ds_read_b64_tr_b16 v[58:59], v61 offset:41728\n"
    "v_mfma_f32_16x16x32_bf16 a[88:91], v[36:39], v[216:219], a[88:91]\n"
    "buffer_load_dwordx4 a[192:195], v0, s[20:23], s92 offen offset:0 sc1\n"
    "v_mfma_f32_16x16x32_bf16 a[92:95], v[36:39], v[248:251], a[92:95]\n"
    "s_waitcnt lgkmcnt(4)\n"
    "v_mfma_f32_16x16x32_bf16 a[96:99], v[48:51], v[152:155], a[96:99]\n"
    "v_mfma_f32_16x16x32_bf16 a[100:103], v[48:51], v[184:187], a[100:103]\n"
    "v_mfma_f32_16x16x32_bf16 a[104:107], v[48:51], v[216:219], a[104:107]\n"
    "s_waitcnt lgkmcnt(0)\n"
    "s_barrier\n"
    "ds_read_b64_tr_b16 v[16:17], v61 offset:46080\n"
    "v_mfma_f32_16x16x32_bf16 a[108:111], v[48:51], v[248:251], a[108:111]\n"
    "s_waitcnt lgkmcnt(3)\n"
    "ds_read_b64_tr_b16 v[18:19], v61 offset:50688\n"
    "v_mfma_f32_16x16x32_bf16 a[112:115], v[52:55], v[152:155], a[112:115]\n"
    "ds_read_b64_tr_b16 v[20:21], v61 offset:46112\n"
    "v_mfma_f32_16x16x32_bf16 a[116:119], v[52:55], v[184:187], a[116:119]\n"
    "s_xor_b32 s92, s19, 114688\n"
    "ds_read_b64_tr_b16 v[22:23], v61 offset:50720\n"
    "v_mfma_f32_16x16x32_bf16 a[120:123], v[52:55], v[216:219], a[120:123]\n"
    "buffer_load_dwordx4 a[220:223], v0, s[20:23], s92 offen offset:3072 sc1\n"
    "v_mfma_f32_16x16x32_bf16 a[124:127], v[52:55], v[248:251], a[124:127]\n"
    "s_waitcnt lgkmcnt(4)\n"
    "v_mfma_f32_16x16x32_bf16 a[128:131], v[56:59], v[152:155], a[128:131]\n"
    "ds_read_b64_tr_b16 v[24:25], v61 offset:46144\n"
    "v_mfma_f32_16x16x32_bf16 a[132:135], v[56:59], v[184:187], a[132:135]\n"
    "ds_read_b64_tr_b16 v[26:27], v61 offset:50752\n"
    "v_mfma_f32_16x16x32_bf16 a[136:139], v[56:59], v[216:219], a[136:139]\n"
    "buffer_store_dwordx4 v[208:211], v0, s[64:67], s87 offen offset:2048 sc1\n"
    "v_mfma_f32_16x16x32_bf16 a[140:143], v[56:59], v[248:251], a[140:143]\n"
    "s_waitcnt vmcnt(14) lgkmcnt(4)\n"
    "v_mfma_f32_16x16x32_bf16 a[0:3], v[16:19], a[160:163], a[0:3]\n"
    "ds_read_b64_tr_b16 v[28:29], v61 offset:46176\n"
    "v_mfma_f32_16x16x32_bf16 a[4:7], v[16:19], a[164:167], a[4:7]\n"
    "ds_read_b64_tr_b16 v[30:31], v61 offset:50784\n"
    "v_mfma_f32_16x16x32_bf16 a[8:11], v[16:19], a[168:171], a[8:11]\n"
    "buffer_load_dwordx4 a[216:219], v0, s[20:23], s92 offen offset:2048 sc1\n"
    "v_mfma_f32_16x16x32_bf16 a[12:15], v[16:19], a[172:175], a[12:15]\n"
    "s_waitcnt lgkmcnt(4)\n"
    "v_mfma_f32_16x16x32_bf16 a[16:19], v[20:23], a[160:163], a[16:19]\n"
    "ds_read_b64_tr_b16 v[32:33], v61 offset:46208\n"
    "v_mfma_f32_16x16x32_bf16 a[20:23], v[20:23], a[164:167], a[20:23]\n"
    "ds_read_b64_tr_b16 v[34:35], v61 offset:50816\n"
    "v_mfma_f32_16x16x32_bf16 a[24:27], v[20:23], a[168:171], a[24:27]\n"
    "buffer_load_dwordx4 a[212:215], v0, s[20:23], s92 offen offset:1024 sc1\n"
    "v_mfma_f32_16x16x32_bf16 a[28:31], v[20:23], a[172:175], a[28:31]\n"
    "s_waitcnt lgkmcnt(4)\n"
    "v_mfma_f32_16x16x32_bf16 a[32:35], v[24:27], a[160:163], a[32:35]\n"
    "ds_read_b64_tr_b16 v[36:37], v61 offset:46240\n"
    "v_mfma_f32_16x16x32_bf16 a[36:39], v[24:27], a[164:167], a[36:39]\n"
    "ds_read_b64_tr_b16 v[38:39], v61 offset:50848\n"
    "v_mfma_f32_16x16x32_bf16 a[40:43], v[24:27], a[168:171], a[40:43]\n"
    "v_mfma_f32_16x16x32_bf16 a[44:47], v[24:27], a[172:175], a[44:47]\n"
    "s_waitcnt lgkmcnt(4)\n"
    "buffer_load_dwordx4 a[208:211], v0, s[20:23], s92 offen offset:0 sc1\n"
    "v_mfma_f32_16x16x32_bf16 a[48:51], v[28:31], a[160:163], a[48:51]\n"
    "ds_read_b64_tr_b16 v[48:49], v61 offset:46272\n"
    "v_mfma_f32_16x16x32_bf16 a[52:55], v[28:31], a[164:167], a[52:55]\n"
    "ds_read_b64_tr_b16 v[50:51], v61 offset:50880\n"
    "v_mfma_f32_16x16x32_bf16 a[56:59], v[28:31], a[168:171], a[56:59]\n"
    "v_mfma_f32_16x16x32_bf16 a[60:63], v[28:31], a[172:175], a[60:63]\n"
    "s_waitcnt lgkmcnt(4)\n"
    "v_mfma_f32_16x16x32_bf16 a[64:67], v[32:35], a[160:163], a[64:67]\n"
    "ds_read_b64_tr_b16 v[52:53], v61 offset:46304\n"
    "v_mfma_f32_16x16x32_bf16 a[68:71], v[32:35], a[164:167], a[68:71]\n"
    "ds_read_b64_tr_b16 v[54:55], v61 offset:50912\n"
    "v_mfma_f32_16x16x32_bf16 a[72:75], v[32:35], a[168:171], a[72:75]\n"
    "buffer_store_dwordx4 v[240:243], v0, s[64:67], s87 offen offset:3072 sc1\n"
    "v_mfma_f32_16x16x32_bf16 a[76:79], v[32:35], a[172:175], a[76:79]\n"
    "s_waitcnt lgkmcnt(4)\n"
    "v_mfma_f32_16x16x32_bf16 a[80:83], v[36:39], a[160:163], a[80:83]\n"
    "ds_read_b64_tr_b16 v[56:57], v61 offset:46336\n"
    "v_mfma_f32_16x16x32_bf16 a[84:87], v[36:39], a[164:167], a[84:87]\n"
    "ds_read_b64_tr_b16 v[58:59], v61 offset:50944\n"
    "v_mfma_f32_16x16x32_bf16 a[88:91], v[36:39], a[168:171], a[88:91]\n"
    "buffer_load_dwordx4 v8, s[40:43], s91 offen offset:2048 lds\n"
    "v_mfma_f32_16x16x32_bf16 a[92:95], v[36:39], a[172:175], a[92:95]\n"
    "s_waitcnt lgkmcnt(4)\n"
    "v_mfma_f32_16x16x32_bf16 a[96:99], v[48:51], a[160:163], a[96:99]\n"
    "ds_read_b64_tr_b16 v[16:17], v61 offset:55296\n"
    "v_mfma_f32_16x16x32_bf16 a[100:103], v[48:51], a[164:167], a[100:103]\n"
    "ds_read_b64_tr_b16 v[18:19], v61 offset:59904\n"
    "v_mfma_f32_16x16x32_bf16 a[104:107], v[48:51], a[168:171], a[104:107]\n"
    "buffer_load_dwordx4 v9, s[40:43], s91 offen offset:3072 lds\n"
    "v_mfma_f32_16x16x32_bf16 a[108:111], v[48:51], a[172:175], a[108:111]\n"
    "s_waitcnt lgkmcnt(4)\n"
    "v_mfma_f32_16x16x32_bf16 a[112:115], v[52:55], a[160:163], a[112:115]\n"
    "ds_read_b64_tr_b16 v[20:21], v61 offset:55328\n"
    "v_mfma_f32_16x16x32_bf16 a[116:119], v[52:55], a[164:167], a[116:119]\n"
    "ds_read_b64_tr_b16 v[22:23], v61 offset:59936\n"
    "v_mfma_f32_16x16x32_bf16 a[120:123], v[52:55], a[168:171], a[120:123]\n"
    "s_add_u32 m0, s17, 24576\n"
    "v_mfma_f32_16x16x32_bf16 a[124:127], v[52:55], a[172:175], a[124:127]\n"
    "s_waitcnt lgkmcnt(4)\n"
    "buffer_load_dwordx4 v10, s[40:43], s91 offen lds\n"
    "v_mfma_f32_16x16x32_bf16 a[128:131], v[56:59], a[160:163], a[128:131]\n"
    "ds_read_b64_tr_b16 v[24:25], v61 offset:55360\n"
    "v_mfma_f32_16x16x32_bf16 a[132:135], v[56:59], a[164:167], a[132:135]\n"
    "ds_read_b64_tr_b16 v[26:27], v61 offset:59968\n"
    "v_mfma_f32_16x16x32_bf16 a[136:139], v[56:59], a[168:171], a[136:139]\n"
    "v_mfma_f32_16x16x32_bf16 a[140:143], v[56:59], a[172:175], a[140:143]\n"
    "s_waitcnt vmcnt(13)\n"
    "v_subrev_u32 v14, s38, v14\n"
    "v_xor_b32 v15, v47, v15\n"
    "v_cmp_lt_u64 vcc, 15, v[14:15]\n"
    "s_nop 0\n"
    "s_cbranch_vccnz L_SP2_%=\n"
    "L_BK2_%=:\n"
    "s_waitcnt lgkmcnt(4)\n"
    "v_mfma_f32_16x16x32_bf16 a[0:3], v[16:19], a[176:179], a[0:3]\n"
    "ds_read_b64_tr_b16 v[28:29], v61 offset:55392\n"
    "v_mfma_f32_16x16x32_bf16 a[4:7], v[16:19], a[180:183], a[4:7]\n"
    "s_mov_b32 s87, 12288\n"
    "ds_read_b64_tr_b16 v[30:31], v61 offset:60000\n"
    "v_mfma_f32_16x16x32_bf16 a[8:11], v[16:19], a[184:187], a[8:11]\n"
    "buffer_store_dwordx4 v[152:155], v0, s[64:67], s87 offen offset:0 sc1\n"
    "v_mfma_f32_16x16x32_bf16 a[12:15], v[16:19], a[188:191], a[12:15]\n"
    "s_waitcnt lgkmcnt(4)\n"
    "v_mfma_f32_16x16x32_bf16 a[16:19], v[20:23], a[176:179], a[16:19]\n"
    "ds_read_b64_tr_b16 v[32:33], v61 offset:55424\n"
    "v_mfma_f32_16x16x32_bf16 a[20:23], v[20:23], a[180:183], a[20:23]\n"
    "s_xor_b32 s92, s19, 16384\n"
    "ds_read_b64_tr_b16 v[34:35], v61 offset:60032\n"
    "v_mfma_f32_16x16x32_bf16 a[24:27], v[20:23], a[184:187], a[24:27]\n"
    "buffer_load_dwordx4 a[236:239], v0, s[20:23], s92 offen offset:3072 sc1\n"
    "v_mfma_f32_16x16x32_bf16 a[28:31], v[20:23], a[188:191], a[28:31]\n"
    "s_waitcnt lgkmcnt(4)\n"
    "v_mfma_f32_16x16x32_bf16 a[32:35], v[24:27], a[176:179], a[32:35]\n"
    "ds_read_b64_tr_b16 v[36:37], v61 offset:55456\n"
    "v_mfma_f32_16x16x32_bf16 a[36:39], v[24:27], a[180:183], a[36:39]\n"
    "ds_read_b64_tr_b16 v[38:39], v61 offset:60064\n"
    "v_mfma_f32_16x16x32_bf16 a[40:43], v[24:27], a[184:187], a[40:43]\n"
    "buffer_load_dwordx4 a[232:235], v0, s[20:23], s92 offen offset:2048 sc1\n"
    "v_mfma_f32_16x16x32_bf16 a[44:47], v[24:27], a[188:191], a[44:47]\n"
    "s_waitcnt lgkmcnt(4)\n"
    "v_mfma_f32_16x16x32_bf16 a[48:51], v[28:31], a[176:179], a[48:51]\n"
    "ds_read_b64_tr_b16 v[48:49], v61 offset:55488\n"
    "v_mfma_f32_16x16x32_bf16 a[52:55], v[28:31], a[180:183], a[52:55]\n"
    "ds_read_b64_tr_b16 v[50:51], v61 offset:60096\n"
    "v_mfma_f32_16x16x32_bf16 a[56:59], v[28:31], a[184:187], a[56:59]\n"
    "v_mfma_f32_16x16x32_bf16 a[60:63], v[28:31], a[188:191], a[60:63]\n"
    "s_waitcnt lgkmcnt(4)\n"
    "buffer_load_dwordx4 a[228:231], v0, s[20:23], s92 offen offset:1024 sc1\n"
    "v_mfma_f32_16x16x32_bf16 a[64:67], v[32:35], a[176:179], a[64:67]\n"
    "ds_read_b64_tr_b16 v[52:53], v61 offset:55520\n"
    "v_mfma_f32_16x16x32_bf16 a[68:71], v[32:35], a[180:183], a[68:71]\n"
    "ds_read_b64_tr_b16 v[54:55], v61 offset:60128\n"
    "v_mfma_f32_16x16x32_bf16 a[72:75], v[32:35], a[184:187], a[72:75]\n"
    "v_mfma_f32_16x16x32_bf16 a[76:79], v[32:35], a[188:191], a[76:79]\n"
    "s_waitcnt lgkmcnt(4)\n"
    "v_mfma_f32_16x16x32_bf16 a[80:83], v[36:39], a[176:179], a[80:83]\n"
    "ds_read_b64_tr_b16 v[56:57], v61 offset:55552\n"
    "v_mfma_f32_16x16x32_bf16 a[84:87], v[36:39], a[180:183], a[84:87]\n"
    "ds_read_b64_tr_b16 v[58:59], v61 offset:60160\n"
    "v_add_u32 v46, 1, v46\n"
    "v_mfma_f32_16x16x32_bf16 a[88:91], v[36:39], a[184:187], a[88:91]\n"
    "buffer_store_dwordx4 v[184:187], v0, s[64:67], s87 offen offset:1024 sc1\n"
    "v_mfma_f32_16x16x32_bf16 a[92:95], v[36:39], a[188:191], a[92:95]\n"
    "s_waitcnt lgkmcnt(4)\n"
    "buffer_store_dwordx2 v[46:47], v11, s[76:79], 0 offen sc1\n"
    "v_mfma_f32_16x16x32_bf16 a[96:99], v[48:51], a[176:179], a[96:99]\n"
    "ds_read_b64_tr_b16 v[16:17], v62 offset:0\n"
    "v_mfma_f32_16x16x32_bf16 a[100:103], v[48:51], a[180:183], a[100:103]\n"
    "ds_read_b64_tr_b16 v[18:19], v62 offset:4608\n"
    "v_mfma_f32_16x16x32_bf16 a[104:107], v[48:51], a[184:187], a[104:107]\n"
    "v_mfma_f32_16x16x32_bf16 a[108:111], v[48:51], a[188:191], a[108:111]\n"
    "s_waitcnt lgkmcnt(4)\n"
    "v_mfma_f32_16x16x32_bf16 a[112:115], v[52:55], a[176:179], a[112:115]\n"
    "ds_read_b64_tr_b16 v[20:21], v62 offset:32\n"
    "v_mfma_f32_16x16x32_bf16 a[116:119], v[52:55], a[180:183], a[116:119]\n"
    "ds_read_b64_tr_b16 v[22:23], v62 offset:4640\n"
    "v_mfma_f32_16x16x32_bf16 a[120:123], v[52:55], a[184:187], a[120:123]\n"
    "buffer_load_dwordx4 a[224:227], v0, s[20:23], s92 offen offset:0 sc1\n"
    "v_mfma_f32_16x16x32_bf16 a[124:127], v[52:55], a[188:191], a[124:127]\n"
    "s_waitcnt lgkmcnt(4)\n"
    "v_mfma_f32_16x16x32_bf16 a[128:131], v[56:59], a[176:179], a[128:131]\n"
    "ds_read_b64_tr_b16 v[24:25], v62 offset:64\n"
    "v_mfma_f32_16x16x32_bf16 a[132:135], v[56:59], a[180:183], a[132:135]\n"
    "ds_read_b64_tr_b16 v[26:27], v62 offset:4672\n"
    "v_mfma_f32_16x16x32_bf16 a[136:139], v[56:59], a[184:187], a[136:139]\n"
    "s_xor_b32 s92, s19, 32768\n"
    "v_mfma_f32_16x16x32_bf16 a[140:143], v[56:59], a[188:191], a[140:143]\n"
    "s_waitcnt vmcnt(16) lgkmcnt(4)\n"
    "buffer_load_dwordx4 a[252:255], v0, s[20:23], s92 offen offset:3072 sc1\n"
    "v_mfma_f32_16x16x32_bf16 a[0:3], v[16:19], a[192:195], a[0:3]\n"
    "ds_read_b64_tr_b16 v[28:29], v62 offset:96\n"
    "v_mfma_f32_16x16x32_bf16 a[4:7], v[16:19], a[196:199], a[4:7]\n"
    "ds_read_b64_tr_b16 v[30:31], v62 offset:4704\n"
    "v_mfma_f32_16x16x32_bf16 a[8:11], v[16:19], a[200:203], a[8:11]\n"
    "v_mfma_f32_16x16x32_bf16 a[12:15], v[16:19], a[204:207], a[12:15]\n"
    "s_waitcnt lgkmcnt(4)\n"
    "v_mfma_f32_16x16x32_bf16 a[16:19], v[20:23], a[192:195], a[16:19]\n"
    "ds_read_b64_tr_b16 v[32:33], v62 offset:128\n"
    "v_mfma_f32_16x16x32_bf16 a[20:23], v[20:23], a[196:199], a[20:23]\n"
    "ds_read_b64_tr_b16 v[34:35], v62 offset:4736\n"
    "v_mfma_f32_16x16x32_bf16 a[24:27], v[20:23], a[200:203], a[24:27]\n"
    "buffer_store_dwordx4 v[216:219], v0, s[64:67], s87 offen offset:2048 sc1\n"
    "v_mfma_f32_16x16x32_bf16 a[28:31], v[20:23], a[204:207], a[28:31]\n"
    "s_waitcnt lgkmcnt(4)\n"
    "v_mfma_f32_16x16x32_bf16 a[32:35], v[24:27], a[192:195], a[32:35]\n"
    "ds_read_b64_tr_b16 v[36:37], v62 offset:160\n"
    "v_mfma_f32_16x16x32_bf16 a[36:39], v[24:27], a[196:199], a[36:39]\n"
    "ds_read_b64_tr_b16 v[38:39], v62 offset:4768\n"
    "v_mfma_f32_16x16x32_bf16 a[40:43], v[24:27], a[200:203], a[40:43]\n"
    "buffer_load_dwordx4 a[248:251], v0, s[20:23], s92 offen offset:2048 sc1\n"
    "v_mfma_f32_16x16x32_bf16 a[44:47], v[24:27], a[204:207], a[44:47]\n"
    "s_waitcnt lgkmcnt(4)\n"
    "v_mfma_f32_16x16x32_bf16 a[48:51], v[28:31], a[192:195], a[48:51]\n"
    "ds_read_b64_tr_b16 v[48:49], v62 offset:192\n"
    "v_mfma_f32_16x16x32_bf16 a[52:55], v[28:31], a[196:199], a[52:55]\n"
    "ds_read_b64_tr_b16 v[50:51], v62 offset:4800\n"
    "v_mfma_f32_16x16x32_bf16 a[56:59], v[28:31], a[200:203], a[56:59]\n"
    "buffer_load_dwordx4 a[244:247], v0, s[20:23], s92 offen offset:1024 sc1\n"
    "v_mfma_f32_16x16x32_bf16 a[60:63], v[28:31], a[204:207], a[60:63]\n"
    "s_waitcnt lgkmcnt(4)\n"
    "v_mfma_f32_16x16x32_bf16 a[64:67], v[32:35], a[192:195], a[64:67]\n"
    "ds_read_b64_tr_b16 v[52:53], v62 offset:224\n"
    "v_mfma_f32_16x16x32_bf16 a[68:71], v[32:35], a[196:199], a[68:71]\n"
    "ds_read_b64_tr_b16 v[54:55], v62 offset:4832\n"
    "v_mfma_f32_16x16x32_bf16 a[72:75], v[32:35], a[200:203], a[72:75]\n"
    "v_mfma_f32_16x16x32_bf16 a[76:79], v[32:35], a[204:207], a[76:79]\n"
    "s_waitcnt lgkmcnt(4)\n"
    "buffer_load_dwordx4 a[240:243], v0, s[20:23], s92 offen offset:0 sc1\n"
    "v_mfma_f32_16x16x32_bf16 a[80:83], v[36:39], a[192:195], a[80:83]\n"
    "ds_read_b64_tr_b16 v[56:57], v62 offset:256\n"
    "v_mfma_f32_16x16x32_bf16 a[84:87], v[36:39], a[196:199], a[84:87]\n"
    "ds_read_b64_tr_b16 v[58:59], v62 offset:4864\n"
    "v_mfma_f32_16x16x32_bf16 a[88:91], v[36:39], a[200:203], a[88:91]\n"
    "v_mfma_f32_16x16x32_bf16 a[92:95], v[36:39], a[204:207], a[92:95]\n"
    "s_waitcnt lgkmcnt(4)\n"
    "v_mfma_f32_16x16x32_bf16 a[96:99], v[48:51], a[192:195], a[96:99]\n"
    "ds_read_b64_tr_b16 v[16:17], v62 offset:9216\n"
    "v_mfma_f32_16x16x32_bf16 a[100:103], v[48:51], a[196:199], a[100:103]\n"
    "ds_read_b64_tr_b16 v[18:19], v62 offset:13824\n"
    "v_mfma_f32_16x16x32_bf16 a[104:107], v[48:51], a[200:203], a[104:107]\n"
    "buffer_store_dwordx4 v[248:251], v0, s[64:67], s87 offen offset:3072 sc1\n"
    "v_mfma_f32_16x16x32_bf16 a[108:111], v[48:51], a[204:207], a[108:111]\n"
    "s_waitcnt lgkmcnt(4)\n"
    "v_mfma_f32_16x16x32_bf16 a[112:115], v[52:55], a[192:195], a[112:115]\n"
    "ds_read_b64_tr_b16 v[20:21], v62 offset:9248\n"
    "v_mfma_f32_16x16x32_bf16 a[116:119], v[52:55], a[196:199], a[116:119]\n"
    "s_xor_b32 s92, s19, 49152\n"
    "ds_read_b64_tr_b16 v[22:23], v62 offset:13856\n"
    "v_mfma_f32_16x16x32_bf16 a[120:123], v[52:55], a[200:203], a[120:123]\n"
    "buffer_load_dwordx4 a[156:159], v0, s[20:23], s92 offen offset:3072 sc1\n"
    "v_mfma_f32_16x16x32_bf16 a[124:127], v[52:55], a[204:207], a[124:127]\n"
    "s_waitcnt lgkmcnt(4)\n"
    "v_mfma_f32_16x16x32_bf16 a[128:131], v[56:59], a[192:195], a[128:131]\n"
    "ds_read_b64_tr_b16 v[24:25], v62 offset:9280\n"
    "v_mfma_f32_16x16x32_bf16 a[132:135], v[56:59], a[196:199], a[132:135]\n"
    "ds_read_b64_tr_b16 v[26:27], v62 offset:13888\n"
    "v_mfma_f32_16x16x32_bf16 a[136:139], v[56:59], a[200:203], a[136:139]\n"
    "buffer_load_dwordx4 a[152:155], v0, s[20:23], s92 offen offset:2048 sc1\n"
    "v_mfma_f32_16x16x32_bf16 a[140:143], v[56:59], a[204:207], a[140:143]\n"
    "s_waitcnt vmcnt(19) lgkmcnt(4)\n"
    "v_mfma_f32_16x16x32_bf16 a[0:3], v[16:19], a[208:211], a[0:3]\n"
    "ds_read_b64_tr_b16 v[28:29], v62 offset:9312\n"
    "v_mfma_f32_16x16x32_bf16 a[4:7], v[16:19], a[212:215], a[4:7]\n"
    "s_add_u32 s38, s95, 4\n"
    "ds_read_b64_tr_b16 v[30:31], v62 offset:13920\n"
    "v_mfma_f32_16x16x32_bf16 a[8:11], v[16:19], a[216:219], a[8:11]\n"
    "buffer_load_dwordx2 v[14:15], v1, s[76:79], s12 offen sc1\n"
    "v_mfma_f32_16x16x32_bf16 a[12:15], v[16:19], a[220:223], a[12:15]\n"
    "s_waitcnt lgkmcnt(4)\n"
    "v_mfma_f32_16x16x32_bf16 a[16:19], v[20:23], a[208:211], a[16:19]\n"
    "ds_read_b64_tr_b16 v[32:33], v62 offset:9344\n"
    "v_mfma_f32_16x16x32_bf16 a[20:23], v[20:23], a[212:215], a[20:23]\n"
    "ds_read_b64_tr_b16 v[34:35], v62 offset:13952\n"
    "v_mfma_f32_16x16x32_bf16 a[24:27], v[20:23], a[216:219], a[24:27]\n"
    "v_mfma_f32_16x16x32_bf16 a[28:31], v[20:23], a[220:223], a[28:31]\n"
    "s_waitcnt lgkmcnt(4)\n"
    "v_mfma_f32_16x16x32_bf16 a[32:35], v[24:27], a[208:211], a[32:35]\n"
    "ds_read_b64_tr_b16 v[36:37], v62 offset:9376\n"
    "v_mfma_f32_16x16x32_bf16 a[36:39], v[24:27], a[212:215], a[36:39]\n"
    "ds_read_b64_tr_b16 v[38:39], v62 offset:13984\n"
    "v_mfma_f32_16x16x32_bf16 a[40:43], v[24:27], a[216:219], a[40:43]\n"
    "buffer_load_dwordx4 a[148:151], v0, s[20:23], s92 offen offset:1024 sc1\n"
    "v_mfma_f32_16x16x32_bf16 a[44:47], v[24:27], a[220:223], a[44:47]\n"
    "s_waitcnt lgkmcnt(4)\n"
    "v_mfma_f32_16x16x32_bf16 a[48:51], v[28:31], a[208:211], a[48:51]\n"
    "ds_read_b64_tr_b16 v[48:49], v62 offset:9408\n"
    "v_mfma_f32_16x16x32_bf16 a[52:55], v[28:31], a[212:215], a[52:55]\n"
    "ds_read_b64_tr_b16 v[50:51], v62 offset:14016\n"
    "v_mfma_f32_16x16x32_bf16 a[56:59], v[28:31], a[216:219], a[56:59]\n"
    "s_mov_b32 s91, 319950089\n"
    "buffer_load_dwordx4 a[144:147], v0, s[20:23], s92 offen offset:0 sc1\n"
    "v_mfma_f32_16x16x32_bf16 a[60:63], v[28:31], a[220:223], a[60:63]\n"
    "s_waitcnt lgkmcnt(4)\n"
    "s_lshr_b32 s91, s91, s36\n"
    "s_xor_b32 s91, s91, s16\n"
    "s_and_b32 s91, s91, 31\n"
    "v_mfma_f32_16x16x32_bf16 a[64:67], v[32:35], a[208:211], a[64:67]\n"
    "s_mul_i32 s91, s91, 36864\n"
    "ds_read_b64_tr_b16 v[52:53], v62 offset:9440\n"
    "v_mfma_f32_16x16x32_bf16 a[68:71], v[32:35], a[212:215], a[68:71]\n"
    "s_add_u32 m0, s17, 90112\n"
    "ds_read_b64_tr_b16 v[54:55], v62 offset:14048\n"
    "v_mfma_f32_16x16x32_bf16 a[72:75], v[32:35], a[216:219], a[72:75]\n"
    "buffer_load_dwordx4 v2, s[40:43], s91 offen lds\n"
    "v_mfma_f32_16x16x32_bf16 a[76:79], v[32:35], a[220:223], a[76:79]\n"
    "s_waitcnt lgkmcnt(4)\n"
    "v_mfma_f32_16x16x32_bf16 a[80:83], v[36:39], a[208:211], a[80:83]\n"
    "ds_read_b64_tr_b16 v[56:57], v62 offset:9472\n"
    "v_mfma_f32_16x16x32_bf16 a[84:87], v[36:39], a[212:215], a[84:87]\n"
    "ds_read_b64_tr_b16 v[58:59], v62 offset:14080\n"
    "v_mfma_f32_16x16x32_bf16 a[88:91], v[36:39], a[216:219], a[88:91]\n"
    "v_mfma_f32_16x16x32_bf16 a[92:95], v[36:39], a[220:223], a[92:95]\n"
    "s_waitcnt lgkmcnt(4)\n"
    "buffer_load_dwordx4 v3, s[40:43], s91 offen offset:1024 lds\n"
    "v_mfma_f32_16x16x32_bf16 a[96:99], v[48:51], a[208:211], a[96:99]\n"
    "v_mfma_f32_16x16x32_bf16 a[100:103], v[48:51], a[212:215], a[100:103]\n"
    "v_mfma_f32_16x16x32_bf16 a[104:107], v[48:51], a[216:219], a[104:107]\n"
    "s_waitcnt lgkmcnt(0)\n"
    "s_barrier\n"
    "ds_read_b64_tr_b16 v[16:17], v60 offset:36864\n"
    "v_mfma_f32_16x16x32_bf16 a[108:111], v[48:51], a[220:223], a[108:111]\n"
    "s_waitcnt lgkmcnt(3)\n"
    "ds_read_b64_tr_b16 v[18:19], v60 offset:41472\n"
    "v_mfma_f32_16x16x32_bf16 a[112:115], v[52:55], a[208:211], a[112:115]\n"
    "ds_read_b64_tr_b16 v[20:21], v60 offset:36896\n"
    "v_mfma_f32_16x16x32_bf16 a[116:119], v[52:55], a[212:215], a[116:119]\n"
    "ds_read_b64_tr_b16 v[22:23], v60 offset:41504\n"
    "v_mfma_f32_16x16x32_bf16 a[120:123], v[52:55], a[216:219], a[120:123]\n"
    "buffer_load_dwordx4 v4, s[40:43], s91 offen offset:2048 lds\n"
    "v_mfma_f32_16x16x32_bf16 a[124:127], v[52:55], a[220:223], a[124:127]\n"
    "s_waitcnt lgkmcnt(4)\n"
    "v_mfma_f32_16x16x32_bf16 a[128:131], v[56:59], a[208:211], a[128:131]\n"
    "ds_read_b64_tr_b16 v[24:25], v60 offset:36928\n"
    "v_mfma_f32_16x16x32_bf16 a[132:135], v[56:59], a[212:215], a[132:135]\n"
    "ds_read_b64_tr_b16 v[26:27], v60 offset:41536\n"
    "v_mfma_f32_16x16x32_bf16 a[136:139], v[56:59], a[216:219], a[136:139]\n"
    "buffer_load_dwordx4 v5, s[40:43], s91 offen offset:3072 lds\n"
    "v_mfma_f32_16x16x32_bf16 a[140:143], v[56:59], a[220:223], a[140:143]\n"
    "s_waitcnt vmcnt(15) lgkmcnt(4)\n"
    "v_mfma_f32_16x16x32_bf16 a[0:3], v[16:19], a[224:227], a[0:3]\n"
    "ds_read_b64_tr_b16 v[28:29], v60 offset:36960\n"
    "v_mfma_f32_16x16x32_bf16 a[4:7], v[16:19], a[228:231], a[4:7]\n"
    "s_add_u32 m0, s17, 94208\n"
    "ds_read_b64_tr_b16 v[30:31], v60 offset:41568\n"
    "v_mfma_f32_16x16x32_bf16 a[8:11], v[16:19], a[232:235], a[8:11]\n"
    "buffer_load_dwordx4 v6, s[40:43], s91 offen lds\n"
    "v_mfma_f32_16x16x32_bf16 a[12:15], v[16:19], a[236:239], a[12:15]\n"
    "s_waitcnt lgkmcnt(4)\n"
    "v_mfma_f32_16x16x32_bf16 a[16:19], v[20:23], a[224:227], a[16:19]\n"
    "ds_read_b64_tr_b16 v[32:33], v60 offset:36992\n"
    "v_mfma_f32_16x16x32_bf16 a[20:23], v[20:23], a[228:231], a[20:23]\n"
    "ds_read_b64_tr_b16 v[34:35], v60 offset:41600\n"
    "v_mfma_f32_16x16x32_bf16 a[24:27], v[20:23], a[232:235], a[24:27]\n"
    "v_mfma_f32_16x16x32_bf16 a[28:31], v[20:23], a[236:239], a[28:31]\n"
    "s_waitcnt lgkmcnt(4)\n"
    "buffer_load_dwordx4 v7, s[40:43], s91 offen offset:1024 lds\n"
    "v_mfma_f32_16x16x32_bf16 a[32:35], v[24:27], a[224:227], a[32:35]\n"
    "ds_read_b64_tr_b16 v[36:37], v60 offset:37024\n"
    "v_mfma_f32_16x16x32_bf16 a[36:39], v[24:27], a[228:231], a[36:39]\n"
    "ds_read_b64_tr_b16 v[38:39], v60 offset:41632\n"
    "v_mfma_f32_16x16x32_bf16 a[40:43], v[24:27], a[232:235], a[40:43]\n"
    "v_mfma_f32_16x16x32_bf16 a[44:47], v[24:27], a[236:239], a[44:47]\n"
    "s_waitcnt lgkmcnt(4)\n"
    "v_mfma_f32_16x16x32_bf16 a[48:51], v[28:31], a[224:227], a[48:51]\n"
    "ds_read_b64_tr_b16 v[48:49], v60 offset:37056\n"
    "v_mfma_f32_16x16x32_bf16 a[52:55], v[28:31], a[228:231], a[52:55]\n"
    "ds_read_b64_tr_b16 v[50:51], v60 offset:41664\n"
    "v_mfma_f32_16x16x32_bf16 a[56:59], v[28:31], a[232:235], a[56:59]\n"
    "buffer_load_dwordx4 v8, s[40:43], s91 offen offset:2048 lds\n"
    "v_mfma_f32_16x16x32_bf16 a[60:63], v[28:31], a[236:239], a[60:63]\n"
    "s_waitcnt lgkmcnt(4)\n"
    "v_mfma_f32_16x16x32_bf16 a[64:67], v[32:35], a[224:227], a[64:67]\n"
    "ds_read_b64_tr_b16 v[52:53], v60 offset:37088\n"
    "v_mfma_f32_16x16x32_bf16 a[68:71], v[32:35], a[228:231], a[68:71]\n"
    "ds_read_b64_tr_b16 v[54:55], v60 offset:41696\n"
    "v_mfma_f32_16x16x32_bf16 a[72:75], v[32:35], a[232:235], a[72:75]\n"
    "buffer_load_dwordx4 v9, s[40:43], s91 offen offset:3072 lds\n"
    "v_mfma_f32_16x16x32_bf16 a[76:79], v[32:35], a[236:239], a[76:79]\n"
    "s_waitcnt lgkmcnt(4)\n"
    "v_mfma_f32_16x16x32_bf16 a[80:83], v[36:39], a[224:227], a[80:83]\n"
    "ds_read_b64_tr_b16 v[56:57], v60 offset:37120\n"
    "v_mfma_f32_16x16x32_bf16 a[84:87], v[36:39], a[228:231], a[84:87]\n"
    "s_add_u32 m0, s17, 98304\n"
    "ds_read_b64_tr_b16 v[58:59], v60 offset:41728\n"
    "v_mfma_f32_16x16x32_bf16 a[88:91], v[36:39], a[232:235], a[88:91]\n"
    "buffer_load_dwordx4 v10, s[40:43], s91 offen lds\n"
    "v_mfma_f32_16x16x32_bf16 a[92:95], v[36:39], a[236:239], a[92:95]\n"
    "s_waitcnt lgkmcnt(4)\n"
    "s_mov_b32 s91, 454694678\n"
    "v_mfma_f32_16x16x32_bf16 a[96:99], v[48:51], a[224:227], a[96:99]\n"
    "s_lshr_b32 s91, s91, s36\n"
    "s_xor_b32 s91, s91, s16\n"
    "s_and_b32 s91, s91, 31\n"
    "ds_read_b64_tr_b16 v[16:17], v60 offset:46080\n"
    "v_mfma_f32_16x16x32_bf16 a[100:103], v[48:51], a[228:231], a[100:103]\n"
    "s_mul_i32 s91, s91, 36864\n"
    "ds_read_b64_tr_b16 v[18:19], v60 offset:50688\n"
    "v_mfma_f32_16x16x32_bf16 a[104:107], v[48:51], a[232:235], a[104:107]\n"
    "s_add_u32 m0, s17, 126976\n"
    "v_mfma_f32_16x16x32_bf16 a[108:111], v[48:51], a[236:239], a[108:111]\n"
    "s_waitcnt lgkmcnt(4)\n"
    "buffer_load_dwordx4 v2, s[40:43], s91 offen lds\n"
    "v_mfma_f32_16x16x32_bf16 a[112:115], v[52:55], a[224:227], a[112:115]\n"
    "ds_read_b64_tr_b16 v[20:21], v60 offset:46112\n"
    "v_mfma_f32_16x16x32_bf16 a[116:119], v[52:55], a[228:231], a[116:119]\n"
    "ds_read_b64_tr_b16 v[22:23], v60 offset:50720\n"
    "v_mfma_f32_16x16x32_bf16 a[120:123], v[52:55], a[232:235], a[120:123]\n"
    "v_mfma_f32_16x16x32_bf16 a[124:127], v[52:55], a[236:239], a[124:127]\n"
    "s_waitcnt lgkmcnt(4)\n"
    "v_mfma_f32_16x16x32_bf16 a[128:131], v[56:59], a[224:227], a[128:131]\n"
    "ds_read_b64_tr_b16 v[24:25], v60 offset:46144\n"
    "v_mfma_f32_16x16x32_bf16 a[132:135], v[56:59], a[228:231], a[132:135]\n"
    "ds_read_b64_tr_b16 v[26:27], v60 offset:50752\n"
    "v_mfma_f32_16x16x32_bf16 a[136:139], v[56:59], a[232:235], a[136:139]\n"
    "buffer_load_dwordx4 v3, s[40:43], s91 offen offset:1024 lds\n"
    "v_mfma_f32_16x16x32_bf16 a[140:143], v[56:59], a[236:239], a[140:143]\n"
    "s_waitcnt vmcnt(13)\n"
    "v_subrev_u32 v14, s38, v14\n"
    "v_xor_b32 v15, v47, v15\n"
    "v_cmp_lt_u64 vcc, 15, v[14:15]\n"
    "s_nop 0\n"
    "s_cbranch_vccnz L_SP3_%=\n"
    "L_BK3_%=:\n"
    "s_waitcnt lgkmcnt(4)\n"
    "v_mfma_f32_16x16x32_bf16 a[0:3], v[16:19], a[240:243], a[0:3]\n"
    "ds_read_b64_tr_b16 v[28:29], v60 offset:46176\n"
    "v_mfma_f32_16x16x32_bf16 a[4:7], v[16:19], a[244:247], a[4:7]\n"
    "s_xor_b32 s92, s19, 69632\n"
    "ds_read_b64_tr_b16 v[30:31], v60 offset:50784\n"
    "v_mfma_f32_16x16x32_bf16 a[8:11], v[16:19], a[248:251], a[8:11]\n"
    "buffer_load_dwordx4 v[76:79], v0, s[20:23], s92 offen offset:3072 sc1\n"
    "v_mfma_f32_16x16x32_bf16 a[12:15], v[16:19], a[252:255], a[12:15]\n"
    "s_waitcnt lgkmcnt(4)\n"
    "v_mfma_f32_16x16x32_bf16 a[16:19], v[20:23], a[240:243], a[16:19]\n"
    "ds_read_b64_tr_b16 v[32:33], v60 offset:46208\n"
    "v_mfma_f32_16x16x32_bf16 a[20:23], v[20:23], a[244:247], a[20:23]\n"
    "ds_read_b64_tr_b16 v[34:35], v60 offset:50816\n"
    "v_mfma_f32_16x16x32_bf16 a[24:27], v[20:23], a[248:251], a[24:27]\n"
    "buffer_load_dwordx4 v[72:75], v0, s[20:23], s92 offen offset:2048 sc1\n"
    "v_mfma_f32_16x16x32_bf16 a[28:31], v[20:23], a[252:255], a[28:31]\n"
    "s_waitcnt lgkmcnt(4)\n"
    "v_mfma_f32_16x16x32_bf16 a[32:35], v[24:27], a[240:243], a[32:35]\n"
    "ds_read_b64_tr_b16 v[36:37], v60 offset:46240\n"
    "v_mfma_f32_16x16x32_bf16 a[36:39], v[24:27], a[244:247], a[36:39]\n"
    "ds_read_b64_tr_b16 v[38:39], v60 offset:50848\n"
    "v_mfma_f32_16x16x32_bf16 a[40:43], v[24:27], a[248:251], a[40:43]\n"
    "v_mfma_f32_16x16x32_bf16 a[44:47], v[24:27], a[252:255], a[44:47]\n"
    "s_waitcnt lgkmcnt(4)\n"
    "buffer_load_dwordx4 v[68:71], v0, s[20:23], s92 offen offset:1024 sc1\n"
    "v_mfma_f32_16x16x32_bf16 a[48:51], v[28:31], a[240:243], a[48:51]\n"
    "ds_read_b64_tr_b16 v[48:49], v60 offset:46272\n"
    "v_mfma_f32_16x16x32_bf16 a[52:55], v[28:31], a[244:247], a[52:55]\n"
    "ds_read_b64_tr_b16 v[50:51], v60 offset:50880\n"
    "v_mfma_f32_16x16x32_bf16 a[56:59], v[28:31], a[248:251], a[56:59]\n"
    "v_mfma_f32_16x16x32_bf16 a[60:63], v[28:31], a[252:255], a[60:63]\n"
    "s_waitcnt lgkmcnt(4)\n"
    "v_mfma_f32_16x16x32_bf16 a[64:67], v[32:35], a[240:243], a[64:67]\n"
    "ds_read_b64_tr_b16 v[52:53], v60 offset:46304\n"
    "v_mfma_f32_16x16x32_bf16 a[68:71], v[32:35], a[244:247], a[68:71]\n"
    "ds_read_b64_tr_b16 v[54:55], v60 offset:50912\n"
    "v_mfma_f32_16x16x32_bf16 a[72:75], v[32:35], a[248:251], a[72:75]\n"
    "buffer_load_dwordx4 v[64:67], v0, s[20:23], s92 offen offset:0 sc1\n"
    "v_mfma_f32_16x16x32_bf16 a[76:79], v[32:35], a[252:255], a[76:79]\n"
    "s_waitcnt lgkmcnt(4)\n"
    "v_mfma_f32_16x16x32_bf16 a[80:83], v[36:39], a[240:243], a[80:83]\n"
    "ds_read_b64_tr_b16 v[56:57], v60 offset:46336\n"
    "v_mfma_f32_16x16x32_bf16 a[84:87], v[36:39], a[244:247], a[84:87]\n"
    "s_xor_b32 s92, s19, 86016\n"
    "ds_read_b64_tr_b16 v[58:59], v60 offset:50944\n"
    "v_mfma_f32_16x16x32_bf16 a[88:91], v[36:39], a[248:251], a[88:91]\n"
    "buffer_load_dwordx4 v[92:95], v0, s[20:23], s92 offen offset:3072 sc1\n"
    "v_mfma_f32_16x16x32_bf16 a[92:95], v[36:39], a[252:255], a[92:95]\n"
    "s_waitcnt lgkmcnt(4)\n"
    "v_mfma_f32_16x16x32_bf16 a[96:99], v[48:51], a[240:243], a[96:99]\n"
    "ds_read_b64_tr_b16 v[16:17], v60 offset:55296\n"
    "v_mfma_f32_16x16x32_bf16 a[100:103], v[48:51], a[244:247], a[100:103]\n"
    "ds_read_b64_tr_b16 v[18:19], v60 offset:59904\n"
    "v_mfma_f32_16x16x32_bf16 a[104:107], v[48:51], a[248:251], a[104:107]\n"
    "buffer_load_dwordx4 v[88:91], v0, s[20:23], s92 offen offset:2048 sc1\n"
    "v_mfma_f32_16x16x32_bf16 a[108:111], v[48:51], a[252:255], a[108:111]\n"
    "s_waitcnt lgkmcnt(4)\n"
    "v_mfma_f32_16x16x32_bf16 a[112:115], v[52:55], a[240:243], a[112:115]\n"
    "ds_read_b64_tr_b16 v[20:21], v60 offset:55328\n"
    "v_mfma_f32_16x16x32_bf16 a[116:119], v[52:55], a[244:247], a[116:119]\n"
    "ds_read_b64_tr_b16 v[22:23], v60 offset:59936\n"
    "v_mfma_f32_16x16x32_bf16 a[120:123], v[52:55], a[248:251], a[120:123]\n"
    "v_mfma_f32_16x16x32_bf16 a[124:127], v[52:55], a[252:255], a[124:127]\n"
    "s_waitcnt lgkmcnt(4)\n"
    "buffer_load_dwordx4 v[84:87], v0, s[20:23], s92 offen offset:1024 sc1\n"
    "v_mfma_f32_16x16x32_bf16 a[128:131], v[56:59], a[240:243], a[128:131]\n"
    "ds_read_b64_tr_b16 v[24:25], v60 offset:55360\n"
    "v_mfma_f32_16x16x32_bf16 a[132:135], v[56:59], a[244:247], a[132:135]\n"
    "ds_read_b64_tr_b16 v[26:27], v60 offset:59968\n"
    "v_mfma_f32_16x16x32_bf16 a[136:139], v[56:59], a[248:251], a[136:139]\n"
    "v_mfma_f32_16x16x32_bf16 a[140:143], v[56:59], a[252:255], a[140:143]\n"
    "s_waitcnt vmcnt(18) lgkmcnt(4)\n"
    "v_mfma_f32_16x16x32_bf16 a[0:3], v[16:19], a[144:147], a[0:3]\n"
    "ds_read_b64_tr_b16 v[28:29], v60 offset:55392\n"
    "v_mfma_f32_16x16x32_bf16 a[4:7], v[16:19], a[148:151], a[4:7]\n"
    "ds_read_b64_tr_b16 v[30:31], v60 offset:60000\n"
    "v_add_u32 v46, 1, v46\n"
    "v_mfma_f32_16x16x32_bf16 a[8:11], v[16:19], a[152:155], a[8:11]\n"
    "buffer_store_dwordx2 v[46:47], v11, s[76:79], 0 offen sc1\n"
    "v_mfma_f32_16x16x32_bf16 a[12:15], v[16:19], a[156:159], a[12:15]\n"
    "s_waitcnt lgkmcnt(4)\n"
    "v_mfma_f32_16x16x32_bf16 a[16:19], v[20:23], a[144:147], a[16:19]\n"
    "ds_read_b64_tr_b16 v[32:33], v60 offset:55424\n"
    "v_mfma_f32_16x16x32_bf16 a[20:23], v[20:23], a[148:151], a[20:23]\n"
    "ds_read_b64_tr_b16 v[34:35], v60 offset:60032\n"
    "v_mfma_f32_16x16x32_bf16 a[24:27], v[20:23], a[152:155], a[24:27]\n"
    "buffer_load_dwordx4 v[80:83], v0, s[20:23], s92 offen offset:0 sc1\n"
    "v_mfma_f32_16x16x32_bf16 a[28:31], v[20:23], a[156:159], a[28:31]\n"
    "s_waitcnt lgkmcnt(4)\n"
    "v_mfma_f32_16x16x32_bf16 a[32:35], v[24:27], a[144:147], a[32:35]\n"
    "ds_read_b64_tr_b16 v[36:37], v60 offset:55456\n"
    "v_mfma_f32_16x16x32_bf16 a[36:39], v[24:27], a[148:151], a[36:39]\n"
    "s_xor_b32 s92, s19, 102400\n"
    "ds_read_b64_tr_b16 v[38:39], v60 offset:60064\n"
    "v_mfma_f32_16x16x32_bf16 a[40:43], v[24:27], a[152:155], a[40:43]\n"
    "buffer_load_dwordx4 a[172:175], v0, s[20:23], s92 offen offset:3072 sc1\n"
    "v_mfma_f32_16x16x32_bf16 a[44:47], v[24:27], a[156:159], a[44:47]\n"
    "s_waitcnt lgkmcnt(4)\n"
    "v_mfma_f32_16x16x32_bf16 a[48:51], v[28:31], a[144:147], a[48:51]\n"
    "ds_read_b64_tr_b16 v[48:49], v60 offset:55488\n"
    "v_mfma_f32_16x16x32_bf16 a[52:55], v[28:31], a[148:151], a[52:55]\n"
    "ds_read_b64_tr_b16 v[50:51], v60 offset:60096\n"
    "v_mfma_f32_16x16x32_bf16 a[56:59], v[28:31], a[152:155], a[56:59]\n"
    "v_mfma_f32_16x16x32_bf16 a[60:63], v[28:31], a[156:159], a[60:63]\n"
    "s_waitcnt lgkmcnt(4)\n"
    "buffer_load_dwordx4 a[168:171], v0, s[20:23], s92 offen offset:2048 sc1\n"
    "v_mfma_f32_16x16x32_bf16 a[64:67], v[32:35], a[144:147], a[64:67]\n"
    "ds_read_b64_tr_b16 v[52:53], v60 offset:55520\n"
    "v_mfma_f32_16x16x32_bf16 a[68:71], v[32:35], a[148:151], a[68:71]\n"
    "ds_read_b64_tr_b16 v[54:55], v60 offset:60128\n"
    "v_mfma_f32_16x16x32_bf16 a[72:75], v[32:35], a[152:155], a[72:75]\n"
    "v_mfma_f32_16x16x32_bf16 a[76:79], v[32:35], a[156:159], a[76:79]\n"
    "s_waitcnt lgkmcnt(4)\n"
    "v_mfma_f32_16x16x32_bf16 a[80:83], v[36:39], a[144:147], a[80:83]\n"
    "ds_read_b64_tr_b16 v[56:57], v60 offset:55552\n"
    "v_mfma_f32_16x16x32_bf16 a[84:87], v[36:39], a[148:151], a[84:87]\n"
    "ds_read_b64_tr_b16 v[58:59], v60 offset:60160\n"
    "v_mfma_f32_16x16x32_bf16 a[88:91], v[36:39], a[152:155], a[88:91]\n"
    "buffer_load_dwordx4 a[164:167], v0, s[20:23], s92 offen offset:1024 sc1\n"
    "v_mfma_f32_16x16x32_bf16 a[92:95], v[36:39], a[156:159], a[92:95]\n"
    "s_waitcnt lgkmcnt(4)\n"
    "v_mfma_f32_16x16x32_bf16 a[96:99], v[48:51], a[144:147], a[96:99]\n"
    "ds_read_b64_tr_b16 v[16:17], v61 offset:0\n"
    "v_mfma_f32_16x16x32_bf16 a[100:103], v[48:51], a[148:151], a[100:103]\n"
    "ds_read_b64_tr_b16 v[18:19], v61 offset:4608\n"
    "v_mfma_f32_16x16x32_bf16 a[104:107], v[48:51], a[152:155], a[104:107]\n"
    "buffer_load_dwordx4 a[160:163], v0, s[20:23], s92 offen offset:0 sc1\n"
    "v_mfma_f32_16x16x32_bf16 a[108:111], v[48:51], a[156:159], a[108:111]\n"
    "s_waitcnt lgkmcnt(4)\n"
    "v_mfma_f32_16x16x32_bf16 a[112:115], v[52:55], a[144:147], a[112:115]\n"
    "ds_read_b64_tr_b16 v[20:21], v61 offset:32\n"
    "v_mfma_f32_16x16x32_bf16 a[116:119], v[52:55], a[148:151], a[116:119]\n"
    "s_xor_b32 s92, s19, 118784\n"
    "ds_read_b64_tr_b16 v[22:23], v61 offset:4640\n"
    "v_mfma_f32_16x16x32_bf16 a[120:123], v[52:55], a[152:155], a[120:123]\n"
    "buffer_load_dwordx4 a[188:191], v0, s[20:23], s92 offen offset:3072 sc1\n"
    "v_mfma_f32_16x16x32_bf16 a[124:127], v[52:55], a[156:159], a[124:127]\n"
    "s_waitcnt lgkmcnt(4)\n"
    "v_mfma_f32_16x16x32_bf16 a[128:131], v[56:59], a[144:147], a[128:131]\n"
    "ds_read_b64_tr_b16 v[24:25], v61 offset:64\n"
    "v_mfma_f32_16x16x32_bf16 a[132:135], v[56:59], a[148:151], a[132:135]\n"
    "ds_read_b64_tr_b16 v[26:27], v61 offset:4672\n"
    "v_mfma_f32_16x16x32_bf16 a[136:139], v[56:59], a[152:155], a[136:139]\n"
    "v_mfma_f32_16x16x32_bf16 a[140:143], v[56:59], a[156:159], a[140:143]\n"
    "s_waitcnt vmcnt(10) lgkmcnt(4)\n"
    "buffer_load_dwordx4 a[184:187], v0, s[20:23], s92 offen offset:2048 sc1\n"
    "v_mfma_f32_16x16x32_bf16 a[0:3], v[16:19], v[64:67], a[0:3]\n"
    "ds_read_b64_tr_b16 v[28:29], v61 offset:96\n"
    "v_mfma_f32_16x16x32_bf16 a[4:7], v[16:19], v[68:71], a[4:7]\n"
    "ds_read_b64_tr_b16 v[30:31], v61 offset:4704\n"
    "v_mfma_f32_16x16x32_bf16 a[8:11], v[16:19], v[72:75], a[8:11]\n"
    "v_mfma_f32_16x16x32_bf16 a[12:15], v[16:19], v[76:79], a[12:15]\n"
    "s_waitcnt lgkmcnt(4)\n"
    "v_mfma_f32_16x16x32_bf16 a[16:19], v[20:23], v[64:67], a[16:19]\n"
    "ds_read_b64_tr_b16 v[32:33], v61 offset:128\n"
    "v_mfma_f32_16x16x32_bf16 a[20:23], v[20:23], v[68:71], a[20:23]\n"
    "ds_read_b64_tr_b16 v[34:35], v61 offset:4736\n"
    "v_mfma_f32_16x16x32_bf16 a[24:27], v[20:23], v[72:75], a[24:27]\n"
    "buffer_load_dwordx4 a[180:183], v0, s[20:23], s92 offen offset:1024 sc1\n"
    "v_mfma_f32_16x16x32_bf16 a[28:31], v[20:23], v[76:79], a[28:31]\n"
    "s_waitcnt lgkmcnt(4)\n"
    "v_mfma_f32_16x16x32_bf16 a[32:35], v[24:27], v[64:67], a[32:35]\n"
    "ds_read_b64_tr_b16 v[36:37], v61 offset:160\n"
    "v_mfma_f32_16x16x32_bf16 a[36:39], v[24:27], v[68:71], a[36:39]\n"
    "ds_read_b64_tr_b16 v[38:39], v61 offset:4768\n"
    "v_mfma_f32_16x16x32_bf16 a[40:43], v[24:27], v[72:75], a[40:43]\n"
    "buffer_load_dwordx4 a[176:179], v0, s[20:23], s92 offen offset:0 sc1\n"
    "v_mfma_f32_16x16x32_bf16 a[44:47], v[24:27], v[76:79], a[44:47]\n"
    "s_waitcnt lgkmcnt(4)\n"
    "v_mfma_f32_16x16x32_bf16 a[48:51], v[28:31], v[64:67], a[48:51]\n"
    "ds_read_b64_tr_b16 v[48:49], v61 offset:192\n"
    "v_mfma_f32_16x16x32_bf16 a[52:55], v[28:31], v[68:71], a[52:55]\n"
    "s_xor_b32 s92, s19, 20480\n"
    "ds_read_b64_tr_b16 v[50:51], v61 offset:4800\n"
    "v_mfma_f32_16x16x32_bf16 a[56:59], v[28:31], v[72:75], a[56:59]\n"
    "buffer_load_dwordx4 v[140:143], v0, s[20:23], s92 offen offset:3072 sc1\n"
    "v_mfma_f32_16x16x32_bf16 a[60:63], v[28:31], v[76:79], a[60:63]\n"
    "s_waitcnt lgkmcnt(4)\n"
    "v_mfma_f32_16x16x32_bf16 a[64:67], v[32:35], v[64:67], a[64:67]\n"
    "ds_read_b64_tr_b16 v[52:53], v61 offset:224\n"
    "v_mfma_f32_16x16x32_bf16 a[68:71], v[32:35], v[68:71], a[68:71]\n"
    "s_add_u32 s38, s95, 5\n"
    "ds_read_b64_tr_b16 v[54:55], v61 offset:4832\n"
    "v_mfma_f32_16x16x32_bf16 a[72:75], v[32:35], v[72:75], a[72:75]\n"
    "buffer_load_dwordx2 v[14:15], v1, s[76:79], s12 offen sc1\n"
    "v_mfma_f32_16x16x32_bf16 a[76:79], v[32:35], v[76:79], a[76:79]\n"
    "s_waitcnt lgkmcnt(4)\n"
    "v_mfma_f32_16x16x32_bf16 a[80:83], v[36:39], v[64:67], a[80:83]\n"
    "ds_read_b64_tr_b16 v[56:57], v61 offset:256\n"
    "v_mfma_f32_16x16x32_bf16 a[84:87], v[36:39], v[68:71], a[84:87]\n"
    "ds_read_b64_tr_b16 v[58:59], v61 offset:4864\n"
    "v_mfma_f32_16x16x32_bf16 a[88:91], v[36:39], v[72:75], a[88:91]\n"
    "v_mfma_f32_16x16x32_bf16 a[92:95], v[36:39], v[76:79], a[92:95]\n"
    "s_waitcnt lgkmcnt(4)\n"
    "v_mfma_f32_16x16x32_bf16 a[96:99], v[48:51], v[64:67], a[96:99]\n"
    "buffer_load_dwordx4 v[136:139], v0, s[20:23], s92 offen offset:2048 sc1\n"
    "v_mfma_f32_16x16x32_bf16 a[100:103], v[48:51], v[68:71], a[100:103]\n"
    "v_mfma_f32_16x16x32_bf16 a[104:107], v[48:51], v[72:75], a[104:107]\n"
    "s_barrier\n"
    "ds_read_b64_tr_b16 v[16:17], v60 offset:0\n"
    "v_mfma_f32_16x16x32_bf16 a[108:111], v[48:51], v[76:79], a[108:111]\n"
    "s_waitcnt lgkmcnt(3)\n"
    "ds_read_b64_tr_b16 v[18:19], v60 offset:4608\n"
    "v_mfma_f32_16x16x32_bf16 a[112:115], v[52:55], v[64:67], a[112:115]\n"
    "ds_read_b64_tr_b16 v[20:21], v60 offset:9216\n"
    "v_mfma_f32_16x16x32_bf16 a[116:119], v[52:55], v[68:71], a[116:119]\n"
    "ds_read_b64_tr_b16 v[22:23], v60 offset:13824\n"
    "v_mfma_f32_16x16x32_bf16 a[120:123], v[52:55], v[72:75], a[120:123]\n"
    "buffer_load_dwordx4 v[132:135], v0, s[20:23], s92 offen offset:1024 sc1\n"
    "v_mfma_f32_16x16x32_bf16 a[124:127], v[52:55], v[76:79], a[124:127]\n"
    "s_waitcnt lgkmcnt(4)\n"
    "v_mfma_f32_16x16x32_bf16 a[128:131], v[56:59], v[64:67], a[128:131]\n"
    "ds_read_b64_tr_b16 v[24:25], v60 offset:32\n"
    "v_mfma_f32_16x16x32_bf16 a[132:135], v[56:59], v[68:71], a[132:135]\n"
    "ds_read_b64_tr_b16 v[26:27], v60 offset:4640\n"
    "v_mfma_f32_16x16x32_bf16 a[136:139], v[56:59], v[72:75], a[136:139]\n"
    "ds_read_b64_tr_b16 v[28:29], v60 offset:9248\n"
    "v_mfma_f32_16x16x32_bf16 a[140:143], v[56:59], v[76:79], a[140:143]\n"
    "s_waitcnt vmcnt(12) lgkmcnt(5)\n"
    "ds_read_b64_tr_b16 v[30:31], v60 offset:13856\n"
    "v_mfma_f32_16x16x32_bf16 a[0:3], v[16:19], v[80:83], a[0:3]\n"
    "s_waitcnt vmcnt(8) lgkmcnt(4)\n"
    "buffer_load_dwordx4 v[128:131], v0, s[20:23], s92 offen offset:0 sc1\n"
    "v_mfma_f32_16x16x32_bf16 a[0:3], v[20:23], a[160:163], a[0:3]\n"
    "v_mfma_f32_16x16x32_bf16 a[4:7], v[16:19], v[84:87], a[4:7]\n"
    "s_xor_b32 s92, s19, 36864\n"
    "v_mfma_f32_16x16x32_bf16 a[4:7], v[20:23], a[164:167], a[4:7]\n"
    "buffer_load_dwordx4 v[156:159], v0, s[20:23], s92 offen offset:3072 sc1\n"
    "v_mfma_f32_16x16x32_bf16 a[8:11], v[16:19], v[88:91], a[8:11]\n"
    "ds_read_b64_tr_b16 v[32:33], v60 offset:64\n"
    "v_mfma_f32_16x16x32_bf16 a[8:11], v[20:23], a[168:171], a[8:11]\n"
    "ds_read_b64_tr_b16 v[34:35], v60 offset:4672\n"
    "v_mfma_f32_16x16x32_bf16 a[12:15], v[16:19], v[92:95], a[12:15]\n"
    "ds_read_b64_tr_b16 v[36:37], v60 offset:9280\n"
    "v_mfma_f32_16x16x32_bf16 a[12:15], v[20:23], a[172:175], a[12:15]\n"
    "s_waitcnt lgkmcnt(5)\n"
    "ds_read_b64_tr_b16 v[38:39], v60 offset:13888\n"
    "v_mfma_f32_16x16x32_bf16 a[16:19], v[24:27], v[80:83], a[16:19]\n"
    "s_waitcnt lgkmcnt(4)\n"
    "buffer_load_dwordx4 v[152:155], v0, s[20:23], s92 offen offset:2048 sc1\n"
    "v_mfma_f32_16x16x32_bf16 a[16:19], v[28:31], a[160:163], a[16:19]\n"
    "v_mfma_f32_16x16x32_bf16 a[20:23], v[24:27], v[84:87], a[20:23]\n"
    "v_mfma_f32_16x16x32_bf16 a[20:23], v[28:31], a[164:167], a[20:23]\n"
    "v_mfma_f32_16x16x32_bf16 a[24:27], v[24:27], v[88:91], a[24:27]\n"
    "ds_read_b64_tr_b16 v[48:49], v60 offset:96\n"
    "v_mfma_f32_16x16x32_bf16 a[24:27], v[28:31], a[168:171], a[24:27]\n"
    "ds_read_b64_tr_b16 v[50:51], v60 offset:4704\n"
    "v_mfma_f32_16x16x32_bf16 a[28:31], v[24:27], v[92:95], a[28:31]\n"
    "ds_read_b64_tr_b16 v[52:53], v60 offset:9312\n"
    "v_mfma_f32_16x16x32_bf16 a[28:31], v[28:31], a[172:175], a[28:31]\n"
    "s_waitcnt lgkmcnt(5)\n"
    "ds_read_b64_tr_b16 v[54:55], v60 offset:13920\n"
    "v_mfma_f32_16x16x32_bf16 a[32:35], v[32:35], v[80:83], a[32:35]\n"
    "s_waitcnt lgkmcnt(4)\n"
    "buffer_load_dwordx4 v[148:151], v0, s[20:23], s92 offen offset:1024 sc1\n"
    "v_mfma_f32_16x16x32_bf16 a[32:35], v[36:39], a[160:163], a[32:35]\n"
    "v_mfma_f32_16x16x32_bf16 a[36:39], v[32:35], v[84:87], a[36:39]\n"
    "buffer_load_dwordx4 v[144:147], v0, s[20:23], s92 offen offset:0 sc1\n"
    "v_mfma_f32_16x16x32_bf16 a[36:39], v[36:39], a[164:167], a[36:39]\n"
    "v_mfma_f32_16x16x32_bf16 a[40:43], v[32:35], v[88:91], a[40:43]\n"
    "ds_read_b64_tr_b16 v[56:57], v60 offset:128\n"
    "v_mfma_f32_16x16x32_bf16 a[40:43], v[36:39], a[168:171], a[40:43]\n"
    "ds_read_b64_tr_b16 v[58:59], v60 offset:4736\n"
    "v_mfma_f32_16x16x32_bf16 a[44:47], v[32:35], v[92:95], a[44:47]\n"
    "ds_read_b64_tr_b16 v[16:17], v60 offset:9344\n"
    "v_mfma_f32_16x16x32_bf16 a[44:47], v[36:39], a[172:175], a[44:47]\n"
    "s_waitcnt lgkmcnt(5)\n"
    "s_xor_b32 s92, s19, 53248\n"
    "ds_read_b64_tr_b16 v[18:19], v60 offset:13952\n"
    "v_mfma_f32_16x16x32_bf16 a[48:51], v[48:51], v[80:83], a[48:51]\n"
    "s_waitcnt lgkmcnt(4)\n"
    "buffer_load_dwordx4 v[172:175], v0, s[20:23], s92 offen offset:3072 sc1\n"
    "v_mfma_f32_16x16x32_bf16 a[48:51], v[52:55], a[160:163], a[48:51]\n"
    "v_mfma_f32_16x16x32_bf16 a[52:55], v[48:51], v[84:87], a[52:55]\n"
    "v_mfma_f32_16x16x32_bf16 a[52:55], v[52:55], a[164:167], a[52:55]\n"
    "v_mfma_f32_16x16x32_bf16 a[56:59], v[48:51], v[88:91], a[56:59]\n"
    "ds_read_b64_tr_b16 v[20:21], v60 offset:160\n"
    "v_mfma_f32_16x16x32_bf16 a[56:59], v[52:55], a[168:171], a[56:59]\n"
    "ds_read_b64_tr_b16 v[22:23], v60 offset:4768\n"
    "v_mfma_f32_16x16x32_bf16 a[60:63], v[48:51], v[92:95], a[60:63]\n"
    "ds_read_b64_tr_b16 v[24:25], v60 offset:9376\n"
    "v_mfma_f32_16x16x32_bf16 a[60:63], v[52:55], a[172:175], a[60:63]\n"
    "s_waitcnt lgkmcnt(5)\n"
    "ds_read_b64_tr_b16 v[26:27], v60 offset:13984\n"
    "v_mfma_f32_16x16x32_bf16 a[64:67], v[56:59], v[80:83], a[64:67]\n"
    "s_waitcnt lgkmcnt(4)\n"
    "buffer_load_dwordx4 v[168:171], v0, s[20:23], s92 offen offset:2048 sc1\n"
    "v_mfma_f32_16x16x32_bf16 a[64:67], v[16:19], a[160:163], a[64:67]\n"
    "v_mfma_f32_16x16x32_bf16 a[68:71], v[56:59], v[84:87], a[68:71]\n"
    "buffer_load_dwordx4 v[164:167], v0, s[20:23], s92 offen offset:1024 sc1\n"
    "v_mfma_f32_16x16x32_bf16 a[68:71], v[16:19], a[164:167], a[68:71]\n"
    "v_mfma_f32_16x16x32_bf16 a[72:75], v[56:59], v[88:91], a[72:75]\n"
    "ds_read_b64_tr_b16 v[28:29], v60 offset:192\n"
    "v_mfma_f32_16x16x32_bf16 a[72:75], v[16:19], a[168:171], a[72:75]\n"
    "ds_read_b64_tr_b16 v[30:31], v60 offset:4800\n"
    "v_mfma_f32_16x16x32_bf16 a[76:79], v[56:59], v[92:95], a[76:79]\n"
    "ds_read_b64_tr_b16 v[32:33], v60 offset:9408\n"
    "v_mfma_f32_16x16x32_bf16 a[76:79], v[16:19], a[172:175], a[76:79]\n"
    "s_waitcnt lgkmcnt(5)\n"
    "ds_read_b64_tr_b16 v[34:35], v60 offset:14016\n"
    "v_mfma_f32_16x16x32_bf16 a[80:83], v[20:23], v[80:83], a[80:83]\n"
    "s_waitcnt lgkmcnt(4)\n"
    "buffer_load_dwordx4 v[160:163], v0, s[20:23], s92 offen offset:0 sc1\n"
    "v_mfma_f32_16x16x32_bf16 a[80:83], v[24:27], a[160:163], a[80:83]\n"
    "v_mfma_f32_16x16x32_bf16 a[84:87], v[20:23], v[84:87], a[84:87]\n"
    "v_mfma_f32_16x16x32_bf16 a[84:87], v[24:27], a[164:167], a[84:87]\n"
    "v_mfma_f32_16x16x32_bf16 a[88:91], v[20:23], v[88:91], a[88:91]\n"
    "ds_read_b64_tr_b16 v[36:37], v60 offset:224\n"
    "v_mfma_f32_16x16x32_bf16 a[88:91], v[24:27], a[168:171], a[88:91]\n"
    "ds_read_b64_tr_b16 v[38:39], v60 offset:4832\n"
    "v_mfma_f32_16x16x32_bf16 a[92:95], v[20:23], v[92:95], a[92:95]\n"
    "ds_read_b64_tr_b16 v[48:49], v60 offset:9440\n"
    "v_mfma_f32_16x16x32_bf16 a[92:95], v[24:27], a[172:175], a[92:95]\n"
    "s_waitcnt lgkmcnt(5)\n"
    "ds_read_b64_tr_b16 v[50:51], v60 offset:14048\n"
    "v_mfma_f32_16x16x32_bf16 a[96:99], v[28:31], v[80:83], a[96:99]\n"
    "s_waitcnt lgkmcnt(4)\n"
    "buffer_load_dwordx4 v4, s[40:43], s91 offen offset:2048 lds\n"
    "v_mfma_f32_16x16x32_bf16 a[96:99], v[32:35], a[160:163], a[96:99]\n"
    "v_mfma_f32_16x16x32_bf16 a[100:103], v[28:31], v[84:87], a[100:103]\n"
    "s_waitcnt vmcnt(12)\n"
    "v_subrev_u32 v14, s38, v14\n"
    "v_xor_b32 v15, v47, v15\n"
    "v_cmp_lt_u64 vcc, 15, v[14:15]\n"
    "s_nop 0\n"
    "s_cbranch_vccnz L_SP4_%=\n"
    "L_BK4_%=:\n"
    "v_mfma_f32_16x16x32_bf16 a[100:103], v[32:35], a[164:167], a[100:103]\n"
    "buffer_load_dwordx4 v5, s[40:43], s91 offen offset:3072 lds\n"
    "v_mfma_f32_16x16x32_bf16 a[104:107], v[28:31], v[88:91], a[104:107]\n"
    "ds_read_b64_tr_b16 v[52:53], v60 offset:256\n"
    "v_mfma_f32_16x16x32_bf16 a[104:107], v[32:35], a[168:171], a[104:107]\n"
    "ds_read_b64_tr_b16 v[54:55], v60 offset:4864\n"
    "v_mfma_f32_16x16x32_bf16 a[108:111], v[28:31], v[92:95], a[108:111]\n"
    "ds_read_b64_tr_b16 v[56:57], v60 offset:9472\n"
    "v_mfma_f32_16x16x32_bf16 a[108:111], v[32:35], a[172:175], a[108:111]\n"
    "s_waitcnt lgkmcnt(5)\n"
    "s_xor_b32 s92, s19, 73728\n"
    "ds_read_b64_tr_b16 v[58:59], v60 offset:14080\n"
    "v_mfma_f32_16x16x32_bf16 a[112:115], v[36:39], v[80:83], a[112:115]\n"
    "s_waitcnt lgkmcnt(4)\n"
    "buffer_load_dwordx4 v[188:191], v0, s[20:23], s92 offen offset:3072 sc1\n"
    "v_mfma_f32_16x16x32_bf16 a[112:115], v[48:51], a[160:163], a[112:115]\n"
    "v_mfma_f32_16x16x32_bf16 a[116:119], v[36:39], v[84:87], a[116:119]\n"
    "v_mfma_f32_16x16x32_bf16 a[116:119], v[48:51], a[164:167], a[116:119]\n"
    "v_mfma_f32_16x16x32_bf16 a[120:123], v[36:39], v[88:91], a[120:123]\n"
    "ds_read_b64_tr_b16 v[16:17], v60 offset:18432\n"
    "v_mfma_f32_16x16x32_bf16 a[120:123], v[48:51], a[168:171], a[120:123]\n"
    "ds_read_b64_tr_b16 v[18:19], v60 offset:23040\n"
    "v_mfma_f32_16x16x32_bf16 a[124:127], v[36:39], v[92:95], a[124:127]\n"
    "ds_read_b64_tr_b16 v[20:21], v60 offset:27648\n"
    "v_mfma_f32_16x16x32_bf16 a[124:127], v[48:51], a[172:175], a[124:127]\n"
    "s_waitcnt lgkmcnt(5)\n"
    "ds_read_b64_tr_b16 v[22:23], v60 offset:32256\n"
    "v_mfma_f32_16x16x32_bf16 a[128:131], v[52:55], v[80:83], a[128:131]\n"
    "s_waitcnt lgkmcnt(4)\n"
    "buffer_load_dwordx4 v[184:187], v0, s[20:23], s92 offen offset:2048 sc1\n"
    "v_mfma_f32_16x16x32_bf16 a[128:131], v[56:59], a[160:163], a[128:131]\n"
    "v_mfma_f32_16x16x32_bf16 a[132:135], v[52:55], v[84:87], a[132:135]\n"
    "buffer_load_dwordx4 v[180:183], v0, s[20:23], s92 offen offset:1024 sc1\n"
    "v_mfma_f32_16x16x32_bf16 a[132:135], v[56:59], a[164:167], a[132:135]\n"
    "v_mfma_f32_16x16x32_bf16 a[136:139], v[52:55], v[88:91], a[136:139]\n"
    "ds_read_b64_tr_b16 v[24:25], v60 offset:18464\n"
    "v_mfma_f32_16x16x32_bf16 a[136:139], v[56:59], a[168:171], a[136:139]\n"
    "ds_read_b64_tr_b16 v[26:27], v60 offset:23072\n"
    "v_mfma_f32_16x16x32_bf16 a[140:143], v[52:55], v[92:95], a[140:143]\n"
    "ds_read_b64_tr_b16 v[28:29], v60 offset:27680\n"
    "v_mfma_f32_16x16x32_bf16 a[140:143], v[56:59], a[172:175], a[140:143]\n"
    "s_waitcnt lgkmcnt(5)\n"
    "ds_read_b64_tr_b16 v[30:31], v60 offset:32288\n"
    "v_mfma_f32_16x16x32_bf16 a[0:3], v[16:19], a[176:179], a[0:3]\n"
    "s_waitcnt vmcnt(13) lgkmcnt(4)\n"
    "buffer_load_dwordx4 v[176:179], v0, s[20:23], s92 offen offset:0 sc1\n"
    "v_mfma_f32_16x16x32_bf16 a[0:3], v[20:23], v[128:131], a[0:3]\n"
    "v_mfma_f32_16x16x32_bf16 a[4:7], v[16:19], a[180:183], a[4:7]\n"
    "v_mfma_f32_16x16x32_bf16 a[4:7], v[20:23], v[132:135], a[4:7]\n"
    "v_mfma_f32_16x16x32_bf16 a[8:11], v[16:19], a[184:187], a[8:11]\n"
    "ds_read_b64_tr_b16 v[32:33], v60 offset:18496\n"
    "v_mfma_f32_16x16x32_bf16 a[8:11], v[20:23], v[136:139], a[8:11]\n"
    "ds_read_b64_tr_b16 v[34:35], v60 offset:23104\n"
    "v_mfma_f32_16x16x32_bf16 a[12:15], v[16:19], a[188:191], a[12:15]\n"
    "ds_read_b64_tr_b16 v[36:37], v60 offset:27712\n"
    "v_mfma_f32_16x16x32_bf16 a[12:15], v[20:23], v[140:143], a[12:15]\n"
    "s_waitcnt lgkmcnt(5)\n"
    "s_xor_b32 s92, s19, 77824\n"
    "ds_read_b64_tr_b16 v[38:39], v60 offset:32320\n"
    "v_mfma_f32_16x16x32_bf16 a[16:19], v[24:27], a[176:179], a[16:19]\n"
    "s_waitcnt lgkmcnt(4)\n"
    "buffer_load_dwordx4 v[204:207], v0, s[20:23], s92 offen offset:3072 sc1\n"
    "v_mfma_f32_16x16x32_bf16 a[16:19], v[28:31], v[128:131], a[16:19]\n"
    "v_mfma_f32_16x16x32_bf16 a[20:23], v[24:27], a[180:183], a[20:23]\n"
    "buffer_load_dwordx4 v[200:203], v0, s[20:23], s92 offen offset:2048 sc1\n"
    "v_mfma_f32_16x16x32_bf16 a[20:23], v[28:31], v[132:135], a[20:23]\n"
    "v_mfma_f32_16x16x32_bf16 a[24:27], v[24:27], a[184:187], a[24:27]\n"
    "ds_read_b64_tr_b16 v[48:49], v60 offset:18528\n"
    "v_mfma_f32_16x16x32_bf16 a[24:27], v[28:31], v[136:139], a[24:27]\n"
    "ds_read_b64_tr_b16 v[50:51], v60 offset:23136\n"
    "v_mfma_f32_16x16x32_bf16 a[28:31], v[24:27], a[188:191], a[28:31]\n"
    "ds_read_b64_tr_b16 v[52:53], v60 offset:27744\n"
    "v_mfma_f32_16x16x32_bf16 a[28:31], v[28:31], v[140:143], a[28:31]\n"
    "s_waitcnt lgkmcnt(5)\n"
    "ds_read_b64_tr_b16 v[54:55], v60 offset:32352\n"
    "v_mfma_f32_16x16x32_bf16 a[32:35], v[32:35], a[176:179], a[32:35]\n"
    "s_waitcnt lgkmcnt(4)\n"
    "buffer_load_dwordx4 v[196:199], v0, s[20:23], s92 offen offset:1024 sc1\n"
    "v_mfma_f32_16x16x32_bf16 a[32:35], v[36:39], v[128:131], a[32:35]\n"
    "v_mfma_f32_16x16x32_bf16 a[36:39], v[32:35], a[180:183], a[36:39]\n"
    "v_mfma_f32_16x16x32_bf16 a[36:39], v[36:39], v[132:135], a[36:39]\n"
    "v_mfma_f32_16x16x32_bf16 a[40:43], v[32:35], a[184:187], a[40:43]\n"
    "ds_read_b64_tr_b16 v[56:57], v60 offset:18560\n"
    "v_mfma_f32_16x16x32_bf16 a[40:43], v[36:39], v[136:139], a[40:43]\n"
    "ds_read_b64_tr_b16 v[58:59], v60 offset:23168\n"
    "v_mfma_f32_16x16x32_bf16 a[44:47], v[32:35], a[188:191], a[44:47]\n"
    "ds_read_b64_tr_b16 v[16:17], v60 offset:27776\n"
    "v_mfma_f32_16x16x32_bf16 a[44:47], v[36:39], v[140:143], a[44:47]\n"
    "s_waitcnt lgkmcnt(5)\n"
    "ds_read_b64_tr_b16 v[18:19], v60 offset:32384\n"
    "v_mfma_f32_16x16x32_bf16 a[48:51], v[48:51], a[176:179], a[48:51]\n"
    "s_waitcnt lgkmcnt(4)\n"
    "buffer_load_dwordx4 v[192:195], v0, s[20:23], s92 offen offset:0 sc1\n"
    "v_mfma_f32_16x16x32_bf16 a[48:51], v[52:55], v[128:131], a[48:51]\n"
    "s_add_u32 m0, s17, 131072\n"
    "v_mfma_f32_16x16x32_bf16 a[52:55], v[48:51], a[180:183], a[52:55]\n"
    "buffer_load_dwordx4 v6, s[40:43], s91 offen lds\n"
    "v_mfma_f32_16x16x32_bf16 a[52:55], v[52:55], v[132:135], a[52:55]\n"
    "v_mfma_f32_16x16x32_bf16 a[56:59], v[48:51], a[184:187], a[56:59]\n"
    "ds_read_b64_tr_b16 v[20:21], v60 offset:18592\n"
    "v_mfma_f32_16x16x32_bf16 a[56:59], v[52:55], v[136:139], a[56:59]\n"
    "ds_read_b64_tr_b16 v[22:23], v60 offset:23200\n"
    "v_mfma_f32_16x16x32_bf16 a[60:63], v[48:51], a[188:191], a[60:63]\n"
    "ds_read_b64_tr_b16 v[24:25], v60 offset:27808\n"
    "v_mfma_f32_16x16x32_bf16 a[60:63], v[52:55], v[140:143], a[60:63]\n"
    "s_waitcnt lgkmcnt(5)\n"
    "ds_read_b64_tr_b16 v[26:27], v60 offset:32416\n"
    "v_mfma_f32_16x16x32_bf16 a[64:67], v[56:59], a[176:179], a[64:67]\n"
    "s_waitcnt lgkmcnt(4)\n"
    "buffer_load_dwordx4 v7, s[40:43], s91 offen offset:1024 lds\n"
    "v_mfma_f32_16x16x32_bf16 a[64:67], v[16:19], v[128:131], a[64:67]\n"
    "v_mfma_f32_16x16x32_bf16 a[68:71], v[56:59], a[180:183], a[68:71]\n"
    "v_mfma_f32_16x16x32_bf16 a[68:71], v[16:19], v[132:135], a[68:71]\n"
    "v_mfma_f32_16x16x32_bf16 a[72:75], v[56:59], a[184:187], a[72:75]\n"
    "ds_read_b64_tr_b16 v[28:29], v60 offset:18624\n"
    "v_mfma_f32_16x16x32_bf16 a[72:75], v[16:19], v[136:139], a[72:75]\n"
    "ds_read_b64_tr_b16 v[30:31], v60 offset:23232\n"
    "v_mfma_f32_16x16x32_bf16 a[76:79], v[56:59], a[188:191], a[76:79]\n"
    "ds_read_b64_tr_b16 v[32:33], v60 offset:27840\n"
    "v_mfma_f32_16x16x32_bf16 a[76:79], v[16:19], v[140:143], a[76:79]\n"
    "s_waitcnt lgkmcnt(5)\n"
    "ds_read_b64_tr_b16 v[34:35], v60 offset:32448\n"
    "v_mfma_f32_16x16x32_bf16 a[80:83], v[20:23], a[176:179], a[80:83]\n"
    "s_waitcnt lgkmcnt(4)\n"
    "buffer_load_dwordx4 v8, s[40:43], s91 offen offset:2048 lds\n"
    "v_mfma_f32_16x16x32_bf16 a[80:83], v[24:27], v[128:131], a[80:83]\n"
    "v_mfma_f32_16x16x32_bf16 a[84:87], v[20:23], a[180:183], a[84:87]\n"
    "buffer_load_dwordx4 v9, s[40:43], s91 offen offset:3072 lds\n"
    "v_mfma_f32_16x16x32_bf16 a[84:87], v[24:27], v[132:135], a[84:87]\n"
    "v_mfma_f32_16x16x32_bf16 a[88:91], v[20:23], a[184:187], a[88:91]\n"
    "ds_read_b64_tr_b16 v[36:37], v60 offset:18656\n"
    "v_mfma_f32_16x16x32_bf16 a[88:91], v[24:27], v[136:139], a[88:91]\n"
    "ds_read_b64_tr_b16 v[38:39], v60 offset:23264\n"
    "v_mfma_f32_16x16x32_bf16 a[92:95], v[20:23], a[188:191], a[92:95]\n"
    "ds_read_b64_tr_b16 v[48:49], v60 offset:27872\n"
    "v_mfma_f32_16x16x32_bf16 a[92:95], v[24:27], v[140:143], a[92:95]\n"
    "s_waitcnt lgkmcnt(5)\n"
    "s_add_u32 m0, s17, 135168\n"
    "ds_read_b64_tr_b16 v[50:51], v60 offset:32480\n"
    "v_mfma_f32_16x16x32_bf16 a[96:99], v[28:31], a[176:179], a[96:99]\n"
    "s_waitcnt lgkmcnt(4)\n"
    "buffer_load_dwordx4 v10, s[40:43], s91 offen lds\n"
    "v_mfma_f32_16x16x32_bf16 a[96:99], v[32:35], v[128:131], a[96:99]\n"
    "v_mfma_f32_16x16x32_bf16 a[100:103], v[28:31], a[180:183], a[100:103]\n"
    "v_mfma_f32_16x16x32_bf16 a[100:103], v[32:35], v[132:135], a[100:103]\n"
    "v_mfma_f32_16x16x32_bf16 a[104:107], v[28:31], a[184:187], a[104:107]\n"
    "ds_read_b64_tr_b16 v[52:53], v60 offset:18688\n"
    "v_mfma_f32_16x16x32_bf16 a[104:107], v[32:35], v[136:139], a[104:107]\n"
    "ds_read_b64_tr_b16 v[54:55], v60 offset:23296\n"
    "v_mfma_f32_16x16x32_bf16 a[108:111], v[28:31], a[188:191], a[108:111]\n"
    "ds_read_b64_tr_b16 v[56:57], v60 offset:27904\n"
    "v_mfma_f32_16x16x32_bf16 a[108:111], v[32:35], v[140:143], a[108:111]\n"
    "s_waitcnt lgkmcnt(5)\n"
    "s_xor_b32 s92, s19, 90112\n"
    "ds_read_b64_tr_b16 v[58:59], v60 offset:32512\n"
    "v_mfma_f32_16x16x32_bf16 a[112:115], v[36:39], a[176:179], a[112:115]\n"
    "s_waitcnt lgkmcnt(4)\n"
    "buffer_load_dwordx4 v[220:223], v0, s[20:23], s92 offen offset:3072 sc1\n"
    "v_mfma_f32_16x16x32_bf16 a[112:115], v[48:51], v[128:131], a[112:115]\n"
    "v_mfma_f32_16x16x32_bf16 a[116:119], v[36:39], a[180:183], a[116:119]\n"
    "buffer_load_dwordx4 v[216:219], v0, s[20:23], s92 offen offset:2048 sc1\n"
    "v_mfma_f32_16x16x32_bf16 a[116:119], v[48:51], v[132:135], a[116:119]\n"
    "v_mfma_f32_16x16x32_bf16 a[120:123], v[36:39], a[184:187], a[120:123]\n"
    "v_mfma_f32_16x16x32_bf16 a[120:123], v[48:51], v[136:139], a[120:123]\n"
    "v_mfma_f32_16x16x32_bf16 a[124:127], v[36:39], a[188:191], a[124:127]\n"
    "s_barrier\n"
    "ds_read_b64_tr_b16 v[16:17], v61 offset:9216\n"
    "v_mfma_f32_16x16x32_bf16 a[124:127], v[48:51], v[140:143], a[124:127]\n"
    "s_waitcnt lgkmcnt(3)\n"
    "ds_read_b64_tr_b16 v[18:19], v61 offset:13824\n"
    "v_mfma_f32_16x16x32_bf16 a[128:131], v[52:55], a[176:179], a[128:131]\n"
    "s_waitcnt lgkmcnt(2)\n"
    "ds_read_b64_tr_b16 v[20:21], v61 offset:18432\n"
    "v_mfma_f32_16x16x32_bf16 a[128:131], v[56:59], v[128:131], a[128:131]\n"
    "ds_read_b64_tr_b16 v[22:23], v61 offset:23040\n"
    "v_mfma_f32_16x16x32_bf16 a[132:135], v[52:55], a[180:183], a[132:135]\n"
    "buffer_load_dwordx4 v[212:215], v0, s[20:23], s92 offen offset:1024 sc1\n"
    "v_mfma_f32_16x16x32_bf16 a[132:135], v[56:59], v[132:135], a[132:135]\n"
    "v_mfma_f32_16x16x32_bf16 a[136:139], v[52:55], a[184:187], a[136:139]\n"
    "ds_read_b64_tr_b16 v[24:25], v61 offset:9248\n"
    "v_mfma_f32_16x16x32_bf16 a[136:139], v[56:59], v[136:139], a[136:139]\n"
    "ds_read_b64_tr_b16 v[26:27], v61 offset:13856\n"
    "v_mfma_f32_16x16x32_bf16 a[140:143], v[52:55], a[188:191], a[140:143]\n"
    "ds_read_b64_tr_b16 v[28:29], v61 offset:18464\n"
    "v_mfma_f32_16x16x32_bf16 a[140:143], v[56:59], v[140:143], a[140:143]\n"
    "s_waitcnt vmcnt(22) lgkmcnt(5)\n"
    "ds_read_b64_tr_b16 v[30:31], v61 offset:23072\n"
    "v_mfma_f32_16x16x32_bf16 a[0:3], v[16:19], v[144:147], a[0:3]\n"
    "s_waitcnt vmcnt(18) lgkmcnt(4)\n"
    "buffer_load_dwordx4 v[208:211], v0, s[20:23], s92 offen offset:0 sc1\n"
    "v_mfma_f32_16x16x32_bf16 a[0:3], v[20:23], v[160:163], a[0:3]\n"
    "s_xor_b32 s92, s19, 94208\n"
    "v_mfma_f32_16x16x32_bf16 a[4:7], v[16:19], v[148:151], a[4:7]\n"
    "buffer_load_dwordx4 v[236:239], v0, s[20:23], s92 offen offset:3072 sc1\n"
    "v_mfma_f32_16x16x32_bf16 a[4:7], v[20:23], v[164:167], a[4:7]\n"
    "v_mfma_f32_16x16x32_bf16 a[8:11], v[16:19], v[152:155], a[8:11]\n"
    "ds_read_b64_tr_b16 v[32:33], v61 offset:9280\n"
    "v_mfma_f32_16x16x32_bf16 a[8:11], v[20:23], v[168:171], a[8:11]\n"
    "ds_read_b64_tr_b16 v[34:35], v61 offset:13888\n"
    "v_mfma_f32_16x16x32_bf16 a[12:15], v[16:19], v[156:159], a[12:15]\n"
    "ds_read_b64_tr_b16 v[36:37], v61 offset:18496\n"
    "v_mfma_f32_16x16x32_bf16 a[12:15], v[20:23], v[172:175], a[12:15]\n"
    "s_waitcnt lgkmcnt(5)\n"
    "ds_read_b64_tr_b16 v[38:39], v61 offset:23104\n"
    "v_mfma_f32_16x16x32_bf16 a[16:19], v[24:27], v[144:147], a[16:19]\n"
    "s_waitcnt lgkmcnt(4)\n"
    "buffer_load_dwordx4 v[232:235], v0, s[20:23], s92 offen offset:2048 sc1\n"
    "v_mfma_f32_16x16x32_bf16 a[16:19], v[28:31], v[160:163], a[16:19]\n"
    "v_mfma_f32_16x16x32_bf16 a[20:23], v[24:27], v[148:151], a[20:23]\n"
    "v_mfma_f32_16x16x32_bf16 a[20:23], v[28:31], v[164:167], a[20:23]\n"
    "v_mfma_f32_16x16x32_bf16 a[24:27], v[24:27], v[152:155], a[24:27]\n"
    "ds_read_b64_tr_b16 v[48:49], v61 offset:9312\n"
    "v_mfma_f32_16x16x32_bf16 a[24:27], v[28:31], v[168:171], a[24:27]\n"
    "ds_read_b64_tr_b16 v[50:51], v61 offset:13920\n"
    "v_mfma_f32_16x16x32_bf16 a[28:31], v[24:27], v[156:159], a[28:31]\n"
    "ds_read_b64_tr_b16 v[52:53], v61 offset:18528\n"
    "v_mfma_f32_16x16x32_bf16 a[28:31], v[28:31], v[172:175], a[28:31]\n"
    "s_waitcnt lgkmcnt(5)\n"
    "ds_read_b64_tr_b16 v[54:55], v61 offset:23136\n"
    "v_mfma_f32_16x16x32_bf16 a[32:35], v[32:35], v[144:147], a[32:35]\n"
    "s_waitcnt lgkmcnt(4)\n"
    "buffer_load_dwordx4 v[228:231], v0, s[20:23], s92 offen offset:1024 sc1\n"
    "v_mfma_f32_16x16x32_bf16 a[32:35], v[36:39], v[160:163], a[32:35]\n"
    "v_mfma_f32_16x16x32_bf16 a[36:39], v[32:35], v[148:151], a[36:39]\n"
    "buffer_load_dwordx4 v[224:227], v0, s[20:23], s92 offen offset:0 sc1\n"
    "v_mfma_f32_16x16x32_bf16 a[36:39], v[36:39], v[164:167], a[36:39]\n"
    "v_mfma_f32_16x16x32_bf16 a[40:43], v[32:35], v[152:155], a[40:43]\n"
    "ds_read_b64_tr_b16 v[56:57], v61 offset:9344\n"
    "v_mfma_f32_16x16x32_bf16 a[40:43], v[36:39], v[168:171], a[40:43]\n"
    "ds_read_b64_tr_b16 v[58:59], v61 offset:13952\n"
    "v_mfma_f32_16x16x32_bf16 a[44:47], v[32:35], v[156:159], a[44:47]\n"
    "ds_read_b64_tr_b16 v[16:17], v61 offset:18560\n"
    "v_mfma_f32_16x16x32_bf16 a[44:47], v[36:39], v[172:175], a[44:47]\n"
    "s_waitcnt lgkmcnt(5)\n"
    "s_xor_b32 s92, s19, 106496\n"
    "ds_read_b64_tr_b16 v[18:19], v61 offset:23168\n"
    "v_mfma_f32_16x16x32_bf16 a[48:51], v[48:51], v[144:147], a[48:51]\n"
    "s_waitcnt lgkmcnt(4)\n"
    "buffer_load_dwordx4 v[252:255], v0, s[20:23], s92 offen offset:3072 sc1\n"
    "v_mfma_f32_16x16x32_bf16 a[48:51], v[52:55], v[160:163], a[48:51]\n"
    "v_mfma_f32_16x16x32_bf16 a[52:55], v[48:51], v[148:151], a[52:55]\n"
    "v_mfma_f32_16x16x32_bf16 a[52:55], v[52:55], v[164:167], a[52:55]\n"
    "v_mfma_f32_16x16x32_bf16 a[56:59], v[48:51], v[152:155], a[56:59]\n"
    "ds_read_b64_tr_b16 v[20:21], v61 offset:9376\n"
    "v_mfma_f32_16x16x32_bf16 a[56:59], v[52:55], v[168:171], a[56:59]\n"
    "ds_read_b64_tr_b16 v[22:23], v61 offset:13984\n"
    "v_mfma_f32_16x16x32_bf16 a[60:63], v[48:51], v[156:159], a[60:63]\n"
    "ds_read_b64_tr_b16 v[24:25], v61 offset:18592\n"
    "v_mfma_f32_16x16x32_bf16 a[60:63], v[52:55], v[172:175], a[60:63]\n"
    "s_waitcnt lgkmcnt(5)\n"
    "ds_read_b64_tr_b16 v[26:27], v61 offset:23200\n"
    "v_mfma_f32_16x16x32_bf16 a[64:67], v[56:59], v[144:147], a[64:67]\n"
    "s_waitcnt lgkmcnt(4)\n"
    "buffer_load_dwordx4 v[248:251], v0, s[20:23], s92 offen offset:2048 sc1\n"
    "v_mfma_f32_16x16x32_bf16 a[64:67], v[16:19], v[160:163], a[64:67]\n"
    "v_mfma_f32_16x16x32_bf16 a[68:71], v[56:59], v[148:151], a[68:71]\n"
    "buffer_load_dwordx4 v[244:247], v0, s[20:23], s92 offen offset:1024 sc1\n"
    "v_mfma_f32_16x16x32_bf16 a[68:71], v[16:19], v[164:167], a[68:71]\n"
    "v_mfma_f32_16x16x32_bf16 a[72:75], v[56:59], v[152:155], a[72:75]\n"
    "ds_read_b64_tr_b16 v[28:29], v61 offset:9408\n"
    "v_mfma_f32_16x16x32_bf16 a[72:75], v[16:19], v[168:171], a[72:75]\n"
    "ds_read_b64_tr_b16 v[30:31], v61 offset:14016\n"
    "v_mfma_f32_16x16x32_bf16 a[76:79], v[56:59], v[156:159], a[76:79]\n"
    "ds_read_b64_tr_b16 v[32:33], v61 offset:18624\n"
    "v_mfma_f32_16x16x32_bf16 a[76:79], v[16:19], v[172:175], a[76:79]\n"
    "s_waitcnt lgkmcnt(5)\n"
    "ds_read_b64_tr_b16 v[34:35], v61 offset:23232\n"
    "v_mfma_f32_16x16x32_bf16 a[80:83], v[20:23], v[144:147], a[80:83]\n"
    "s_waitcnt lgkmcnt(4)\n"
    "buffer_load_dwordx4 v[240:243], v0, s[20:23], s92 offen offset:0 sc1\n"
    "v_mfma_f32_16x16x32_bf16 a[80:83], v[24:27], v[160:163], a[80:83]\n"
    "v_mfma_f32_16x16x32_bf16 a[84:87], v[20:23], v[148:151], a[84:87]\n"
    "v_mfma_f32_16x16x32_bf16 a[84:87], v[24:27], v[164:167], a[84:87]\n"
    "v_mfma_f32_16x16x32_bf16 a[88:91], v[20:23], v[152:155], a[88:91]\n"
    "ds_read_b64_tr_b16 v[36:37], v61 offset:9440\n"
    "v_mfma_f32_16x16x32_bf16 a[88:91], v[24:27], v[168:171], a[88:91]\n"
    "ds_read_b64_tr_b16 v[38:39], v61 offset:14048\n"
    "v_mfma_f32_16x16x32_bf16 a[92:95], v[20:23], v[156:159], a[92:95]\n"
    "ds_read_b64_tr_b16 v[48:49], v61 offset:18656\n"
    "v_mfma_f32_16x16x32_bf16 a[92:95], v[24:27], v[172:175], a[92:95]\n"
    "s_waitcnt lgkmcnt(5)\n"
    "s_xor_b32 s92, s19, 110592\n"
    "ds_read_b64_tr_b16 v[50:51], v61 offset:23264\n"
    "v_mfma_f32_16x16x32_bf16 a[96:99], v[28:31], v[144:147], a[96:99]\n"
    "s_waitcnt lgkmcnt(4)\n"
    "buffer_load_dwordx4 a[204:207], v0, s[20:23], s92 offen offset:3072 sc1\n"
    "v_mfma_f32_16x16x32_bf16 a[96:99], v[32:35], v[160:163], a[96:99]\n"
    "v_mfma_f32_16x16x32_bf16 a[100:103], v[28:31], v[148:151], a[100:103]\n"
    "buffer_load_dwordx4 a[200:203], v0, s[20:23], s92 offen offset:2048 sc1\n"
    "v_mfma_f32_16x16x32_bf16 a[100:103], v[32:35], v[164:167], a[100:103]\n"
    "v_mfma_f32_16x16x32_bf16 a[104:107], v[28:31], v[152:155], a[104:107]\n"
    "ds_read_b64_tr_b16 v[52:53], v61 offset:9472\n"
    "v_mfma_f32_16x16x32_bf16 a[104:107], v[32:35], v[168:171], a[104:107]\n"
    "ds_read_b64_tr_b16 v[54:55], v61 offset:14080\n"
    "v_mfma_f32_16x16x32_bf16 a[108:111], v[28:31], v[156:159], a[108:111]\n"
    "ds_read_b64_tr_b16 v[56:57], v61 offset:18688\n"
    "v_mfma_f32_16x16x32_bf16 a[108:111], v[32:35], v[172:175], a[108:111]\n"
    "s_waitcnt lgkmcnt(5)\n"
    "ds_read_b64_tr_b16 v[58:59], v61 offset:23296\n"
    "v_mfma_f32_16x16x32_bf16 a[112:115], v[36:39], v[144:147], a[112:115]\n"
    "s_waitcnt lgkmcnt(4)\n"
    "buffer_load_dwordx4 a[196:199], v0, s[20:23], s92 offen offset:1024 sc1\n"
    "v_mfma_f32_16x16x32_bf16 a[112:115], v[48:51], v[160:163], a[112:115]\n"
    "v_mfma_f32_16x16x32_bf16 a[116:119], v[36:39], v[148:151], a[116:119]\n"
    "v_mfma_f32_16x16x32_bf16 a[116:119], v[48:51], v[164:167], a[116:119]\n"
    "v_mfma_f32_16x16x32_bf16 a[120:123], v[36:39], v[152:155], a[120:123]\n"
    "ds_read_b64_tr_b16 v[16:17], v61 offset:27648\n"
    "v_mfma_f32_16x16x32_bf16 a[120:123], v[48:51], v[168:171], a[120:123]\n"
    "ds_read_b64_tr_b16 v[18:19], v61 offset:32256\n"
    "v_mfma_f32_16x16x32_bf16 a[124:127], v[36:39], v[156:159], a[124:127]\n"
    "s_mov_b32 s91, 117841694\n"
    "ds_read_b64_tr_b16 v[20:21], v61 offset:36864\n"
    "v_mfma_f32_16x16x32_bf16 a[124:127], v[48:51], v[172:175], a[124:127]\n"
    "s_waitcnt lgkmcnt(5)\n"
    "s_lshr_b32 s91, s91, s36\n"
    "s_xor_b32 s91, s91, s16\n"
    "s_and_b32 s91, s91, 31\n"
    "ds_read_b64_tr_b16 v[22:23], v61 offset:41472\n"
    "v_mfma_f32_16x16x32_bf16 a[128:131], v[52:55], v[144:147], a[128:131]\n"
    "s_waitcnt lgkmcnt(4)\n"
    "s_mul_i32 s91, s91, 36864\n"
    "buffer_load_dwordx4 a[192:195], v0, s[20:23], s92 offen offset:0 sc1\n"
    "v_mfma_f32_16x16x32_bf16 a[128:131], v[56:59], v[160:163], a[128:131]\n"
    "s_add_u32 m0, s17, 53248\n"
    "v_mfma_f32_16x16x32_bf16 a[132:135], v[52:55], v[148:151], a[132:135]\n"
    "buffer_load_dwordx4 v2, s[40:43], s91 offen lds\n"
    "v_mfma_f32_16x16x32_bf16 a[132:135], v[56:59], v[164:167], a[132:135]\n"
    "v_mfma_f32_16x16x32_bf16 a[136:139], v[52:55], v[152:155], a[136:139]\n"
    "ds_read_b64_tr_b16 v[24:25], v61 offset:27680\n"
    "v_mfma_f32_16x16x32_bf16 a[136:139], v[56:59], v[168:171], a[136:139]\n"
    "ds_read_b64_tr_b16 v[26:27], v61 offset:32288\n"
    "v_mfma_f32_16x16x32_bf16 a[140:143], v[52:55], v[156:159], a[140:143]\n"
    "ds_read_b64_tr_b16 v[28:29], v61 offset:36896\n"
    "v_mfma_f32_16x16x32_bf16 a[140:143], v[56:59], v[172:175], a[140:143]\n"
    "s_waitcnt vmcnt(26) lgkmcnt(5)\n"
    "ds_read_b64_tr_b16 v[30:31], v61 offset:41504\n"
    "v_mfma_f32_16x16x32_bf16 a[0:3], v[16:19], v[176:179], a[0:3]\n"
    "s_waitcnt vmcnt(22) lgkmcnt(4)\n"
    "buffer_load_dwordx4 v3, s[40:43], s91 offen offset:1024 lds\n"
    "v_mfma_f32_16x16x32_bf16 a[0:3], v[20:23], v[192:195], a[0:3]\n"
    "v_mfma_f32_16x16x32_bf16 a[4:7], v[16:19], v[180:183], a[4:7]\n"
    "v_mfma_f32_16x16x32_bf16 a[4:7], v[20:23], v[196:199], a[4:7]\n"
    "v_mfma_f32_16x16x32_bf16 a[8:11], v[16:19], v[184:187], a[8:11]\n"
    "ds_read_b64_tr_b16 v[32:33], v61 offset:27712\n"
    "v_mfma_f32_16x16x32_bf16 a[8:11], v[20:23], v[200:203], a[8:11]\n"
    "ds_read_b64_tr_b16 v[34:35], v61 offset:32320\n"
    "v_mfma_f32_16x16x32_bf16 a[12:15], v[16:19], v[188:191], a[12:15]\n"
    "ds_read_b64_tr_b16 v[36:37], v61 offset:36928\n"
    "v_mfma_f32_16x16x32_bf16 a[12:15], v[20:23], v[204:207], a[12:15]\n"
    "s_waitcnt lgkmcnt(5)\n"
    "ds_read_b64_tr_b16 v[38:39], v61 offset:41536\n"
    "v_mfma_f32_16x16x32_bf16 a[16:19], v[24:27], v[176:179], a[16:19]\n"
    "s_waitcnt lgkmcnt(4)\n"
    "buffer_load_dwordx4 v4, s[40:43], s91 offen offset:2048 lds\n"
    "v_mfma_f32_16x16x32_bf16 a[16:19], v[28:31], v[192:195], a[16:19]\n"
    "v_mfma_f32_16x16x32_bf16 a[20:23], v[24:27], v[180:183], a[20:23]\n"
    "buffer_load_dwordx4 v5, s[40:43], s91 offen offset:3072 lds\n"
    "v_mfma_f32_16x16x32_bf16 a[20:23], v[28:31], v[196:199], a[20:23]\n"
    "v_mfma_f32_16x16x32_bf16 a[24:27], v[24:27], v[184:187], a[24:27]\n"
    "ds_read_b64_tr_b16 v[48:49], v61 offset:27744\n"
    "v_mfma_f32_16x16x32_bf16 a[24:27], v[28:31], v[200:203], a[24:27]\n"
    "ds_read_b64_tr_b16 v[50:51], v61 offset:32352\n"
    "v_mfma_f32_16x16x32_bf16 a[28:31], v[24:27], v[188:191], a[28:31]\n"
    "ds_read_b64_tr_b16 v[52:53], v61 offset:36960\n"
    "v_mfma_f32_16x16x32_bf16 a[28:31], v[28:31], v[204:207], a[28:31]\n"
    "s_waitcnt lgkmcnt(5)\n"
    "s_add_u32 m0, s17, 57344\n"
    "ds_read_b64_tr_b16 v[54:55], v61 offset:41568\n"
    "v_mfma_f32_16x16x32_bf16 a[32:35], v[32:35], v[176:179], a[32:35]\n"
    "s_waitcnt lgkmcnt(4)\n"
    "buffer_load_dwordx4 v6, s[40:43], s91 offen lds\n"
    "v_mfma_f32_16x16x32_bf16 a[32:35], v[36:39], v[192:195], a[32:35]\n"
    "v_mfma_f32_16x16x32_bf16 a[36:39], v[32:35], v[180:183], a[36:39]\n"
    "v_mfma_f32_16x16x32_bf16 a[36:39], v[36:39], v[196:199], a[36:39]\n"
    "v_mfma_f32_16x16x32_bf16 a[40:43], v[32:35], v[184:187], a[40:43]\n"
    "ds_read_b64_tr_b16 v[56:57], v61 offset:27776\n"
    "v_mfma_f32_16x16x32_bf16 a[40:43], v[36:39], v[200:203], a[40:43]\n"
    "ds_read_b64_tr_b16 v[58:59], v61 offset:32384\n"
    "v_mfma_f32_16x16x32_bf16 a[44:47], v[32:35], v[188:191], a[44:47]\n"
    "ds_read_b64_tr_b16 v[16:17], v61 offset:36992\n"
    "v_mfma_f32_16x16x32_bf16 a[44:47], v[36:39], v[204:207], a[44:47]\n"
    "s_waitcnt lgkmcnt(5)\n"
    "ds_read_b64_tr_b16 v[18:19], v61 offset:41600\n"
    "v_mfma_f32_16x16x32_bf16 a[48:51], v[48:51], v[176:179], a[48:51]\n"
    "s_waitcnt lgkmcnt(4)\n"
    "buffer_load_dwordx4 v7, s[40:43], s91 offen offset:1024 lds\n"
    "v_mfma_f32_16x16x32_bf16 a[48:51], v[52:55], v[192:195], a[48:51]\n"
    "v_mfma_f32_16x16x32_bf16 a[52:55], v[48:51], v[180:183], a[52:55]\n"
    "buffer_load_dwordx4 v8, s[40:43], s91 offen offset:2048 lds\n"
    "v_mfma_f32_16x16x32_bf16 a[52:55], v[52:55], v[196:199], a[52:55]\n"
    "v_mfma_f32_16x16x32_bf16 a[56:59], v[48:51], v[184:187], a[56:59]\n"
    "ds_read_b64_tr_b16 v[20:21], v61 offset:27808\n"
    "v_mfma_f32_16x16x32_bf16 a[56:59], v[52:55], v[200:203], a[56:59]\n"
    "ds_read_b64_tr_b16 v[22:23], v61 offset:32416\n"
    "v_mfma_f32_16x16x32_bf16 a[60:63], v[48:51], v[188:191], a[60:63]\n"
    "ds_read_b64_tr_b16 v[24:25], v61 offset:37024\n"
    "v_mfma_f32_16x16x32_bf16 a[60:63], v[52:55], v[204:207], a[60:63]\n"
    "s_waitcnt lgkmcnt(5)\n"
    "ds_read_b64_tr_b16 v[26:27], v61 offset:41632\n"
    "v_mfma_f32_16x16x32_bf16 a[64:67], v[56:59], v[176:179], a[64:67]\n"
    "s_waitcnt lgkmcnt(4)\n"
    "buffer_load_dwordx4 v9, s[40:43], s91 offen offset:3072 lds\n"
    "v_mfma_f32_16x16x32_bf16 a[64:67], v[16:19], v[192:195], a[64:67]\n"
    "v_mfma_f32_16x16x32_bf16 a[68:71], v[56:59], v[180:183], a[68:71]\n"
    "v_mfma_f32_16x16x32_bf16 a[68:71], v[16:19], v[196:199], a[68:71]\n"
    "v_mfma_f32_16x16x32_bf16 a[72:75], v[56:59], v[184:187], a[72:75]\n"
    "ds_read_b64_tr_b16 v[28:29], v61 offset:27840\n"
    "v_mfma_f32_16x16x32_bf16 a[72:75], v[16:19], v[200:203], a[72:75]\n"
    "ds_read_b64_tr_b16 v[30:31], v61 offset:32448\n"
    "v_mfma_f32_16x16x32_bf16 a[76:79], v[56:59], v[188:191], a[76:79]\n"
    "ds_read_b64_tr_b16 v[32:33], v61 offset:37056\n"
    "v_mfma_f32_16x16x32_bf16 a[76:79], v[16:19], v[204:207], a[76:79]\n"
    "s_waitcnt lgkmcnt(5)\n"
    "s_add_u32 m0, s17, 61440\n"
    "ds_read_b64_tr_b16 v[34:35], v61 offset:41664\n"
    "v_mfma_f32_16x16x32_bf16 a[80:83], v[20:23], v[176:179], a[80:83]\n"
    "s_waitcnt lgkmcnt(4)\n"
    "buffer_load_dwordx4 v10, s[40:43], s91 offen lds\n"
    "v_mfma_f32_16x16x32_bf16 a[80:83], v[24:27], v[192:195], a[80:83]\n"
    "s_xor_b32 s92, s19, 122880\n"
    "v_mfma_f32_16x16x32_bf16 a[84:87], v[20:23], v[180:183], a[84:87]\n"
    "buffer_load_dwordx4 a[220:223], v0, s[20:23], s92 offen offset:3072 sc1\n"
    "v_mfma_f32_16x16x32_bf16 a[84:87], v[24:27], v[196:199], a[84:87]\n"
    "v_mfma_f32_16x16x32_bf16 a[88:91], v[20:23], v[184:187], a[88:91]\n"
    "ds_read_b64_tr_b16 v[36:37], v61 offset:27872\n"
    "v_mfma_f32_16x16x32_bf16 a[88:91], v[24:27], v[200:203], a[88:91]\n"
    "ds_read_b64_tr_b16 v[38:39], v61 offset:32480\n"
    "v_mfma_f32_16x16x32_bf16 a[92:95], v[20:23], v[188:191], a[92:95]\n"
    "ds_read_b64_tr_b16 v[48:49], v61 offset:37088\n"
    "v_mfma_f32_16x16x32_bf16 a[92:95], v[24:27], v[204:207], a[92:95]\n"
    "s_waitcnt lgkmcnt(5)\n"
    "ds_read_b64_tr_b16 v[50:51], v61 offset:41696\n"
    "v_mfma_f32_16x16x32_bf16 a[96:99], v[28:31], v[176:179], a[96:99]\n"
    "s_waitcnt lgkmcnt(4)\n"
    "buffer_load_dwordx4 a[216:219], v0, s[20:23], s92 offen offset:2048 sc1\n"
    "v_mfma_f32_16x16x32_bf16 a[96:99], v[32:35], v[192:195], a[96:99]\n"
    "v_mfma_f32_16x16x32_bf16 a[100:103], v[28:31], v[180:183], a[100:103]\n"
    "v_mfma_f32_16x16x32_bf16 a[100:103], v[32:35], v[196:199], a[100:103]\n"
    "v_mfma_f32_16x16x32_bf16 a[104:107], v[28:31], v[184:187], a[104:107]\n"
    "ds_read_b64_tr_b16 v[52:53], v61 offset:27904\n"
    "v_mfma_f32_16x16x32_bf16 a[104:107], v[32:35], v[200:203], a[104:107]\n"
    "ds_read_b64_tr_b16 v[54:55], v61 offset:32512\n"
    "v_mfma_f32_16x16x32_bf16 a[108:111], v[28:31], v[188:191], a[108:111]\n"
    "ds_read_b64_tr_b16 v[56:57], v61 offset:37120\n"
    "v_mfma_f32_16x16x32_bf16 a[108:111], v[32:35], v[204:207], a[108:111]\n"
    "s_waitcnt lgkmcnt(5)\n"
    "ds_read_b64_tr_b16 v[58:59], v61 offset:41728\n"
    "v_mfma_f32_16x16x32_bf16 a[112:115], v[36:39], v[176:179], a[112:115]\n"
    "s_waitcnt lgkmcnt(4)\n"
    "buffer_load_dwordx4 a[212:215], v0, s[20:23], s92 offen offset:1024 sc1\n"
    "v_mfma_f32_16x16x32_bf16 a[112:115], v[48:51], v[192:195], a[112:115]\n"
    "v_mfma_f32_16x16x32_bf16 a[116:119], v[36:39], v[180:183], a[116:119]\n"
    "buffer_load_dwordx4 a[208:211], v0, s[20:23], s92 offen offset:0 sc1\n"
    "v_mfma_f32_16x16x32_bf16 a[116:119], v[48:51], v[196:199], a[116:119]\n"
    "v_mfma_f32_16x16x32_bf16 a[120:123], v[36:39], v[184:187], a[120:123]\n"
    "v_mfma_f32_16x16x32_bf16 a[120:123], v[48:51], v[200:203], a[120:123]\n"
    "v_mfma_f32_16x16x32_bf16 a[124:127], v[36:39], v[188:191], a[124:127]\n"
    "s_waitcnt vmcnt(29)\n"
    "s_barrier\n"
    "ds_read_b64_tr_b16 v[16:17], v61 offset:46080\n"
    "v_mfma_f32_16x16x32_bf16 a[124:127], v[48:51], v[204:207], a[124:127]\n"
    "s_waitcnt lgkmcnt(3)\n"
    "ds_read_b64_tr_b16 v[18:19], v61 offset:50688\n"
    "v_mfma_f32_16x16x32_bf16 a[128:131], v[52:55], v[176:179], a[128:131]\n"
    "s_waitcnt lgkmcnt(2)\n"
    "ds_read_b64_tr_b16 v[20:21], v61 offset:55296\n"
    "v_mfma_f32_16x16x32_bf16 a[128:131], v[56:59], v[192:195], a[128:131]\n"
    "s_xor_b32 s92, s19, 126976\n"
    "ds_read_b64_tr_b16 v[22:23], v61 offset:59904\n"
    "v_mfma_f32_16x16x32_bf16 a[132:135], v[52:55], v[180:183], a[132:135]\n"
    "buffer_load_dwordx4 a[236:239], v0, s[20:23], s92 offen offset:3072 sc1\n"
    "v_mfma_f32_16x16x32_bf16 a[132:135], v[56:59], v[196:199], a[132:135]\n"
    "v_mfma_f32_16x16x32_bf16 a[136:139], v[52:55], v[184:187], a[136:139]\n"
    "ds_read_b64_tr_b16 v[24:25], v61 offset:46112\n"
    "v_mfma_f32_16x16x32_bf16 a[136:139], v[56:59], v[200:203], a[136:139]\n"
    "ds_read_b64_tr_b16 v[26:27], v61 offset:50720\n"
    "v_mfma_f32_16x16x32_bf16 a[140:143], v[52:55], v[188:191], a[140:143]\n"
    "ds_read_b64_tr_b16 v[28:29], v61 offset:55328\n"
    "v_mfma_f32_16x16x32_bf16 a[140:143], v[56:59], v[204:207], a[140:143]\n"
    "s_waitcnt vmcnt(26) lgkmcnt(5)\n"
    "ds_read_b64_tr_b16 v[30:31], v61 offset:59936\n"
    "v_mfma_f32_16x16x32_bf16 a[0:3], v[16:19], v[208:211], a[0:3]\n"
    "s_waitcnt vmcnt(22) lgkmcnt(4)\n"
    "buffer_load_dwordx4 a[232:235], v0, s[20:23], s92 offen offset:2048 sc1\n"
    "v_mfma_f32_16x16x32_bf16 a[0:3], v[20:23], v[224:227], a[0:3]\n"
    "v_mfma_f32_16x16x32_bf16 a[4:7], v[16:19], v[212:215], a[4:7]\n"
    "buffer_load_dwordx4 a[228:231], v0, s[20:23], s92 offen offset:1024 sc1\n"
    "v_mfma_f32_16x16x32_bf16 a[4:7], v[20:23], v[228:231], a[4:7]\n"
    "v_mfma_f32_16x16x32_bf16 a[8:11], v[16:19], v[216:219], a[8:11]\n"
    "ds_read_b64_tr_b16 v[32:33], v61 offset:46144\n"
    "v_mfma_f32_16x16x32_bf16 a[8:11], v[20:23], v[232:235], a[8:11]\n"
    "ds_read_b64_tr_b16 v[34:35], v61 offset:50752\n"
    "v_mfma_f32_16x16x32_bf16 a[12:15], v[16:19], v[220:223], a[12:15]\n"
    "ds_read_b64_tr_b16 v[36:37], v61 offset:55360\n"
    "v_mfma_f32_16x16x32_bf16 a[12:15], v[20:23], v[236:239], a[12:15]\n"
    "s_waitcnt lgkmcnt(5)\n"
    "ds_read_b64_tr_b16 v[38:39], v61 offset:59968\n"
    "v_mfma_f32_16x16x32_bf16 a[16:19], v[24:27], v[208:211], a[16:19]\n"
    "s_waitcnt lgkmcnt(4)\n"
    "buffer_load_dwordx4 a[224:227], v0, s[20:23], s92 offen offset:0 sc1\n"
    "v_mfma_f32_16x16x32_bf16 a[16:19], v[28:31], v[224:227], a[16:19]\n"
    "v_mfma_f32_16x16x32_bf16 a[20:23], v[24:27], v[212:215], a[20:23]\n"
    "v_mfma_f32_16x16x32_bf16 a[20:23], v[28:31], v[228:231], a[20:23]\n"
    "v_mfma_f32_16x16x32_bf16 a[24:27], v[24:27], v[216:219], a[24:27]\n"
    "s_mov_b32 s91, 252578570\n"
    "ds_read_b64_tr_b16 v[48:49], v61 offset:46176\n"
    "v_mfma_f32_16x16x32_bf16 a[24:27], v[28:31], v[232:235], a[24:27]\n"
    "s_lshr_b32 s91, s91, s36\n"
    "s_xor_b32 s91, s91, s16\n"
    "s_and_b32 s91, s91, 31\n"
    "ds_read_b64_tr_b16 v[50:51], v61 offset:50784\n"
    "v_mfma_f32_16x16x32_bf16 a[28:31], v[24:27], v[220:223], a[28:31]\n"
    "s_mul_i32 s91, s91, 36864\n"
    "ds_read_b64_tr_b16 v[52:53], v61 offset:55392\n"
    "v_mfma_f32_16x16x32_bf16 a[28:31], v[28:31], v[236:239], a[28:31]\n"
    "s_waitcnt lgkmcnt(5)\n"
    "s_add_u32 m0, s17, 16384\n"
    "ds_read_b64_tr_b16 v[54:55], v61 offset:60000\n"
    "v_mfma_f32_16x16x32_bf16 a[32:35], v[32:35], v[208:211], a[32:35]\n"
    "s_waitcnt lgkmcnt(4)\n"
    "buffer_load_dwordx4 v2, s[40:43], s91 offen lds\n"
    "v_mfma_f32_16x16x32_bf16 a[32:35], v[36:39], v[224:227], a[32:35]\n"
    "v_mfma_f32_16x16x32_bf16 a[36:39], v[32:35], v[212:215], a[36:39]\n"
    "buffer_load_dwordx4 v3, s[40:43], s91 offen offset:1024 lds\n"
    "v_mfma_f32_16x16x32_bf16 a[36:39], v[36:39], v[228:231], a[36:39]\n"
    "v_mfma_f32_16x16x32_bf16 a[40:43], v[32:35], v[216:219], a[40:43]\n"
    "ds_read_b64_tr_b16 v[56:57], v61 offset:46208\n"
    "v_mfma_f32_16x16x32_bf16 a[40:43], v[36:39], v[232:235], a[40:43]\n"
    "ds_read_b64_tr_b16 v[58:59], v61 offset:50816\n"
    "v_mfma_f32_16x16x32_bf16 a[44:47], v[32:35], v[220:223], a[44:47]\n"
    "ds_read_b64_tr_b16 v[16:17], v61 offset:55424\n"
    "v_mfma_f32_16x16x32_bf16 a[44:47], v[36:39], v[236:239], a[44:47]\n"
    "s_waitcnt lgkmcnt(5)\n"
    "ds_read_b64_tr_b16 v[18:19], v61 offset:60032\n"
    "v_mfma_f32_16x16x32_bf16 a[48:51], v[48:51], v[208:211], a[48:51]\n"
    "s_waitcnt lgkmcnt(4)\n"
    "buffer_load_dwordx4 v4, s[40:43], s91 offen offset:2048 lds\n"
    "v_mfma_f32_16x16x32_bf16 a[48:51], v[52:55], v[224:227], a[48:51]\n"
    "v_mfma_f32_16x16x32_bf16 a[52:55], v[48:51], v[212:215], a[52:55]\n"
    "v_mfma_f32_16x16x32_bf16 a[52:55], v[52:55], v[228:231], a[52:55]\n"
    "v_mfma_f32_16x16x32_bf16 a[56:59], v[48:51], v[216:219], a[56:59]\n"
    "ds_read_b64_tr_b16 v[20:21], v61 offset:46240\n"
    "v_mfma_f32_16x16x32_bf16 a[56:59], v[52:55], v[232:235], a[56:59]\n"
    "ds_read_b64_tr_b16 v[22:23], v61 offset:50848\n"
    "v_mfma_f32_16x16x32_bf16 a[60:63], v[48:51], v[220:223], a[60:63]\n"
    "ds_read_b64_tr_b16 v[24:25], v61 offset:55456\n"
    "v_mfma_f32_16x16x32_bf16 a[60:63], v[52:55], v[236:239], a[60:63]\n"
    "s_waitcnt lgkmcnt(5)\n"
    "ds_read_b64_tr_b16 v[26:27], v61 offset:60064\n"
    "v_mfma_f32_16x16x32_bf16 a[64:67], v[56:59], v[208:211], a[64:67]\n"
    "s_waitcnt lgkmcnt(4)\n"
    "buffer_load_dwordx4 v5, s[40:43], s91 offen offset:3072 lds\n"
    "v_mfma_f32_16x16x32_bf16 a[64:67], v[16:19], v[224:227], a[64:67]\n"
    "s_add_u32 m0, s17, 20480\n"
    "v_mfma_f32_16x16x32_bf16 a[68:71], v[56:59], v[212:215], a[68:71]\n"
    "buffer_load_dwordx4 v6, s[40:43], s91 offen lds\n"
    "v_mfma_f32_16x16x32_bf16 a[68:71], v[16:19], v[228:231], a[68:71]\n"
    "v_mfma_f32_16x16x32_bf16 a[72:75], v[56:59], v[216:219], a[72:75]\n"
    "ds_read_b64_tr_b16 v[28:29], v61 offset:46272\n"
    "v_mfma_f32_16x16x32_bf16 a[72:75], v[16:19], v[232:235], a[72:75]\n"
    "ds_read_b64_tr_b16 v[30:31], v61 offset:50880\n"
    "v_mfma_f32_16x16x32_bf16 a[76:79], v[56:59], v[220:223], a[76:79]\n"
    "ds_read_b64_tr_b16 v[32:33], v61 offset:55488\n"
    "v_mfma_f32_16x16x32_bf16 a[76:79], v[16:19], v[236:239], a[76:79]\n"
    "s_waitcnt lgkmcnt(5)\n"
    "ds_read_b64_tr_b16 v[34:35], v61 offset:60096\n"
    "v_mfma_f32_16x16x32_bf16 a[80:83], v[20:23], v[208:211], a[80:83]\n"
    "s_waitcnt lgkmcnt(4)\n"
    "buffer_load_dwordx4 v7, s[40:43], s91 offen offset:1024 lds\n"
    "v_mfma_f32_16x16x32_bf16 a[80:83], v[24:27], v[224:227], a[80:83]\n"
    "v_mfma_f32_16x16x32_bf16 a[84:87], v[20:23], v[212:215], a[84:87]\n"
    "v_mfma_f32_16x16x32_bf16 a[84:87], v[24:27], v[228:231], a[84:87]\n"
    "v_mfma_f32_16x16x32_bf16 a[88:91], v[20:23], v[216:219], a[88:91]\n"
    "ds_read_b64_tr_b16 v[36:37], v61 offset:46304\n"
    "v_mfma_f32_16x16x32_bf16 a[88:91], v[24:27], v[232:235], a[88:91]\n"
    "ds_read_b64_tr_b16 v[38:39], v61 offset:50912\n"
    "v_mfma_f32_16x16x32_bf16 a[92:95], v[20:23], v[220:223], a[92:95]\n"
    "ds_read_b64_tr_b16 v[48:49], v61 offset:55520\n"
    "v_mfma_f32_16x16x32_bf16 a[92:95], v[24:27], v[236:239], a[92:95]\n"
    "s_waitcnt lgkmcnt(5)\n"
    "ds_read_b64_tr_b16 v[50:51], v61 offset:60128\n"
    "v_mfma_f32_16x16x32_bf16 a[96:99], v[28:31], v[208:211], a[96:99]\n"
    "s_waitcnt lgkmcnt(4)\n"
    "buffer_load_dwordx4 v8, s[40:43], s91 offen offset:2048 lds\n"
    "v_mfma_f32_16x16x32_bf16 a[96:99], v[32:35], v[224:227], a[96:99]\n"
    "v_mfma_f32_16x16x32_bf16 a[100:103], v[28:31], v[212:215], a[100:103]\n"
    "buffer_load_dwordx4 v9, s[40:43], s91 offen offset:3072 lds\n"
    "v_mfma_f32_16x16x32_bf16 a[100:103], v[32:35], v[228:231], a[100:103]\n"
    "v_mfma_f32_16x16x32_bf16 a[104:107], v[28:31], v[216:219], a[104:107]\n"
    "ds_read_b64_tr_b16 v[52:53], v61 offset:46336\n"
    "v_mfma_f32_16x16x32_bf16 a[104:107], v[32:35], v[232:235], a[104:107]\n"
    "ds_read_b64_tr_b16 v[54:55], v61 offset:50944\n"
    "v_mfma_f32_16x16x32_bf16 a[108:111], v[28:31], v[220:223], a[108:111]\n"
    "ds_read_b64_tr_b16 v[56:57], v61 offset:55552\n"
    "v_mfma_f32_16x16x32_bf16 a[108:111], v[32:35], v[236:239], a[108:111]\n"
    "s_waitcnt lgkmcnt(5)\n"
    "s_add_u32 m0, s17, 24576\n"
    "ds_read_b64_tr_b16 v[58:59], v61 offset:60160\n"
    "v_mfma_f32_16x16x32_bf16 a[112:115], v[36:39], v[208:211], a[112:115]\n"
    "s_waitcnt lgkmcnt(4)\n"
    "buffer_load_dwordx4 v10, s[40:43], s91 offen lds\n"
    "v_mfma_f32_16x16x32_bf16 a[112:115], v[48:51], v[224:227], a[112:115]\n"
    "v_mfma_f32_16x16x32_bf16 a[116:119], v[36:39], v[212:215], a[116:119]\n"
    "v_mfma_f32_16x16x32_bf16 a[116:119], v[48:51], v[228:231], a[116:119]\n"
    "v_mfma_f32_16x16x32_bf16 a[120:123], v[36:39], v[216:219], a[120:123]\n"
    "ds_read_b64_tr_b16 v[16:17], v62 offset:0\n"
    "v_mfma_f32_16x16x32_bf16 a[120:123], v[48:51], v[232:235], a[120:123]\n"
    "ds_read_b64_tr_b16 v[18:19], v62 offset:4608\n"
    "v_mfma_f32_16x16x32_bf16 a[124:127], v[36:39], v[220:223], a[124:127]\n"
    "ds_read_b64_tr_b16 v[20:21], v62 offset:9216\n"
    "v_mfma_f32_16x16x32_bf16 a[124:127], v[48:51], v[236:239], a[124:127]\n"
    "s_waitcnt lgkmcnt(5)\n"
    "s_xor_b32 s92, s19, 24576\n"
    "ds_read_b64_tr_b16 v[22:23], v62 offset:13824\n"
    "v_mfma_f32_16x16x32_bf16 a[128:131], v[52:55], v[208:211], a[128:131]\n"
    "s_waitcnt lgkmcnt(4)\n"
    "buffer_load_dwordx4 a[252:255], v0, s[20:23], s92 offen offset:3072 sc1\n"
    "v_mfma_f32_16x16x32_bf16 a[128:131], v[56:59], v[224:227], a[128:131]\n"
    "v_mfma_f32_16x16x32_bf16 a[132:135], v[52:55], v[212:215], a[132:135]\n"
    "buffer_load_dwordx4 a[248:251], v0, s[20:23], s92 offen offset:2048 sc1\n"
    "v_mfma_f32_16x16x32_bf16 a[132:135], v[56:59], v[228:231], a[132:135]\n"
    "v_mfma_f32_16x16x32_bf16 a[136:139], v[52:55], v[216:219], a[136:139]\n"
    "ds_read_b64_tr_b16 v[24:25], v62 offset:32\n"
    "v_mfma_f32_16x16x32_bf16 a[136:139], v[56:59], v[232:235], a[136:139]\n"
    "ds_read_b64_tr_b16 v[26:27], v62 offset:4640\n"
    "v_mfma_f32_16x16x32_bf16 a[140:143], v[52:55], v[220:223], a[140:143]\n"
    "ds_read_b64_tr_b16 v[28:29], v62 offset:9248\n"
    "v_mfma_f32_16x16x32_bf16 a[140:143], v[56:59], v[236:239], a[140:143]\n"
    "s_waitcnt vmcnt(32) lgkmcnt(5)\n"
    "ds_read_b64_tr_b16 v[30:31], v62 offset:13856\n"
    "v_mfma_f32_16x16x32_bf16 a[0:3], v[16:19], v[240:243], a[0:3]\n"
    "s_waitcnt vmcnt(28) lgkmcnt(4)\n"
    "buffer_load_dwordx4 a[244:247], v0, s[20:23], s92 offen offset:1024 sc1\n"
    "v_mfma_f32_16x16x32_bf16 a[0:3], v[20:23], a[192:195], a[0:3]\n"
    "v_mfma_f32_16x16x32_bf16 a[4:7], v[16:19], v[244:247], a[4:7]\n"
    "v_mfma_f32_16x16x32_bf16 a[4:7], v[20:23], a[196:199], a[4:7]\n"
    "v_mfma_f32_16x16x32_bf16 a[8:11], v[16:19], v[248:251], a[8:11]\n"
    "ds_read_b64_tr_b16 v[32:33], v62 offset:64\n"
    "v_mfma_f32_16x16x32_bf16 a[8:11], v[20:23], a[200:203], a[8:11]\n"
    "ds_read_b64_tr_b16 v[34:35], v62 offset:4672\n"
    "v_mfma_f32_16x16x32_bf16 a[12:15], v[16:19], v[252:255], a[12:15]\n"
    "ds_read_b64_tr_b16 v[36:37], v62 offset:9280\n"
    "v_mfma_f32_16x16x32_bf16 a[12:15], v[20:23], a[204:207], a[12:15]\n"
    "s_waitcnt lgkmcnt(5)\n"
    "ds_read_b64_tr_b16 v[38:39], v62 offset:13888\n"
    "v_mfma_f32_16x16x32_bf16 a[16:19], v[24:27], v[240:243], a[16:19]\n"
    "s_waitcnt lgkmcnt(4)\n"
    "buffer_load_dwordx4 a[240:243], v0, s[20:23], s92 offen offset:0 sc1\n"
    "v_mfma_f32_16x16x32_bf16 a[16:19], v[28:31], a[192:195], a[16:19]\n"
    "s_xor_b32 s92, s19, 28672\n"
    "v_mfma_f32_16x16x32_bf16 a[20:23], v[24:27], v[244:247], a[20:23]\n"
    "buffer_load_dwordx4 a[156:159], v0, s[20:23], s92 offen offset:3072 sc1\n"
    "v_mfma_f32_16x16x32_bf16 a[20:23], v[28:31], a[196:199], a[20:23]\n"
    "v_mfma_f32_16x16x32_bf16 a[24:27], v[24:27], v[248:251], a[24:27]\n"
    "ds_read_b64_tr_b16 v[48:49], v62 offset:96\n"
    "v_mfma_f32_16x16x32_bf16 a[24:27], v[28:31], a[200:203], a[24:27]\n"
    "ds_read_b64_tr_b16 v[50:51], v62 offset:4704\n"
    "v_mfma_f32_16x16x32_bf16 a[28:31], v[24:27], v[252:255], a[28:31]\n"
    "ds_read_b64_tr_b16 v[52:53], v62 offset:9312\n"
    "v_mfma_f32_16x16x32_bf16 a[28:31], v[28:31], a[204:207], a[28:31]\n"
    "s_waitcnt lgkmcnt(5)\n"
    "ds_read_b64_tr_b16 v[54:55], v62 offset:13920\n"
    "v_mfma_f32_16x16x32_bf16 a[32:35], v[32:35], v[240:243], a[32:35]\n"
    "s_waitcnt lgkmcnt(4)\n"
    "buffer_load_dwordx4 a[152:155], v0, s[20:23], s92 offen offset:2048 sc1\n"
    "v_mfma_f32_16x16x32_bf16 a[32:35], v[36:39], a[192:195], a[32:35]\n"
    "v_mfma_f32_16x16x32_bf16 a[36:39], v[32:35], v[244:247], a[36:39]\n"
    "v_mfma_f32_16x16x32_bf16 a[36:39], v[36:39], a[196:199], a[36:39]\n"
    "v_mfma_f32_16x16x32_bf16 a[40:43], v[32:35], v[248:251], a[40:43]\n"
    "ds_read_b64_tr_b16 v[56:57], v62 offset:128\n"
    "v_mfma_f32_16x16x32_bf16 a[40:43], v[36:39], a[200:203], a[40:43]\n"
    "ds_read_b64_tr_b16 v[58:59], v62 offset:4736\n"
    "v_mfma_f32_16x16x32_bf16 a[44:47], v[32:35], v[252:255], a[44:47]\n"
    "ds_read_b64_tr_b16 v[16:17], v62 offset:9344\n"
    "v_mfma_f32_16x16x32_bf16 a[44:47], v[36:39], a[204:207], a[44:47]\n"
    "s_waitcnt lgkmcnt(5)\n"
    "ds_read_b64_tr_b16 v[18:19], v62 offset:13952\n"
    "v_mfma_f32_16x16x32_bf16 a[48:51], v[48:51], v[240:243], a[48:51]\n"
    "s_waitcnt lgkmcnt(4)\n"
    "buffer_load_dwordx4 a[148:151], v0, s[20:23], s92 offen offset:1024 sc1\n"
    "v_mfma_f32_16x16x32_bf16 a[48:51], v[52:55], a[192:195], a[48:51]\n"
    "v_mfma_f32_16x16x32_bf16 a[52:55], v[48:51], v[244:247], a[52:55]\n"
    "buffer_load_dwordx4 a[144:147], v0, s[20:23], s92 offen offset:0 sc1\n"
    "v_mfma_f32_16x16x32_bf16 a[52:55], v[52:55], a[196:199], a[52:55]\n"
    "v_mfma_f32_16x16x32_bf16 a[56:59], v[48:51], v[248:251], a[56:59]\n"
    "ds_read_b64_tr_b16 v[20:21], v62 offset:160\n"
    "v_mfma_f32_16x16x32_bf16 a[56:59], v[52:55], a[200:203], a[56:59]\n"
    "ds_read_b64_tr_b16 v[22:23], v62 offset:4768\n"
    "v_mfma_f32_16x16x32_bf16 a[60:63], v[48:51], v[252:255], a[60:63]\n"
    "ds_read_b64_tr_b16 v[24:25], v62 offset:9376\n"
    "v_mfma_f32_16x16x32_bf16 a[60:63], v[52:55], a[204:207], a[60:63]\n"
    "s_waitcnt lgkmcnt(5)\n"
    "s_xor_b32 s92, s19, 40960\n"
    "ds_read_b64_tr_b16 v[26:27], v62 offset:13984\n"
    "v_mfma_f32_16x16x32_bf16 a[64:67], v[56:59], v[240:243], a[64:67]\n"
    "s_waitcnt lgkmcnt(4)\n"
    "buffer_load_dwordx4 v[76:79], v0, s[20:23], s92 offen offset:3072 sc1\n"
    "v_mfma_f32_16x16x32_bf16 a[64:67], v[16:19], a[192:195], a[64:67]\n"
    "v_mfma_f32_16x16x32_bf16 a[68:71], v[56:59], v[244:247], a[68:71]\n"
    "v_mfma_f32_16x16x32_bf16 a[68:71], v[16:19], a[196:199], a[68:71]\n"
    "v_mfma_f32_16x16x32_bf16 a[72:75], v[56:59], v[248:251], a[72:75]\n"
    "ds_read_b64_tr_b16 v[28:29], v62 offset:192\n"
    "v_mfma_f32_16x16x32_bf16 a[72:75], v[16:19], a[200:203], a[72:75]\n"
    "ds_read_b64_tr_b16 v[30:31], v62 offset:4800\n"
    "v_mfma_f32_16x16x32_bf16 a[76:79], v[56:59], v[252:255], a[76:79]\n"
    "ds_read_b64_tr_b16 v[32:33], v62 offset:9408\n"
    "v_mfma_f32_16x16x32_bf16 a[76:79], v[16:19], a[204:207], a[76:79]\n"
    "s_waitcnt lgkmcnt(5)\n"
    "ds_read_b64_tr_b16 v[34:35], v62 offset:14016\n"
    "v_mfma_f32_16x16x32_bf16 a[80:83], v[20:23], v[240:243], a[80:83]\n"
    "s_waitcnt lgkmcnt(4)\n"
    "buffer_load_dwordx4 v[72:75], v0, s[20:23], s92 offen offset:2048 sc1\n"
    "v_mfma_f32_16x16x32_bf16 a[80:83], v[24:27], a[192:195], a[80:83]\n"
    "v_mfma_f32_16x16x32_bf16 a[84:87], v[20:23], v[244:247], a[84:87]\n"
    "buffer_load_dwordx4 v[68:71], v0, s[20:23], s92 offen offset:1024 sc1\n"
    "v_mfma_f32_16x16x32_bf16 a[84:87], v[24:27], a[196:199], a[84:87]\n"
    "v_mfma_f32_16x16x32_bf16 a[88:91], v[20:23], v[248:251], a[88:91]\n"
    "ds_read_b64_tr_b16 v[36:37], v62 offset:224\n"
    "v_mfma_f32_16x16x32_bf16 a[88:91], v[24:27], a[200:203], a[88:91]\n"
    "ds_read_b64_tr_b16 v[38:39], v62 offset:4832\n"
    "v_mfma_f32_16x16x32_bf16 a[92:95], v[20:23], v[252:255], a[92:95]\n"
    "ds_read_b64_tr_b16 v[48:49], v62 offset:9440\n"
    "v_mfma_f32_16x16x32_bf16 a[92:95], v[24:27], a[204:207], a[92:95]\n"
    "s_waitcnt lgkmcnt(5)\n"
    "ds_read_b64_tr_b16 v[50:51], v62 offset:14048\n"
    "v_mfma_f32_16x16x32_bf16 a[96:99], v[28:31], v[240:243], a[96:99]\n"
    "s_waitcnt lgkmcnt(4)\n"
    "buffer_load_dwordx4 v[64:67], v0, s[20:23], s92 offen offset:0 sc1\n"
    "v_mfma_f32_16x16x32_bf16 a[96:99], v[32:35], a[192:195], a[96:99]\n"
    "v_mfma_f32_16x16x32_bf16 a[100:103], v[28:31], v[244:247], a[100:103]\n"
    "v_mfma_f32_16x16x32_bf16 a[100:103], v[32:35], a[196:199], a[100:103]\n"
    "v_mfma_f32_16x16x32_bf16 a[104:107], v[28:31], v[248:251], a[104:107]\n"
    "ds_read_b64_tr_b16 v[52:53], v62 offset:256\n"
    "v_mfma_f32_16x16x32_bf16 a[104:107], v[32:35], a[200:203], a[104:107]\n"
    "ds_read_b64_tr_b16 v[54:55], v62 offset:4864\n"
    "v_mfma_f32_16x16x32_bf16 a[108:111], v[28:31], v[252:255], a[108:111]\n"
    "ds_read_b64_tr_b16 v[56:57], v62 offset:9472\n"
    "v_mfma_f32_16x16x32_bf16 a[108:111], v[32:35], a[204:207], a[108:111]\n"
    "s_waitcnt lgkmcnt(5)\n"
    "s_xor_b32 s92, s19, 45056\n"
    "ds_read_b64_tr_b16 v[58:59], v62 offset:14080\n"
    "v_mfma_f32_16x16x32_bf16 a[112:115], v[36:39], v[240:243], a[112:115]\n"
    "s_waitcnt lgkmcnt(4)\n"
    "buffer_load_dwordx4 v[92:95], v0, s[20:23], s92 offen offset:3072 sc1\n"
    "v_mfma_f32_16x16x32_bf16 a[112:115], v[48:51], a[192:195], a[112:115]\n"
    "v_mfma_f32_16x16x32_bf16 a[116:119], v[36:39], v[244:247], a[116:119]\n"
    "buffer_load_dwordx4 v[88:91], v0, s[20:23], s92 offen offset:2048 sc1\n"
    "v_mfma_f32_16x16x32_bf16 a[116:119], v[48:51], a[196:199], a[116:119]\n"
    "v_mfma_f32_16x16x32_bf16 a[120:123], v[36:39], v[248:251], a[120:123]\n"
    "v_mfma_f32_16x16x32_bf16 a[120:123], v[48:51], a[200:203], a[120:123]\n"
    "v_mfma_f32_16x16x32_bf16 a[124:127], v[36:39], v[252:255], a[124:127]\n"
    "s_waitcnt vmcnt(31)\n"
    "s_barrier\n"
    "ds_read_b64_tr_b16 v[16:17], v60 offset:36864\n"
    "v_mfma_f32_16x16x32_bf16 a[124:127], v[48:51], a[204:207], a[124:127]\n"
    "s_waitcnt lgkmcnt(3)\n"
    "ds_read_b64_tr_b16 v[18:19], v60 offset:41472\n"
    "v_mfma_f32_16x16x32_bf16 a[128:131], v[52:55], v[240:243], a[128:131]\n"
    "s_waitcnt lgkmcnt(2)\n"
    "ds_read_b64_tr_b16 v[20:21], v60 offset:46080\n"
    "v_mfma_f32_16x16x32_bf16 a[128:131], v[56:59], a[192:195], a[128:131]\n"
    "ds_read_b64_tr_b16 v[22:23], v60 offset:50688\n"
    "v_mfma_f32_16x16x32_bf16 a[132:135], v[52:55], v[244:247], a[132:135]\n"
    "buffer_load_dwordx4 v[84:87], v0, s[20:23], s92 offen offset:1024 sc1\n"
    "v_mfma_f32_16x16x32_bf16 a[132:135], v[56:59], a[196:199], a[132:135]\n"
    "v_mfma_f32_16x16x32_bf16 a[136:139], v[52:55], v[248:251], a[136:139]\n"
    "ds_read_b64_tr_b16 v[24:25], v60 offset:36896\n"
    "v_mfma_f32_16x16x32_bf16 a[136:139], v[56:59], a[200:203], a[136:139]\n"
    "ds_read_b64_tr_b16 v[26:27], v60 offset:41504\n"
    "v_mfma_f32_16x16x32_bf16 a[140:143], v[52:55], v[252:255], a[140:143]\n"
    "ds_read_b64_tr_b16 v[28:29], v60 offset:46112\n"
    "v_mfma_f32_16x16x32_bf16 a[140:143], v[56:59], a[204:207], a[140:143]\n"
    "s_waitcnt vmcnt(28) lgkmcnt(5)\n"
    "ds_read_b64_tr_b16 v[30:31], v60 offset:50720\n"
    "v_mfma_f32_16x16x32_bf16 a[0:3], v[16:19], a[208:211], a[0:3]\n"
    "s_waitcnt vmcnt(24) lgkmcnt(4)\n"
    "buffer_load_dwordx4 v[80:83], v0, s[20:23], s92 offen offset:0 sc1\n"
    "v_mfma_f32_16x16x32_bf16 a[0:3], v[20:23], a[224:227], a[0:3]\n"
    "s_xor_b32 s92, s19, 57344\n"
    "v_mfma_f32_16x16x32_bf16 a[4:7], v[16:19], a[212:215], a[4:7]\n"
    "buffer_load_dwordx4 a[172:175], v0, s[20:23], s92 offen offset:3072 sc1\n"
    "v_mfma_f32_16x16x32_bf16 a[4:7], v[20:23], a[228:231], a[4:7]\n"
    "v_mfma_f32_16x16x32_bf16 a[8:11], v[16:19], a[216:219], a[8:11]\n"
    "ds_read_b64_tr_b16 v[32:33], v60 offset:36928\n"
    "v_mfma_f32_16x16x32_bf16 a[8:11], v[20:23], a[232:235], a[8:11]\n"
    "ds_read_b64_tr_b16 v[34:35], v60 offset:41536\n"
    "v_mfma_f32_16x16x32_bf16 a[12:15], v[16:19], a[220:223], a[12:15]\n"
    "ds_read_b64_tr_b16 v[36:37], v60 offset:46144\n"
    "v_mfma_f32_16x16x32_bf16 a[12:15], v[20:23], a[236:239], a[12:15]\n"
    "s_waitcnt lgkmcnt(5)\n"
    "ds_read_b64_tr_b16 v[38:39], v60 offset:50752\n"
    "v_mfma_f32_16x16x32_bf16 a[16:19], v[24:27], a[208:211], a[16:19]\n"
    "s_waitcnt lgkmcnt(4)\n"
    "buffer_load_dwordx4 a[168:171], v0, s[20:23], s92 offen offset:2048 sc1\n"
    "v_mfma_f32_16x16x32_bf16 a[16:19], v[28:31], a[224:227], a[16:19]\n"
    "v_mfma_f32_16x16x32_bf16 a[20:23], v[24:27], a[212:215], a[20:23]\n"
    "v_mfma_f32_16x16x32_bf16 a[20:23], v[28:31], a[228:231], a[20:23]\n"
    "v_mfma_f32_16x16x32_bf16 a[24:27], v[24:27], a[216:219], a[24:27]\n"
    "s_lshr_b32 s61, s7, 1\n"
    "s_cmp_eq_u32 s61, s80\n"
    "s_cselect_b32 s60, 0x40000000, 0\n"
    "s_cselect_b32 s49, 0, 90112\n"
    "s_or_b32 s15, s15, s60\n"
    "s_lshl_b32 s56, 1, s7\n"
    "s_xor_b32 s56, s56, 0xff\n"
    "s_and_b32 s58, s56, 0x55\n"
    "s_mov_b32 s57, 0\n"
    "s_mov_b32 s59, 0\n"
    "s_mul_i32 s50, s80, 3072\n"
    "s_xor_b32 s52, s61, 0\n"
    "s_mul_i32 s52, s52, 18432\n"
    "s_xor_b32 s53, s61, 1\n"
    "s_mul_i32 s53, s53, 18432\n"
    "s_xor_b32 s54, s61, 2\n"
    "s_mul_i32 s54, s54, 18432\n"
    "s_xor_b32 s55, s61, 3\n"
    "s_mul_i32 s55, s55, 18432\n"
    "s_lshl_b32 s51, s37, 2\n"
    "s_add_u32 s51, s51, s80\n"
    "s_cmp_eq_u32 s51, 3\n"
    "s_addc_u32 s39, s7, 0\n"
    "s_lshl_b32 s51, s51, 2\n"
    "s_lshr_b32 s51, 0x33102210, s51\n"
    "s_and_b32 s51, s51, 3\n"
    "s_add_u32 s38, s95, s51\n"
    "s_add_u32 s38, s38, 6\n"
    "s_mul_i32 s39, s39, 24\n"
    "s_add_u32 s39, s39, s51\n"
    "s_mul_i32 s39, s39, 3072\n"
    "ds_read_b64_tr_b16 v[48:49], v60 offset:36960\n"
    "v_lshrrev_b32 v124, 8, v0\n"
    "v_mfma_f32_16x16x32_bf16 a[24:27], v[28:31], a[232:235], a[24:27]\n"
    "ds_read_b64_tr_b16 v[50:51], v60 offset:41568\n"
    "v_bfe_u32 v125, v0, 6, 2\n"
    "v_mfma_f32_16x16x32_bf16 a[28:31], v[24:27], a[220:223], a[28:31]\n"
    "ds_read_b64_tr_b16 v[52:53], v60 offset:46176\n"
    "v_cmp_eq_u32 vcc, v124, v125\n"
    "v_mfma_f32_16x16x32_bf16 a[28:31], v[28:31], a[236:239], a[28:31]\n"
    "s_waitcnt lgkmcnt(5)\n"
    "ds_read_b64_tr_b16 v[54:55], v60 offset:50784\n"
    "v_and_b32 v124, 16, v0\n"
    "v_mfma_f32_16x16x32_bf16 a[32:35], v[32:35], a[208:211], a[32:35]\n"
    "s_waitcnt lgkmcnt(4)\n"
    "buffer_load_dwordx4 a[164:167], v0, s[20:23], s92 offen offset:1024 sc1\n"
    "v_mov_b32 v125, 0x3f80\n"
    "v_mfma_f32_16x16x32_bf16 a[32:35], v[36:39], a[224:227], a[32:35]\n"
    "v_lshlrev_b32 v124, v124, v125\n"
    "v_cndmask_b32 v124, 0, v124, vcc\n"
    "v_mfma_f32_16x16x32_bf16 a[36:39], v[32:35], a[212:215], a[36:39]\n"
    "buffer_load_dwordx4 a[160:163], v0, s[20:23], s92 offen offset:0 sc1\n"
    "v_and_b32 v125, 32, v0\n"
    "v_mfma_f32_16x16x32_bf16 a[36:39], v[36:39], a[228:231], a[36:39]\n"
    "v_cmp_eq_u32 vcc, 0, v125\n"
    "s_nop 1\n"
    "v_cndmask_b32 v40, 0, v124, vcc\n"
    "v_mfma_f32_16x16x32_bf16 a[40:43], v[32:35], a[216:219], a[40:43]\n"
    "ds_read_b64_tr_b16 v[56:57], v60 offset:36992\n"
    "v_cndmask_b32 v41, v124, 0, vcc\n"
    "v_mfma_f32_16x16x32_bf16 a[40:43], v[36:39], a[232:235], a[40:43]\n"
    "ds_read_b64_tr_b16 v[58:59], v60 offset:41600\n"
    "v_mov_b32 v42, 0\n"
    "v_mfma_f32_16x16x32_bf16 a[44:47], v[32:35], a[220:223], a[44:47]\n"
    "ds_read_b64_tr_b16 v[16:17], v60 offset:46208\n"
    "v_mov_b32 v43, 0\n"
    "v_mfma_f32_16x16x32_bf16 a[44:47], v[36:39], a[236:239], a[44:47]\n"
    "s_waitcnt lgkmcnt(5)\n"
    "s_xor_b32 s92, s19, 61440\n"
    "ds_read_b64_tr_b16 v[18:19], v60 offset:50816\n"
    "v_mov_b32 v44, v40\n"
    "v_mfma_f32_16x16x32_bf16 a[48:51], v[48:51], a[208:211], a[48:51]\n"
    "s_waitcnt lgkmcnt(4)\n"
    "buffer_load_dwordx4 a[188:191], v0, s[20:23], s92 offen offset:3072 sc1\n"
    "v_mov_b32 v45, v41\n"
    "v_mfma_f32_16x16x32_bf16 a[48:51], v[52:55], a[224:227], a[48:51]\n"
    "v_lshlrev_b32 v1, 2, v1\n"
    "v_add_u32 v9, s49, v0\n"
    "v_mfma_f32_16x16x32_bf16 a[52:55], v[48:51], a[212:215], a[52:55]\n"
    "v_add_u32 v8, s50, v0\n"
    "v_mfma_f32_16x16x32_bf16 a[52:55], v[52:55], a[228:231], a[52:55]\n"
    "v_mfma_f32_16x16x32_bf16 a[56:59], v[48:51], a[216:219], a[56:59]\n"
    "ds_read_b64_tr_b16 v[20:21], v60 offset:37024\n"
    "v_mfma_f32_16x16x32_bf16 a[56:59], v[52:55], a[232:235], a[56:59]\n"
    "ds_read_b64_tr_b16 v[22:23], v60 offset:41632\n"
    "v_mfma_f32_16x16x32_bf16 a[60:63], v[48:51], a[220:223], a[60:63]\n"
    "ds_read_b64_tr_b16 v[24:25], v60 offset:46240\n"
    "v_mfma_f32_16x16x32_bf16 a[60:63], v[52:55], a[236:239], a[60:63]\n"
    "s_waitcnt lgkmcnt(5)\n"
    "ds_read_b64_tr_b16 v[26:27], v60 offset:50848\n"
    "v_mfma_f32_16x16x32_bf16 a[64:67], v[56:59], a[208:211], a[64:67]\n"
    "s_waitcnt lgkmcnt(4)\n"
    "buffer_load_dwordx4 a[184:187], v0, s[20:23], s92 offen offset:2048 sc1\n"
    "v_mfma_f32_16x16x32_bf16 a[64:67], v[16:19], a[224:227], a[64:67]\n"
    "v_mfma_f32_16x16x32_bf16 a[68:71], v[56:59], a[212:215], a[68:71]\n"
    "buffer_load_dwordx4 a[180:183], v0, s[20:23], s92 offen offset:1024 sc1\n"
    "v_mfma_f32_16x16x32_bf16 a[68:71], v[16:19], a[228:231], a[68:71]\n"
    "v_mfma_f32_16x16x32_bf16 a[72:75], v[56:59], a[216:219], a[72:75]\n"
    "ds_read_b64_tr_b16 v[28:29], v60 offset:37056\n"
    "v_mfma_f32_16x16x32_bf16 a[72:75], v[16:19], a[232:235], a[72:75]\n"
    "ds_read_b64_tr_b16 v[30:31], v60 offset:41664\n"
    "v_mfma_f32_16x16x32_bf16 a[76:79], v[56:59], a[220:223], a[76:79]\n"
    "ds_read_b64_tr_b16 v[32:33], v60 offset:46272\n"
    "v_mfma_f32_16x16x32_bf16 a[76:79], v[16:19], a[236:239], a[76:79]\n"
    "s_waitcnt lgkmcnt(5)\n"
    "ds_read_b64_tr_b16 v[34:35], v60 offset:50880\n"
    "v_mfma_f32_16x16x32_bf16 a[80:83], v[20:23], a[208:211], a[80:83]\n"
    "s_waitcnt lgkmcnt(4)\n"
    "buffer_load_dwordx4 a[176:179], v0, s[20:23], s92 offen offset:0 sc1\n"
    "v_mfma_f32_16x16x32_bf16 a[80:83], v[24:27], a[224:227], a[80:83]\n"
    "v_mfma_f32_16x16x32_bf16 a[84:87], v[20:23], a[212:215], a[84:87]\n"
    "v_mfma_f32_16x16x32_bf16 a[84:87], v[24:27], a[228:231], a[84:87]\n"
    "v_mfma_f32_16x16x32_bf16 a[88:91], v[20:23], a[216:219], a[88:91]\n"
    "ds_read_b64_tr_b16 v[36:37], v60 offset:37088\n"
    "v_mfma_f32_16x16x32_bf16 a[88:91], v[24:27], a[232:235], a[88:91]\n"
    "ds_read_b64_tr_b16 v[38:39], v60 offset:41696\n"
    "v_mfma_f32_16x16x32_bf16 a[92:95], v[20:23], a[220:223], a[92:95]\n"
    "ds_read_b64_tr_b16 v[48:49], v60 offset:46304\n"
    "v_mfma_f32_16x16x32_bf16 a[92:95], v[24:27], a[236:239], a[92:95]\n"
    "s_waitcnt lgkmcnt(5)\n"
    "ds_read_b64_tr_b16 v[50:51], v60 offset:50912\n"
    "v_mfma_f32_16x16x32_bf16 a[96:99], v[28:31], a[208:211], a[96:99]\n"
    "s_waitcnt lgkmcnt(4)\n"
    "v_mfma_f32_16x16x32_bf16 a[96:99], v[32:35], a[224:227], a[96:99]\n"
    "v_mfma_f32_16x16x32_bf16 a[100:103], v[28:31], a[212:215], a[100:103]\n"
    "v_mfma_f32_16x16x32_bf16 a[100:103], v[32:35], a[228:231], a[100:103]\n"
    "v_mfma_f32_16x16x32_bf16 a[104:107], v[28:31], a[216:219], a[104:107]\n"
    "ds_read_b64_tr_b16 v[52:53], v60 offset:37120\n"
    "v_mfma_f32_16x16x32_bf16 a[104:107], v[32:35], a[232:235], a[104:107]\n"
    "ds_read_b64_tr_b16 v[54:55], v60 offset:41728\n"
    "v_mfma_f32_16x16x32_bf16 a[108:111], v[28:31], a[220:223], a[108:111]\n"
    "ds_read_b64_tr_b16 v[56:57], v60 offset:46336\n"
    "v_mfma_f32_16x16x32_bf16 a[108:111], v[32:35], a[236:239], a[108:111]\n"
    "s_waitcnt lgkmcnt(5)\n"
    "ds_read_b64_tr_b16 v[58:59], v60 offset:50944\n"
    "v_mfma_f32_16x16x32_bf16 a[112:115], v[36:39], a[208:211], a[112:115]\n"
    "s_waitcnt lgkmcnt(4)\n"
    "v_mfma_f32_16x16x32_bf16 a[112:115], v[48:51], a[224:227], a[112:115]\n"
    "v_mfma_f32_16x16x32_bf16 a[116:119], v[36:39], a[212:215], a[116:119]\n"
    "v_mfma_f32_16x16x32_bf16 a[116:119], v[48:51], a[228:231], a[116:119]\n"
    "v_mfma_f32_16x16x32_bf16 a[120:123], v[36:39], a[216:219], a[120:123]\n"
    "ds_read_b64_tr_b16 v[16:17], v60 offset:55296\n"
    "v_mfma_f32_16x16x32_bf16 a[120:123], v[48:51], a[232:235], a[120:123]\n"
    "ds_read_b64_tr_b16 v[18:19], v60 offset:59904\n"
    "v_mfma_f32_16x16x32_bf16 a[124:127], v[36:39], a[220:223], a[124:127]\n"
    "ds_read_b64_tr_b16 v[20:21], v61 offset:0\n"
    "v_mfma_f32_16x16x32_bf16 a[124:127], v[48:51], a[236:239], a[124:127]\n"
    "s_waitcnt lgkmcnt(5)\n"
    "ds_read_b64_tr_b16 v[22:23], v61 offset:4608\n"
    "v_mfma_f32_16x16x32_bf16 a[128:131], v[52:55], a[208:211], a[128:131]\n"
    "s_waitcnt lgkmcnt(4)\n"
    "v_mfma_f32_16x16x32_bf16 a[128:131], v[56:59], a[224:227], a[128:131]\n"
    "v_mfma_f32_16x16x32_bf16 a[132:135], v[52:55], a[212:215], a[132:135]\n"
    "v_bfe_u32 v12, v0, 4, 4\n"
    "v_mul_u32_u24 v12, 1152, v12\n"
    "v_mfma_f32_16x16x32_bf16 a[132:135], v[56:59], a[228:231], a[132:135]\n"
    "v_lshrrev_b32 v63, 8, v0\n"
    "v_lshl_add_u32 v12, v63, 3, v12\n"
    "v_mfma_f32_16x16x32_bf16 a[136:139], v[52:55], a[216:219], a[136:139]\n"
    "v_add_u32 v63, 18432, v12\n"
    "v_mfma_f32_16x16x32_bf16 a[136:139], v[56:59], a[232:235], a[136:139]\n"
    "v_mfma_f32_16x16x32_bf16 a[140:143], v[52:55], a[220:223], a[140:143]\n"
    "s_waitcnt vmcnt(24)\n"
    "s_barrier\n"
    "ds_read_b64_tr_b16 v[24:25], v60 offset:0\n"
    "v_mfma_f32_16x16x32_bf16 a[140:143], v[56:59], a[236:239], a[140:143]\n"
    "s_waitcnt vmcnt(20) lgkmcnt(3)\n"
    "ds_read_b64_tr_b16 v[26:27], v60 offset:4608\n"
    "v_mfma_f32_16x16x32_bf16 a[0:3], v[16:19], a[240:243], a[0:3]\n"
    "s_waitcnt vmcnt(16) lgkmcnt(2)\n"
    "ds_read_b64_tr_b16 v[28:29], v60 offset:9216\n"
    "v_mfma_f32_16x16x32_bf16 a[0:3], v[20:23], a[144:147], a[0:3]\n"
    "ds_read_b64_tr_b16 v[30:31], v60 offset:13824\n"
    "v_mfma_f32_16x16x32_bf16 a[4:7], v[16:19], a[244:247], a[4:7]\n"
    "v_mfma_f32_16x16x32_bf16 a[4:7], v[20:23], a[148:151], a[4:7]\n"
    "v_mfma_f32_16x16x32_bf16 a[8:11], v[16:19], a[248:251], a[8:11]\n"
    "ds_read_b64_tr_b16 v[32:33], v60 offset:18432\n"
    "v_mfma_f32_16x16x32_bf16 a[8:11], v[20:23], a[152:155], a[8:11]\n"
    "ds_read_b64_tr_b16 v[34:35], v60 offset:23040\n"
    "v_mfma_f32_16x16x32_bf16 a[12:15], v[16:19], a[252:255], a[12:15]\n"
    "ds_read_b64_tr_b16 v[36:37], v60 offset:27648\n"
    "v_mfma_f32_16x16x32_bf16 a[12:15], v[20:23], a[156:159], a[12:15]\n"
    "s_waitcnt vmcnt(12) lgkmcnt(5)\n"
    "ds_read_b64_tr_b16 v[38:39], v60 offset:32256\n"
    "v_mfma_f32_16x16x32_bf16 a[0:3], v[24:27], v[64:67], a[0:3]\n"
    "s_waitcnt vmcnt(8) lgkmcnt(4)\n"
    "buffer_load_dwordx2 v[112:113], v12, s[24:27], 0 offen offset:0\n"
    "v_mfma_f32_16x16x32_bf16 a[0:3], v[28:31], v[80:83], a[0:3]\n"
    "v_mfma_f32_16x16x32_bf16 a[4:7], v[24:27], v[68:71], a[4:7]\n"
    "v_mfma_f32_16x16x32_bf16 a[4:7], v[28:31], v[84:87], a[4:7]\n"
    "v_mfma_f32_16x16x32_bf16 a[8:11], v[24:27], v[72:75], a[8:11]\n"
    "ds_read_b64_tr_b16 v[48:49], v60 offset:55328\n"
    "v_mfma_f32_16x16x32_bf16 a[8:11], v[28:31], v[88:91], a[8:11]\n"
    "ds_read_b64_tr_b16 v[50:51], v60 offset:59936\n"
    "v_mfma_f32_16x16x32_bf16 a[12:15], v[24:27], v[76:79], a[12:15]\n"
    "ds_read_b64_tr_b16 v[52:53], v61 offset:32\n"
    "v_mfma_f32_16x16x32_bf16 a[12:15], v[28:31], v[92:95], a[12:15]\n"
    "s_waitcnt vmcnt(5) lgkmcnt(5)\n"
    "ds_read_b64_tr_b16 v[54:55], v61 offset:4640\n"
    "v_mfma_f32_16x16x32_bf16 a[0:3], v[32:35], a[160:163], a[0:3]\n"
    "s_waitcnt vmcnt(1) lgkmcnt(4)\n"
    "buffer_load_dwordx2 v[114:115], v63, s[24:27], 0 offen offset:0\n"
    "v_mfma_f32_16x16x32_bf16 a[0:3], v[36:39], a[176:179], a[0:3]\n"
    "v_mfma_f32_16x16x32_bf16 a[4:7], v[32:35], a[164:167], a[4:7]\n"
    "v_mfma_f32_16x16x32_bf16 a[4:7], v[36:39], a[180:183], a[4:7]\n"
    "v_mfma_f32_16x16x32_bf16 a[8:11], v[32:35], a[168:171], a[8:11]\n"
    "ds_read_b64_tr_b16 v[56:57], v60 offset:32\n"
    "v_mfma_f32_16x16x32_bf16 a[8:11], v[36:39], a[184:187], a[8:11]\n"
    "ds_read_b64_tr_b16 v[58:59], v60 offset:4640\n"
    "v_mfma_f32_16x16x32_bf16 a[12:15], v[32:35], a[172:175], a[12:15]\n"
    "ds_read_b64_tr_b16 v[16:17], v60 offset:9248\n"
    "v_mfma_f32_16x16x32_bf16 a[12:15], v[36:39], a[188:191], a[12:15]\n"
    "s_waitcnt lgkmcnt(5)\n"
    "ds_read_b64_tr_b16 v[18:19], v60 offset:13856\n"
    "v_mfma_f32_16x16x32_bf16 a[16:19], v[48:51], a[240:243], a[16:19]\n"
    "s_waitcnt lgkmcnt(4)\n"
    "buffer_load_dwordx2 v[116:117], v12, s[24:27], 0 offen offset:32\n"
    "v_mfma_f32_16x16x32_bf16 a[16:19], v[52:55], a[144:147], a[16:19]\n"
    "v_mfma_f32_16x16x32_bf16 a[20:23], v[48:51], a[244:247], a[20:23]\n"
    "v_accvgpr_read_b32 v96, a0\n"
    "v_accvgpr_read_b32 v97, a1\n"
    "v_mfma_f32_16x16x32_bf16 a[20:23], v[52:55], a[148:151], a[20:23]\n"
    "v_accvgpr_read_b32 v98, a2\n"
    "v_accvgpr_read_b32 v99, a3\n"
    "v_mfma_f32_16x16x32_bf16 a[24:27], v[48:51], a[248:251], a[24:27]\n"
    "ds_read_b64_tr_b16 v[20:21], v60 offset:18464\n"
    "v_accvgpr_read_b32 v100, a4\n"
    "v_mfma_f32_16x16x32_bf16 a[24:27], v[52:55], a[152:155], a[24:27]\n"
    "ds_read_b64_tr_b16 v[22:23], v60 offset:23072\n"
    "v_accvgpr_read_b32 v101, a5\n"
    "v_mfma_f32_16x16x32_bf16 a[28:31], v[48:51], a[252:255], a[28:31]\n"
    "ds_read_b64_tr_b16 v[24:25], v60 offset:27680\n"
    "v_accvgpr_read_b32 v102, a6\n"
    "v_mfma_f32_16x16x32_bf16 a[28:31], v[52:55], a[156:159], a[28:31]\n"
    "s_waitcnt lgkmcnt(5)\n"
    "ds_read_b64_tr_b16 v[26:27], v60 offset:32288\n"
    "v_accvgpr_read_b32 v103, a7\n"
    "v_mfma_f32_16x16x32_bf16 a[16:19], v[56:59], v[64:67], a[16:19]\n"
    "s_waitcnt lgkmcnt(4)\n"
    "buffer_load_dwordx2 v[118:119], v63, s[24:27], 0 offen offset:32\n"
    "v_cvt_pk_bf16_f32 v96, v96, v97\n"
    "v_mfma_f32_16x16x32_bf16 a[16:19], v[16:19], v[80:83], a[16:19]\n"
    "v_cvt_pk_bf16_f32 v97, v98, v99\n"
    "v_cvt_pk_bf16_f32 v98, v100, v101\n"
    "v_mfma_f32_16x16x32_bf16 a[20:23], v[56:59], v[68:71], a[20:23]\n"
    "v_cvt_pk_bf16_f32 v99, v102, v103\n"
    "v_accvgpr_read_b32 v104, a8\n"
    "v_mfma_f32_16x16x32_bf16 a[20:23], v[16:19], v[84:87], a[20:23]\n"
    "buffer_store_dwordx4 v[96:99], v0, s[72:75], s14 offen offset:0 sc1\n"
    "v_accvgpr_read_b32 v105, a9\n"
    "v_mfma_f32_16x16x32_bf16 a[24:27], v[56:59], v[72:75], a[24:27]\n"
    "ds_read_b64_tr_b16 v[28:29], v60 offset:55360\n"
    "v_accvgpr_read_b32 v106, a10\n"
    "v_mfma_f32_16x16x32_bf16 a[24:27], v[16:19], v[88:91], a[24:27]\n"
    "ds_read_b64_tr_b16 v[30:31], v60 offset:59968\n"
    "v_accvgpr_read_b32 v107, a11\n"
    "v_mfma_f32_16x16x32_bf16 a[28:31], v[56:59], v[76:79], a[28:31]\n"
    "ds_read_b64_tr_b16 v[32:33], v61 offset:64\n"
    "v_accvgpr_read_b32 v108, a12\n"
    "v_mfma_f32_16x16x32_bf16 a[28:31], v[16:19], v[92:95], a[28:31]\n"
    "s_waitcnt lgkmcnt(5)\n"
    "ds_read_b64_tr_b16 v[34:35], v61 offset:4672\n"
    "v_accvgpr_read_b32 v109, a13\n"
    "v_mfma_f32_16x16x32_bf16 a[16:19], v[20:23], a[160:163], a[16:19]\n"
    "s_waitcnt lgkmcnt(4)\n"
    "buffer_load_dwordx2 v[120:121], v12, s[24:27], 0 offen offset:64\n"
    "v_accvgpr_read_b32 v110, a14\n"
    "v_mfma_f32_16x16x32_bf16 a[16:19], v[24:27], a[176:179], a[16:19]\n"
    "v_accvgpr_read_b32 v111, a15\n"
    "v_cvt_pk_bf16_f32 v104, v104, v105\n"
    "v_mfma_f32_16x16x32_bf16 a[20:23], v[20:23], a[164:167], a[20:23]\n"
    "v_cvt_pk_bf16_f32 v105, v106, v107\n"
    "v_cvt_pk_bf16_f32 v106, v108, v109\n"
    "v_mfma_f32_16x16x32_bf16 a[20:23], v[24:27], a[180:183], a[20:23]\n"
    "v_cvt_pk_bf16_f32 v107, v110, v111\n"
    "v_mfma_f32_16x16x32_bf16 a[24:27], v[20:23], a[168:171], a[24:27]\n"
    "ds_read_b64_tr_b16 v[36:37], v60 offset:64\n"
    "buffer_store_dwordx4 v[104:107], v0, s[72:75], s15 offen offset:0 sc1\n"
    "v_mfma_f32_16x16x32_bf16 a[24:27], v[24:27], a[184:187], a[24:27]\n"
    "ds_read_b64_tr_b16 v[38:39], v60 offset:4672\n"
    "ds_write_b128 v9, v[104:107] offset:0\n"
    "v_mfma_f32_16x16x32_bf16 a[28:31], v[20:23], a[172:175], a[28:31]\n"
    "ds_read_b64_tr_b16 v[48:49], v60 offset:9280\n"
    "v_mfma_f32_16x16x32_bf16 a[28:31], v[24:27], a[188:191], a[28:31]\n"
    "s_waitcnt lgkmcnt(6)\n"
    "ds_read_b64_tr_b16 v[50:51], v60 offset:13888\n"
    "v_mfma_f32_16x16x32_bf16 a[32:35], v[28:31], a[240:243], a[32:35]\n"
    "s_waitcnt lgkmcnt(5)\n"
    "buffer_load_dwordx2 v[122:123], v63, s[24:27], 0 offen offset:64\n"
    "v_mfma_f32_16x16x32_bf16 a[32:35], v[32:35], a[144:147], a[32:35]\n"
    "v_mfma_f32_16x16x32_bf16 a[36:39], v[28:31], a[244:247], a[36:39]\n"
    "v_accvgpr_read_b32 v96, a16\n"
    "v_accvgpr_read_b32 v97, a17\n"
    "v_mfma_f32_16x16x32_bf16 a[36:39], v[32:35], a[148:151], a[36:39]\n"
    "v_accvgpr_read_b32 v98, a18\n"
    "v_accvgpr_read_b32 v99, a19\n"
    "v_mfma_f32_16x16x32_bf16 a[40:43], v[28:31], a[248:251], a[40:43]\n"
    "ds_read_b64_tr_b16 v[52:53], v60 offset:18496\n"
    "v_accvgpr_read_b32 v100, a20\n"
    "v_mfma_f32_16x16x32_bf16 a[40:43], v[32:35], a[152:155], a[40:43]\n"
    "ds_read_b64_tr_b16 v[54:55], v60 offset:23104\n"
    "v_accvgpr_read_b32 v101, a21\n"
    "v_mfma_f32_16x16x32_bf16 a[44:47], v[28:31], a[252:255], a[44:47]\n"
    "ds_read_b64_tr_b16 v[56:57], v60 offset:27712\n"
    "v_accvgpr_read_b32 v102, a22\n"
    "v_mfma_f32_16x16x32_bf16 a[44:47], v[32:35], a[156:159], a[44:47]\n"
    "s_waitcnt lgkmcnt(6)\n"
    "ds_read_b64_tr_b16 v[58:59], v60 offset:32320\n"
    "v_accvgpr_read_b32 v103, a23\n"
    "v_mfma_f32_16x16x32_bf16 a[32:35], v[36:39], v[64:67], a[32:35]\n"
    "s_waitcnt lgkmcnt(4)\n"
    "v_cvt_pk_bf16_f32 v96, v96, v97\n"
    "v_cvt_pk_bf16_f32 v97, v98, v99\n"
    "v_mfma_f32_16x16x32_bf16 a[32:35], v[48:51], v[80:83], a[32:35]\n"
    "v_cvt_pk_bf16_f32 v98, v100, v101\n"
    "v_cvt_pk_bf16_f32 v99, v102, v103\n"
    "v_mfma_f32_16x16x32_bf16 a[36:39], v[36:39], v[68:71], a[36:39]\n"
    "buffer_store_dwordx4 v[96:99], v0, s[72:75], s14 offen offset:1024 sc1\n"
    "v_accvgpr_read_b32 v104, a24\n"
    "v_mfma_f32_16x16x32_bf16 a[36:39], v[48:51], v[84:87], a[36:39]\n"
    "v_accvgpr_read_b32 v105, a25\n"
    "v_accvgpr_read_b32 v106, a26\n"
    "v_mfma_f32_16x16x32_bf16 a[40:43], v[36:39], v[72:75], a[40:43]\n"
    "ds_read_b64_tr_b16 v[16:17], v60 offset:55392\n"
    "v_accvgpr_read_b32 v107, a27\n"
    "v_mfma_f32_16x16x32_bf16 a[40:43], v[48:51], v[88:91], a[40:43]\n"
    "ds_read_b64_tr_b16 v[18:19], v60 offset:60000\n"
    "v_accvgpr_read_b32 v108, a28\n"
    "v_mfma_f32_16x16x32_bf16 a[44:47], v[36:39], v[76:79], a[44:47]\n"
    "ds_read_b64_tr_b16 v[20:21], v61 offset:96\n"
    "v_accvgpr_read_b32 v109, a29\n"
    "v_mfma_f32_16x16x32_bf16 a[44:47], v[48:51], v[92:95], a[44:47]\n"
    "s_waitcnt lgkmcnt(5)\n"
    "ds_read_b64_tr_b16 v[22:23], v61 offset:4704\n"
    "v_accvgpr_read_b32 v110, a30\n"
    "v_mfma_f32_16x16x32_bf16 a[32:35], v[52:55], a[160:163], a[32:35]\n"
    "s_waitcnt lgkmcnt(4)\n"
    "v_accvgpr_read_b32 v111, a31\n"
    "v_cvt_pk_bf16_f32 v104, v104, v105\n"
    "v_mfma_f32_16x16x32_bf16 a[32:35], v[56:59], a[176:179], a[32:35]\n"
    "v_cvt_pk_bf16_f32 v105, v106, v107\n"
    "v_cvt_pk_bf16_f32 v106, v108, v109\n"
    "v_mfma_f32_16x16x32_bf16 a[36:39], v[52:55], a[164:167], a[36:39]\n"
    "v_cvt_pk_bf16_f32 v107, v110, v111\n"
    "v_mfma_f32_16x16x32_bf16 a[36:39], v[56:59], a[180:183], a[36:39]\n"
    "buffer_store_dwordx4 v[104:107], v0, s[72:75], s15 offen offset:1024 sc1\n"
    "ds_write_b128 v9, v[104:107] offset:1024\n"
    "v_mfma_f32_16x16x32_bf16 a[40:43], v[52:55], a[168:171], a[40:43]\n"
    "ds_read_b64_tr_b16 v[24:25], v60 offset:96\n"
    "v_mfma_f32_16x16x32_bf16 a[40:43], v[56:59], a[184:187], a[40:43]\n"
    "ds_read_b64_tr_b16 v[26:27], v60 offset:4704\n"
    "v_mfma_f32_16x16x32_bf16 a[44:47], v[52:55], a[172:175], a[44:47]\n"
    "ds_read_b64_tr_b16 v[28:29], v60 offset:9312\n"
    "v_mfma_f32_16x16x32_bf16 a[44:47], v[56:59], a[188:191], a[44:47]\n"
    "s_waitcnt lgkmcnt(6)\n"
    "ds_read_b64_tr_b16 v[30:31], v60 offset:13920\n"
    "v_mfma_f32_16x16x32_bf16 a[48:51], v[16:19], a[240:243], a[48:51]\n"
    "s_waitcnt lgkmcnt(5)\n"
    "v_mfma_f32_16x16x32_bf16 a[48:51], v[20:23], a[144:147], a[48:51]\n"
    "v_mfma_f32_16x16x32_bf16 a[52:55], v[16:19], a[244:247], a[52:55]\n"
    "v_accvgpr_read_b32 v96, a32\n"
    "v_accvgpr_read_b32 v97, a33\n"
    "v_mfma_f32_16x16x32_bf16 a[52:55], v[20:23], a[148:151], a[52:55]\n"
    "v_accvgpr_read_b32 v98, a34\n"
    "v_accvgpr_read_b32 v99, a35\n"
    "v_mfma_f32_16x16x32_bf16 a[56:59], v[16:19], a[248:251], a[56:59]\n"
    "ds_read_b64_tr_b16 v[32:33], v60 offset:18528\n"
    "v_accvgpr_read_b32 v100, a36\n"
    "v_mfma_f32_16x16x32_bf16 a[56:59], v[20:23], a[152:155], a[56:59]\n"
    "ds_read_b64_tr_b16 v[34:35], v60 offset:23136\n"
    "v_accvgpr_read_b32 v101, a37\n"
    "v_mfma_f32_16x16x32_bf16 a[60:63], v[16:19], a[252:255], a[60:63]\n"
    "ds_read_b64_tr_b16 v[36:37], v60 offset:27744\n"
    "v_accvgpr_read_b32 v102, a38\n"
    "v_mfma_f32_16x16x32_bf16 a[60:63], v[20:23], a[156:159], a[60:63]\n"
    "s_waitcnt lgkmcnt(5)\n"
    "ds_read_b64_tr_b16 v[38:39], v60 offset:32352\n"
    "v_accvgpr_read_b32 v103, a39\n"
    "v_mfma_f32_16x16x32_bf16 a[48:51], v[24:27], v[64:67], a[48:51]\n"
    "s_waitcnt lgkmcnt(4)\n"
    "v_cvt_pk_bf16_f32 v96, v96, v97\n"
    "v_cvt_pk_bf16_f32 v97, v98, v99\n"
    "v_mfma_f32_16x16x32_bf16 a[48:51], v[28:31], v[80:83], a[48:51]\n"
    "v_cvt_pk_bf16_f32 v98, v100, v101\n"
    "v_cvt_pk_bf16_f32 v99, v102, v103\n"
    "v_mfma_f32_16x16x32_bf16 a[52:55], v[24:27], v[68:71], a[52:55]\n"
    "buffer_store_dwordx4 v[96:99], v0, s[72:75], s14 offen offset:2048 sc1\n"
    "v_accvgpr_read_b32 v104, a40\n"
    "v_mfma_f32_16x16x32_bf16 a[52:55], v[28:31], v[84:87], a[52:55]\n"
    "v_accvgpr_read_b32 v105, a41\n"
    "v_accvgpr_read_b32 v106, a42\n"
    "v_mfma_f32_16x16x32_bf16 a[56:59], v[24:27], v[72:75], a[56:59]\n"
    "ds_read_b64_tr_b16 v[48:49], v60 offset:55424\n"
    "v_accvgpr_read_b32 v107, a43\n"
    "v_mfma_f32_16x16x32_bf16 a[56:59], v[28:31], v[88:91], a[56:59]\n"
    "ds_read_b64_tr_b16 v[50:51], v60 offset:60032\n"
    "v_accvgpr_read_b32 v108, a44\n"
    "v_mfma_f32_16x16x32_bf16 a[60:63], v[24:27], v[76:79], a[60:63]\n"
    "ds_read_b64_tr_b16 v[52:53], v61 offset:128\n"
    "v_accvgpr_read_b32 v109, a45\n"
    "v_mfma_f32_16x16x32_bf16 a[60:63], v[28:31], v[92:95], a[60:63]\n"
    "s_waitcnt lgkmcnt(5)\n"
    "ds_read_b64_tr_b16 v[54:55], v61 offset:4736\n"
    "v_accvgpr_read_b32 v110, a46\n"
    "v_mfma_f32_16x16x32_bf16 a[48:51], v[32:35], a[160:163], a[48:51]\n"
    "s_waitcnt lgkmcnt(4)\n"
    "v_accvgpr_read_b32 v111, a47\n"
    "v_cvt_pk_bf16_f32 v104, v104, v105\n"
    "v_mfma_f32_16x16x32_bf16 a[48:51], v[36:39], a[176:179], a[48:51]\n"
    "v_cvt_pk_bf16_f32 v105, v106, v107\n"
    "v_cvt_pk_bf16_f32 v106, v108, v109\n"
    "v_mfma_f32_16x16x32_bf16 a[52:55], v[32:35], a[164:167], a[52:55]\n"
    "v_cvt_pk_bf16_f32 v107, v110, v111\n"
    "v_mfma_f32_16x16x32_bf16 a[52:55], v[36:39], a[180:183], a[52:55]\n"
    "buffer_store_dwordx4 v[104:107], v0, s[72:75], s15 offen offset:2048 sc1\n"
    "ds_write_b128 v9, v[104:107] offset:2048\n"
    "v_mfma_f32_16x16x32_bf16 a[56:59], v[32:35], a[168:171], a[56:59]\n"
    "ds_read_b64_tr_b16 v[56:57], v60 offset:128\n"
    "v_mfma_f32_16x16x32_bf16 a[56:59], v[36:39], a[184:187], a[56:59]\n"
    "ds_read_b64_tr_b16 v[58:59], v60 offset:4736\n"
    "v_mfma_f32_16x16x32_bf16 a[60:63], v[32:35], a[172:175], a[60:63]\n"
    "ds_read_b64_tr_b16 v[16:17], v60 offset:9344\n"
    "v_mfma_f32_16x16x32_bf16 a[60:63], v[36:39], a[188:191], a[60:63]\n"
    "s_waitcnt lgkmcnt(6)\n"
    "ds_read_b64_tr_b16 v[18:19], v60 offset:13952\n"
    "v_mfma_f32_16x16x32_bf16 a[64:67], v[48:51], a[240:243], a[64:67]\n"
    "s_waitcnt lgkmcnt(5)\n"
    "v_mfma_f32_16x16x32_bf16 a[64:67], v[52:55], a[144:147], a[64:67]\n"
    "v_mfma_f32_16x16x32_bf16 a[68:71], v[48:51], a[244:247], a[68:71]\n"
    "v_accvgpr_read_b32 v96, a48\n"
    "v_accvgpr_read_b32 v97, a49\n"
    "v_mfma_f32_16x16x32_bf16 a[68:71], v[52:55], a[148:151], a[68:71]\n"
    "v_accvgpr_read_b32 v98, a50\n"
    "v_accvgpr_read_b32 v99, a51\n"
    "v_mfma_f32_16x16x32_bf16 a[72:75], v[48:51], a[248:251], a[72:75]\n"
    "ds_read_b64_tr_b16 v[20:21], v60 offset:18560\n"
    "v_accvgpr_read_b32 v100, a52\n"
    "v_mfma_f32_16x16x32_bf16 a[72:75], v[52:55], a[152:155], a[72:75]\n"
    "ds_read_b64_tr_b16 v[22:23], v60 offset:23168\n"
    "v_accvgpr_read_b32 v101, a53\n"
    "v_mfma_f32_16x16x32_bf16 a[76:79], v[48:51], a[252:255], a[76:79]\n"
    "ds_read_b64_tr_b16 v[24:25], v60 offset:27776\n"
    "v_accvgpr_read_b32 v102, a54\n"
    "v_mfma_f32_16x16x32_bf16 a[76:79], v[52:55], a[156:159], a[76:79]\n"
    "s_waitcnt lgkmcnt(5)\n"
    "ds_read_b64_tr_b16 v[26:27], v60 offset:32384\n"
    "v_accvgpr_read_b32 v103, a55\n"
    "v_mfma_f32_16x16x32_bf16 a[64:67], v[56:59], v[64:67], a[64:67]\n"
    "s_waitcnt lgkmcnt(4)\n"
    "v_cvt_pk_bf16_f32 v96, v96, v97\n"
    "v_cvt_pk_bf16_f32 v97, v98, v99\n"
    "v_mfma_f32_16x16x32_bf16 a[64:67], v[16:19], v[80:83], a[64:67]\n"
    "v_cvt_pk_bf16_f32 v98, v100, v101\n"
    "v_cvt_pk_bf16_f32 v99, v102, v103\n"
    "v_mfma_f32_16x16x32_bf16 a[68:71], v[56:59], v[68:71], a[68:71]\n"
    "buffer_store_dwordx4 v[96:99], v0, s[72:75], s14 offen offset:3072 sc1\n"
    "v_accvgpr_read_b32 v104, a56\n"
    "v_mfma_f32_16x16x32_bf16 a[68:71], v[16:19], v[84:87], a[68:71]\n"
    "v_accvgpr_read_b32 v105, a57\n"
    "v_accvgpr_read_b32 v106, a58\n"
    "v_mfma_f32_16x16x32_bf16 a[72:75], v[56:59], v[72:75], a[72:75]\n"
    "ds_read_b64_tr_b16 v[28:29], v60 offset:55456\n"
    "v_accvgpr_read_b32 v107, a59\n"
    "v_mfma_f32_16x16x32_bf16 a[72:75], v[16:19], v[88:91], a[72:75]\n"
    "ds_read_b64_tr_b16 v[30:31], v60 offset:60064\n"
    "v_accvgpr_read_b32 v108, a60\n"
    "v_mfma_f32_16x16x32_bf16 a[76:79], v[56:59], v[76:79], a[76:79]\n"
    "ds_read_b64_tr_b16 v[32:33], v61 offset:160\n"
    "v_accvgpr_read_b32 v109, a61\n"
    "v_mfma_f32_16x16x32_bf16 a[76:79], v[16:19], v[92:95], a[76:79]\n"
    "s_waitcnt lgkmcnt(5)\n"
    "ds_read_b64_tr_b16 v[34:35], v61 offset:4768\n"
    "v_accvgpr_read_b32 v110, a62\n"
    "v_mfma_f32_16x16x32_bf16 a[64:67], v[20:23], a[160:163], a[64:67]\n"
    "s_waitcnt lgkmcnt(4)\n"
    "v_accvgpr_read_b32 v111, a63\n"
    "v_cvt_pk_bf16_f32 v104, v104, v105\n"
    "v_mfma_f32_16x16x32_bf16 a[64:67], v[24:27], a[176:179], a[64:67]\n"
    "v_cvt_pk_bf16_f32 v105, v106, v107\n"
    "v_cvt_pk_bf16_f32 v106, v108, v109\n"
    "v_mfma_f32_16x16x32_bf16 a[68:71], v[20:23], a[164:167], a[68:71]\n"
    "v_cvt_pk_bf16_f32 v107, v110, v111\n"
    "v_mfma_f32_16x16x32_bf16 a[68:71], v[24:27], a[180:183], a[68:71]\n"
    "buffer_store_dwordx4 v[104:107], v0, s[72:75], s15 offen offset:3072 sc1\n"
    "ds_write_b128 v9, v[104:107] offset:3072\n"
    "v_mfma_f32_16x16x32_bf16 a[72:75], v[20:23], a[168:171], a[72:75]\n"
    "ds_read_b64_tr_b16 v[36:37], v60 offset:160\n"
    "v_mfma_f32_16x16x32_bf16 a[72:75], v[24:27], a[184:187], a[72:75]\n"
    "ds_read_b64_tr_b16 v[38:39], v60 offset:4768\n"
    "v_mfma_f32_16x16x32_bf16 a[76:79], v[20:23], a[172:175], a[76:79]\n"
    "ds_read_b64_tr_b16 v[48:49], v60 offset:9376\n"
    "v_mfma_f32_16x16x32_bf16 a[76:79], v[24:27], a[188:191], a[76:79]\n"
    "s_waitcnt lgkmcnt(6)\n"
    "ds_read_b64_tr_b16 v[50:51], v60 offset:13984\n"
    "v_mfma_f32_16x16x32_bf16 a[80:83], v[28:31], a[240:243], a[80:83]\n"
    "s_waitcnt lgkmcnt(5)\n"
    "v_mfma_f32_16x16x32_bf16 a[80:83], v[32:35], a[144:147], a[80:83]\n"
    "v_mfma_f32_16x16x32_bf16 a[84:87], v[28:31], a[244:247], a[84:87]\n"
    "s_add_u32 s14, s14, 4096\n"
    "s_add_u32 s15, s15, 4096\n"
    "v_accvgpr_read_b32 v96, a64\n"
    "v_accvgpr_read_b32 v97, a65\n"
    "v_mfma_f32_16x16x32_bf16 a[84:87], v[32:35], a[148:151], a[84:87]\n"
    "v_accvgpr_read_b32 v98, a66\n"
    "v_accvgpr_read_b32 v99, a67\n"
    "v_mfma_f32_16x16x32_bf16 a[88:91], v[28:31], a[248:251], a[88:91]\n"
    "ds_read_b64_tr_b16 v[52:53], v60 offset:18592\n"
    "v_accvgpr_read_b32 v100, a68\n"
    "v_mfma_f32_16x16x32_bf16 a[88:91], v[32:35], a[152:155], a[88:91]\n"
    "ds_read_b64_tr_b16 v[54:55], v60 offset:23200\n"
    "v_accvgpr_read_b32 v101, a69\n"
    "v_mfma_f32_16x16x32_bf16 a[92:95], v[28:31], a[252:255], a[92:95]\n"
    "ds_read_b64_tr_b16 v[56:57], v60 offset:27808\n"
    "v_accvgpr_read_b32 v102, a70\n"
    "v_mfma_f32_16x16x32_bf16 a[92:95], v[32:35], a[156:159], a[92:95]\n"
    "s_waitcnt lgkmcnt(5)\n"
    "ds_read_b64_tr_b16 v[58:59], v60 offset:32416\n"
    "v_accvgpr_read_b32 v103, a71\n"
    "v_mfma_f32_16x16x32_bf16 a[80:83], v[36:39], v[64:67], a[80:83]\n"
    "s_waitcnt lgkmcnt(4)\n"
    "v_cvt_pk_bf16_f32 v96, v96, v97\n"
    "v_cvt_pk_bf16_f32 v97, v98, v99\n"
    "v_mfma_f32_16x16x32_bf16 a[80:83], v[48:51], v[80:83], a[80:83]\n"
    "v_cvt_pk_bf16_f32 v98, v100, v101\n"
    "v_cvt_pk_bf16_f32 v99, v102, v103\n"
    "v_mfma_f32_16x16x32_bf16 a[84:87], v[36:39], v[68:71], a[84:87]\n"
    "buffer_store_dwordx4 v[96:99], v0, s[72:75], s14 offen offset:0 sc1\n"
    "v_accvgpr_read_b32 v104, a72\n"
    "v_mfma_f32_16x16x32_bf16 a[84:87], v[48:51], v[84:87], a[84:87]\n"
    "v_accvgpr_read_b32 v105, a73\n"
    "v_accvgpr_read_b32 v106, a74\n"
    "v_mfma_f32_16x16x32_bf16 a[88:91], v[36:39], v[72:75], a[88:91]\n"
    "ds_read_b64_tr_b16 v[16:17], v60 offset:55488\n"
    "v_accvgpr_read_b32 v107, a75\n"
    "v_mfma_f32_16x16x32_bf16 a[88:91], v[48:51], v[88:91], a[88:91]\n"
    "ds_read_b64_tr_b16 v[18:19], v60 offset:60096\n"
    "v_accvgpr_read_b32 v108, a76\n"
    "v_mfma_f32_16x16x32_bf16 a[92:95], v[36:39], v[76:79], a[92:95]\n"
    "ds_read_b64_tr_b16 v[20:21], v61 offset:192\n"
    "v_accvgpr_read_b32 v109, a77\n"
    "v_mfma_f32_16x16x32_bf16 a[92:95], v[48:51], v[92:95], a[92:95]\n"
    "s_waitcnt lgkmcnt(5)\n"
    "ds_read_b64_tr_b16 v[22:23], v61 offset:4800\n"
    "v_accvgpr_read_b32 v110, a78\n"
    "v_mfma_f32_16x16x32_bf16 a[80:83], v[52:55], a[160:163], a[80:83]\n"
    "s_waitcnt lgkmcnt(4)\n"
    "v_accvgpr_read_b32 v111, a79\n"
    "v_cvt_pk_bf16_f32 v104, v104, v105\n"
    "v_mfma_f32_16x16x32_bf16 a[80:83], v[56:59], a[176:179], a[80:83]\n"
    "v_cvt_pk_bf16_f32 v105, v106, v107\n"
    "v_cvt_pk_bf16_f32 v106, v108, v109\n"
    "v_mfma_f32_16x16x32_bf16 a[84:87], v[52:55], a[164:167], a[84:87]\n"
    "v_cvt_pk_bf16_f32 v107, v110, v111\n"
    "v_mfma_f32_16x16x32_bf16 a[84:87], v[56:59], a[180:183], a[84:87]\n"
    "buffer_store_dwordx4 v[104:107], v0, s[72:75], s15 offen offset:0 sc1\n"
    "ds_write_b128 v9, v[104:107] offset:4096\n"
    "v_mfma_f32_16x16x32_bf16 a[88:91], v[52:55], a[168:171], a[88:91]\n"
    "ds_read_b64_tr_b16 v[24:25], v60 offset:192\n"
    "v_mfma_f32_16x16x32_bf16 a[88:91], v[56:59], a[184:187], a[88:91]\n"
    "ds_read_b64_tr_b16 v[26:27], v60 offset:4800\n"
    "v_mfma_f32_16x16x32_bf16 a[92:95], v[52:55], a[172:175], a[92:95]\n"
    "ds_read_b64_tr_b16 v[28:29], v60 offset:9408\n"
    "v_mfma_f32_16x16x32_bf16 a[92:95], v[56:59], a[188:191], a[92:95]\n"
    "s_waitcnt lgkmcnt(6)\n"
    "ds_read_b64_tr_b16 v[30:31], v60 offset:14016\n"
    "v_mfma_f32_16x16x32_bf16 a[96:99], v[16:19], a[240:243], a[96:99]\n"
    "s_waitcnt lgkmcnt(5)\n"
    "v_mfma_f32_16x16x32_bf16 a[96:99], v[20:23], a[144:147], a[96:99]\n"
    "v_add_u32 v46, 1, v46\n"
    "v_mfma_f32_16x16x32_bf16 a[100:103], v[16:19], a[244:247], a[100:103]\n"
    "s_waitcnt vmcnt(4)\n"
    "buffer_store_dwordx2 v[46:47], v11, s[76:79], 0 offen sc1\n"
    "v_accvgpr_read_b32 v96, a80\n"
    "v_mfma_f32_16x16x32_bf16 a[100:103], v[20:23], a[148:151], a[100:103]\n"
    "v_accvgpr_read_b32 v97, a81\n"
    "v_accvgpr_read_b32 v98, a82\n"
    "v_mfma_f32_16x16x32_bf16 a[104:107], v[16:19], a[248:251], a[104:107]\n"
    "ds_read_b64_tr_b16 v[32:33], v60 offset:18624\n"
    "v_accvgpr_read_b32 v99, a83\n"
    "v_mfma_f32_16x16x32_bf16 a[104:107], v[20:23], a[152:155], a[104:107]\n"
    "ds_read_b64_tr_b16 v[34:35], v60 offset:23232\n"
    "v_accvgpr_read_b32 v100, a84\n"
    "v_mfma_f32_16x16x32_bf16 a[108:111], v[16:19], a[252:255], a[108:111]\n"
    "ds_read_b64_tr_b16 v[36:37], v60 offset:27840\n"
    "v_accvgpr_read_b32 v101, a85\n"
    "v_mfma_f32_16x16x32_bf16 a[108:111], v[20:23], a[156:159], a[108:111]\n"
    "s_waitcnt lgkmcnt(5)\n"
    "ds_read_b64_tr_b16 v[38:39], v60 offset:32448\n"
    "v_accvgpr_read_b32 v102, a86\n"
    "v_mfma_f32_16x16x32_bf16 a[96:99], v[24:27], v[64:67], a[96:99]\n"
    "s_waitcnt lgkmcnt(4)\n"
    "v_accvgpr_read_b32 v103, a87\n"
    "v_cvt_pk_bf16_f32 v96, v96, v97\n"
    "v_mfma_f32_16x16x32_bf16 a[96:99], v[28:31], v[80:83], a[96:99]\n"
    "v_cvt_pk_bf16_f32 v97, v98, v99\n"
    "v_cvt_pk_bf16_f32 v98, v100, v101\n"
    "v_mfma_f32_16x16x32_bf16 a[100:103], v[24:27], v[68:71], a[100:103]\n"
    "v_cvt_pk_bf16_f32 v99, v102, v103\n"
    "v_accvgpr_read_b32 v104, a88\n"
    "v_mfma_f32_16x16x32_bf16 a[100:103], v[28:31], v[84:87], a[100:103]\n"
    "buffer_store_dwordx4 v[96:99], v0, s[72:75], s14 offen offset:1024 sc1\n"
    "v_accvgpr_read_b32 v105, a89\n"
    "v_mfma_f32_16x16x32_bf16 a[104:107], v[24:27], v[72:75], a[104:107]\n"
    "ds_read_b64_tr_b16 v[48:49], v60 offset:55520\n"
    "v_accvgpr_read_b32 v106, a90\n"
    "v_mfma_f32_16x16x32_bf16 a[104:107], v[28:31], v[88:91], a[104:107]\n"
    "ds_read_b64_tr_b16 v[50:51], v60 offset:60128\n"
    "v_accvgpr_read_b32 v107, a91\n"
    "v_mfma_f32_16x16x32_bf16 a[108:111], v[24:27], v[76:79], a[108:111]\n"
    "ds_read_b64_tr_b16 v[52:53], v61 offset:224\n"
    "v_accvgpr_read_b32 v108, a92\n"
    "v_mfma_f32_16x16x32_bf16 a[108:111], v[28:31], v[92:95], a[108:111]\n"
    "s_waitcnt lgkmcnt(5)\n"
    "ds_read_b64_tr_b16 v[54:55], v61 offset:4832\n"
    "v_accvgpr_read_b32 v109, a93\n"
    "v_mfma_f32_16x16x32_bf16 a[96:99], v[32:35], a[160:163], a[96:99]\n"
    "s_waitcnt lgkmcnt(4)\n"
    "v_accvgpr_read_b32 v110, a94\n"
    "v_accvgpr_read_b32 v111, a95\n"
    "v_mfma_f32_16x16x32_bf16 a[96:99], v[36:39], a[176:179], a[96:99]\n"
    "v_cvt_pk_bf16_f32 v104, v104, v105\n"
    "v_cvt_pk_bf16_f32 v105, v106, v107\n"
    "v_mfma_f32_16x16x32_bf16 a[100:103], v[32:35], a[164:167], a[100:103]\n"
    "v_cvt_pk_bf16_f32 v106, v108, v109\n"
    "v_cvt_pk_bf16_f32 v107, v110, v111\n"
    "v_mfma_f32_16x16x32_bf16 a[100:103], v[36:39], a[180:183], a[100:103]\n"
    "buffer_store_dwordx4 v[104:107], v0, s[72:75], s15 offen offset:1024 sc1\n"
    "ds_write_b128 v9, v[104:107] offset:5120\n"
    "v_mfma_f32_16x16x32_bf16 a[104:107], v[32:35], a[168:171], a[104:107]\n"
    "ds_read_b64_tr_b16 v[56:57], v60 offset:224\n"
    "v_mfma_f32_16x16x32_bf16 a[104:107], v[36:39], a[184:187], a[104:107]\n"
    "ds_read_b64_tr_b16 v[58:59], v60 offset:4832\n"
    "v_mfma_f32_16x16x32_bf16 a[108:111], v[32:35], a[172:175], a[108:111]\n"
    "ds_read_b64_tr_b16 v[16:17], v60 offset:9440\n"
    "v_mfma_f32_16x16x32_bf16 a[108:111], v[36:39], a[188:191], a[108:111]\n"
    "s_waitcnt lgkmcnt(6)\n"
    "ds_read_b64_tr_b16 v[18:19], v60 offset:14048\n"
    "v_mfma_f32_16x16x32_bf16 a[112:115], v[48:51], a[240:243], a[112:115]\n"
    "s_waitcnt lgkmcnt(5)\n"
    "v_mfma_f32_16x16x32_bf16 a[112:115], v[52:55], a[144:147], a[112:115]\n"
    "v_mfma_f32_16x16x32_bf16 a[116:119], v[48:51], a[244:247], a[116:119]\n"
    "s_xor_b32 s15, s15, s60\n"
    "v_accvgpr_read_b32 v96, a96\n"
    "v_accvgpr_read_b32 v97, a97\n"
    "v_mfma_f32_16x16x32_bf16 a[116:119], v[52:55], a[148:151], a[116:119]\n"
    "v_accvgpr_read_b32 v98, a98\n"
    "v_accvgpr_read_b32 v99, a99\n"
    "v_mfma_f32_16x16x32_bf16 a[120:123], v[48:51], a[248:251], a[120:123]\n"
    "ds_read_b64_tr_b16 v[20:21], v60 offset:18656\n"
    "v_accvgpr_read_b32 v100, a100\n"
    "v_mfma_f32_16x16x32_bf16 a[120:123], v[52:55], a[152:155], a[120:123]\n"
    "ds_read_b64_tr_b16 v[22:23], v60 offset:23264\n"
    "v_accvgpr_read_b32 v101, a101\n"
    "v_mfma_f32_16x16x32_bf16 a[124:127], v[48:51], a[252:255], a[124:127]\n"
    "ds_read_b64_tr_b16 v[24:25], v60 offset:27872\n"
    "v_accvgpr_read_b32 v102, a102\n"
    "v_mfma_f32_16x16x32_bf16 a[124:127], v[52:55], a[156:159], a[124:127]\n"
    "s_waitcnt lgkmcnt(5)\n"
    "ds_read_b64_tr_b16 v[26:27], v60 offset:32480\n"
    "v_accvgpr_read_b32 v103, a103\n"
    "v_mfma_f32_16x16x32_bf16 a[112:115], v[56:59], v[64:67], a[112:115]\n"
    "s_waitcnt lgkmcnt(4)\n"
    "v_cvt_pk_bf16_f32 v96, v96, v97\n"
    "v_cvt_pk_bf16_f32 v97, v98, v99\n"
    "v_mfma_f32_16x16x32_bf16 a[112:115], v[16:19], v[80:83], a[112:115]\n"
    "v_cvt_pk_bf16_f32 v98, v100, v101\n"
    "v_cvt_pk_bf16_f32 v99, v102, v103\n"
    "v_mfma_f32_16x16x32_bf16 a[116:119], v[56:59], v[68:71], a[116:119]\n"
    "buffer_store_dwordx4 v[96:99], v0, s[72:75], s14 offen offset:2048 sc1\n"
    "v_accvgpr_read_b32 v104, a104\n"
    "v_mfma_f32_16x16x32_bf16 a[116:119], v[16:19], v[84:87], a[116:119]\n"
    "v_accvgpr_read_b32 v105, a105\n"
    "v_accvgpr_read_b32 v106, a106\n"
    "v_mfma_f32_16x16x32_bf16 a[120:123], v[56:59], v[72:75], a[120:123]\n"
    "ds_read_b64_tr_b16 v[28:29], v60 offset:55552\n"
    "v_accvgpr_read_b32 v107, a107\n"
    "v_mfma_f32_16x16x32_bf16 a[120:123], v[16:19], v[88:91], a[120:123]\n"
    "ds_read_b64_tr_b16 v[30:31], v60 offset:60160\n"
    "v_accvgpr_read_b32 v108, a108\n"
    "v_mfma_f32_16x16x32_bf16 a[124:127], v[56:59], v[76:79], a[124:127]\n"
    "ds_read_b64_tr_b16 v[32:33], v61 offset:256\n"
    "v_accvgpr_read_b32 v109, a109\n"
    "v_mfma_f32_16x16x32_bf16 a[124:127], v[16:19], v[92:95], a[124:127]\n"
    "s_waitcnt lgkmcnt(5)\n"
    "ds_read_b64_tr_b16 v[34:35], v61 offset:4864\n"
    "v_accvgpr_read_b32 v110, a110\n"
    "v_mfma_f32_16x16x32_bf16 a[112:115], v[20:23], a[160:163], a[112:115]\n"
    "s_waitcnt lgkmcnt(4)\n"
    "v_accvgpr_read_b32 v111, a111\n"
    "v_cvt_pk_bf16_f32 v104, v104, v105\n"
    "v_mfma_f32_16x16x32_bf16 a[112:115], v[24:27], a[176:179], a[112:115]\n"
    "v_cvt_pk_bf16_f32 v105, v106, v107\n"
    "v_cvt_pk_bf16_f32 v106, v108, v109\n"
    "v_mfma_f32_16x16x32_bf16 a[116:119], v[20:23], a[164:167], a[116:119]\n"
    "v_cvt_pk_bf16_f32 v107, v110, v111\n"
    "v_mfma_f32_16x16x32_bf16 a[116:119], v[24:27], a[180:183], a[116:119]\n"
    "buffer_store_dwordx4 v[104:107], v0, s[72:75], s15 offen offset:2048 sc1\n"
    "v_mfma_f32_16x16x32_bf16 a[120:123], v[20:23], a[168:171], a[120:123]\n"
    "ds_read_b64_tr_b16 v[36:37], v60 offset:256\n"
    "v_mfma_f32_16x16x32_bf16 a[120:123], v[24:27], a[184:187], a[120:123]\n"
    "ds_read_b64_tr_b16 v[38:39], v60 offset:4864\n"
    "v_mfma_f32_16x16x32_bf16 a[124:127], v[20:23], a[172:175], a[124:127]\n"
    "ds_read_b64_tr_b16 v[48:49], v60 offset:9472\n"
    "v_mfma_f32_16x16x32_bf16 a[124:127], v[24:27], a[188:191], a[124:127]\n"
    "s_waitcnt lgkmcnt(5)\n"
    "ds_read_b64_tr_b16 v[50:51], v60 offset:14080\n"
    "v_mfma_f32_16x16x32_bf16 a[128:131], v[28:31], a[240:243], a[128:131]\n"
    "s_waitcnt lgkmcnt(4)\n"
    "v_mfma_f32_16x16x32_bf16 a[128:131], v[32:35], a[144:147], a[128:131]\n"
    "v_mfma_f32_16x16x32_bf16 a[132:135], v[28:31], a[244:247], a[132:135]\n"
    "v_accvgpr_read_b32 v96, a112\n"
    "v_accvgpr_read_b32 v97, a113\n"
    "v_mfma_f32_16x16x32_bf16 a[132:135], v[32:35], a[148:151], a[132:135]\n"
    "v_accvgpr_read_b32 v98, a114\n"
    "v_accvgpr_read_b32 v99, a115\n"
    "v_mfma_f32_16x16x32_bf16 a[136:139], v[28:31], a[248:251], a[136:139]\n"
    "ds_read_b64_tr_b16 v[52:53], v60 offset:18688\n"
    "v_accvgpr_read_b32 v100, a116\n"
    "v_mfma_f32_16x16x32_bf16 a[136:139], v[32:35], a[152:155], a[136:139]\n"
    "ds_read_b64_tr_b16 v[54:55], v60 offset:23296\n"
    "v_accvgpr_read_b32 v101, a117\n"
    "v_mfma_f32_16x16x32_bf16 a[140:143], v[28:31], a[252:255], a[140:143]\n"
    "ds_read_b64_tr_b16 v[56:57], v60 offset:27904\n"
    "v_accvgpr_read_b32 v102, a118\n"
    "v_mfma_f32_16x16x32_bf16 a[140:143], v[32:35], a[156:159], a[140:143]\n"
    "s_waitcnt lgkmcnt(5)\n"
    "ds_read_b64_tr_b16 v[58:59], v60 offset:32512\n"
    "v_accvgpr_read_b32 v103, a119\n"
    "v_mfma_f32_16x16x32_bf16 a[128:131], v[36:39], v[64:67], a[128:131]\n"
    "s_waitcnt lgkmcnt(4)\n"
    "v_cvt_pk_bf16_f32 v96, v96, v97\n"
    "v_cvt_pk_bf16_f32 v97, v98, v99\n"
    "v_mfma_f32_16x16x32_bf16 a[128:131], v[48:51], v[80:83], a[128:131]\n"
    "v_cvt_pk_bf16_f32 v98, v100, v101\n"
    "v_cvt_pk_bf16_f32 v99, v102, v103\n"
    "v_mfma_f32_16x16x32_bf16 a[132:135], v[36:39], v[68:71], a[132:135]\n"
    "buffer_store_dwordx4 v[96:99], v0, s[72:75], s14 offen offset:3072 sc1\n"
    "v_accvgpr_read_b32 v104, a120\n"
    "v_mfma_f32_16x16x32_bf16 a[132:135], v[48:51], v[84:87], a[132:135]\n"
    "v_accvgpr_read_b32 v105, a121\n"
    "v_accvgpr_read_b32 v106, a122\n"
    "v_mfma_f32_16x16x32_bf16 a[136:139], v[36:39], v[72:75], a[136:139]\n"
    "v_accvgpr_read_b32 v107, a123\n"
    "v_accvgpr_read_b32 v108, a124\n"
    "v_mfma_f32_16x16x32_bf16 a[136:139], v[48:51], v[88:91], a[136:139]\n"
    "v_accvgpr_read_b32 v109, a125\n"
    "v_accvgpr_read_b32 v110, a126\n"
    "v_mfma_f32_16x16x32_bf16 a[140:143], v[36:39], v[76:79], a[140:143]\n"
    "v_accvgpr_read_b32 v111, a127\n"
    "v_cvt_pk_bf16_f32 v104, v104, v105\n"
    "v_mfma_f32_16x16x32_bf16 a[140:143], v[48:51], v[92:95], a[140:143]\n"
    "s_waitcnt lgkmcnt(2)\n"
    "v_cvt_pk_bf16_f32 v105, v106, v107\n"
    "v_cvt_pk_bf16_f32 v106, v108, v109\n"
    "v_mfma_f32_16x16x32_bf16 a[128:131], v[52:55], a[160:163], a[128:131]\n"
    "s_waitcnt lgkmcnt(0)\n"
    "v_cvt_pk_bf16_f32 v107, v110, v111\n"
    "v_mfma_f32_16x16x32_bf16 a[128:131], v[56:59], a[176:179], a[128:131]\n"
    "buffer_store_dwordx4 v[104:107], v0, s[72:75], s15 offen offset:3072 sc1\n"
    "v_mfma_f32_16x16x32_bf16 a[132:135], v[52:55], a[164:167], a[132:135]\n"
    "v_mfma_f32_16x16x32_bf16 a[132:135], v[56:59], a[180:183], a[132:135]\n"
    "v_mfma_f32_16x16x32_bf16 a[136:139], v[52:55], a[168:171], a[136:139]\n"
    "v_mfma_f32_16x16x32_bf16 a[136:139], v[56:59], a[184:187], a[136:139]\n"
    "v_mfma_f32_16x16x32_bf16 a[140:143], v[52:55], a[172:175], a[140:143]\n"
    "v_mfma_f32_16x16x32_bf16 a[140:143], v[56:59], a[188:191], a[140:143]\n"
    "v_add_u32 v46, 1, v46\n"
    "s_waitcnt vmcnt(4)\n"
    "buffer_store_dwordx2 v[46:47], v11, s[76:79], 0 offen sc1\n"
    "L_FIN1%=:\n"
    "s_add_u32 s14, s14, 4096\n"
    "v_accvgpr_read_b32 v96, a128\n"
    "v_accvgpr_read_b32 v97, a129\n"
    "v_accvgpr_read_b32 v98, a130\n"
    "v_accvgpr_read_b32 v99, a131\n"
    "v_accvgpr_read_b32 v100, a132\n"
    "v_accvgpr_read_b32 v101, a133\n"
    "v_accvgpr_read_b32 v102, a134\n"
    "v_accvgpr_read_b32 v103, a135\n"
    "v_cvt_pk_bf16_f32 v96, v96, v97\n"
    "v_cvt_pk_bf16_f32 v97, v98, v99\n"
    "v_cvt_pk_bf16_f32 v98, v100, v101\n"
    "v_cvt_pk_bf16_f32 v99, v102, v103\n"
    "buffer_store_dwordx4 v[96:99], v0, s[72:75], s14 offen offset:0 sc1\n"
    "s_add_u32 s15, s15, 4096\n"
    "v_accvgpr_read_b32 v104, a136\n"
    "v_accvgpr_read_b32 v105, a137\n"
    "v_accvgpr_read_b32 v106, a138\n"
    "v_accvgpr_read_b32 v107, a139\n"
    "v_accvgpr_read_b32 v108, a140\n"
    "v_accvgpr_read_b32 v109, a141\n"
    "v_accvgpr_read_b32 v110, a142\n"
    "v_accvgpr_read_b32 v111, a143\n"
    "v_cvt_pk_bf16_f32 v104, v104, v105\n"
    "v_cvt_pk_bf16_f32 v105, v106, v107\n"
    "v_cvt_pk_bf16_f32 v106, v108, v109\n"
    "v_cvt_pk_bf16_f32 v107, v110, v111\n"
    "buffer_store_dwordx4 v[104:107], v0, s[72:75], s15 offen offset:0 sc1\n"
    "v_add_u32 v46, 1, v46\n"
    "buffer_load_dwordx2 v[14:15], v1, s[76:79], s13 offen sc1\n"
    "s_barrier\n"
    "ds_read_b128 a[48:51], v8 offset:0\n"
    "ds_read_b128 a[64:67], v8 offset:1024\n"
    "ds_read_b128 a[80:83], v8 offset:2048\n"
    "s_waitcnt vmcnt(1)\n"
    "buffer_store_dwordx2 v[46:47], v11, s[76:79], 0 offen sc1\n"
    "s_cmp_eq_u32 s51, 3\n"
    "s_cbranch_scc1 L_END%=\n"
    "s_waitcnt vmcnt(1)\n"
    "v_subrev_u32 v14, s38, v14\n"
    "v_xor_b32 v15, v47, v15\n"
    "v_cmp_lt_u64 vcc, 15, v[14:15]\n"
    "s_nop 0\n"
    "s_and_b64 s[98:99], vcc, s[58:59]\n"
    "s_cbranch_scc1 L_SP7_%=\n"
    "L_BK7_%=:\n"
    "s_add_u32 s92, s39, s52\n"
    "buffer_load_dwordx4 a[0:3], v0, s[72:75], s92 offen offset:0 sc1\n"
    "buffer_load_dwordx4 a[16:19], v0, s[72:75], s92 offen offset:1024 sc1\n"
    "buffer_load_dwordx4 a[32:35], v0, s[72:75], s92 offen offset:2048 sc1\n"
    "s_add_u32 s92, s39, s53\n"
    "buffer_load_dwordx4 a[4:7], v0, s[72:75], s92 offen offset:0 sc1\n"
    "buffer_load_dwordx4 a[20:23], v0, s[72:75], s92 offen offset:1024 sc1\n"
    "buffer_load_dwordx4 a[36:39], v0, s[72:75], s92 offen offset:2048 sc1\n"
    "s_add_u32 s92, s39, s54\n"
    "buffer_load_dwordx4 a[8:11], v0, s[72:75], s92 offen offset:0 sc1\n"
    "buffer_load_dwordx4 a[24:27], v0, s[72:75], s92 offen offset:1024 sc1\n"
    "buffer_load_dwordx4 a[40:43], v0, s[72:75], s92 offen offset:2048 sc1\n"
    "s_add_u32 s92, s39, s55\n"
    "buffer_load_dwordx4 a[12:15], v0, s[72:75], s92 offen offset:0 sc1\n"
    "buffer_load_dwordx4 a[28:31], v0, s[72:75], s92 offen offset:1024 sc1\n"
    "buffer_load_dwordx4 a[44:47], v0, s[72:75], s92 offen offset:2048 sc1\n"
    "s_add_u32 s39, s39, 9216\n"
    "s_and_b64 s[98:99], vcc, s[56:57]\n"
    "s_cselect_b32 s48, 0x40000000, s39\n"
    "s_add_u32 s92, s48, s53\n"
    "buffer_load_dwordx4 a[52:55], v0, s[72:75], s92 offen offset:0 sc1\n"
    "buffer_load_dwordx4 a[68:71], v0, s[72:75], s92 offen offset:1024 sc1\n"
    "buffer_load_dwordx4 a[84:87], v0, s[72:75], s92 offen offset:2048 sc1\n"
    "s_add_u32 s92, s48, s54\n"
    "buffer_load_dwordx4 a[56:59], v0, s[72:75], s92 offen offset:0 sc1\n"
    "buffer_load_dwordx4 a[72:75], v0, s[72:75], s92 offen offset:1024 sc1\n"
    "buffer_load_dwordx4 a[88:91], v0, s[72:75], s92 offen offset:2048 sc1\n"
    "s_add_u32 s92, s48, s55\n"
    "buffer_load_dwordx4 a[60:63], v0, s[72:75], s92 offen offset:0 sc1\n"
    "buffer_load_dwordx4 a[76:79], v0, s[72:75], s92 offen offset:1024 sc1\n"
    "buffer_load_dwordx4 a[92:95], v0, s[72:75], s92 offen offset:2048 sc1\n"
    "buffer_load_dwordx2 v[14:15], v1, s[76:79], s13 offen sc1\n"
    "s_waitcnt vmcnt(21)\n"
    "v_mfma_f32_16x16x32_bf16 v[64:67], v[40:43], a[0:3], 0\n"
    "v_lshlrev_b32 v16, 16, v112\n"
    "v_mfma_f32_16x16x32_bf16 v[68:71], v[42:45], a[0:3], 0\n"
    "s_waitcnt vmcnt(20)\n"
    "v_and_b32 v17, 0xffff0000, v112\n"
    "v_mfma_f32_16x16x32_bf16 v[72:75], v[40:43], a[16:19], 0\n"
    "v_lshlrev_b32 v18, 16, v113\n"
    "v_mfma_f32_16x16x32_bf16 v[76:79], v[42:45], a[16:19], 0\n"
    "s_waitcnt vmcnt(19)\n"
    "v_and_b32 v19, 0xffff0000, v113\n"
    "v_mfma_f32_16x16x32_bf16 v[80:83], v[40:43], a[32:35], 0\n"
    "v_lshlrev_b32 v20, 16, v114\n"
    "v_mfma_f32_16x16x32_bf16 v[84:87], v[42:45], a[32:35], 0\n"
    "s_waitcnt vmcnt(18)\n"
    "v_and_b32 v21, 0xffff0000, v114\n"
    "v_mfma_f32_16x16x32_bf16 v[64:67], v[40:43], a[4:7], v[64:67]\n"
    "v_lshlrev_b32 v22, 16, v115\n"
    "v_mfma_f32_16x16x32_bf16 v[68:71], v[42:45], a[4:7], v[68:71]\n"
    "s_waitcnt vmcnt(17)\n"
    "v_and_b32 v23, 0xffff0000, v115\n"
    "v_mfma_f32_16x16x32_bf16 v[72:75], v[40:43], a[20:23], v[72:75]\n"
    "v_lshlrev_b32 v24, 16, v116\n"
    "v_mfma_f32_16x16x32_bf16 v[76:79], v[42:45], a[20:23], v[76:79]\n"
    "s_waitcnt vmcnt(16)\n"
    "v_and_b32 v25, 0xffff0000, v116\n"
    "v_mfma_f32_16x16x32_bf16 v[80:83], v[40:43], a[36:39], v[80:83]\n"
    "v_lshlrev_b32 v26, 16, v117\n"
    "v_mfma_f32_16x16x32_bf16 v[84:87], v[42:45], a[36:39], v[84:87]\n"
    "s_waitcnt vmcnt(15)\n"
    "v_and_b32 v27, 0xffff0000, v117\n"
    "v_mfma_f32_16x16x32_bf16 v[64:67], v[40:43], a[8:11], v[64:67]\n"
    "v_lshlrev_b32 v28, 16, v118\n"
    "v_mfma_f32_16x16x32_bf16 v[68:71], v[42:45], a[8:11], v[68:71]\n"
    "s_waitcnt vmcnt(14)\n"
    "v_and_b32 v29, 0xffff0000, v118\n"
    "v_mfma_f32_16x16x32_bf16 v[72:75], v[40:43], a[24:27], v[72:75]\n"
    "v_lshlrev_b32 v30, 16, v119\n"
    "v_mfma_f32_16x16x32_bf16 v[76:79], v[42:45], a[24:27], v[76:79]\n"
    "s_waitcnt vmcnt(13)\n"
    "v_and_b32 v31, 0xffff0000, v119\n"
    "v_mfma_f32_16x16x32_bf16 v[80:83], v[40:43], a[40:43], v[80:83]\n"
    "v_lshlrev_b32 v32, 16, v120\n"
    "v_mfma_f32_16x16x32_bf16 v[84:87], v[42:45], a[40:43], v[84:87]\n"
    "s_waitcnt vmcnt(12)\n"
    "v_and_b32 v33, 0xffff0000, v120\n"
    "v_mfma_f32_16x16x32_bf16 v[64:67], v[40:43], a[12:15], v[64:67]\n"
    "v_lshlrev_b32 v34, 16, v121\n"
    "v_mfma_f32_16x16x32_bf16 v[68:71], v[42:45], a[12:15], v[68:71]\n"
    "s_waitcnt vmcnt(11)\n"
    "v_and_b32 v35, 0xffff0000, v121\n"
    "v_mfma_f32_16x16x32_bf16 v[72:75], v[40:43], a[28:31], v[72:75]\n"
    "v_lshlrev_b32 v36, 16, v122\n"
    "v_mfma_f32_16x16x32_bf16 v[76:79], v[42:45], a[28:31], v[76:79]\n"
    "s_waitcnt vmcnt(10)\n"
    "v_and_b32 v37, 0xffff0000, v122\n"
    "v_mfma_f32_16x16x32_bf16 v[80:83], v[40:43], a[44:47], v[80:83]\n"
    "v_lshlrev_b32 v38, 16, v123\n"
    "v_mfma_f32_16x16x32_bf16 v[84:87], v[42:45], a[44:47], v[84:87]\n"
    "v_and_b32 v39, 0xffff0000, v123\n"
    "v_mul_f32 v152, 0xbfb8aa3b, v64\n"
    "v_mul_f32 v153, 0xbfb8aa3b, v65\n"
    "v_mul_f32 v154, 0xbfb8aa3b, v66\n"
    "v_mul_f32 v155, 0xbfb8aa3b, v67\n"
    "v_mul_f32 v156, 0xbfb8aa3b, v68\n"
    "v_mul_f32 v157, 0xbfb8aa3b, v69\n"
    "v_mul_f32 v158, 0xbfb8aa3b, v70\n"
    "v_mul_f32 v159, 0xbfb8aa3b, v71\n"
    "v_mul_f32 v160, 0xbfb8aa3b, v72\n"
    "v_mul_f32 v161, 0xbfb8aa3b, v73\n"
    "v_mul_f32 v162, 0xbfb8aa3b, v74\n"
    "v_mul_f32 v163, 0xbfb8aa3b, v75\n"
    "v_mul_f32 v164, 0xbfb8aa3b, v76\n"
    "v_mul_f32 v165, 0xbfb8aa3b, v77\n"
    "v_mul_f32 v166, 0xbfb8aa3b, v78\n"
    "v_mul_f32 v167, 0xbfb8aa3b, v79\n"
    "v_mul_f32 v168, 0xbfb8aa3b, v80\n"
    "v_mul_f32 v169, 0xbfb8aa3b, v81\n"
    "v_mul_f32 v170, 0xbfb8aa3b, v82\n"
    "v_mul_f32 v171, 0xbfb8aa3b, v83\n"
    "v_mul_f32 v172, 0xbfb8aa3b, v84\n"
    "v_mul_f32 v173, 0xbfb8aa3b, v85\n"
    "v_mul_f32 v174, 0xbfb8aa3b, v86\n"
    "v_mul_f32 v175, 0xbfb8aa3b, v87\n"
    "v_exp_f32 v152, v152\n"
    "v_exp_f32 v153, v153\n"
    "v_exp_f32 v154, v154\n"
    "v_exp_f32 v155, v155\n"
    "v_exp_f32 v156, v156\n"
    "v_exp_f32 v157, v157\n"
    "v_exp_f32 v158, v158\n"
    "v_exp_f32 v159, v159\n"
    "v_exp_f32 v160, v160\n"
    "v_exp_f32 v161, v161\n"
    "v_exp_f32 v162, v162\n"
    "v_exp_f32 v163, v163\n"
    "v_exp_f32 v164, v164\n"
    "v_exp_f32 v165, v165\n"
    "v_exp_f32 v166, v166\n"
    "v_exp_f32 v167, v167\n"
    "v_exp_f32 v168, v168\n"
    "v_exp_f32 v169, v169\n"
    "v_exp_f32 v170, v170\n"
    "v_exp_f32 v171, v171\n"
    "v_exp_f32 v172, v172\n"
    "v_exp_f32 v173, v173\n"
    "v_exp_f32 v174, v174\n"
    "v_exp_f32 v175, v175\n"
    "v_add_f32 v152, 1.0, v152\n"
    "v_add_f32 v153, 1.0, v153\n"
    "v_add_f32 v154, 1.0, v154\n"
    "v_add_f32 v155, 1.0, v155\n"
    "v_add_f32 v156, 1.0, v156\n"
    "v_add_f32 v157, 1.0, v157\n"
    "v_add_f32 v158, 1.0, v158\n"
    "v_add_f32 v159, 1.0, v159\n"
    "v_add_f32 v160, 1.0, v160\n"
    "v_add_f32 v161, 1.0, v161\n"
    "v_add_f32 v162, 1.0, v162\n"
    "v_add_f32 v163, 1.0, v163\n"
    "v_add_f32 v164, 1.0, v164\n"
    "v_add_f32 v165, 1.0, v165\n"
    "v_add_f32 v166, 1.0, v166\n"
    "v_add_f32 v167, 1.0, v167\n"
    "v_add_f32 v168, 1.0, v168\n"
    "v_add_f32 v169, 1.0, v169\n"
    "v_add_f32 v170, 1.0, v170\n"
    "v_add_f32 v171, 1.0, v171\n"
    "v_add_f32 v172, 1.0, v172\n"
    "v_add_f32 v173, 1.0, v173\n"
    "v_add_f32 v174, 1.0, v174\n"
    "v_add_f32 v175, 1.0, v175\n"
    "v_rcp_f32 v152, v152\n"
    "v_rcp_f32 v153, v153\n"
    "v_rcp_f32 v154, v154\n"
    "v_rcp_f32 v155, v155\n"
    "v_rcp_f32 v156, v156\n"
    "v_rcp_f32 v157, v157\n"
    "v_rcp_f32 v158, v158\n"
    "v_rcp_f32 v159, v159\n"
    "v_rcp_f32 v160, v160\n"
    "v_rcp_f32 v161, v161\n"
    "v_rcp_f32 v162, v162\n"
    "v_rcp_f32 v163, v163\n"
    "v_rcp_f32 v164, v164\n"
    "v_rcp_f32 v165, v165\n"
    "v_rcp_f32 v166, v166\n"
    "v_rcp_f32 v167, v167\n"
    "v_rcp_f32 v168, v168\n"
    "v_rcp_f32 v169, v169\n"
    "v_rcp_f32 v170, v170\n"
    "v_rcp_f32 v171, v171\n"
    "v_rcp_f32 v172, v172\n"
    "v_rcp_f32 v173, v173\n"
    "v_rcp_f32 v174, v174\n"
    "v_rcp_f32 v175, v175\n"
    "v_mul_f32 v64, v64, v152\n"
    "v_mul_f32 v65, v65, v153\n"
    "v_mul_f32 v66, v66, v154\n"
    "v_mul_f32 v67, v67, v155\n"
    "v_mul_f32 v68, v68, v156\n"
    "v_mul_f32 v69, v69, v157\n"
    "v_mul_f32 v70, v70, v158\n"
    "v_mul_f32 v71, v71, v159\n"
    "v_mul_f32 v72, v72, v160\n"
    "v_mul_f32 v73, v73, v161\n"
    "v_mul_f32 v74, v74, v162\n"
    "v_mul_f32 v75, v75, v163\n"
    "v_mul_f32 v76, v76, v164\n"
    "v_mul_f32 v77, v77, v165\n"
    "v_mul_f32 v78, v78, v166\n"
    "v_mul_f32 v79, v79, v167\n"
    "v_mul_f32 v80, v80, v168\n"
    "v_mul_f32 v81, v81, v169\n"
    "v_mul_f32 v82, v82, v170\n"
    "v_mul_f32 v83, v83, v171\n"
    "v_mul_f32 v84, v84, v172\n"
    "v_mul_f32 v85, v85, v173\n"
    "v_mul_f32 v86, v86, v174\n"
    "v_mul_f32 v87, v87, v175\n"
    "s_cmp_eq_u32 s48, 0x40000000\n"
    "s_cbranch_scc0 L_SR1%=\n"
    "s_waitcnt vmcnt(0)\n"
    "v_subrev_u32 v14, s38, v14\n"
    "v_xor_b32 v15, v47, v15\n"
    "v_cmp_lt_u64 vcc, 15, v[14:15]\n"
    "s_nop 0\n"
    "s_and_b64 s[98:99], vcc, s[56:57]\n"
    "s_cbranch_scc1 L_SP8_%=\n"
    "L_BK8_%=:\n"
    "s_add_u32 s92, s39, s53\n"
    "buffer_load_dwordx4 a[52:55], v0, s[72:75], s92 offen offset:0 sc1\n"
    "buffer_load_dwordx4 a[68:71], v0, s[72:75], s92 offen offset:1024 sc1\n"
    "buffer_load_dwordx4 a[84:87], v0, s[72:75], s92 offen offset:2048 sc1\n"
    "s_add_u32 s92, s39, s54\n"
    "buffer_load_dwordx4 a[56:59], v0, s[72:75], s92 offen offset:0 sc1\n"
    "buffer_load_dwordx4 a[72:75], v0, s[72:75], s92 offen offset:1024 sc1\n"
    "buffer_load_dwordx4 a[88:91], v0, s[72:75], s92 offen offset:2048 sc1\n"
    "s_add_u32 s92, s39, s55\n"
    "buffer_load_dwordx4 a[60:63], v0, s[72:75], s92 offen offset:0 sc1\n"
    "buffer_load_dwordx4 a[76:79], v0, s[72:75], s92 offen offset:1024 sc1\n"
    "buffer_load_dwordx4 a[92:95], v0, s[72:75], s92 offen offset:2048 sc1\n"
    "L_SR1%=:\n"
    "s_waitcnt lgkmcnt(2)\n"
    "v_mfma_f32_16x16x32_bf16 v[128:131], v[40:43], a[48:51], 0\n"
    "v_mfma_f32_16x16x32_bf16 v[132:135], v[42:45], a[48:51], 0\n"
    "s_waitcnt lgkmcnt(1)\n"
    "v_mfma_f32_16x16x32_bf16 v[136:139], v[40:43], a[64:67], 0\n"
    "v_mfma_f32_16x16x32_bf16 v[140:143], v[42:45], a[64:67], 0\n"
    "s_waitcnt lgkmcnt(0)\n"
    "v_mfma_f32_16x16x32_bf16 v[144:147], v[40:43], a[80:83], 0\n"
    "v_mfma_f32_16x16x32_bf16 v[148:151], v[42:45], a[80:83], 0\n"
    "s_waitcnt vmcnt(8)\n"
    "v_mfma_f32_16x16x32_bf16 v[128:131], v[40:43], a[52:55], v[128:131]\n"
    "v_mfma_f32_16x16x32_bf16 v[132:135], v[42:45], a[52:55], v[132:135]\n"
    "s_waitcnt vmcnt(7)\n"
    "v_mfma_f32_16x16x32_bf16 v[136:139], v[40:43], a[68:71], v[136:139]\n"
    "v_mfma_f32_16x16x32_bf16 v[140:143], v[42:45], a[68:71], v[140:143]\n"
    "s_waitcnt vmcnt(6)\n"
    "v_mfma_f32_16x16x32_bf16 v[144:147], v[40:43], a[84:87], v[144:147]\n"
    "v_mfma_f32_16x16x32_bf16 v[148:151], v[42:45], a[84:87], v[148:151]\n"
    "s_waitcnt vmcnt(5)\n"
    "v_mfma_f32_16x16x32_bf16 v[128:131], v[40:43], a[56:59], v[128:131]\n"
    "v_mfma_f32_16x16x32_bf16 v[132:135], v[42:45], a[56:59], v[132:135]\n"
    "s_waitcnt vmcnt(4)\n"
    "v_mfma_f32_16x16x32_bf16 v[136:139], v[40:43], a[72:75], v[136:139]\n"
    "v_mfma_f32_16x16x32_bf16 v[140:143], v[42:45], a[72:75], v[140:143]\n"
    "s_waitcnt vmcnt(3)\n"
    "v_mfma_f32_16x16x32_bf16 v[144:147], v[40:43], a[88:91], v[144:147]\n"
    "v_mfma_f32_16x16x32_bf16 v[148:151], v[42:45], a[88:91], v[148:151]\n"
    "s_waitcnt vmcnt(2)\n"
    "v_mfma_f32_16x16x32_bf16 v[128:131], v[40:43], a[60:63], v[128:131]\n"
    "v_mfma_f32_16x16x32_bf16 v[132:135], v[42:45], a[60:63], v[132:135]\n"
    "s_waitcnt vmcnt(1)\n"
    "v_mfma_f32_16x16x32_bf16 v[136:139], v[40:43], a[76:79], v[136:139]\n"
    "v_mfma_f32_16x16x32_bf16 v[140:143], v[42:45], a[76:79], v[140:143]\n"
    "s_waitcnt vmcnt(0)\n"
    "v_mfma_f32_16x16x32_bf16 v[144:147], v[40:43], a[92:95], v[144:147]\n"
    "v_mfma_f32_16x16x32_bf16 v[148:151], v[42:45], a[92:95], v[148:151]\n"
    "s_nop 2\n"
    "v_fma_f32 v128, v128, v64, v16\n"
    "v_fma_f32 v129, v129, v65, v17\n"
    "v_fma_f32 v130, v130, v66, v18\n"
    "v_fma_f32 v131, v131, v67, v19\n"
    "v_fma_f32 v132, v132, v68, v20\n"
    "v_fma_f32 v133, v133, v69, v21\n"
    "v_fma_f32 v134, v134, v70, v22\n"
    "v_fma_f32 v135, v135, v71, v23\n"
    "v_cvt_pk_bf16_f32 v128, v128, v129\n"
    "v_cvt_pk_bf16_f32 v129, v130, v131\n"
    "v_cvt_pk_bf16_f32 v130, v132, v133\n"
    "v_cvt_pk_bf16_f32 v131, v134, v135\n"
    "buffer_store_dwordx2 v[128:129], v12, s[28:31], 0 offen offset:0\n"
    "buffer_store_dwordx2 v[130:131], v63, s[28:31], 0 offen offset:0\n"
    "v_fma_f32 v136, v136, v72, v24\n"
    "v_fma_f32 v137, v137, v73, v25\n"
    "v_fma_f32 v138, v138, v74, v26\n"
    "v_fma_f32 v139, v139, v75, v27\n"
    "v_fma_f32 v140, v140, v76, v28\n"
    "v_fma_f32 v141, v141, v77, v29\n"
    "v_fma_f32 v142, v142, v78, v30\n"
    "v_fma_f32 v143, v143, v79, v31\n"
    "v_cvt_pk_bf16_f32 v136, v136, v137\n"
    "v_cvt_pk_bf16_f32 v137, v138, v139\n"
    "v_cvt_pk_bf16_f32 v138, v140, v141\n"
    "v_cvt_pk_bf16_f32 v139, v142, v143\n"
    "buffer_store_dwordx2 v[136:137], v12, s[28:31], 0 offen offset:32\n"
    "buffer_store_dwordx2 v[138:139], v63, s[28:31], 0 offen offset:32\n"
    "v_fma_f32 v144, v144, v80, v32\n"
    "v_fma_f32 v145, v145, v81, v33\n"
    "v_fma_f32 v146, v146, v82, v34\n"
    "v_fma_f32 v147, v147, v83, v35\n"
    "v_fma_f32 v148, v148, v84, v36\n"
    "v_fma_f32 v149, v149, v85, v37\n"
    "v_fma_f32 v150, v150, v86, v38\n"
    "v_fma_f32 v151, v151, v87, v39\n"
    "v_cvt_pk_bf16_f32 v144, v144, v145\n"
    "v_cvt_pk_bf16_f32 v145, v146, v147\n"
    "v_cvt_pk_bf16_f32 v146, v148, v149\n"
    "v_cvt_pk_bf16_f32 v147, v150, v151\n"
    "buffer_store_dwordx2 v[144:145], v12, s[28:31], 0 offen offset:64\n"
    "buffer_store_dwordx2 v[146:147], v63, s[28:31], 0 offen offset:64\n"
    "s_branch L_END%=\n"
    "L_FB%=:\n"
    "s_waitcnt vmcnt(0)\n"
    "s_mov_b32 s93, 1\n"
    "s_lshl_b32 s92, s80, 12\n"
    "s_mov_b32 s88, 32\n"
    "v_mov_b32 v48, 0xff800000\n"
    "v_mov_b32 v52, 0xff800000\n"
    "v_mov_b32 v56, 0xff800000\n"
    "v_mov_b32 v60, 0xff800000\n"
    "v_bfe_u32 v13, v0, 4, 4\n"
    "v_lshrrev_b32 v4, 8, v0\n"
    "v_xor_b32 v4, v4, v13\n"
    "v_lshlrev_b32 v4, 4, v4\n"
    "v_lshl_add_u32 v4, v13, 8, v4\n"
    "v_add_u32 v3, 98304, v4\n"
    "v_lshrrev_b32 v13, 6, v0\n"
    "v_bfe_u32 v6, v0, 1, 5\n"
    "v_lshl_add_u32 v6, v13, 8, v6\n"
    "v_and_b32 v13, 7, v13\n"
    "v_lshl_add_u32 v6, v13, 5, v6\n"
    "v_add_u32 v5, 98304, v6\n"
    "s_waitcnt lgkmcnt(5)\n"
    "v_xor_b32 v16, 0, v3\n"
    "v_xor_b32 v17, 64, v3\n"
    "s_waitcnt lgkmcnt(4)\n"
    "v_xor_b32 v18, 128, v3\n"
    "v_xor_b32 v19, 192, v3\n"
    "s_waitcnt lgkmcnt(3)\n"
    "v_xor_b32 v20, 0, v4\n"
    "v_xor_b32 v21, 64, v4\n"
    "s_waitcnt lgkmcnt(2)\n"
    "v_xor_b32 v22, 128, v4\n"
    "v_xor_b32 v23, 192, v4\n"
    "s_waitcnt lgkmcnt(1)\n"
    "v_xor_b32 v24, 0, v5\n"
    "v_xor_b32 v25, 32, v5\n"
    "s_waitcnt lgkmcnt(0)\n"
    "v_xor_b32 v26, 64, v5\n"
    "v_xor_b32 v27, 96, v5\n"
    "v_xor_b32 v28, 128, v5\n"
    "v_xor_b32 v29, 160, v5\n"
    "v_xor_b32 v30, 192, v5\n"
    "v_xor_b32 v31, 224, v5\n"
    "v_xor_b32 v32, 0, v6\n"
    "v_xor_b32 v33, 32, v6\n"
    "v_xor_b32 v34, 64, v6\n"
    "v_xor_b32 v35, 96, v6\n"
    "v_xor_b32 v36, 128, v6\n"
    "v_xor_b32 v37, 160, v6\n"
    "v_xor_b32 v38, 192, v6\n"
    "v_xor_b32 v39, 224, v6\n"
    "v_lshlrev_b32 v12, 3, v0\n"
    "v_and_b32 v1, 0x70, v0\n"
    "v_lshlrev_b32 v1, 5, v1\n"
    "s_mov_b32 s46, 0\n"
    "L_FBL%=:\n"
    "s_barrier\n"
    "s_add_u32 m0, s86, 98304\n"
    "s_nop 0\n"
    "buffer_load_dwordx4 v40, s[56:59], s92 offen lds\n"
    "buffer_load_dwordx4 v41, s[56:59], s92 offen offset:1024 lds\n"
    "buffer_load_dwordx4 v42, s[56:59], s92 offen offset:2048 lds\n"
    "buffer_load_dwordx4 v43, s[56:59], s92 offen offset:3072 lds\n"
    "s_add_u32 m0, s86, 114688\n"
    "s_nop 0\n"
    "buffer_load_dwordx4 v44, s[60:63], s92 offen lds\n"
    "buffer_load_dwordx4 v45, s[60:63], s92 offen offset:1024 lds\n"
    "buffer_load_dwordx4 v44, s[60:63], s92 offen offset:2048 lds\n"
    "buffer_load_dwordx4 v45, s[60:63], s92 offen offset:3072 lds\n"
    "s_add_u32 s92, s92, 16384\n"
    "s_waitcnt vmcnt(0)\n"
    "s_barrier\n"
    "ds_read_b128 a[64:67], v16 offset:0\n"
    "ds_read_b128 a[68:71], v17 offset:0\n"
    "ds_read_b128 a[72:75], v18 offset:0\n"
    "ds_read_b128 a[76:79], v19 offset:0\n"
    "ds_read_b128 a[80:83], v16 offset:4096\n"
    "ds_read_b128 a[84:87], v17 offset:4096\n"
    "ds_read_b128 a[88:91], v18 offset:4096\n"
    "ds_read_b128 a[92:95], v19 offset:4096\n"
    "s_waitcnt lgkmcnt(7)\n"
    "v_mfma_f32_16x16x32_bf16 v[96:99], a[64:67], a[0:3], 0\n"
    "s_waitcnt lgkmcnt(6)\n"
    "v_mfma_f32_16x16x32_bf16 v[96:99], a[68:71], a[4:7], v[96:99]\n"
    "s_waitcnt lgkmcnt(5)\n"
    "v_mfma_f32_16x16x32_bf16 v[96:99], a[72:75], a[8:11], v[96:99]\n"
    "s_waitcnt lgkmcnt(4)\n"
    "v_mfma_f32_16x16x32_bf16 v[96:99], a[76:79], a[12:15], v[96:99]\n"
    "v_mfma_f32_16x16x32_bf16 v[104:107], a[64:67], a[16:19], 0\n"
    "v_mfma_f32_16x16x32_bf16 v[104:107], a[68:71], a[20:23], v[104:107]\n"
    "v_mfma_f32_16x16x32_bf16 v[104:107], a[72:75], a[24:27], v[104:107]\n"
    "v_mfma_f32_16x16x32_bf16 v[104:107], a[76:79], a[28:31], v[104:107]\n"
    "v_mfma_f32_16x16x32_bf16 v[112:115], a[64:67], a[32:35], 0\n"
    "v_mfma_f32_16x16x32_bf16 v[112:115], a[68:71], a[36:39], v[112:115]\n"
    "v_mfma_f32_16x16x32_bf16 v[112:115], a[72:75], a[40:43], v[112:115]\n"
    "v_mfma_f32_16x16x32_bf16 v[112:115], a[76:79], a[44:47], v[112:115]\n"
    "v_mfma_f32_16x16x32_bf16 v[120:123], a[64:67], a[48:51], 0\n"
    "v_mfma_f32_16x16x32_bf16 v[120:123], a[68:71], a[52:55], v[120:123]\n"
    "v_mfma_f32_16x16x32_bf16 v[120:123], a[72:75], a[56:59], v[120:123]\n"
    "v_mfma_f32_16x16x32_bf16 v[120:123], a[76:79], a[60:63], v[120:123]\n"
    "s_waitcnt lgkmcnt(3)\n"
    "v_mfma_f32_16x16x32_bf16 v[100:103], a[80:83], a[0:3], 0\n"
    "s_waitcnt lgkmcnt(2)\n"
    "v_mfma_f32_16x16x32_bf16 v[100:103], a[84:87], a[4:7], v[100:103]\n"
    "s_waitcnt lgkmcnt(1)\n"
    "v_mfma_f32_16x16x32_bf16 v[100:103], a[88:91], a[8:11], v[100:103]\n"
    "s_waitcnt lgkmcnt(0)\n"
    "v_mfma_f32_16x16x32_bf16 v[100:103], a[92:95], a[12:15], v[100:103]\n"
    "v_mfma_f32_16x16x32_bf16 v[108:111], a[80:83], a[16:19], 0\n"
    "v_mfma_f32_16x16x32_bf16 v[108:111], a[84:87], a[20:23], v[108:111]\n"
    "v_mfma_f32_16x16x32_bf16 v[108:111], a[88:91], a[24:27], v[108:111]\n"
    "v_mfma_f32_16x16x32_bf16 v[108:111], a[92:95], a[28:31], v[108:111]\n"
    "v_mfma_f32_16x16x32_bf16 v[116:119], a[80:83], a[32:35], 0\n"
    "v_mfma_f32_16x16x32_bf16 v[116:119], a[84:87], a[36:39], v[116:119]\n"
    "v_mfma_f32_16x16x32_bf16 v[116:119], a[88:91], a[40:43], v[116:119]\n"
    "v_mfma_f32_16x16x32_bf16 v[116:119], a[92:95], a[44:47], v[116:119]\n"
    "v_mfma_f32_16x16x32_bf16 v[124:127], a[80:83], a[48:51], 0\n"
    "v_mfma_f32_16x16x32_bf16 v[124:127], a[84:87], a[52:55], v[124:127]\n"
    "v_mfma_f32_16x16x32_bf16 v[124:127], a[88:91], a[56:59], v[124:127]\n"
    "v_mfma_f32_16x16x32_bf16 v[124:127], a[92:95], a[60:63], v[124:127]\n"
    "v_max3_f32 v48, v48, v96, v97\n"
    "v_max3_f32 v48, v48, v98, v99\n"
    "v_max3_f32 v48, v48, v100, v101\n"
    "v_max3_f32 v48, v48, v102, v103\n"
    "v_max3_f32 v52, v52, v104, v105\n"
    "v_max3_f32 v52, v52, v106, v107\n"
    "v_max3_f32 v52, v52, v108, v109\n"
    "v_max3_f32 v52, v52, v110, v111\n"
    "v_max3_f32 v56, v56, v112, v113\n"
    "v_max3_f32 v56, v56, v114, v115\n"
    "v_max3_f32 v56, v56, v116, v117\n"
    "v_max3_f32 v56, v56, v118, v119\n"
    "v_max3_f32 v60, v60, v120, v121\n"
    "v_max3_f32 v60, v60, v122, v123\n"
    "v_max3_f32 v60, v60, v124, v125\n"
    "v_max3_f32 v60, v60, v126, v127\n"
    "ds_read_b128 a[64:67], v16 offset:8192\n"
    "ds_read_b128 a[68:71], v17 offset:8192\n"
    "ds_read_b128 a[72:75], v18 offset:8192\n"
    "ds_read_b128 a[76:79], v19 offset:8192\n"
    "ds_read_b128 a[80:83], v16 offset:12288\n"
    "ds_read_b128 a[84:87], v17 offset:12288\n"
    "ds_read_b128 a[88:91], v18 offset:12288\n"
    "ds_read_b128 a[92:95], v19 offset:12288\n"
    "s_waitcnt lgkmcnt(7)\n"
    "v_mfma_f32_16x16x32_bf16 v[96:99], a[64:67], a[0:3], 0\n"
    "s_waitcnt lgkmcnt(6)\n"
    "v_mfma_f32_16x16x32_bf16 v[96:99], a[68:71], a[4:7], v[96:99]\n"
    "s_waitcnt lgkmcnt(5)\n"
    "v_mfma_f32_16x16x32_bf16 v[96:99], a[72:75], a[8:11], v[96:99]\n"
    "s_waitcnt lgkmcnt(4)\n"
    "v_mfma_f32_16x16x32_bf16 v[96:99], a[76:79], a[12:15], v[96:99]\n"
    "v_mfma_f32_16x16x32_bf16 v[104:107], a[64:67], a[16:19], 0\n"
    "v_mfma_f32_16x16x32_bf16 v[104:107], a[68:71], a[20:23], v[104:107]\n"
    "v_mfma_f32_16x16x32_bf16 v[104:107], a[72:75], a[24:27], v[104:107]\n"
    "v_mfma_f32_16x16x32_bf16 v[104:107], a[76:79], a[28:31], v[104:107]\n"
    "v_mfma_f32_16x16x32_bf16 v[112:115], a[64:67], a[32:35], 0\n"
    "v_mfma_f32_16x16x32_bf16 v[112:115], a[68:71], a[36:39], v[112:115]\n"
    "v_mfma_f32_16x16x32_bf16 v[112:115], a[72:75], a[40:43], v[112:115]\n"
    "v_mfma_f32_16x16x32_bf16 v[112:115], a[76:79], a[44:47], v[112:115]\n"
    "v_mfma_f32_16x16x32_bf16 v[120:123], a[64:67], a[48:51], 0\n"
    "v_mfma_f32_16x16x32_bf16 v[120:123], a[68:71], a[52:55], v[120:123]\n"
    "v_mfma_f32_16x16x32_bf16 v[120:123], a[72:75], a[56:59], v[120:123]\n"
    "v_mfma_f32_16x16x32_bf16 v[120:123], a[76:79], a[60:63], v[120:123]\n"
    "s_waitcnt lgkmcnt(3)\n"
    "v_mfma_f32_16x16x32_bf16 v[100:103], a[80:83], a[0:3], 0\n"
    "s_waitcnt lgkmcnt(2)\n"
    "v_mfma_f32_16x16x32_bf16 v[100:103], a[84:87], a[4:7], v[100:103]\n"
    "s_waitcnt lgkmcnt(1)\n"
    "v_mfma_f32_16x16x32_bf16 v[100:103], a[88:91], a[8:11], v[100:103]\n"
    "s_waitcnt lgkmcnt(0)\n"
    "v_mfma_f32_16x16x32_bf16 v[100:103], a[92:95], a[12:15], v[100:103]\n"
    "v_mfma_f32_16x16x32_bf16 v[108:111], a[80:83], a[16:19], 0\n"
    "v_mfma_f32_16x16x32_bf16 v[108:111], a[84:87], a[20:23], v[108:111]\n"
    "v_mfma_f32_16x16x32_bf16 v[108:111], a[88:91], a[24:27], v[108:111]\n"
    "v_mfma_f32_16x16x32_bf16 v[108:111], a[92:95], a[28:31], v[108:111]\n"
    "v_mfma_f32_16x16x32_bf16 v[116:119], a[80:83], a[32:35], 0\n"
    "v_mfma_f32_16x16x32_bf16 v[116:119], a[84:87], a[36:39], v[116:119]\n"
    "v_mfma_f32_16x16x32_bf16 v[116:119], a[88:91], a[40:43], v[116:119]\n"
    "v_mfma_f32_16x16x32_bf16 v[116:119], a[92:95], a[44:47], v[116:119]\n"
    "v_mfma_f32_16x16x32_bf16 v[124:127], a[80:83], a[48:51], 0\n"
    "v_mfma_f32_16x16x32_bf16 v[124:127], a[84:87], a[52:55], v[124:127]\n"
    "v_mfma_f32_16x16x32_bf16 v[124:127], a[88:91], a[56:59], v[124:127]\n"
    "v_mfma_f32_16x16x32_bf16 v[124:127], a[92:95], a[60:63], v[124:127]\n"
    "v_max3_f32 v48, v48, v96, v97\n"
    "v_max3_f32 v48, v48, v98, v99\n"
    "v_max3_f32 v48, v48, v100, v101\n"
    "v_max3_f32 v48, v48, v102, v103\n"
    "v_max3_f32 v52, v52, v104, v105\n"
    "v_max3_f32 v52, v52, v106, v107\n"
    "v_max3_f32 v52, v52, v108, v109\n"
    "v_max3_f32 v52, v52, v110, v111\n"
    "v_max3_f32 v56, v56, v112, v113\n"
    "v_max3_f32 v56, v56, v114, v115\n"
    "v_max3_f32 v56, v56, v116, v117\n"
    "v_max3_f32 v56, v56, v118, v119\n"
    "v_max3_f32 v60, v60, v120, v121\n"
    "v_max3_f32 v60, v60, v122, v123\n"
    "v_max3_f32 v60, v60, v124, v125\n"
    "v_max3_f32 v60, v60, v126, v127\n"
    "s_sub_u32 s88, s88, 1\n"
    "s_cmp_lg_u32 s88, 0\n"
    "s_cbranch_scc1 L_FBL%=\n"
    "v_mov_b32 v49, v48\n"
    "v_mov_b32 v53, v52\n"
    "v_mov_b32 v57, v56\n"
    "v_mov_b32 v61, v60\n"
    "v_permlane16_swap_b32 v48, v49\n"
    "v_permlane16_swap_b32 v52, v53\n"
    "v_permlane16_swap_b32 v56, v57\n"
    "v_permlane16_swap_b32 v60, v61\n"
    "v_max_f32 v48, v48, v49\n"
    "v_max_f32 v52, v52, v53\n"
    "v_max_f32 v56, v56, v57\n"
    "v_max_f32 v60, v60, v61\n"
    "v_mov_b32 v49, v48\n"
    "v_mov_b32 v53, v52\n"
    "v_mov_b32 v57, v56\n"
    "v_mov_b32 v61, v60\n"
    "v_permlane32_swap_b32 v48, v49\n"
    "v_permlane32_swap_b32 v52, v53\n"
    "v_permlane32_swap_b32 v56, v57\n"
    "v_permlane32_swap_b32 v60, v61\n"
    "v_max_f32 v48, v48, v49\n"
    "v_max_f32 v52, v52, v53\n"
    "v_max_f32 v56, v56, v57\n"
    "v_max_f32 v60, v60, v61\n"
    "v_sub_f32 v48, 0, v48\n"
    "v_mov_b32 v49, v48\n"
    "v_mov_b32 v50, v48\n"
    "v_mov_b32 v51, v48\n"
    "v_sub_f32 v52, 0, v52\n"
    "v_mov_b32 v53, v52\n"
    "v_mov_b32 v54, v52\n"
    "v_mov_b32 v55, v52\n"
    "v_sub_f32 v56, 0, v56\n"
    "v_mov_b32 v57, v56\n"
    "v_mov_b32 v58, v56\n"
    "v_mov_b32 v59, v56\n"
    "v_sub_f32 v60, 0, v60\n"
    "v_mov_b32 v61, v60\n"
    "v_mov_b32 v62, v60\n"
    "v_mov_b32 v63, v60\n"
    "s_lshl_b32 s92, s80, 12\n"
    "s_barrier\n"
    "s_add_u32 m0, s86, 98304\n"
    "s_nop 0\n"
    "buffer_load_dwordx4 v40, s[56:59], s92 offen lds\n"
    "buffer_load_dwordx4 v41, s[56:59], s92 offen offset:1024 lds\n"
    "buffer_load_dwordx4 v42, s[56:59], s92 offen offset:2048 lds\n"
    "buffer_load_dwordx4 v43, s[56:59], s92 offen offset:3072 lds\n"
    "s_add_u32 m0, s86, 114688\n"
    "s_nop 0\n"
    "buffer_load_dwordx4 v44, s[60:63], s92 offen lds\n"
    "buffer_load_dwordx4 v45, s[60:63], s92 offen offset:1024 lds\n"
    "buffer_load_dwordx4 v44, s[60:63], s92 offen offset:2048 lds\n"
    "buffer_load_dwordx4 v45, s[60:63], s92 offen offset:3072 lds\n"
    "s_add_u32 s92, s92, 16384\n"
    "s_add_u32 m0, s86, 131072\n"
    "s_nop 0\n"
    "buffer_load_dwordx4 v40, s[56:59], s92 offen lds\n"
    "buffer_load_dwordx4 v41, s[56:59], s92 offen offset:1024 lds\n"
    "buffer_load_dwordx4 v42, s[56:59], s92 offen offset:2048 lds\n"
    "buffer_load_dwordx4 v43, s[56:59], s92 offen offset:3072 lds\n"
    "s_add_u32 m0, s86, 147456\n"
    "s_nop 0\n"
    "buffer_load_dwordx4 v44, s[60:63], s92 offen lds\n"
    "buffer_load_dwordx4 v45, s[60:63], s92 offen offset:1024 lds\n"
    "buffer_load_dwordx4 v44, s[60:63], s92 offen offset:2048 lds\n"
    "buffer_load_dwordx4 v45, s[60:63], s92 offen offset:3072 lds\n"
    "s_add_u32 s92, s92, 16384\n"
    "s_add_u32 m0, s86, 32768\n"
    "s_nop 0\n"
    "buffer_load_dwordx4 v40, s[56:59], s92 offen lds\n"
    "buffer_load_dwordx4 v41, s[56:59], s92 offen offset:1024 lds\n"
    "buffer_load_dwordx4 v42, s[56:59], s92 offen offset:2048 lds\n"
    "buffer_load_dwordx4 v43, s[56:59], s92 offen offset:3072 lds\n"
    "s_add_u32 m0, s86, 49152\n"
    "s_nop 0\n"
    "buffer_load_dwordx4 v44, s[60:63], s92 offen lds\n"
    "buffer_load_dwordx4 v45, s[60:63], s92 offen offset:1024 lds\n"
    "buffer_load_dwordx4 v44, s[60:63], s92 offen offset:2048 lds\n"
    "buffer_load_dwordx4 v45, s[60:63], s92 offen offset:3072 lds\n"
    "s_add_u32 s92, s92, 16384\n"
    "s_waitcnt vmcnt(0)\n"
    "s_barrier\n"
    "ds_read_b128 a[64:67], v16 offset:0\n"
    "ds_read_b128 a[68:71], v17 offset:0\n"
    "ds_read_b128 a[72:75], v18 offset:0\n"
    "ds_read_b128 a[76:79], v19 offset:0\n"
    "ds_read_b128 a[80:83], v16 offset:4096\n"
    "ds_read_b128 a[84:87], v17 offset:4096\n"
    "ds_read_b128 a[88:91], v18 offset:4096\n"
    "ds_read_b128 a[92:95], v19 offset:4096\n"
    "s_waitcnt lgkmcnt(7)\n"
    "v_mfma_f32_16x16x32_bf16 v[96:99], a[64:67], a[0:3], 0\n"
    "s_waitcnt lgkmcnt(6)\n"
    "v_mfma_f32_16x16x32_bf16 v[96:99], a[68:71], a[4:7], v[96:99]\n"
    "s_waitcnt lgkmcnt(5)\n"
    "v_mfma_f32_16x16x32_bf16 v[96:99], a[72:75], a[8:11], v[96:99]\n"
    "s_waitcnt lgkmcnt(4)\n"
    "v_mfma_f32_16x16x32_bf16 v[96:99], a[76:79], a[12:15], v[96:99]\n"
    "v_mfma_f32_16x16x32_bf16 v[104:107], a[64:67], a[16:19], 0\n"
    "v_mfma_f32_16x16x32_bf16 v[104:107], a[68:71], a[20:23], v[104:107]\n"
    "v_mfma_f32_16x16x32_bf16 v[104:107], a[72:75], a[24:27], v[104:107]\n"
    "v_mfma_f32_16x16x32_bf16 v[104:107], a[76:79], a[28:31], v[104:107]\n"
    "v_mfma_f32_16x16x32_bf16 v[112:115], a[64:67], a[32:35], 0\n"
    "v_mfma_f32_16x16x32_bf16 v[112:115], a[68:71], a[36:39], v[112:115]\n"
    "v_mfma_f32_16x16x32_bf16 v[112:115], a[72:75], a[40:43], v[112:115]\n"
    "v_mfma_f32_16x16x32_bf16 v[112:115], a[76:79], a[44:47], v[112:115]\n"
    "v_mfma_f32_16x16x32_bf16 v[120:123], a[64:67], a[48:51], 0\n"
    "v_mfma_f32_16x16x32_bf16 v[120:123], a[68:71], a[52:55], v[120:123]\n"
    "v_mfma_f32_16x16x32_bf16 v[120:123], a[72:75], a[56:59], v[120:123]\n"
    "v_mfma_f32_16x16x32_bf16 v[120:123], a[76:79], a[60:63], v[120:123]\n"
    "s_waitcnt lgkmcnt(3)\n"
    "v_mfma_f32_16x16x32_bf16 v[100:103], a[80:83], a[0:3], 0\n"
    "s_waitcnt lgkmcnt(2)\n"
    "v_mfma_f32_16x16x32_bf16 v[100:103], a[84:87], a[4:7], v[100:103]\n"
    "s_waitcnt lgkmcnt(1)\n"
    "v_mfma_f32_16x16x32_bf16 v[100:103], a[88:91], a[8:11], v[100:103]\n"
    "s_waitcnt lgkmcnt(0)\n"
    "v_mfma_f32_16x16x32_bf16 v[100:103], a[92:95], a[12:15], v[100:103]\n"
    "v_mfma_f32_16x16x32_bf16 v[108:111], a[80:83], a[16:19], 0\n"
    "v_mfma_f32_16x16x32_bf16 v[108:111], a[84:87], a[20:23], v[108:111]\n"
    "v_mfma_f32_16x16x32_bf16 v[108:111], a[88:91], a[24:27], v[108:111]\n"
    "v_mfma_f32_16x16x32_bf16 v[108:111], a[92:95], a[28:31], v[108:111]\n"
    "v_mfma_f32_16x16x32_bf16 v[116:119], a[80:83], a[32:35], 0\n"
    "v_mfma_f32_16x16x32_bf16 v[116:119], a[84:87], a[36:39], v[116:119]\n"
    "v_mfma_f32_16x16x32_bf16 v[116:119], a[88:91], a[40:43], v[116:119]\n"
    "v_mfma_f32_16x16x32_bf16 v[116:119], a[92:95], a[44:47], v[116:119]\n"
    "v_mfma_f32_16x16x32_bf16 v[124:127], a[80:83], a[48:51], 0\n"
    "v_mfma_f32_16x16x32_bf16 v[124:127], a[84:87], a[52:55], v[124:127]\n"
    "v_mfma_f32_16x16x32_bf16 v[124:127], a[88:91], a[56:59], v[124:127]\n"
    "v_mfma_f32_16x16x32_bf16 v[124:127], a[92:95], a[60:63], v[124:127]\n"
    "ds_read_b128 a[64:67], v16 offset:8192\n"
    "ds_read_b128 a[68:71], v17 offset:8192\n"
    "ds_read_b128 a[72:75], v18 offset:8192\n"
    "ds_read_b128 a[76:79], v19 offset:8192\n"
    "ds_read_b128 a[80:83], v16 offset:12288\n"
    "ds_read_b128 a[84:87], v17 offset:12288\n"
    "ds_read_b128 a[88:91], v18 offset:12288\n"
    "ds_read_b128 a[92:95], v19 offset:12288\n"
    "s_branch L_PRO2%=\n"
    "L_SP0_%=:\n"
    "s_mov_b32 s96, 1048576\n"
    "L_SL0_%=:\n"
    "s_sleep 1\n"
    "buffer_load_dwordx2 v[14:15], v1, s[76:79], s12 offen sc1\n"
    "s_waitcnt vmcnt(0)\n"
    "v_subrev_u32 v14, s38, v14\n"
    "v_xor_b32 v15, v47, v15\n"
    "v_cmp_lt_u64 vcc, 15, v[14:15]\n"
    "s_nop 0\n"
    "s_cbranch_vccz L_BK0_%=\n"
    "s_sub_u32 s96, s96, 1\n"
    "s_cmp_lg_u32 s96, 0\n"
    "s_cbranch_scc1 L_SL0_%=\n"
    "s_branch L_BK0_%=\n"
    "L_SP1_%=:\n"
    "s_mov_b32 s96, 1048576\n"
    "L_SL1_%=:\n"
    "s_sleep 1\n"
    "buffer_load_dwordx2 v[14:15], v1, s[76:79], s12 offen sc1\n"
    "s_waitcnt vmcnt(0)\n"
    "v_subrev_u32 v14, s38, v14\n"
    "v_xor_b32 v15, v47, v15\n"
    "v_cmp_lt_u64 vcc, 15, v[14:15]\n"
    "s_nop 0\n"
    "s_and_b64 s[98:99], vcc, 15\n"
    "s_cbranch_scc0 L_BK1_%=\n"
    "s_sub_u32 s96, s96, 1\n"
    "s_cmp_lg_u32 s96, 0\n"
    "s_cbranch_scc1 L_SL1_%=\n"
    "s_branch L_BK1_%=\n"
    "L_SP2_%=:\n"
    "s_mov_b32 s96, 1048576\n"
    "L_SL2_%=:\n"
    "s_sleep 1\n"
    "buffer_load_dwordx2 v[14:15], v1, s[76:79], s12 offen sc1\n"
    "s_waitcnt vmcnt(0)\n"
    "v_subrev_u32 v14, s38, v14\n"
    "v_xor_b32 v15, v47, v15\n"
    "v_cmp_lt_u64 vcc, 15, v[14:15]\n"
    "s_nop 0\n"
    "s_cbranch_vccz L_BK2_%=\n"
    "s_sub_u32 s96, s96, 1\n"
    "s_cmp_lg_u32 s96, 0\n"
    "s_cbranch_scc1 L_SL2_%=\n"
    "s_branch L_BK2_%=\n"
    "L_SP3_%=:\n"
    "s_mov_b32 s96, 1048576\n"
    "L_SL3_%=:\n"
    "s_sleep 1\n"
    "buffer_load_dwordx2 v[14:15], v1, s[76:79], s12 offen sc1\n"
    "s_waitcnt vmcnt(0)\n"
    "v_subrev_u32 v14, s38, v14\n"
    "v_xor_b32 v15, v47, v15\n"
    "v_cmp_lt_u64 vcc, 15, v[14:15]\n"
    "s_nop 0\n"
    "s_cbranch_vccz L_BK3_%=\n"
    "s_sub_u32 s96, s96, 1\n"
    "s_cmp_lg_u32 s96, 0\n"
    "s_cbranch_scc1 L_SL3_%=\n"
    "s_branch L_BK3_%=\n"
    "L_SP4_%=:\n"
    "s_mov_b32 s96, 1048576\n"
    "L_SL4_%=:\n"
    "s_sleep 1\n"
    "buffer_load_dwordx2 v[14:15], v1, s[76:79], s12 offen sc1\n"
    "s_waitcnt vmcnt(0)\n"
    "v_subrev_u32 v14, s38, v14\n"
    "v_xor_b32 v15, v47, v15\n"
    "v_cmp_lt_u64 vcc, 15, v[14:15]\n"
    "s_nop 0\n"
    "s_cbranch_vccz L_BK4_%=\n"
    "s_sub_u32 s96, s96, 1\n"
    "s_cmp_lg_u32 s96, 0\n"
    "s_cbranch_scc1 L_SL4_%=\n"
    "s_branch L_BK4_%=\n"
    "L_SP5_%=:\n"
    "s_mov_b32 s96, 1048576\n"
    "L_SL5_%=:\n"
    "s_sleep 1\n"
    "buffer_load_dwordx2 v[14:15], v1, s[76:79], s13 offen sc1\n"
    "s_waitcnt vmcnt(0)\n"
    "v_subrev_u32 v14, s38, v14\n"
    "v_xor_b32 v15, v47, v15\n"
    "v_cmp_lt_u64 vcc, 15, v[14:15]\n"
    "s_nop 0\n"
    "s_and_b64 s[98:99], vcc, s[58:59]\n"
    "s_cbranch_scc0 L_BK5_%=\n"
    "s_sub_u32 s96, s96, 1\n"
    "s_cmp_lg_u32 s96, 0\n"
    "s_cbranch_scc1 L_SL5_%=\n"
    "s_branch L_BK5_%=\n"
    "L_SP6_%=:\n"
    "s_mov_b32 s96, 1048576\n"
    "L_SL6_%=:\n"
    "s_sleep 1\n"
    "buffer_load_dwordx2 v[14:15], v1, s[76:79], s13 offen sc1\n"
    "s_waitcnt vmcnt(0)\n"
    "v_subrev_u32 v14, s38, v14\n"
    "v_xor_b32 v15, v47, v15\n"
    "v_cmp_lt_u64 vcc, 15, v[14:15]\n"
    "s_nop 0\n"
    "s_and_b64 s[98:99], vcc, s[56:57]\n"
    "s_cbranch_scc0 L_BK6_%=\n"
    "s_sub_u32 s96, s96, 1\n"
    "s_cmp_lg_u32 s96, 0\n"
    "s_cbranch_scc1 L_SL6_%=\n"
    "s_branch L_BK6_%=\n"
    "L_SP7_%=:\n"
    "s_mov_b32 s96, 1048576\n"
    "L_SL7_%=:\n"
    "s_sleep 1\n"
    "buffer_load_dwordx2 v[14:15], v1, s[76:79], s13 offen sc1\n"
    "s_waitcnt vmcnt(0)\n"
    "v_subrev_u32 v14, s38, v14\n"
    "v_xor_b32 v15, v47, v15\n"
    "v_cmp_lt_u64 vcc, 15, v[14:15]\n"
    "s_nop 0\n"
    "s_and_b64 s[98:99], vcc, s[58:59]\n"
    "s_cbranch_scc0 L_BK7_%=\n"
    "s_sub_u32 s96, s96, 1\n"
    "s_cmp_lg_u32 s96, 0\n"
    "s_cbranch_scc1 L_SL7_%=\n"
    "s_branch L_BK7_%=\n"
    "L_SP8_%=:\n"
    "s_mov_b32 s96, 1048576\n"
    "L_SL8_%=:\n"
    "s_sleep 1\n"
    "buffer_load_dwordx2 v[14:15], v1, s[76:79], s13 offen sc1\n"
    "s_waitcnt vmcnt(0)\n"
    "v_subrev_u32 v14, s38, v14\n"
    "v_xor_b32 v15, v47, v15\n"
    "v_cmp_lt_u64 vcc, 15, v[14:15]\n"
    "s_nop 0\n"
    "s_and_b64 s[98:99], vcc, s[56:57]\n"
    "s_cbranch_scc0 L_BK8_%=\n"
    "s_sub_u32 s96, s96, 1\n"
    "s_cmp_lg_u32 s96, 0\n"
    "s_cbranch_scc1 L_SL8_%=\n"
    "s_branch L_BK8_%=\n"
    "L_END%=:\n"
    ::"s"(w),"s"(blockIdx.x),"s"(__builtin_amdgcn_kernarg_segment_ptr()):CLOB);
}

extern "C" void kernel_launch(void* const* d_in, const int* in_sizes, int n_in, void* d_out, int out_size, void* d_ws, size_t ws_size, hipStream_t stream){
  (void)hipFuncSetAttribute((const void*)k1, hipFuncAttributeMaxDynamicSharedMemorySize, 163840);
  k1<<<256,256,163840,stream>>>(d_ws,d_in[3],d_in[6],d_in[7],d_in[1],d_in[2],d_in[8],d_in[4],d_in[0],d_out);
}
```
